```python
import torch

def _softmax(x, axis=-1):
    return torch.softmax(x, dim=axis)
def _gelu(x):
    return torch.nn.functional.gelu(x, approximate="tanh")
def _silu(x):
    return torch.nn.functional.silu(x)
def _sigmoid(x):
    return torch.sigmoid(x)
def _layernorm(x, axis=-1, eps=1e-5):
    m = x.mean(dim=axis, keepdim=True)
    v = x.var(dim=axis, keepdim=True, unbiased=False)
    return (x - m) / torch.sqrt(v + eps)
def _scatter_add(tgt, idx, src, axis=0):
    return tgt.clone().index_add_(axis, idx, src)
def _segment_sum(src, seg, n):
    out = torch.zeros((n,) + src.shape[1:], dtype=src.dtype)
    return out.index_add_(0, seg, src)
def _pad(x, pw):
    flat = tuple(v for pair in reversed(pw) for v in pair)
    return torch.nn.functional.pad(x, flat)

kernel_name = 'interleave_layers__sliding_local_dilated__glu_gated_bilinear__prompt'

def setup_inputs():

    _w = (lambda a: (lambda t: t.to(torch.bfloat16) if t.is_floating_point() else t)(torch.as_tensor(a)))
    def _A_setup():
        B, S, D = 1, 2048, 640
        return {
            "x": _w((lambda *s: torch.randn(*s, dtype=torch.bfloat16) * 0.05)(B, S, D)),
            "wqkv": _w((lambda *s: torch.randn(*s, dtype=torch.bfloat16) * 0.05)(D, 3 * D)),
            "wo": _w((lambda *s: torch.randn(*s, dtype=torch.bfloat16) * 0.05)(D, D)),
        }
    _A_in = _A_setup()

    def _B_setup():
        B, S, D, F = 1, 2048, 640, 1728
        return {
            "x": _w((lambda *s: torch.randn(*s, dtype=torch.bfloat16) * 0.05)(B, S, D)),
            "w_gate": _w((lambda *s: torch.randn(*s, dtype=torch.bfloat16) * 0.8)(D, F) * (D ** -0.5)),
            "w_up": _w((lambda *s: torch.randn(*s, dtype=torch.bfloat16) * 0.8)(D, F) * (D ** -0.5)),
            "w_down": _w((lambda *s: torch.randn(*s, dtype=torch.bfloat16) * 0.8)(F, D) * (F ** -0.5)),
        }
    _B_in = _B_setup()

    return {
        "A_x": _A_in["x"],
        "A_wqkv": _A_in["wqkv"],
        "A_wo": _A_in["wo"],
        "B_x": _B_in["x"],
        "B_w_gate": _B_in["w_gate"],
        "B_w_up": _B_in["w_up"],
        "B_w_down": _B_in["w_down"],
    }

def reference(A_x, A_wqkv, A_wo, B_x, B_w_gate, B_w_up, B_w_down):
    def _A_ref(x, wqkv, wo):
        B, S, D = x.shape
        H = 32; Dh = D // H
        W, dil = 8, 2
        qkv = (lambda x, *d: x.permute(*d))((x @ wqkv).reshape(B, S, 3, H, Dh), 2, 0, 3, 1, 4)
        q, k, v = qkv[0], qkv[1], qkv[2]
        scale = Dh ** -0.5
        pos = torch.arange(S)
        off = torch.arange(W) * dil
        idx = pos[:, None] - off[None, :]
        mask = idx >= 0
        idx_c = torch.where(mask, idx, 0).reshape(-1)
        kg = k[:, :, idx_c, :].reshape(B, H, S, W, Dh)
        vg = v[:, :, idx_c, :].reshape(B, H, S, W, Dh)
        logits = torch.einsum("bhsd,bhswd->bhsw", q, kg) * scale
        logits = torch.where(mask[None, None, :, :], logits, -1e9)
        attn = _softmax(logits, axis=-1)
        o = torch.einsum("bhsw,bhswd->bhsd", attn, vg)
        return (lambda x, *d: x.permute(*d))(o, 0, 2, 1, 3).reshape(B, S, D) @ wo

    def _B_ref(x, w_gate, w_up, w_down):
        g = torch.einsum("bsd,df->bsf", x, w_gate)
        u = torch.einsum("bsd,df->bsf", x, w_up)
        h = g * u
        return torch.einsum("bsf,fd->bsd", h, w_down)

    x = A_x
    x = x + _A_ref(_layernorm(x, axis=-1), A_wqkv, A_wo)
    x = x + _B_ref(_layernorm(x, axis=-1), B_w_gate, B_w_up, B_w_down)
    x = x + _A_ref(_layernorm(x, axis=-1), A_wqkv, A_wo)
    x = x + _B_ref(_layernorm(x, axis=-1), B_w_gate, B_w_up, B_w_down)
    return x
```

```cpp
#include <hip/hip_runtime.h>
#include <hip/hip_bf16.h>
typedef unsigned short u16;
__device__ __forceinline__ float b2f(u16 b){ return __uint_as_float(((unsigned)b)<<16); }
__device__ __forceinline__ u16 f2b(float f){ unsigned u=__float_as_uint(f); u += 0x7FFFu + ((u>>16)&1u); return (u16)(u>>16); }
constexpr int S=2048, D=640, F=1728, H=32, DH=20;

__global__ void ln_k(const u16* x, u16* y){
  int row=blockIdx.x; int t=threadIdx.x;
  float v[10]; float s=0;
  for(int i=0;i<10;i++){ v[i]=b2f(x[row*D+t+64*i]); s+=v[i]; }
  for(int o=32;o>=1;o>>=1) s+=__shfl_xor(s,o);
  float m=s/D; float q=0;
  for(int i=0;i<10;i++){ float d=v[i]-m; q+=d*d; }
  for(int o=32;o>=1;o>>=1) q+=__shfl_xor(q,o);
  float r=1.0f/sqrtf(q/D+1e-5f);
  for(int i=0;i<10;i++) y[row*D+t+64*i]=f2b((v[i]-m)*r);
}
__global__ void gemm_k(const u16* A,const u16* B,u16* C,const u16* res,int M,int N,int K,int mode){
  __shared__ float As[16][17], Bs[16][17];
  int tx=threadIdx.x&15, ty=threadIdx.x>>4;
  int row=blockIdx.y*16+ty, col=blockIdx.x*16+tx;
  float acc=0;
  for(int k0=0;k0<K;k0+=16){
    As[ty][tx]=b2f(A[row*K+k0+tx]);
    Bs[ty][tx]=b2f(B[(k0+ty)*N+col]);
    __syncthreads();
    for(int k=0;k<16;k++) acc+=As[ty][k]*Bs[k][tx];
    __syncthreads();
  }
  if(mode==0) C[row*N+col]=f2b(acc);
  else C[row*N+col]=f2b(b2f(res[row*N+col])+b2f(f2b(acc)));
}
__global__ void attn_k(const u16* qkv,u16* o){
  int idx=blockIdx.x*blockDim.x+threadIdx.x;
  int t=idx/H, h=idx%H;
  float q[DH]; for(int d=0;d<DH;d++) q[d]=b2f(qkv[t*3*D+h*DH+d]);
  float l[8]; float mx=-1e30f;
  for(int w=0;w<8;w++){ int p=t-2*w; if(p>=0){ float s=0; for(int d=0;d<DH;d++) s+=q[d]*b2f(qkv[p*3*D+D+h*DH+d]); l[w]=s*0.22360679775f; } else l[w]=-1e9f; mx=fmaxf(mx,l[w]); }
  float sum=0; for(int w=0;w<8;w++){ l[w]=__expf(l[w]-mx); sum+=l[w]; }
  float inv=1.0f/sum;
  for(int d=0;d<DH;d++){ float a=0; for(int w=0;w<8;w++){ int p=t-2*w; if(p>=0) a+=l[w]*inv*b2f(qkv[p*3*D+2*D+h*DH+d]); } o[t*D+h*DH+d]=f2b(a); }
}
__global__ void mul_k(const u16* g,const u16* u,u16* h,int n){ int i=blockIdx.x*blockDim.x+threadIdx.x; if(i<n) h[i]=f2b(b2f(g[i])*b2f(u[i])); }

extern "C" void kernel_launch(void* const* d_in, const int* in_sizes, int n_in,
                              void* d_out, int out_size, void* d_ws, size_t ws_size,
                              hipStream_t stream) {
  const u16* x0=(const u16*)d_in[0]; const u16* wqkv=(const u16*)d_in[1]; const u16* wo=(const u16*)d_in[2];
  const u16* wg=(const u16*)d_in[4]; const u16* wu=(const u16*)d_in[5]; const u16* wd=(const u16*)d_in[6];
  u16* ws=(u16*)d_ws;
  u16* xn=ws; u16* qkv=xn+S*D; u16* o=qkv+S*3*D; u16* x1=o+S*D; u16* g=x1+S*D; u16* u=g+S*F; u16* h=u+S*F; u16* x2=h+S*F;
  const u16* x=x0;
  for(int it=0;it<2;it++){
    u16* xo = it==0? x2 : (u16*)d_out;
    ln_k<<<S,64,0,stream>>>(x,xn);
    gemm_k<<<dim3(3*D/16,S/16),256,0,stream>>>(xn,wqkv,qkv,nullptr,S,3*D,D,0);
    attn_k<<<S*H/256,256,0,stream>>>(qkv,o);
    gemm_k<<<dim3(D/16,S/16),256,0,stream>>>(o,wo,x1,x,S,D,D,1);
    ln_k<<<S,64,0,stream>>>(x1,xn);
    gemm_k<<<dim3(F/16,S/16),256,0,stream>>>(xn,wg,g,nullptr,S,F,D,0);
    gemm_k<<<dim3(F/16,S/16),256,0,stream>>>(xn,wu,u,nullptr,S,F,D,0);
    mul_k<<<(S*F+255)/256,256,0,stream>>>(g,u,h,S*F);
    gemm_k<<<dim3(D/16,S/16),256,0,stream>>>(h,wd,xo,x1,S,D,F,1);
    x=x2;
  }
}
```

```cpp
#include <hip/hip_runtime.h>
#include <stdint.h>
#include <utility>

#define LAS __attribute__((address_space(3)))
#define GAS __attribute__((address_space(1)))
#define DI __device__ __forceinline__
typedef unsigned u32;
typedef unsigned short u16;
typedef u32 u32x4 __attribute__((ext_vector_type(4)));
typedef u32 u32x2 __attribute__((ext_vector_type(2)));
typedef float f32x2 __attribute__((ext_vector_type(2)));
typedef float f32x4 __attribute__((ext_vector_type(4)));
typedef float f32x16 __attribute__((ext_vector_type(16)));
typedef short s16x8 __attribute__((ext_vector_type(8)));
typedef short s16x4 __attribute__((ext_vector_type(4)));
typedef __bf16 bf16x2 __attribute__((ext_vector_type(2)));
typedef __amdgpu_buffer_rsrc_t rsrc_t;

constexpr u32 FLAG_BYTES = 32768;
constexpr u32 OFF_WQ = 65536;
constexpr u32 OFF_WO = OFF_WQ + 16 * 163840;
constexpr u32 OFF_WG = OFF_WO + 8 * 20 * 5 * 1024;
constexpr u32 OFF_WD = OFF_WG + 108 * 40960;
constexpr u32 OFF_CSQ = OFF_WD + 8 * 54 * 5 * 1024;
constexpr u32 OFF_CSG = OFF_CSQ + 16384;
constexpr u32 SZ_XF = 64 * 40 * 1024;
constexpr u32 OFF_XF = OFF_CSG + 28672;
constexpr u32 SZ_ST = 8 * 2048 * 8;
constexpr u32 OFF_ST = OFF_XF + 4 * SZ_XF;
constexpr u32 SZ_OF = 128 * 20 * 1024;
constexpr u32 OFF_OF = OFF_ST + 4 * SZ_ST;
constexpr u32 SZ_HF = 128 * 54 * 1024;
constexpr u32 OFF_HF = OFF_OF + 2 * SZ_OF;
constexpr u32 WS_USED = OFF_HF + 2 * SZ_HF;
constexpr u32 FG_O = 0, FG_X1 = 16, FG_H = 48, FG_X2 = 64, FG_PER_IT = 96;
constexpr u32 FG_TMO = 2 * FG_PER_IT;

constexpr u32 L_HALO = 122880;
constexpr u32 L_KST = 40960;
constexpr u32 L_VST = L_KST + 144 * 80;
constexpr u32 L_QST = L_VST + 144 * 80;
constexpr u32 L_RED = 100352;
constexpr u32 L_CSQ = 161792;
constexpr u32 L_CSG = L_CSQ + 512;
constexpr u32 LDS_BYTES = 163840;

constexpr u32 AUX_SC1 = 16;
#define RLX_AGENT __ATOMIC_RELAXED, __HIP_MEMORY_SCOPE_AGENT
#define SB() __builtin_amdgcn_sched_barrier(0)
#define MEMFENCE() asm volatile("" ::: "memory")
#define WAITV(n) asm volatile("s_waitcnt vmcnt(%0)" ::"i"(n) : "memory")
#define WAITL0() asm volatile("s_waitcnt lgkmcnt(0)" ::: "memory")
#define BAR() do { MEMFENCE(); __builtin_amdgcn_s_barrier(); MEMFENCE(); } while (0)
#define CERT(tag) asm volatile("; CERT " tag ::: "memory")

template <int... I, class Fn> DI void sfor_impl(std::integer_sequence<int, I...>, Fn&& f) { (f(std::integral_constant<int, I>{}), ...); }
template <int N, class Fn> DI void sfor(Fn&& f) { sfor_impl(std::make_integer_sequence<int, N>{}, f); }
DI f32x16 mfma32(u32x4 a, u32x4 b, f32x16 c) { return __builtin_amdgcn_mfma_f32_32x32x16_bf16(__builtin_bit_cast(s16x8, a), __builtin_bit_cast(s16x8, b), c, 0, 0, 0); }
DI f32x4 mfma16(u32x4 a, u32x4 b, f32x4 c) { return __builtin_amdgcn_mfma_f32_16x16x32_bf16(__builtin_bit_cast(s16x8, a), __builtin_bit_cast(s16x8, b), c, 0, 0, 0); }
DI u32 cvtpk(float lo, float hi) { f32x2 v = {lo, hi}; bf16x2 b = __builtin_convertvector(v, bf16x2); return __builtin_bit_cast(u32, b); }
DI float u2f(u32 v) { return __builtin_bit_cast(float, v); }
DI u32 f2u(float v) { return __builtin_bit_cast(u32, v); }
DI float bflo(u32 v) { return __builtin_bit_cast(float, v << 16); }
DI float bfhi(u32 v) { return __builtin_bit_cast(float, v & 0xffff0000u); }
DI float fma_s(float a, float b, float c) { float r; asm("v_fma_f32 %0, %1, %2, %3" : "=v"(r) : "v"(a), "v"(b), "v"(c)); return r; }
DI float mul_s(float a, float b) { float r; asm("v_mul_f32_e32 %0, %1, %2" : "=v"(r) : "v"(a), "v"(b)); return r; }
DI float red16_32_add(float v) {
  auto a = __builtin_amdgcn_permlane16_swap(f2u(v), f2u(v), false, false);
  v = u2f(a[0]) + u2f(a[1]);
  auto b = __builtin_amdgcn_permlane32_swap(f2u(v), f2u(v), false, false);
  return u2f(b[0]) + u2f(b[1]);
}
DI float red16_32_max(float v) {
  auto a = __builtin_amdgcn_permlane16_swap(f2u(v), f2u(v), false, false);
  v = __builtin_fmaxf(u2f(a[0]), u2f(a[1]));
  auto b = __builtin_amdgcn_permlane32_swap(f2u(v), f2u(v), false, false);
  return __builtin_fmaxf(u2f(b[0]), u2f(b[1]));
}
DI float red32_add(float v) {
  auto b = __builtin_amdgcn_permlane32_swap(f2u(v), f2u(v), false, false);
  return u2f(b[0]) + u2f(b[1]);
}
DI void stg_sc1(u32x4 v, rsrc_t rs, u32 voff, u32 soff) { __builtin_amdgcn_raw_buffer_store_b128(v, rs, voff, soff, AUX_SC1); }
DI void stg2_sc1(u32x2 v, rsrc_t rs, u32 voff, u32 soff) { __builtin_amdgcn_raw_buffer_store_b64(v, rs, voff, soff, AUX_SC1); }

template <int IMM> DI void ald_v_sc1(u32x4& d, rsrc_t rs, u32 voff, u32 soff) {
  asm volatile("buffer_load_dwordx4 %0, %1, %2, %3 offen offset:%4 sc1" : "=v"(d) : "v"(voff), "s"(rs), "s"(soff), "i"(IMM) : "memory");
}
template <int IMM> DI void ald_a(u32x4& d, rsrc_t rs, u32 voff, u32 soff) {
  asm volatile("buffer_load_dwordx4 %0, %1, %2, %3 offen offset:%4" : "=a"(d) : "v"(voff), "s"(rs), "s"(soff), "i"(IMM) : "memory");
}
template <int IMM> DI void ald_v(u32x4& d, rsrc_t rs, u32 voff, u32 soff) {
  asm volatile("buffer_load_dwordx4 %0, %1, %2, %3 offen offset:%4" : "=v"(d) : "v"(voff), "s"(rs), "s"(soff), "i"(IMM) : "memory");
}
DI void ald2_sc1(u32x2& d, rsrc_t rs, u32 voff, u32 soff) {
  asm volatile("buffer_load_dwordx2 %0, %1, %2, %3 offen sc1" : "=v"(d) : "v"(voff), "s"(rs), "s"(soff) : "memory");
}
DI void ald2(u32x2& d, rsrc_t rs, u32 voff, u32 soff) {
  asm volatile("buffer_load_dwordx2 %0, %1, %2, %3 offen" : "=v"(d) : "v"(voff), "s"(rs), "s"(soff) : "memory");
}
DI void ald1(u32& d, rsrc_t rs, u32 voff, u32 soff) {
  asm volatile("buffer_load_dword %0, %1, %2, %3 offen" : "=v"(d) : "v"(voff), "s"(rs), "s"(soff) : "memory");
}
template <int N> DI void waitpin_v(u32x4& r) { asm volatile("s_waitcnt vmcnt(%1)" : "+v"(r) : "i"(N) : "memory"); }
DI void pin_v(u32x4& r) { asm volatile("" : "+v"(r)::"memory"); }
DI void pin_a(u32x4& r) { asm volatile("" : "+a"(r)::"memory"); }
DI void pin2(u32x2& r) { asm volatile("" : "+v"(r)::"memory"); }
DI void pin1(u32& r) { asm volatile("" : "+v"(r)::"memory"); }
#define DMA1(TAG, rs, voff, soff, m0v) \
  asm volatile("s_mov_b32 m0, %2\n\ts_nop 0\n\tbuffer_load_dwordx4 %0, %1, %3 offen lds ; DMA " TAG ::"v"(voff), "s"(rs), "s"(m0v), "s"(soff) : "memory")
#define DMA1_SC1(TAG, rs, voff, soff, m0v) \
  asm volatile("s_mov_b32 m0, %2\n\ts_nop 0\n\tbuffer_load_dwordx4 %0, %1, %3 offen sc1 lds ; DMA " TAG ::"v"(voff), "s"(rs), "s"(m0v), "s"(soff) : "memory")

__global__ __launch_bounds__(256) void prep_kernel(const u16* __restrict__ x0, const u16* __restrict__ wqkv, const u16* __restrict__ wo,
                                                   const u16* __restrict__ wg, const u16* __restrict__ wu, const u16* __restrict__ wd, char* __restrict__ ws) {
  __shared__ __attribute__((aligned(16))) char lds[40960 + 1024];
  const int tid = threadIdx.x, l = tid & 63, w = __builtin_amdgcn_readfirstlane(tid >> 6);
  const int u = blockIdx.x;
  if (u == 0) {
    for (int i = tid; i < (int)(FLAG_BYTES / 16); i += 256) ((u32x4*)ws)[i] = u32x4{0, 0, 0, 0};
  }
  if (u < 248) {
    int type, cg, kk;
    if (u < 60) { type = 0; cg = u >> 1; kk = u & 1; }
    else if (u < 168) { int v = u - 60; type = 1 + v / 54; int r = v % 54; cg = r >> 1; kk = r & 1; }
    else if (u < 188) { int v = u - 168; type = 3; cg = v >> 1; kk = v & 1; }
    else { int v = u - 188; type = 4; cg = v / 6; kk = v % 6; }
    const u16* src = type == 0 ? wqkv : type == 1 ? wg : type == 2 ? wu : type == 3 ? wo : wd;
    const int pitch = type == 0 ? 1920 : type <= 2 ? 1728 : 640;
    const int nrows = type == 4 ? 288 : 320;
    const int row0 = kk * nrows;
    for (int i = w; i < nrows / 8; i += 4) {
      const u16* g = src + (size_t)(row0 + 8 * i + (l >> 3)) * pitch + 64 * cg + (l & 7) * 8;
      __builtin_amdgcn_global_load_lds((const GAS u32*)g, (LAS u32*)(lds + i * 1024), 16, 0, 0);
    }
    __syncthreads();
    const int c = 64 * cg + l;
    u32 dbase; int dstepA = 0;
    int aux0 = 0;
    if (type == 0) {
      const int sec = c / 640, cc = c % 640, p = cc / 40, r40 = cc % 40;
      const int n = (sec == 1 ? 0 : sec == 2 ? 40 : 80) + r40;
      const int b = n >> 5, nl = n & 31;
      dbase = OFF_WQ + p * 163840 + (b < 2 ? b * 1024 : 81920 + (b - 2) * 40960) + nl * 16;
      dstepA = b < 2 ? 2048 : 1024;
      aux0 = p * 128 + n;
    } else if (type <= 2) {
      const int fb = c >> 4, nl = (c & 15) + 16 * (type - 1);
      const int half = fb / 54, rem = fb % 54;
      int idx, B0;
      if (rem < 42) { idx = rem % 7; B0 = 54 * half + rem - idx; } else { idx = (rem - 42) % 6; B0 = 54 * half + rem - idx; }
      const bool paired = idx < 6;
      dbase = OFF_WG + (paired ? (B0 + (idx & ~1)) * 40960 + (idx & 1) * 1024 : (B0 + 6) * 40960) + nl * 16;
      dstepA = paired ? 2048 : 1024;
      aux0 = fb * 32 + nl;
    } else {
      const int c8 = c / 80, nb = (c % 80) >> 4, n = c & 15;
      dbase = (type == 3 ? OFF_WO + c8 * 20 * 5120 : OFF_WD + c8 * 54 * 5120) + nb * 1024 + n * 16;
    }
    const int gam = l >> 4, q = (l & 15) >> 2, pp = l & 3;
    const int noct = nrows / 8;
    float csum = 0.f;
    for (int o = w; o < noct; o += 4) {
      int r0, r1; u32 dst;
      if (type <= 2) {
        r0 = 8 * o + q; r1 = r0 + 4;
        const int ko = 40 * kk + o;
        dst = dbase + (ko >> 1) * dstepA + (ko & 1) * 512;
      } else if (type == 3) {
        const int pl = o / 5, i = o % 5;
        r0 = 40 * pl + 4 * i + q; r1 = r0 + 20;
        const int sg = 40 * kk + o;
        dst = dbase + (sg >> 2) * 5120 + (sg & 3) * 256;
      } else {
        const int sl = o >> 2, g = o & 3;
        r0 = 32 * sl + 16 * (g >> 1) + 4 * (g & 1) + q; r1 = r0 + 8;
        dst = dbase + (9 * kk + sl) * 5120 + g * 256;
      }
      s16x4 v0 = __builtin_amdgcn_ds_read_tr16_b64_v4i16((LAS s16x4*)(lds + r0 * 128 + gam * 32 + pp * 8));
      s16x4 v1 = __builtin_amdgcn_ds_read_tr16_b64_v4i16((LAS s16x4*)(lds + r1 * 128 + gam * 32 + pp * 8));
      u32x2 a = __builtin_bit_cast(u32x2, v0), b2 = __builtin_bit_cast(u32x2, v1);
      u32x4 slot = {a.x, a.y, b2.x, b2.y};
      *(u32x4*)(ws + dst) = slot;
      if (type <= 2) csum += ((bflo(a.x) + bfhi(a.x)) + (bflo(a.y) + bfhi(a.y))) + ((bflo(b2.x) + bfhi(b2.x)) + (bflo(b2.y) + bfhi(b2.y)));
    }
    if (type <= 2) {
      __syncthreads();
      float* red = (float*)lds;
      red[w * 64 + l] = csum;
      __syncthreads();
      if (w == 0) {
        float s = (red[l] + red[64 + l]) + (red[128 + l] + red[192 + l]);
        if (type == 0) ((float*)(ws + OFF_CSQ))[kk * 2048 + aux0] = s;
        else ((float*)(ws + OFF_CSG))[kk * 3456 + aux0] = s;
      }
    }
  } else {
    const int rb = u - 248;
    for (int i = w; i < 40; i += 4) {
      const int cidx = 64 * i + l, row = cidx / 80, cc = cidx % 80;
      const u16* g = x0 + (size_t)(32 * rb + row) * 640 + ((cc ^ (row & 7)) * 8);
      __builtin_amdgcn_global_load_lds((const GAS u32*)g, (LAS u32*)(lds + i * 1024), 16, 0, 0);
    }
    __syncthreads();
    const int tok = l & 31, h = l >> 5;
    float s1 = 0.f, s2 = 0.f;
    for (int s = 10 * w; s < 10 * w + 10; ++s) {
      const int cc = 2 * s + h;
      u32x4 v = *(const u32x4*)(lds + tok * 1280 + ((cc ^ (tok & 7)) * 16));
      *(u32x4*)(ws + OFF_XF + (rb * 40 + s) * 1024 + l * 16) = v;
#pragma unroll
      for (int j = 0; j < 4; ++j) { float a = bflo(v[j]), b = bfhi(v[j]); s1 += a + b; s2 += a * a + b * b; }
    }
    s1 = red32_add(s1); s2 = red32_add(s2);
    if (l < 32) {
      f32x2* st = (f32x2*)(ws + OFF_ST);
      st[w * 2048 + 32 * rb + tok] = f32x2{s1, s2};
      st[(w + 4) * 2048 + 32 * rb + tok] = f32x2{0.f, 0.f};
    }
  }
}

#define STAMP(i) do {} while (0)
constexpr u32 OFF_STAMP = WS_USED + 4096;
constexpr u32 kSpinCap = 1u << 16;

struct K {
  rsrc_t rs, rso;
  LAS char* lds;
  u32 lds0;
  GAS u32* flags;
  int lane, w, m128, p, m64, c8, B0, nblk, r4, jm, c8l, tid;
  u32x2 xr[5];
  u32x4 wo_r[5][5];
  u32x4 wd_r[14][5];
};

#define PHASE_IDS() int lane = k.lane; asm volatile("" : "+v"(lane)); \
  int w = k.w, m128 = k.m128, p = k.p, m64 = k.m64, c8 = k.c8, B0 = k.B0, r4 = k.r4, jm = k.jm, c8l = k.c8l; \
  asm volatile("" : "+s"(w), "+s"(m128), "+s"(p), "+s"(m64), "+s"(c8), "+s"(B0), "+s"(r4), "+s"(jm), "+s"(c8l)); \
  const u32 l16 = lane * 16; const int tok = lane & 31, h = lane >> 5, t16 = lane & 15, g = lane >> 4; \
  const rsrc_t rs = k.rs; LAS char* const lds = k.lds; const u32 lds0 = k.lds0; \
  (void)tok; (void)h; (void)t16; (void)g; (void)l16; (void)m128; (void)p; (void)m64; (void)c8; (void)B0; (void)r4; (void)jm; (void)c8l; (void)lds; (void)lds0; (void)rs

DI void poll_flags(GAS u32* flags, u32 fo, bool act, int lane) {
  for (u32 spins = 0;;) {
    u32 v = act ? __hip_atomic_load(flags + fo, RLX_AGENT) : 1u;
    if (__all(v == 1u)) break;
    __builtin_amdgcn_s_sleep(1);
    if (++spins > kSpinCap) {
      if (lane == 0) __hip_atomic_store(flags + FG_TMO * 32, 1u, RLX_AGENT);
      break;
    }
    if ((spins & 255u) == 0u && __builtin_amdgcn_readfirstlane(__hip_atomic_load(flags + FG_TMO * 32, RLX_AGENT)) != 0u) break;
  }
}
DI void stats_finish(float s1, float s2, float& nmean, float& rstd) {
  const float mean = s1 * (1.0f / 640.0f);
  const float var = __builtin_fmaxf(s2 * (1.0f / 640.0f) - mean * mean, 0.f);
  nmean = -mean; rstd = __builtin_amdgcn_rsqf(var + 1e-5f);
}
DI int mod40(int v) { return v >= 40 ? v - 40 : v; }

template <int C, int SLOT, int I> DI void dma_q(rsrc_t rs, u32 l16, int p, int w, int r4, u32 lds0) {
  u32 so, mo;
  if constexpr (C < 2) {
    so = OFF_WQ + p * 163840 + (2 * mod40(10 * r4 + 20 * C + 5 * w) + I) * 1024;
    mo = lds0 + SLOT * 40960 + (10 * w + I) * 1024;
  } else {
    constexpr int hf = I / 5, ii = I % 5;
    so = OFF_WQ + p * 163840 + 81920 + (C - 2) * 40960 + (mod40(10 * r4 + 20 * hf + 5 * w) + ii) * 1024;
    mo = lds0 + SLOT * 40960 + (20 * hf + 5 * w + ii) * 1024;
  }
  if constexpr (C == 0) DMA1("Q0", rs, l16, so, mo);
  else if constexpr (C == 1) DMA1("Q1", rs, l16, so, mo);
  else if constexpr (C == 2) DMA1("Q2", rs, l16, so, mo);
  else if constexpr (I < 5) DMA1("Q3a", rs, l16, so, mo);
  else DMA1("Q3b", rs, l16, so, mo);
}
template <int HH, int I> DI void dma_g(rsrc_t rs, u32 l16, int B0, int w, int r4, u32 lds0) {
  u32 so;
  if constexpr (HH < 12) {
    constexpr int ps = HH / 4, ph = 20 * (HH % 4);
    int f = ph + 5 * w + 20 * r4; f = f >= 80 ? f - 80 : f;
    so = OFF_WG + (B0 + 2 * ps) * 40960 + (f + I) * 1024;
  } else {
    so = OFF_WG + (B0 + 6) * 40960 + (mod40(10 * r4 + 20 * (HH - 12) + 5 * w) + I) * 1024;
  }
  const u32 mo = lds0 + (HH % 7) * 20480 + (5 * w + I) * 1024;
#define G_CASE(n) if constexpr (HH == n) DMA1("G" #n, rs, l16, so, mo)
  G_CASE(0); G_CASE(1); G_CASE(2); G_CASE(3); G_CASE(4); G_CASE(5); G_CASE(6); G_CASE(7); G_CASE(8); G_CASE(9); G_CASE(10); G_CASE(11); G_CASE(12); G_CASE(13);
#undef G_CASE
}

constexpr int XW = 10;
template <int IT> struct QS {
  static constexpr int Q3F0 = 78, WOF0 = 98;
  static constexpr bool x_at(int f) { return f < 80 && (f & 1) == 0 && (f >> 1) + XW < 40; }
  static constexpr bool q1_at(int f) { return IT == 0 && f < 20 && (f & 1) == 1; }
  static constexpr bool q2_at(int f) { return IT == 0 && f >= 40 && f < 60 && (f & 1) == 1; }
  static constexpr bool q3_at(int f) { return f >= Q3F0 && f < Q3F0 + 20 && ((f - Q3F0) & 1) == 0; }
  static constexpr bool wo_at(int f) { return f >= WOF0 && f < WOF0 + 50 && ((f - WOF0) & 1) == 0; }
  static constexpr int upto(int F) { int n = 0; for (int f = 0; f <= F; ++f) n += x_at(f) + q1_at(f) + q2_at(f) + q3_at(f) + wo_at(f); return n; }
  static constexpr int xidx(int i) { return i < XW ? i : XW + upto(2 * (i - XW)) - 1; }
  static constexpr int xwait(int s) { return XW + upto(2 * s) - 1 - xidx(s); }
};
struct GS {
  static constexpr int NWD = 40;
  static constexpr bool x_at(int f) { return f < 80 && (f & 1) == 0 && (f >> 1) + XW < 40; }
  static constexpr int op(int F) {
    if (F & 1) return -1;
    if (F >= 62 && F < 72) return 6 * 8 + (F - 62) / 2;
    if (F >= 72 && F < 92) return (7 + (F - 72) / 10) * 8 + ((F - 72) % 10) / 2;
    if (F >= 92 && F < 112) return (9 + (F - 92) / 10) * 8 + ((F - 92) % 10) / 2;
    if (F >= 118 && F < 138) return (11 + (F - 118) / 10) * 8 + ((F - 118) % 10) / 2;
    if (F >= 158 && F < 168) return 13 * 8 + (F - 158) / 2;
    if (F >= 138 && F < 158) return 1000 + (F - 138) / 2;
    if (F >= 168 && F < 240 && 10 + (F - 168) / 2 < NWD) return 1000 + 10 + (F - 168) / 2;
    return -1;
  }
  static constexpr bool st_at(int F) { const int ps = F / 80, r = F % 80; return F < 240 && ps > 0 && (r == 28 || r == 60); }
  static constexpr int upto(int F) { int n = 0; for (int f = 0; f <= F; ++f) n += x_at(f) + (op(f) >= 0) + st_at(f); return n; }
  static constexpr int slot(int o) { for (int f = 0; f < 300; ++f) if (op(f) == o) return f; return -1; }
  static constexpr int xidx(int i) { return i < XW ? i : XW + upto(2 * (i - XW)) - 1; }
  static constexpr int xwait(int s) { return XW + upto(2 * s) - 1 - xidx(s); }
};
static_assert(10 + (240 - 168) / 2 >= GS::NWD, "all prefetched Wd fragments need a slot");

template <int IT> DI void iteration(K& k, u16* out) {
  constexpr u32 xin = OFF_XF + (IT == 0 ? 0 : 2) * SZ_XF, stin = OFF_ST + (IT == 0 ? 0 : 2) * SZ_ST;
  constexpr u32 x1o = OFF_XF + (IT == 0 ? 1 : 3) * SZ_XF, st1o = OFF_ST + (IT == 0 ? 1 : 3) * SZ_ST;
  constexpr u32 ofo = OFF_OF + IT * SZ_OF, hfo = OFF_HF + IT * SZ_HF;
  constexpr u32 fgb = IT * FG_PER_IT;
  GAS u32* const flags = k.flags;
  const int tid = k.tid;
  {
    PHASE_IDS();
    STAMP(0);
    u32 csa = 0, csb = 0, cga = 0, cgb = 0;
    if constexpr (IT == 0) {
      sfor<10>([&](auto I) { dma_q<0, 0, decltype(I)::value>(rs, l16, p, w, r4, lds0); });
      const u32 t4 = tid * 4;
      ald1(csa, rs, t4, OFF_CSQ + p * 512); ald1(csb, rs, t4, OFF_CSQ + 8192 + p * 512);
      ald1(cga, rs, t4, OFF_CSG + B0 * 128); ald1(cgb, rs, t4, OFF_CSG + 13824 + B0 * 128);
      const u32 token = 64 * m64 + 16 * w + t16;
      const u32 o = ((token >> 5) * 40 + 5 * c8) * 1024 + ((token & 31) + 32 * (g >> 1)) * 16 + (g & 1) * 8;
#pragma unroll
      for (int nb = 0; nb < 5; ++nb) ald2(k.xr[nb], rs, o, OFF_XF + nb * 1024);
    } else {
      if (w == 0) {
        const int grp = 2 * m128 - 1 + (lane >> 3);
        poll_flags(flags, (FG_X2 + grp) * 32 + (lane & 7), lane < 24 && grp >= 0, lane);
      }
      WAITV(0); CERT("Q0"); CERT("Q1"); CERT("Q2");
      BAR();
    }
    STAMP(1);
    {
      const int rbh = m128 > 0 ? 4 * m128 - 1 : 0;
      const u32 vo = (g >> 1) * 1024 + (g & 1) * 512 + (16 + t16) * 16;
#pragma unroll
      for (int i = 0; i < 5; ++i)
        DMA1_SC1("HALO", rs, vo, xin + rbh * 40960 + mod40(10 * r4 + 2 * (5 * w + i)) * 1024, lds0 + L_HALO + (5 * w + i) * 1024);
    }
    u32x2 hs0, hs1, sa0, sa1, sa2, sa3;
    {
      const int ht = 128 * m128 - 16 + t16;
      const u32 htc = ht < 0 ? 0 : ht;
      ald2_sc1(hs0, rs, ((2 * g) * 2048 + htc) * 8, stin); ald2_sc1(hs1, rs, ((2 * g + 1) * 2048 + htc) * 8, stin);
      const u32 so = (4 * h * 2048 + 128 * m128 + 32 * w + tok) * 8;
      ald2_sc1(sa0, rs, so, stin); ald2_sc1(sa1, rs, so, stin + 16384); ald2_sc1(sa2, rs, so, stin + 32768); ald2_sc1(sa3, rs, so, stin + 49152);
    }
    u32x4 xf[40];
    const u32 xbase = xin + (4 * m128 + w) * 40960;
    u32 xg[4];
#pragma unroll
    for (int gi = 0; gi < 4; ++gi) xg[gi] = xbase + mod40(10 * r4 + 10 * gi) * 1024;
#define XLOAD(i) ald_v_sc1<(((i) % 10) % 4) * 1024>(xf[i], rs, l16, xg[(i) / 10] + (((i) % 10) / 4) * 4096)
    sfor<XW>([&](auto I) { constexpr int i = decltype(I)::value; XLOAD(i); });
    using Q = QS<IT>;
    if constexpr (IT == 0) {
      WAITV(20 + XW); CERT("Q0");
      pin1(csa); pin1(csb); pin1(cga); pin1(cgb);
      BAR();
    }
    constexpr int DQ = 4, NBQ = 6;
    u32x4 wb[NBQ];
    f32x16 acc0, acc1, acc2, acc3;
    f32x4 hacc = {}, hacc4 = {};
    u32x4 hw[2], hx[2];
    float nmean = 0.f, rstd = 0.f, hnm = 0.f, hrs = 0.f;
    u32 lb1 = l16 + 65536, lbh = l16 + L_HALO; asm volatile("" : "+v"(lb1), "+v"(lbh));
    const u32 hwbase = (w >> 1) * 1024 + (g >> 1) * 2048 + (g & 1) * 512 + (16 * (w & 1) + t16) * 16;
    const u32 hw4base = 81920 - 65536 + (g >> 1) * 1024 + (g & 1) * 512 + t16 * 16 + 65536;
    const u32 kvrow = L_KST + (16 + 32 * w + tok) * 80 + h * 8;
    const u32 csb4 = L_CSQ - 131072 + h * 16;
    u32 lcs = 131072 + csb4; asm volatile("" : "+v"(lcs));
    const int hn = 16 * w + 4 * g;
    const u32 haddr = L_KST + t16 * 80 + 2 * hn + (hn >= 40 ? (L_VST - L_KST - 80) : 0);
    const u32 haddr4 = L_VST + t16 * 80 + 2 * (64 + 4 * g - 40);
    const bool hzero = (m128 == 0);
#define LDSQ(F) (((F) / 40 % 3) * 40960 + ((F) % 40) * 1024)
#define RDW(off) (*(const LAS u32x4*)(lds + ((off) < 65536 ? l16 : lb1) + ((off) & 65535)))
    f32x4 gcs;
    float gt[4];
#define GRAN_A(n0) gcs = *(const LAS f32x4*)(lds + lcs + (n0) * 4)
#define GRAN_B(ACC, q) do { gt[0] = fma_s(nmean, gcs[0], ACC[4 * (q)]); gt[1] = fma_s(nmean, gcs[1], ACC[4 * (q) + 1]); \
      gt[2] = fma_s(nmean, gcs[2], ACC[4 * (q) + 2]); gt[3] = fma_s(nmean, gcs[3], ACC[4 * (q) + 3]); } while (0)
#define GRAN_C(n0) do { const u32x2 pk_ = {cvtpk(mul_s(gt[0], rstd), mul_s(gt[1], rstd)), cvtpk(mul_s(gt[2], rstd), mul_s(gt[3], rstd))}; \
      constexpr u32 a_ = (n0) < 40 ? 2 * (n0) : (n0) < 80 ? (L_VST - L_KST) + 2 * ((n0) - 40) : (L_QST - L_KST - 16 * 80) + 2 * ((n0) - 80); \
      *(LAS u32x2*)(lds + kvrow + a_) = pk_; } while (0)
#define HALO_A(ncol) gcs = *(const LAS f32x4*)(lds + L_CSQ + (ncol) * 4)
#define HALO_B(HA) do { gt[0] = fma_s(hnm, gcs[0], HA[0]); gt[1] = fma_s(hnm, gcs[1], HA[1]); gt[2] = fma_s(hnm, gcs[2], HA[2]); gt[3] = fma_s(hnm, gcs[3], HA[3]); } while (0)
#define HALO_C(addr) do { u32x2 pk_ = {cvtpk(mul_s(gt[0], hrs), mul_s(gt[1], hrs)), cvtpk(mul_s(gt[2], hrs), mul_s(gt[3], hrs))}; \
      if (hzero) pk_ = u32x2{0u, 0u}; *(LAS u32x2*)(lds + (addr)) = pk_; } while (0)
#pragma unroll
    for (int F = 0; F < DQ; ++F) wb[F % NBQ] = RDW(LDSQ(F));
    sfor<160>([&](auto FC) {
      constexpr int F = decltype(FC)::value, Fr = F + DQ;
      if constexpr (Q::x_at(F)) XLOAD((F >> 1) + XW);
      if constexpr (Q::q1_at(F)) dma_q<1, 1, (F >> 1)>(rs, l16, p, w, r4, lds0);
      if constexpr (Q::q2_at(F)) dma_q<2, 2, ((F - 40) >> 1)>(rs, l16, p, w, r4, lds0);
      if constexpr (Fr == 80) {
        if constexpr (IT == 0) { WAITV(0); CERT("Q2"); }
        WAITL0(); BAR();
        STAMP(2);
      }
      if constexpr (Q::q3_at(F)) dma_q<3, 0, ((F - Q::Q3F0) >> 1)>(rs, l16, p, w, r4, lds0);
      if constexpr (Q::wo_at(F)) {
        constexpr int i = (F - Q::WOF0) >> 1, s = i / 5, nb = i % 5;
        int st = s + jm % 5; st = st >= 5 ? st - 5 : st;
        const u32 so = OFF_WO + ((c8 * 20 + 5 * w + st) * 5) * 1024 + (nb == 4 ? 4096 : 0);
        ald_a<(nb & 3) * 1024>(k.wo_r[s][nb], rs, l16, so);
      }
      if constexpr (Fr == 40) {
        if constexpr (IT == 0) { WAITV(Q::upto(F) - Q::upto(19)); CERT("Q1"); } else { WAITV(XW); }
        CERT("HALO");
        BAR();
      }
      if constexpr (Fr == 120) { WAITV(Q::upto(F) - Q::upto(Q::Q3F0 + 8)); CERT("Q3a"); BAR(); }
      if constexpr (Fr == 140) { WAITV(Q::upto(F) - Q::upto(Q::Q3F0 + 18)); CERT("Q3b"); BAR(); }
      if constexpr (Fr < 160) wb[Fr % NBQ] = RDW(LDSQ(Fr));
      if constexpr (F < 80) {
        constexpr int s = F >> 1;
        if constexpr ((F & 1) == 0) waitpin_v<Q::xwait(s)>(xf[s]);
        if constexpr (F == 0) acc0 = mfma32(wb[F % NBQ], xf[s], f32x16{});
        else if constexpr (F == 1) acc1 = mfma32(wb[F % NBQ], xf[s], f32x16{});
        else if constexpr (F & 1) acc1 = mfma32(wb[F % NBQ], xf[s], acc1);
        else acc0 = mfma32(wb[F % NBQ], xf[s], acc0);
        if constexpr (F == 4) {
          pin2(hs0); pin2(hs1); pin2(sa0); pin2(sa1); pin2(sa2); pin2(sa3);
          stats_finish(red32_add((u2f(sa0.x) + u2f(sa1.x)) + (u2f(sa2.x) + u2f(sa3.x))), red32_add((u2f(sa0.y) + u2f(sa1.y)) + (u2f(sa2.y) + u2f(sa3.y))), nmean, rstd);
          stats_finish(red16_32_add(u2f(hs0.x) + u2f(hs1.x)), red16_32_add(u2f(hs0.y) + u2f(hs1.y)), hnm, hrs);
          if constexpr (IT == 0) {
#pragma unroll
            for (int nb = 0; nb < 5; ++nb) pin2(k.xr[nb]);
            if (tid < 128) ((LAS float*)(lds + L_CSQ))[tid] = u2f(csa) + u2f(csb);
            if (tid < 224) ((LAS float*)(lds + L_CSG))[tid] = u2f(cga) + u2f(cgb);
          }
        }
        if constexpr (F & 1) {
          if constexpr (s >= 20 && s <= 39) hacc = mfma16(hw[s & 1], hx[s & 1], hacc);
          if constexpr (s >= 18 && s <= 37) {
            constexpr int sg = s - 18;
            hw[s & 1] = *(const LAS u32x4*)(lds + hwbase + (sg < 10 ? sg * 4096 : 40960 + (sg - 10) * 4096));
            hx[s & 1] = *(const LAS u32x4*)(lds + lbh + sg * 1024);
          }
        }
      } else if constexpr (F < 120) {
        constexpr int s = F - 80;
        if constexpr (s == 0) acc2 = mfma32(wb[F % NBQ], xf[s], f32x16{}); else acc2 = mfma32(wb[F % NBQ], xf[s], acc2);
        if constexpr ((s & 1) == 1 && s >= 3) hacc4 = mfma16(hw[((s - 3) >> 1) & 1], hx[((s - 3) >> 1) & 1], hacc4);
        if constexpr ((s & 1) == 1) {
          constexpr int sg = s >> 1;
          hw[sg & 1] = *(const LAS u32x4*)(lds + hw4base + sg * 2048);
          hx[sg & 1] = *(const LAS u32x4*)(lds + lbh + sg * 1024);
        }
        constexpr int gi = s >> 2, st = s & 3;
        if constexpr (gi < 8) {
          constexpr int b = gi >> 2, q = gi & 3, n0 = 32 * b + 8 * q;
          if constexpr (st == 0) GRAN_A(n0);
          if constexpr (st == 2) { if constexpr (b == 0) GRAN_B(acc0, q); else GRAN_B(acc1, q); }
          if constexpr (st == 3) GRAN_C(n0);
        } else if constexpr (gi == 8) {
          if constexpr (st == 0) HALO_A(hn);
          if constexpr (st == 2) HALO_B(hacc);
          if constexpr (st == 3) HALO_C(haddr);
        }
      } else {
        constexpr int s = F - 120;
        if constexpr (s == 0) acc3 = mfma32(wb[F % NBQ], xf[s], f32x16{}); else acc3 = mfma32(wb[F % NBQ], xf[s], acc3);
        if constexpr (s == 1) hacc4 = mfma16(hw[1], hx[1], hacc4);
        constexpr int gi = s >> 2, st = s & 3;
        if constexpr (gi >= 1 && gi < 5) {
          constexpr int q = gi - 1, n0 = 64 + 8 * q;
          if constexpr (st == 0) GRAN_A(n0);
          if constexpr (st == 2) GRAN_B(acc2, q);
          if constexpr (st == 3) GRAN_C(n0);
        } else if constexpr (gi == 5) {
          if constexpr (st == 0) HALO_A(64 + 4 * g);
          if constexpr (st == 2) HALO_B(hacc4);
          if constexpr (st == 3) HALO_C(haddr4);
        }
      }
      SB();
    });
    STAMP(3);
    GRAN_A(96); GRAN_B(acc3, 0); GRAN_C(96); GRAN_A(104); GRAN_B(acc3, 1); GRAN_C(104); GRAN_A(112); GRAN_B(acc3, 2); GRAN_C(112);
    WAITL0(); BAR();
    STAMP(4);
    {
      const int par = t16 & 1;
      const bool ca = (4 * g + par) > t16, cb = (4 * g + 2 + par) > t16;
      const u32 gq = g < 2 ? g : 2;
      const float cexp = 0.22360679774997896f * 1.4426950408889634f;
#pragma unroll
      for (int qb = 0; qb < 2; ++qb) {
        const int T = 32 * w + 16 * qb;
        const bool neg = (m128 == 0) && (T == 0);
        f32x4 o0[2], o1[2];
#pragma unroll
        for (int hd = 0; hd < 2; ++hd) {
          const u32 fo = hd * 32 + gq * 16;
          u32x4 qf = *(const LAS u32x4*)(lds + L_QST + (T + t16) * 80 + fo);
          if (hd == 0) { if (g >= 2) { qf[2] = 0; qf[3] = 0; } if (g == 3) { qf[0] = 0; qf[1] = 0; } }
          else { if (g == 0 || g == 3) { qf[0] = 0; qf[1] = 0; } if (g == 3) { qf[2] = 0; qf[3] = 0; } }
          const u32x4 k0 = *(const LAS u32x4*)(lds + L_KST + (T + t16) * 80 + fo);
          const u32x4 k1 = *(const LAS u32x4*)(lds + L_KST + (T + 16 + t16) * 80 + fo);
          const f32x4 z = {0.f, 0.f, 0.f, 0.f};
          const f32x4 s0 = mfma16(k0, qf, z), s1 = mfma16(k1, qf, z);
          const float s0a = par ? s0[1] : s0[0], s1a = par ? s1[1] : s1[0];
          const float s0b = par ? s0[3] : s0[2], s1b = par ? s1[3] : s1[2];
          float a = ca ? (neg ? -__builtin_inff() : s0a) : s1a;
          float b = cb ? (neg ? -__builtin_inff() : s0b) : s1b;
          const float mx = red16_32_max(__builtin_fmaxf(a, b));
          const float ea = __builtin_amdgcn_exp2f((a - mx) * cexp), eb = __builtin_amdgcn_exp2f((b - mx) * cexp);
          const float inv = __builtin_amdgcn_rcpf(red16_32_add(ea + eb));
          const u32 pk = cvtpk(ea * inv, eb * inv);
          const u32 va = par ? (pk << 16) : (pk & 0xffffu), vb = par ? (pk & 0xffff0000u) : (pk >> 16);
          const u32x4 pf = {ca ? va : 0u, cb ? vb : 0u, ca ? 0u : va, cb ? 0u : vb};
          const u32 vrow = L_VST + (T + 4 * g + (t16 >> 2)) * 80 + hd * 40 + (t16 & 3) * 8;
          const s16x4 t00 = __builtin_amdgcn_ds_read_tr16_b64_v4i16((LAS s16x4*)(lds + vrow));
          const s16x4 t01 = __builtin_amdgcn_ds_read_tr16_b64_v4i16((LAS s16x4*)(lds + vrow + 16 * 80));
          const s16x4 t10 = __builtin_amdgcn_ds_read_tr16_b64_v4i16((LAS s16x4*)(lds + vrow + 32));
          const s16x4 t11 = __builtin_amdgcn_ds_read_tr16_b64_v4i16((LAS s16x4*)(lds + vrow + 32 + 16 * 80));
          const u32x2 a00 = __builtin_bit_cast(u32x2, t00), a01 = __builtin_bit_cast(u32x2, t01), a10 = __builtin_bit_cast(u32x2, t10), a11 = __builtin_bit_cast(u32x2, t11);
          o0[hd] = mfma16(u32x4{a00.x, a00.y, a01.x, a01.y}, pf, z);
          o1[hd] = mfma16(u32x4{a10.x, a10.y, a11.x, a11.y}, pf, z);
        }
        const u32 tb16 = 8 * m128 + 2 * w + qb;
        const u32 sg = 5 * p + g, sg4 = 5 * p + 4;
        const u32x4 v = {cvtpk(o0[0][0], o0[0][1]), cvtpk(o0[0][2], o0[0][3]), cvtpk(o0[1][0], o0[1][1]), cvtpk(o0[1][2], o0[1][3])};
        stg_sc1(v, rs, (tb16 * 20 + (sg >> 2)) * 1024 + (t16 + 16 * (sg & 3)) * 16, ofo);
        const u32x4 v4 = {cvtpk(o1[0][0], o1[0][1]), cvtpk(o1[0][2], o1[0][3]), cvtpk(o1[1][0], o1[1][1]), cvtpk(o1[1][2], o1[1][3])};
        if (g == 0) stg_sc1(v4, rs, (tb16 * 20 + (sg4 >> 2)) * 1024 + (t16 + 16 * (sg4 & 3)) * 16, ofo);
      }
    }
    SB();
    STAMP(5);
    WAITV(0);
    WAITL0();
    BAR();
    if (tid == 0) __hip_atomic_store(flags + (fgb + FG_O + m128) * 32 + p, 1u, RLX_AGENT);
    STAMP(6);
  }
  {
    PHASE_IDS();
    sfor<5>([&](auto I) { dma_g<0, decltype(I)::value>(rs, l16, B0, w, r4, lds0); });
    sfor<5>([&](auto I) { dma_g<1, decltype(I)::value>(rs, l16, B0, w, r4, lds0); });
    if (w == 0) poll_flags(flags, (fgb + FG_O + (m64 >> 1)) * 32 + (lane & 15), lane < 16, lane);
    BAR();
    STAMP(7);
#pragma unroll
    for (int s = 0; s < 5; ++s)
#pragma unroll
      for (int nb = 0; nb < 5; ++nb) pin_a(k.wo_r[s][nb]);
    u32x4 af[20];
    u32 tbo[4], sto[5];
    const int rho0 = c8l % 3;
#pragma unroll
    for (int tbi = 0; tbi < 4; ++tbi) {
      int rho = tbi + rho0; rho = rho >= 3 ? rho - 3 : rho;
      const u32 tb = tbi < 3 ? (w + 1 + rho) & 3 : w;
      tbo[tbi] = ofo + ((4 * m64 + tb) * 20 + 5 * w) * 1024;
    }
#pragma unroll
    for (int s = 0; s < 5; ++s) { int st = s + jm % 5; st = st >= 5 ? st - 5 : st; sto[s] = st * 1024; }
    constexpr int OW = 10;
#define OLOAD(q) ald_v_sc1<0>(af[q], rs, l16, tbo[(q) / 5] + sto[(q) % 5])
    sfor<OW>([&](auto I) { constexpr int q = decltype(I)::value; OLOAD(q); });
    f32x4 acc[2][5];
    sfor<20>([&](auto QC) {
      constexpr int q = decltype(QC)::value, tbi = q / 5, s = q % 5;
      if constexpr (q + OW < 20) OLOAD(q + OW);
      else if constexpr (q < 15) dma_g<2, q - 10>(rs, l16, B0, w, r4, lds0);
      else dma_g<3, q - 15>(rs, l16, B0, w, r4, lds0);
      waitpin_v<OW>(af[q]);
#pragma unroll
      for (int nb = 0; nb < 5; ++nb) acc[tbi & 1][nb] = mfma16(k.wo_r[s][nb], af[q], s == 0 ? f32x4{0.f, 0.f, 0.f, 0.f} : acc[tbi & 1][nb]);
      if constexpr (tbi > 0) {
        int rho = tbi - 1 + rho0; rho = rho >= 3 ? rho - 3 : rho;
        const u32 tbp = (w + 1 + rho) & 3;
        *(LAS f32x4*)(lds + L_RED + ((tbp * 3 + (2 - rho)) * 5 + s) * 1024 + l16) = acc[(tbi - 1) & 1][s];
      }
      SB();
    });
    STAMP(8);
    WAITL0(); BAR();
    float s1 = 0.f, s2 = 0.f;
#pragma unroll
    for (int nb = 0; nb < 5; ++nb) {
      f32x4 v = acc[1][nb];
#pragma unroll
      for (int sl = 0; sl < 3; ++sl) v += *(const LAS f32x4*)(lds + L_RED + ((w * 3 + sl) * 5 + nb) * 1024 + l16);
      const u32 a = cvtpk(bflo(k.xr[nb].x) + v[0], bfhi(k.xr[nb].x) + v[1]), b = cvtpk(bflo(k.xr[nb].y) + v[2], bfhi(k.xr[nb].y) + v[3]);
      k.xr[nb] = u32x2{a, b};
    }
    const u32 token = 64 * m64 + 16 * w + t16;
    const u32 xo = ((token >> 5) * 40 + 5 * c8) * 1024 + ((token & 31) + 32 * (g >> 1)) * 16;
#pragma unroll
    for (int pr = 0; pr < 3; ++pr) {
      const int A = 2 * pr, Bn = pr < 2 ? 2 * pr + 1 : 4;
      auto r0 = __builtin_amdgcn_permlane16_swap(k.xr[A].x, k.xr[Bn].x, false, false);
      auto r1 = __builtin_amdgcn_permlane16_swap(k.xr[A].y, k.xr[Bn].y, false, false);
      const u32x4 v = {r0[0], r1[0], r0[1], r1[1]};
      if (pr < 2) stg_sc1(v, rs, xo + (A + (g & 1)) * 1024, x1o);
      else if ((g & 1) == 0) stg_sc1(v, rs, xo + 4 * 1024, x1o);
    }
#pragma unroll
    for (int nb = 0; nb < 5; ++nb) {
      const float a = bflo(k.xr[nb].x), b = bfhi(k.xr[nb].x), c = bflo(k.xr[nb].y), d = bfhi(k.xr[nb].y);
      s1 += (a + b) + (c + d); s2 += (a * a + b * b) + (c * c + d * d);
    }
    s1 = red16_32_add(s1); s2 = red16_32_add(s2);
    if (g == 0) stg2_sc1(u32x2{f2u(s1), f2u(s2)}, rs, (c8 * 2048 + token) * 8, st1o);
    SB();
    STAMP(9);
    WAITV(0); CERT("G0"); CERT("G1"); CERT("G2"); CERT("G3");
    WAITL0();
    BAR();
    if (tid == 0) __hip_atomic_store(flags + (fgb + FG_X1 + m64) * 32 + c8, 1u, RLX_AGENT);
    STAMP(10);
  }
  {
    PHASE_IDS();
    const int nblk = k.nblk;
    const int ds0 = w < 2 ? 14 * w : 28 + 13 * (w - 2), dns = w < 2 ? 14 : 13;
    sfor<5>([&](auto I) { dma_g<4, decltype(I)::value>(rs, l16, B0, w, r4, lds0); });
    sfor<5>([&](auto I) { dma_g<5, decltype(I)::value>(rs, l16, B0, w, r4, lds0); });
    if (w == 0) poll_flags(flags, (fgb + FG_X1 + 2 * m128 + (lane >> 3)) * 32 + (lane & 7), lane < 16, lane);
    BAR();
    STAMP(11);
    u32x2 sa0, sa1, sa2, sa3;
    {
      const u32 so = (4 * h * 2048 + 128 * m128 + 32 * w + tok) * 8;
      ald2_sc1(sa0, rs, so, st1o); ald2_sc1(sa1, rs, so, st1o + 16384); ald2_sc1(sa2, rs, so, st1o + 32768); ald2_sc1(sa3, rs, so, st1o + 49152);
    }
    u32x4 xf[40];
    const u32 xbase = x1o + (4 * m128 + w) * 40960;
    u32 xg[4];
#pragma unroll
    for (int gi = 0; gi < 4; ++gi) xg[gi] = xbase + mod40(10 * r4 + 10 * gi) * 1024;
    sfor<XW>([&](auto I) { constexpr int i = decltype(I)::value; XLOAD(i); });
    constexpr int NWD = GS::NWD;
    float nmean = 0.f, r2 = 0.f;
    constexpr int DG = 4, NBG = 6;
    u32x4 wb[NBG];
    f32x16 ac[2][2];
    u32 lb1 = l16 + 65536, lb2 = l16 + 131072; asm volatile("" : "+v"(lb1), "+v"(lb2));
    u32 lcs = L_CSG + h * 16; asm volatile("" : "+v"(lcs));
    const u32 hbase = (8 * m128 + 2 * w + (tok >> 4)) * 54 * 1024 + ((tok & 15) + 16 * h) * 16;
    const u32 wd_o = OFF_WD + (c8 * 54 + ds0) * 5120;
#define LDSG(F) ((((F) / 20) % 7) * 20480 + ((F) % 20) * 1024)
#define RDG(off) (*(const LAS u32x4*)(lds + ((off) < 65536 ? l16 : (off) < 131072 ? lb1 : lb2) + ((off) & 65535)))
    f32x4 cg, cu;
    float ht[8];
    u32 hp[4];
#define GU_A(idx, q) do { cg = *(const LAS f32x4*)(lds + lcs + ((idx) * 32 + 8 * (q)) * 4); cu = *(const LAS f32x4*)(lds + lcs + ((idx) * 32 + 16 + 8 * (q)) * 4); } while (0)
#define GU_B(ACC, q) do { _Pragma("unroll") for (int r_ = 0; r_ < 4; ++r_) { ht[r_] = fma_s(nmean, cg[r_], ACC[4 * (q) + r_]); ht[4 + r_] = fma_s(nmean, cu[r_], ACC[8 + 4 * (q) + r_]); } } while (0)
#define GU_C(q) do { hp[2 * (q)] = cvtpk(mul_s(mul_s(ht[0], r2), ht[4]), mul_s(mul_s(ht[1], r2), ht[5])); \
      hp[2 * (q) + 1] = cvtpk(mul_s(mul_s(ht[2], r2), ht[6]), mul_s(mul_s(ht[3], r2), ht[7])); } while (0)
#define GU_S(idx) do { const u32 fb_ = B0 + (idx); stg_sc1(u32x4{hp[0], hp[1], hp[2], hp[3]}, rs, hbase + (fb_ & 1) * 512, hfo + (fb_ >> 1) * 1024); } while (0)
#define GU_STAGE(ACC, idx, t) do { if constexpr ((t) == 0) GU_A(idx, 0); if constexpr ((t) == 3) GU_B(ACC, 0); if constexpr ((t) == 4) GU_A(idx, 1); \
      if constexpr ((t) == 5) GU_C(0); if constexpr ((t) == 7) GU_B(ACC, 1); if constexpr ((t) == 9) GU_C(1); if constexpr ((t) == 10) GU_S(idx); } while (0)
    auto wd_load = [&](auto OC) {
      constexpr int i = decltype(OC)::value, s = i / 5, nb = i % 5;
      int st = s + (2 * jm) % dns; st = st >= dns ? st - dns : st; if (s == 13 && dns == 13) st = 0;
      ald_a<(nb & 3) * 1024>(k.wd_r[s][nb], rs, l16, wd_o + st * 5120 + (nb == 4 ? 4096 : 0));
    };
#pragma unroll
    for (int F = 0; F < DG; ++F) wb[F % NBG] = RDG(LDSG(F));
    sfor<240>([&](auto FC) {
      constexpr int F = decltype(FC)::value, Fr = F + DG;
      if constexpr (GS::x_at(F)) XLOAD((F >> 1) + XW);
      if constexpr (GS::op(F) >= 0 && GS::op(F) < 1000) dma_g<GS::op(F) / 8, GS::op(F) % 8>(rs, l16, B0, w, r4, lds0);
      if constexpr (GS::op(F) >= 1000) wd_load(std::integral_constant<int, GS::op(F) - 1000>{});
      if constexpr (Fr == 40) { WAITL0(); BAR(); }
      if constexpr (Fr == 80) { CERT("G4"); CERT("G5"); WAITL0(); BAR(); STAMP(12); }
      if constexpr (Fr == 120) { STAMP(21); WAITV(GS::upto(F) - GS::upto(GS::slot(7 * 8 + 4))); CERT("G6"); CERT("G7"); WAITL0(); BAR(); STAMP(22); }
      if constexpr (Fr == 160) { STAMP(23); WAITV(GS::upto(F) - GS::upto(GS::slot(9 * 8 + 4))); CERT("G8"); CERT("G9"); WAITL0(); BAR(); STAMP(24); }
      if constexpr (Fr == 200) { STAMP(25); WAITV(GS::upto(F) - GS::upto(GS::slot(11 * 8 + 4))); CERT("G10"); CERT("G11"); BAR(); STAMP(26); }
      if constexpr (Fr < 240) wb[Fr % NBG] = RDG(LDSG(Fr));
      constexpr int ps = F / 80, s = (F % 80) >> 1, b = F & 1;
      if constexpr (ps == 0 && b == 0) waitpin_v<GS::xwait(s)>(xf[s]);
      if constexpr (s == 0) ac[ps & 1][b] = mfma32(wb[F % NBG], xf[0], f32x16{}); else ac[ps & 1][b] = mfma32(wb[F % NBG], xf[s], ac[ps & 1][b]);
      if constexpr (F == 4) {
        pin2(sa0); pin2(sa1); pin2(sa2); pin2(sa3);
        float rstd;
        stats_finish(red32_add((u2f(sa0.x) + u2f(sa1.x)) + (u2f(sa2.x) + u2f(sa3.x))), red32_add((u2f(sa0.y) + u2f(sa1.y)) + (u2f(sa2.y) + u2f(sa3.y))), nmean, rstd);
        r2 = rstd * rstd;
      }
      if constexpr (ps > 0 && b == 0) {
        if constexpr (s >= 4 && s <= 14) GU_STAGE(ac[(ps - 1) & 1][0], 2 * (ps - 1), s - 4);
        if constexpr (s >= 20 && s <= 30) GU_STAGE(ac[(ps - 1) & 1][1], 2 * (ps - 1) + 1, s - 20);
      }
      SB();
    });
    STAMP(13);
    if (nblk == 7) {
      asm volatile("; BR7_BEGIN" ::: "memory");
      WAITV(GS::upto(239) - GS::upto(GS::slot(13 * 8 + 4))); CERT("G12"); CERT("G13"); BAR();
#pragma unroll
      for (int F = 240; F < 240 + DG; ++F) wb[F % NBG] = RDG(LDSG(F));
      sfor<40>([&](auto FC) {
        constexpr int F = 240 + decltype(FC)::value, Fr = F + DG;
        if constexpr (Fr < 280) wb[Fr % NBG] = RDG(LDSG(Fr));
        constexpr int s = F - 240;
        if constexpr (s == 0) ac[1][0] = mfma32(wb[F % NBG], xf[0], f32x16{}); else ac[1][0] = mfma32(wb[F % NBG], xf[s], ac[1][0]);
        if constexpr (s >= 2 && s <= 12) GU_STAGE(ac[0][0], 4, s - 2);
        if constexpr (s >= 14 && s <= 24) GU_STAGE(ac[0][1], 5, s - 14);
        SB();
      });
      sfor<11>([&](auto T) { GU_STAGE(ac[1][0], 6, decltype(T)::value); });
      asm volatile("; BR7_ELSE" ::: "memory");
    } else {
      asm volatile("; BR7_ELSE" ::: "memory");
      sfor<11>([&](auto T) { GU_STAGE(ac[0][0], 4, decltype(T)::value); });
      sfor<11>([&](auto T) { GU_STAGE(ac[0][1], 5, decltype(T)::value); });
      asm volatile("; BR7_END" ::: "memory");
    }
    asm volatile("; BR7_END" ::: "memory");
    SB();
    STAMP(14);
    WAITV(0);
    WAITL0();
    BAR();
    if (tid == 0) __hip_atomic_store(flags + (fgb + FG_H + m128) * 32 + p, 1u, RLX_AGENT);
    STAMP(15);
    sfor<70 - NWD>([&](auto I) {
      constexpr int i = NWD + decltype(I)::value, s = i / 5, nb = i % 5;
      int st = s + (2 * jm) % dns; st = st >= dns ? st - dns : st; if (s == 13 && dns == 13) st = 0;
      if constexpr (i < 60) ald_a<(nb & 3) * 1024>(k.wd_r[s][nb], rs, l16, wd_o + st * 5120 + (nb == 4 ? 4096 : 0));
      else ald_v<(nb & 3) * 1024>(k.wd_r[s][nb], rs, l16, wd_o + st * 5120 + (nb == 4 ? 4096 : 0));
    });
  }
  {
    PHASE_IDS();
    const int ds0 = w < 2 ? 14 * w : 28 + 13 * (w - 2), dns = w < 2 ? 14 : 13;
    STAMP(16);
    if (w == 0) poll_flags(flags, (fgb + FG_H + (m64 >> 1)) * 32 + (lane & 15), lane < 16, lane);
    WAITV(0);
    BAR();
    STAMP(17);
    sfor<70>([&](auto I) { constexpr int i = decltype(I)::value; if constexpr (i < 60) pin_a(k.wd_r[i / 5][i % 5]); else pin_v(k.wd_r[i / 5][i % 5]); });
    constexpr int DH = 14;
    u32x4 af[DH];
    const bool s13 = w < 2;
    const int rho0 = c8l % 3;
    u32 tbo[4], sto[14];
#pragma unroll
    for (int tbi = 0; tbi < 4; ++tbi) {
      int rho = tbi + rho0; rho = rho >= 3 ? rho - 3 : rho;
      const u32 tb = tbi < 3 ? (w + 1 + rho) & 3 : w;
      tbo[tbi] = hfo + ((4 * m64 + tb) * 54 + ds0) * 1024;
    }
    const int rot = (2 * jm) % dns;
#define HSTEP(s) ((((s) + rot >= dns) ? (s) + rot - dns : (s) + rot) * 1024 * (((s) == 13 && dns == 13) ? 0 : 1))
#define HLOAD(q) ald_v_sc1<0>(af[(q) % DH], rs, l16, tbo[(q) / 14] + HSTEP((q) % 14))
    (void)sto;
    sfor<DH>([&](auto I) { constexpr int q = decltype(I)::value; HLOAD(q); });
    f32x4 acc[2][5];
    sfor<56>([&](auto QC) {
      constexpr int q = decltype(QC)::value, tbi = q / 14, s = q % 14;
      constexpr int yf = (q + DH - 1 < 55 ? q + DH - 1 : 55) - q;
      constexpr int yd = (IT == 0 && q > 42) ? (q - 42 < 14 ? (q - 42) + ((q - 42) < 6 ? (q - 42) : 6) : 20) : 0;
      waitpin_v<yf + yd>(af[q % DH]);
      if constexpr (s == 0) {
#pragma unroll
        for (int nb = 0; nb < 5; ++nb) acc[tbi & 1][nb] = mfma16(k.wd_r[s][nb], af[q % DH], f32x4{0.f, 0.f, 0.f, 0.f});
      } else if (s < 13 || s13) {
#pragma unroll
        for (int nb = 0; nb < 5; ++nb) acc[tbi & 1][nb] = mfma16(k.wd_r[s][nb], af[q % DH], acc[tbi & 1][nb]);
      }
      if constexpr (tbi > 0 && s >= 1 && s < 6) {
        int rho = tbi - 1 + rho0; rho = rho >= 3 ? rho - 3 : rho;
        const u32 tbp = (w + 1 + rho) & 3;
        *(LAS f32x4*)(lds + L_RED + ((tbp * 3 + (2 - rho)) * 5 + (s - 1)) * 1024 + l16) = acc[(tbi - 1) & 1][s - 1];
      }
      SB();
      if constexpr (q + DH < 56) HLOAD(q + DH);
      if constexpr (IT == 0 && q >= 42) {
        constexpr int j0 = (q - 42) + ((q - 42) < 6 ? (q - 42) : 6);
        if constexpr (j0 < 10) dma_q<0, 0, j0>(rs, l16, p, w, r4, lds0); else dma_q<1, 1, j0 - 10>(rs, l16, p, w, r4, lds0);
        if constexpr (q - 42 < 6) {
          constexpr int j1 = j0 + 1;
          if constexpr (j1 < 10) dma_q<0, 0, j1>(rs, l16, p, w, r4, lds0); else dma_q<1, 1, j1 - 10>(rs, l16, p, w, r4, lds0);
        }
      }
      SB();
    });
    STAMP(18);
    WAITL0(); BAR();
    float s1 = 0.f, s2 = 0.f;
#pragma unroll
    for (int nb = 0; nb < 5; ++nb) {
      f32x4 v = acc[1][nb];
#pragma unroll
      for (int sl = 0; sl < 3; ++sl) v += *(const LAS f32x4*)(lds + L_RED + ((w * 3 + sl) * 5 + nb) * 1024 + l16);
      const u32 a = cvtpk(bflo(k.xr[nb].x) + v[0], bfhi(k.xr[nb].x) + v[1]), b = cvtpk(bflo(k.xr[nb].y) + v[2], bfhi(k.xr[nb].y) + v[3]);
      k.xr[nb] = u32x2{a, b};
    }
    const u32 token = 64 * m64 + 16 * w + t16;
    STAMP(19);
    if constexpr (IT == 0) {
      const u32 xo = ((token >> 5) * 40 + 5 * c8) * 1024 + ((token & 31) + 32 * (g >> 1)) * 16;
#pragma unroll
      for (int pr = 0; pr < 3; ++pr) {
        const int A = 2 * pr, Bn = pr < 2 ? 2 * pr + 1 : 4;
        auto r0 = __builtin_amdgcn_permlane16_swap(k.xr[A].x, k.xr[Bn].x, false, false);
        auto r1 = __builtin_amdgcn_permlane16_swap(k.xr[A].y, k.xr[Bn].y, false, false);
        const u32x4 v = {r0[0], r1[0], r0[1], r1[1]};
        if (pr < 2) stg_sc1(v, rs, xo + (A + (g & 1)) * 1024, OFF_XF + 2 * SZ_XF);
        else if ((g & 1) == 0) stg_sc1(v, rs, xo + 4 * 1024, OFF_XF + 2 * SZ_XF);
      }
#pragma unroll
      for (int nb = 0; nb < 5; ++nb) {
        const float a = bflo(k.xr[nb].x), b = bfhi(k.xr[nb].x), c = bflo(k.xr[nb].y), d = bfhi(k.xr[nb].y);
        s1 += (a + b) + (c + d); s2 += (a * a + b * b) + (c * c + d * d);
      }
      s1 = red16_32_add(s1); s2 = red16_32_add(s2);
      if (g == 0) stg2_sc1(u32x2{f2u(s1), f2u(s2)}, rs, (c8 * 2048 + token) * 8, OFF_ST + 2 * SZ_ST);
      SB();
      WAITV(0);
      WAITL0();
      BAR();
      if (tid == 0) __hip_atomic_store(flags + (FG_X2 + m64) * 32 + c8, 1u, RLX_AGENT);
      sfor<10>([&](auto I) { dma_q<2, 2, decltype(I)::value>(rs, l16, p, w, r4, lds0); });
    } else {
      const u32 oo = token * 1280 + (80 * c8 + 8 * (g >> 1)) * 2;
#pragma unroll
      for (int pr = 0; pr < 3; ++pr) {
        const int A = 2 * pr, Bn = pr < 2 ? 2 * pr + 1 : 4;
        auto r0 = __builtin_amdgcn_permlane16_swap(k.xr[A].x, k.xr[Bn].x, false, false);
        auto r1 = __builtin_amdgcn_permlane16_swap(k.xr[A].y, k.xr[Bn].y, false, false);
        const u32x4 v = {r0[0], r1[0], r0[1], r1[1]};
        if (pr < 2) __builtin_amdgcn_raw_buffer_store_b128(v, k.rso, oo + (A + (g & 1)) * 32, 0, 0);
        else if ((g & 1) == 0) __builtin_amdgcn_raw_buffer_store_b128(v, k.rso, oo + 4 * 32, 0, 0);
      }
    }
    STAMP(20);
  }
}

__global__ __launch_bounds__(256, 1) void main_kernel(char* __restrict__ ws, u16* __restrict__ out) {
  extern __shared__ __attribute__((aligned(16))) char smem[];
  K k;
  k.lds = (LAS char*)smem;
  k.lds0 = (u32)(size_t)k.lds;
  k.tid = threadIdx.x; k.lane = k.tid & 63; k.w = __builtin_amdgcn_readfirstlane(k.tid >> 6);
  k.rs = __builtin_amdgcn_make_buffer_rsrc(ws, 0, WS_USED, 0x00020000);
  k.rso = __builtin_amdgcn_make_buffer_rsrc(out, 0, 2048 * 640 * 2, 0x00020000);
  k.flags = (GAS u32*)ws;
  const int bid = blockIdx.x, xc = bid & 7, j = bid >> 3, mg = xc >> 1, half = xc & 1;
  k.m128 = 4 * mg + (j >> 3); k.p = 8 * half + (j & 7);
  k.m64 = 8 * mg + (j >> 2); k.c8 = 4 * half + (j & 3);
  const int p7 = j & 7;
  k.B0 = 54 * half + (p7 < 6 ? 7 * p7 : 42 + 6 * (p7 - 6));
  k.nblk = p7 < 6 ? 7 : 6;
  k.r4 = 0; k.jm = 0; k.c8l = 0;
  iteration<0>(k, out);
  iteration<1>(k, out);
}

extern "C" void kernel_launch(void* const* d_in, const int* in_sizes, int n_in, void* d_out, int out_size, void* d_ws, size_t ws_size,
                              hipStream_t stream) {
  const u16* x0 = (const u16*)d_in[0];
  const u16* wqkv = (const u16*)d_in[1];
  const u16* wo = (const u16*)d_in[2];
  const u16* wg = (const u16*)d_in[4];
  const u16* wu = (const u16*)d_in[5];
  const u16* wd = (const u16*)d_in[6];
  (void)hipFuncSetAttribute((const void*)main_kernel, hipFuncAttributeMaxDynamicSharedMemorySize, LDS_BYTES);
  prep_kernel<<<312, 256, 0, stream>>>(x0, wqkv, wo, wg, wu, wd, (char*)d_ws);
  main_kernel<<<256, 256, LDS_BYTES, stream>>>((char*)d_ws, (u16*)d_out);
}
```

```cpp
#include <hip/hip_runtime.h>
#include <stdint.h>
#include <utility>

#define LAS __attribute__((address_space(3)))
#define GAS __attribute__((address_space(1)))
#define DI __device__ __forceinline__
typedef unsigned u32;
typedef unsigned short u16;
typedef u32 u32x4 __attribute__((ext_vector_type(4)));
typedef u32 u32x2 __attribute__((ext_vector_type(2)));
typedef float f32x2 __attribute__((ext_vector_type(2)));
typedef float f32x4 __attribute__((ext_vector_type(4)));
typedef float f32x16 __attribute__((ext_vector_type(16)));
typedef short s16x8 __attribute__((ext_vector_type(8)));
typedef short s16x4 __attribute__((ext_vector_type(4)));
typedef __bf16 bf16x2 __attribute__((ext_vector_type(2)));
typedef __amdgpu_buffer_rsrc_t rsrc_t;

constexpr u32 FLAG_BYTES = 32768;
constexpr u32 OFF_WQ = 65536;
constexpr u32 OFF_WO = OFF_WQ + 16 * 163840;
constexpr u32 OFF_WG = OFF_WO + 8 * 20 * 5 * 1024;
constexpr u32 OFF_WD = OFF_WG + 108 * 40960;
constexpr u32 OFF_CSQ = OFF_WD + 8 * 54 * 5 * 1024;
constexpr u32 OFF_CSG = OFF_CSQ + 16384;
constexpr u32 SZ_XF = 64 * 40 * 1024;
constexpr u32 OFF_XF = OFF_CSG + 28672;
constexpr u32 SZ_ST = 8 * 2048 * 8;
constexpr u32 OFF_ST = OFF_XF + 4 * SZ_XF;
constexpr u32 SZ_OF = 128 * 20 * 1024;
constexpr u32 OFF_OF = OFF_ST + 4 * SZ_ST;
constexpr u32 SZ_HF = 128 * 54 * 1024;
constexpr u32 OFF_HF = OFF_OF + 2 * SZ_OF;
constexpr u32 WS_USED = OFF_HF + 2 * SZ_HF;
constexpr u32 FG_O = 0, FG_X1 = 16, FG_H = 48, FG_X2 = 64, FG_PER_IT = 96;
constexpr u32 FG_TMO = 2 * FG_PER_IT;

constexpr u32 L_HALO = 122880;
constexpr u32 L_KST = 40960;
constexpr u32 L_VST = L_KST + 144 * 80;
constexpr u32 L_QST = L_VST + 144 * 80;
constexpr u32 L_RED = 100352;
constexpr u32 L_H4 = 143360;
constexpr u32 LDS_BYTES = 163840;

constexpr u32 AUX_SC1 = 16;
#define RLX_AGENT __ATOMIC_RELAXED, __HIP_MEMORY_SCOPE_AGENT
#define SB() __builtin_amdgcn_sched_barrier(0)
#define MEMFENCE() asm volatile("" ::: "memory")
#define WAITV(n) asm volatile("s_waitcnt vmcnt(%0)" ::"i"(n) : "memory")
#define WAITL0() asm volatile("s_waitcnt lgkmcnt(0)" ::: "memory")
#define BAR() do { MEMFENCE(); __builtin_amdgcn_s_barrier(); MEMFENCE(); } while (0)
#define CERT(tag) asm volatile("; CERT " tag ::: "memory")

template <int... I, class Fn> DI void sfor_impl(std::integer_sequence<int, I...>, Fn&& f) { (f(std::integral_constant<int, I>{}), ...); }
template <int N, class Fn> DI void sfor(Fn&& f) { sfor_impl(std::make_integer_sequence<int, N>{}, f); }
DI f32x16 mfma32(u32x4 a, u32x4 b, f32x16 c) { return __builtin_amdgcn_mfma_f32_32x32x16_bf16(__builtin_bit_cast(s16x8, a), __builtin_bit_cast(s16x8, b), c, 0, 0, 0); }
DI f32x4 mfma16(u32x4 a, u32x4 b, f32x4 c) { return __builtin_amdgcn_mfma_f32_16x16x32_bf16(__builtin_bit_cast(s16x8, a), __builtin_bit_cast(s16x8, b), c, 0, 0, 0); }
DI u32 cvtpk(float lo, float hi) { f32x2 v = {lo, hi}; bf16x2 b = __builtin_convertvector(v, bf16x2); return __builtin_bit_cast(u32, b); }
DI float u2f(u32 v) { return __builtin_bit_cast(float, v); }
DI u32 f2u(float v) { return __builtin_bit_cast(u32, v); }
DI float bflo(u32 v) { return __builtin_bit_cast(float, v << 16); }
DI float bfhi(u32 v) { return __builtin_bit_cast(float, v & 0xffff0000u); }
DI float fma_s(float a, float b, float c) { float r; asm("v_fma_f32 %0, %1, %2, %3" : "=v"(r) : "v"(a), "v"(b), "v"(c)); return r; }
DI float mul_s(float a, float b) { float r; asm("v_mul_f32_e32 %0, %1, %2" : "=v"(r) : "v"(a), "v"(b)); return r; }
DI float add_s(float a, float b) { float r; asm("v_add_f32_e32 %0, %1, %2" : "=v"(r) : "v"(a), "v"(b)); return r; }
DI float max_s(float a, float b) { float r; asm("v_max_f32_e32 %0, %1, %2" : "=v"(r) : "v"(a), "v"(b)); return r; }
DI float sel_s(float f, float t, unsigned long long m) { float r; asm("v_cndmask_b32_e64 %0, %1, %2, %3" : "=v"(r) : "v"(f), "v"(t), "s"(m)); return r; }
DI float red16_32_add(float v) {
  auto a = __builtin_amdgcn_permlane16_swap(f2u(v), f2u(v), false, false);
  v = u2f(a[0]) + u2f(a[1]);
  auto b = __builtin_amdgcn_permlane32_swap(f2u(v), f2u(v), false, false);
  return u2f(b[0]) + u2f(b[1]);
}
DI float red16_32_max(float v) {
  auto a = __builtin_amdgcn_permlane16_swap(f2u(v), f2u(v), false, false);
  v = __builtin_fmaxf(u2f(a[0]), u2f(a[1]));
  auto b = __builtin_amdgcn_permlane32_swap(f2u(v), f2u(v), false, false);
  return __builtin_fmaxf(u2f(b[0]), u2f(b[1]));
}
DI float red32_add(float v) {
  auto b = __builtin_amdgcn_permlane32_swap(f2u(v), f2u(v), false, false);
  return u2f(b[0]) + u2f(b[1]);
}
DI void stg_sc1(u32x4 v, rsrc_t rs, u32 voff, u32 soff) { __builtin_amdgcn_raw_buffer_store_b128(v, rs, voff, soff, AUX_SC1); }
DI void stg2_sc1(u32x2 v, rsrc_t rs, u32 voff, u32 soff) { __builtin_amdgcn_raw_buffer_store_b64(v, rs, voff, soff, AUX_SC1); }

template <int IMM> DI void ald_v_sc1(u32x4& d, rsrc_t rs, u32 voff, u32 soff) {
  asm volatile("buffer_load_dwordx4 %0, %1, %2, %3 offen offset:%4 sc1" : "=v"(d) : "v"(voff), "s"(rs), "s"(soff), "i"(IMM) : "memory");
}
template <int IMM> DI void ald_a(u32x4& d, rsrc_t rs, u32 voff, u32 soff) {
  asm volatile("buffer_load_dwordx4 %0, %1, %2, %3 offen offset:%4" : "=a"(d) : "v"(voff), "s"(rs), "s"(soff), "i"(IMM) : "memory");
}
template <int IMM> DI void ald_v(u32x4& d, rsrc_t rs, u32 voff, u32 soff) {
  asm volatile("buffer_load_dwordx4 %0, %1, %2, %3 offen offset:%4" : "=v"(d) : "v"(voff), "s"(rs), "s"(soff), "i"(IMM) : "memory");
}
DI void ald2_sc1(u32x2& d, rsrc_t rs, u32 voff, u32 soff) {
  asm volatile("buffer_load_dwordx2 %0, %1, %2, %3 offen sc1" : "=v"(d) : "v"(voff), "s"(rs), "s"(soff) : "memory");
}
DI void ald2(u32x2& d, rsrc_t rs, u32 voff, u32 soff) {
  asm volatile("buffer_load_dwordx2 %0, %1, %2, %3 offen" : "=v"(d) : "v"(voff), "s"(rs), "s"(soff) : "memory");
}
DI void ald1(u32& d, rsrc_t rs, u32 voff, u32 soff) {
  asm volatile("buffer_load_dword %0, %1, %2, %3 offen" : "=v"(d) : "v"(voff), "s"(rs), "s"(soff) : "memory");
}
template <int IMM> DI void ald_a_sc1(u32x4& d, rsrc_t rs, u32 voff, u32 soff) {
  asm volatile("buffer_load_dwordx4 %0, %1, %2, %3 offen offset:%4 sc1" : "=a"(d) : "v"(voff), "s"(rs), "s"(soff), "i"(IMM) : "memory");
}
template <int N> DI void waitpin_a(u32x4& r) { asm volatile("s_waitcnt vmcnt(%1)" : "+a"(r) : "i"(N) : "memory"); }
template <int N> DI void waitpin_a2(u32x4& r0, u32x4& r1) { asm volatile("s_waitcnt vmcnt(%2)" : "+a"(r0), "+a"(r1) : "i"(N) : "memory"); }
DI void mma32(f32x16& acc, const u32x4& wv, const u32x4& xa) { asm volatile("v_mfma_f32_32x32x16_bf16 %0, %1, %2, %0" : "+v"(acc) : "v"(wv), "a"(xa)); }
DI void mma32_0(f32x16& acc, const u32x4& wv, const u32x4& xa) { asm volatile("v_mfma_f32_32x32x16_bf16 %0, %1, %2, 0" : "=&v"(acc) : "v"(wv), "a"(xa)); }
DI void mma32w(f32x16& acc, const u32x4& wv, const u32x4& xa, const u32x4& wn) { asm volatile("v_mfma_f32_32x32x16_bf16 %0, %1, %2, %0" : "+v"(acc) : "v"(wv), "a"(xa), "v"(wn)); }
DI void mma32w_0(f32x16& acc, const u32x4& wv, const u32x4& xa, const u32x4& wn) { asm volatile("v_mfma_f32_32x32x16_bf16 %0, %1, %2, 0" : "=&v"(acc) : "v"(wv), "a"(xa), "v"(wn)); }
DI void mma16(f32x4& acc, const u32x4& av, const u32x4& bv) { asm volatile("v_mfma_f32_16x16x32_bf16 %0, %1, %2, %0" : "+v"(acc) : "v"(av), "v"(bv)); }
DI void mma16_0(f32x4& acc, const u32x4& av, const u32x4& bv) { asm volatile("v_mfma_f32_16x16x32_bf16 %0, %1, %2, 0" : "=&v"(acc) : "v"(av), "v"(bv)); }
DI void pin_vv(u32x4& a, u32x4& b) { asm volatile("" : "+v"(a), "+v"(b)); }
template <int N> DI void waitpin_v(u32x4& r) { asm volatile("s_waitcnt vmcnt(%1)" : "+v"(r) : "i"(N) : "memory"); }
DI void pin_v(u32x4& r) { asm volatile("" : "+v"(r)::"memory"); }
DI void pin_a(u32x4& r) { asm volatile("" : "+a"(r)::"memory"); }
DI void pin2(u32x2& r) { asm volatile("" : "+v"(r)::"memory"); }
DI void pin1(u32& r) { asm volatile("" : "+v"(r)::"memory"); }
#define DMA_F(TAG, IMM, SC, rs, voff, soff, m0v) \
  asm volatile("s_mov_b32 m0, %2\n\ts_nop 0\n\tbuffer_load_dwordx4 %0, %1, %3 offen offset:%4 " SC "lds ; DMA " TAG ::"v"(voff), "s"(rs), "s"(m0v), "s"(soff), "i"(IMM) : "memory")
#define DMA_N(TAG, IMM, SC, rs, voff, soff) \
  asm volatile("buffer_load_dwordx4 %0, %1, %2 offen offset:%3 " SC "lds ; DMA " TAG ::"v"(voff), "s"(rs), "s"(soff), "i"(IMM) : "memory")

constexpr int PREP_UNITS = 84 + 20 + 60 + 64;
__global__ __launch_bounds__(512) void prep_kernel(const u16* __restrict__ x0, const u16* __restrict__ wqkv, const u16* __restrict__ wo,
                                                   const u16* __restrict__ wg, const u16* __restrict__ wu, const u16* __restrict__ wd, char* __restrict__ ws) {
  __shared__ __attribute__((aligned(16))) char lds[81920 + 1024];
  const int tid = threadIdx.x, l = tid & 63, w = __builtin_amdgcn_readfirstlane(tid >> 6);
  const int u = blockIdx.x;
  if (u == PREP_UNITS - 1) {
    for (int i = tid; i < (int)(FLAG_BYTES / 16); i += 512) ((u32x4*)ws)[i] = u32x4{0, 0, 0, 0};
  }
  const int gam = l >> 4, q = (l & 15) >> 2, pp = l & 3;
  if (u < 84) {
    const int type = u < 30 ? 0 : u < 57 ? 1 : 2, cg = u < 30 ? u : u < 57 ? u - 30 : u - 57;
    const u16* src = type == 0 ? wqkv : type == 1 ? wg : wu;
    const int pitch = type == 0 ? 1920 : 1728;
#pragma unroll
    for (int i = 0; i < 10; ++i) {
      const int r = 8 * i + w;
      const u16* g = src + (size_t)(8 * r + (l >> 3)) * pitch + 64 * cg + (l & 7) * 8;
      __builtin_amdgcn_global_load_lds((const GAS u32*)g, (LAS u32*)(lds + r * 1024), 16, 0, 0);
    }
    const int c = 64 * cg + l;
    u32 dbase; int dstep;
    if (type == 0) {
      const int sec = c / 640, cc = c % 640, p = cc / 40, r40 = cc % 40;
      const int n = (sec == 1 ? 0 : sec == 2 ? 40 : 80) + r40;
      const int b = n >> 5, nl = n & 31;
      dbase = OFF_WQ + p * 163840 + (b < 2 ? b * 1024 : 81920 + (b - 2) * 40960) + nl * 16;
      dstep = b < 2 ? 2048 : 1024;
    } else {
      const int fb = c >> 4, nl = (c & 15) + 16 * (type - 1);
      const int half = fb / 54, rem = fb % 54;
      const int idx = rem < 42 ? rem % 7 : (rem - 42) % 6, B0 = 54 * half + rem - idx;
      const bool paired = idx < 6;
      dbase = OFF_WG + (paired ? (B0 + (idx & ~1)) * 40960 + (idx & 1) * 1024 : (B0 + 6) * 40960) + nl * 16;
      dstep = paired ? 2048 : 1024;
    }
    __syncthreads();
    u32x4 sl[10];
    float csum = 0.f;
#pragma unroll
    for (int i = 0; i < 10; ++i) {
      const int o = 8 * i + w;
      const u32x2 a = __builtin_bit_cast(u32x2, __builtin_amdgcn_ds_read_tr16_b64_v4i16((LAS s16x4*)(lds + (8 * o + q) * 128 + gam * 32 + pp * 8)));
      const u32x2 b2 = __builtin_bit_cast(u32x2, __builtin_amdgcn_ds_read_tr16_b64_v4i16((LAS s16x4*)(lds + (8 * o + 4 + q) * 128 + gam * 32 + pp * 8)));
      sl[i] = u32x4{a.x, a.y, b2.x, b2.y};
    }
#pragma unroll
    for (int i = 0; i < 10; ++i)
#pragma unroll
      for (int j = 0; j < 4; ++j) csum += bflo(sl[i][j]) + bfhi(sl[i][j]);
    __syncthreads();
    float* red = (float*)lds;
    red[w * 64 + l] = csum;
    __syncthreads();
    const float mean = (((red[l] + red[64 + l]) + (red[128 + l] + red[192 + l])) + ((red[256 + l] + red[320 + l]) + (red[384 + l] + red[448 + l]))) * (1.0f / 640.0f);
#pragma unroll
    for (int i = 0; i < 10; ++i) {
      const int o = 8 * i + w;
      u32x4 v;
#pragma unroll
      for (int j = 0; j < 4; ++j) v[j] = cvtpk(bflo(sl[i][j]) - mean, bfhi(sl[i][j]) - mean);
      *(u32x4*)(ws + dbase + (o >> 1) * dstep + (o & 1) * 512) = v;
    }
  } else if (u < 164) {
    const bool iswo = u < 104;
    const int v = iswo ? u - 84 : u - 104, cg = iswo ? v >> 1 : v / 6, kk = iswo ? v & 1 : v % 6;
    const u16* src = iswo ? wo : wd;
    const int nrows = iswo ? 320 : 288, row0 = kk * nrows, noct = nrows / 8;
#pragma unroll
    for (int i = 0; i < 5; ++i) {
      const int r = 8 * i + w;
      if (r < noct) {
        const u16* g = src + (size_t)(row0 + 8 * r + (l >> 3)) * 640 + 64 * cg + (l & 7) * 8;
        __builtin_amdgcn_global_load_lds((const GAS u32*)g, (LAS u32*)(lds + r * 1024), 16, 0, 0);
      }
    }
    const int c = 64 * cg + l, c8 = c / 80, nb = (c % 80) >> 4, n = c & 15;
    const u32 dbase = (iswo ? OFF_WO + c8 * 20 * 5120 : OFF_WD + c8 * 54 * 5120) + nb * 1024 + n * 16;
    __syncthreads();
#pragma unroll
    for (int i = 0; i < 5; ++i) {
      const int o = 8 * i + w;
      if (o < noct) {
        int r0, r1; u32 dst;
        if (iswo) {
          const int pl = o / 5, ii = o % 5, sg = 40 * kk + o;
          r0 = 40 * pl + 4 * ii + q; r1 = r0 + 20;
          dst = dbase + (sg >> 2) * 5120 + (sg & 3) * 256;
        } else {
          const int s_ = o >> 2, g = o & 3;
          r0 = 32 * s_ + 16 * (g >> 1) + 4 * (g & 1) + q; r1 = r0 + 8;
          dst = dbase + (9 * kk + s_) * 5120 + g * 256;
        }
        const u32x2 a = __builtin_bit_cast(u32x2, __builtin_amdgcn_ds_read_tr16_b64_v4i16((LAS s16x4*)(lds + r0 * 128 + gam * 32 + pp * 8)));
        const u32x2 b2 = __builtin_bit_cast(u32x2, __builtin_amdgcn_ds_read_tr16_b64_v4i16((LAS s16x4*)(lds + r1 * 128 + gam * 32 + pp * 8)));
        *(u32x4*)(ws + dst) = u32x4{a.x, a.y, b2.x, b2.y};
      }
    }
  } else {
    const int rb = u - 164;
#pragma unroll
    for (int i = 0; i < 5; ++i) {
      const int cidx = 64 * (8 * i + w) + l, row = cidx / 80, cc = cidx % 80;
      const u16* g = x0 + (size_t)(32 * rb + row) * 640 + ((cc ^ (row & 7)) * 8);
      __builtin_amdgcn_global_load_lds((const GAS u32*)g, (LAS u32*)(lds + (8 * i + w) * 1024), 16, 0, 0);
    }
    __syncthreads();
    const int tok = l & 31, h = l >> 5;
    float s1 = 0.f, s2 = 0.f;
#pragma unroll
    for (int i = 0; i < 5; ++i) {
      const int s = 5 * w + i, cc = 2 * s + h;
      const u32x4 v = *(const u32x4*)(lds + tok * 1280 + ((cc ^ (tok & 7)) * 16));
      *(u32x4*)(ws + OFF_XF + (rb * 40 + s) * 1024 + l * 16) = v;
#pragma unroll
      for (int j = 0; j < 4; ++j) { float a = bflo(v[j]), b = bfhi(v[j]); s1 += a + b; s2 += a * a + b * b; }
    }
    s1 = red32_add(s1); s2 = red32_add(s2);
    if (l < 32) {
      f32x2* st = (f32x2*)(ws + OFF_ST);
      st[w * 2048 + 32 * rb + tok] = f32x2{s1, s2};
    }
  }
}

#define STAMP(i) do {} while (0)
#define RSTAMP(i) do {} while (0)
#define GSTAMP(i) STAMP(i)
#define QSTAMP(i) do {} while (0)
constexpr u32 OFF_STAMP = WS_USED + 4096;
constexpr u32 kSpinCap = 1u << 16;

struct K {
  rsrc_t rs, rso;
  LAS char* lds;
  u32 lds0;
  GAS u32* flags;
  int lane, w, m128, p, m64, c8, B0, nblk, tid;
  u32x2 xr[5];
  u32x4 wo_r[5][5];
  u32x4 wd_r[14][5];
};

#define PHASE_IDS() int lane = k.lane; asm volatile("" : "+v"(lane)); \
  int w = k.w, m128 = k.m128, p = k.p, m64 = k.m64, c8 = k.c8, B0 = k.B0; \
  asm volatile("" : "+s"(w), "+s"(m128), "+s"(p), "+s"(m64), "+s"(c8), "+s"(B0)); \
  const u32 l16 = lane * 16; const int tok = lane & 31, h = lane >> 5, t16 = lane & 15, g = lane >> 4; \
  const rsrc_t rs = k.rs; LAS char* const lds = k.lds; const u32 lds0 = k.lds0; \
  (void)tok; (void)h; (void)t16; (void)g; (void)l16; (void)m128; (void)p; (void)m64; (void)c8; (void)B0; (void)lds; (void)lds0; (void)rs

DI void poll_flags(GAS u32* flags, u32 fo, bool act, int lane) {
  for (u32 spins = 0;;) {
    u32 v = act ? __hip_atomic_load(flags + fo, RLX_AGENT) : 1u;
    if (__all(v == 1u)) break;
    __builtin_amdgcn_s_sleep(1);
    if (++spins > kSpinCap) {
      if (lane == 0) __hip_atomic_store(flags + FG_TMO * 32, 1u, RLX_AGENT);
      break;
    }
    if ((spins & 255u) == 0u && __builtin_amdgcn_readfirstlane(__hip_atomic_load(flags + FG_TMO * 32, RLX_AGENT)) != 0u) break;
  }
}
DI void stats_finish(float s1, float s2, float& nmean, float& rstd) {
  const float mean = s1 * (1.0f / 640.0f);
  const float var = __builtin_fmaxf(s2 * (1.0f / 640.0f) - mean * mean, 0.f);
  nmean = -mean; rstd = __builtin_amdgcn_rsqf(var + 1e-5f);
}

template <int C, int SLOT, int I> DI void dma_q(rsrc_t rs, u32 l16, u32 sQ, int w, u32 lds0) {
#define Q_EMIT(TAG, FIRST, IMM, SO, MO) do { if constexpr (FIRST) DMA_F(TAG, IMM, "", rs, l16, SO, MO); else DMA_N(TAG, IMM, "", rs, l16, SO); } while (0)
  if constexpr (C < 2) {
    constexpr int grp = I / 4, imm = (I % 4) * 1024;
    const u32 so = sQ + w * 10240 + C * 40960 + grp * 4096, mo = lds0 + SLOT * 40960 + w * 10240 + grp * 4096;
    if constexpr (C == 0) Q_EMIT("Q0", I % 4 == 0, imm, so, mo); else Q_EMIT("Q1", I % 4 == 0, imm, so, mo);
  } else {
    constexpr int hf = I / 5, ii = I % 5, grp = ii / 4, imm = (ii % 4) * 1024;
    const u32 so = sQ + w * 5120 + 81920 + (C - 2) * 40960 + hf * 20480 + grp * 4096, mo = lds0 + SLOT * 40960 + w * 5120 + hf * 20480 + grp * 4096;
    if constexpr (C == 2) Q_EMIT("Q2", ii % 4 == 0, imm, so, mo);
    else if constexpr (hf == 0) Q_EMIT("Q3a", ii % 4 == 0, imm, so, mo);
    else Q_EMIT("Q3b", ii % 4 == 0, imm, so, mo);
  }
#undef Q_EMIT
}
template <int HH, int I> DI void dma_g(rsrc_t rs, u32 l16, u32 sG, int w, u32 lds0) {
  constexpr int grp = I / 4, imm = (I % 4) * 1024;
  const u32 so = sG + HH * 20480 + grp * 4096, mo = lds0 + w * 5120 + (HH % 7) * 20480 + grp * 4096;
#define G_CASE(n) if constexpr (HH == n) { if constexpr (I % 4 == 0) DMA_F("G" #n, imm, "", rs, l16, so, mo); else DMA_N("G" #n, imm, "", rs, l16, so); }
  G_CASE(0) G_CASE(1) G_CASE(2) G_CASE(3) G_CASE(4) G_CASE(5) G_CASE(6) G_CASE(7) G_CASE(8) G_CASE(9) G_CASE(10) G_CASE(11) G_CASE(12) G_CASE(13)
#undef G_CASE
}

constexpr int XW = 10;
template <int IT> struct QS {
  static constexpr int Q3F0 = 78;
  static constexpr bool x_at(int f) { return f < 80 && (f & 1) == 0 && (f >> 1) + XW < 40; }
  static constexpr int aux_at(int f) { return (IT != 0 && f < 22 && (f & 1) == 1) ? f >> 1 : -1; }
  static constexpr bool q1_at(int f) { return IT == 0 && f < 20 && (f & 1) == 1; }
  static constexpr bool q2_at(int f) { return IT == 0 && f >= 40 && f < 60 && (f & 1) == 1; }
  static constexpr bool q3_at(int f) { return f >= Q3F0 && f < Q3F0 + 20 && ((f - Q3F0) & 1) == 0; }
  static constexpr int wo_at(int f) {
    if (f >= 60 && f < 76 && (f & 1) == 0) return (f - 60) / 2;
    if (f >= 100 && f < 151 && (f - 100) % 3 == 0) return 8 + (f - 100) / 3;
    return -1;
  }
  static constexpr int upto(int F) { int n = 0; for (int f = 0; f <= F; ++f) n += x_at(f) + (aux_at(f) >= 0) + q1_at(f) + q2_at(f) + q3_at(f) + (wo_at(f) >= 0); return n; }
  static constexpr int xidx(int i) { return i < XW ? i : XW + upto(2 * (i - XW)) - 1; }
  static constexpr int xwait(int s) { return XW + upto(2 * s) - 1 - xidx(s); }
};
struct GS {
  static constexpr int NWD = 50;
  static constexpr bool x_at(int f) { return f < 80 && (f & 1) == 0 && (f >> 1) + XW < 40; }
  static constexpr int op(int F) {
    if (F < 8 && (F & 1)) return 3000 + (F >> 1);
    if (F & 1) return -1;
    if (F >= 62 && F < 82) return (7 + (F - 62) / 10) * 8 + ((F - 62) % 10) / 2;
    if (F >= 82 && F < 102) return (9 + (F - 82) / 10) * 8 + ((F - 82) % 10) / 2;
    if (F >= 102 && F < 118) return 1000 + (F - 102) / 2;
    if (F >= 118 && F < 138) return (11 + (F - 118) / 10) * 8 + ((F - 118) % 10) / 2;
    if (F >= 138 && F < 158) return 1008 + (F - 138) / 2;
    if (F >= 158 && F < 168) return 13 * 8 + (F - 158) / 2;
    if (F >= 168 && F < 240 && 18 + (F - 168) / 2 < NWD) return 1018 + (F - 168) / 2;
    return -1;
  }
  static constexpr bool st_at(int F) { const int ps = F / 80, r = F % 80; return F < 240 && ps > 0 && (r == 37 || r == 77); }
  static constexpr int upto(int F) { int n = 0; for (int f = 0; f <= F; ++f) n += x_at(f) + (op(f) >= 0) + st_at(f); return n; }
  static constexpr int slot(int o) { for (int f = 0; f < 300; ++f) if (op(f) == o) return f; return -1; }
  static constexpr int xidx(int i) { return i < XW ? i : XW + upto(2 * (i - XW)) - 1; }
  static constexpr int xwait(int s) { return XW + upto(2 * s) - 1 - xidx(s); }
};
static_assert(18 + (240 - 168) / 2 >= GS::NWD, "all prefetched Wd fragments need a slot");

template <int IT> DI void iteration(K& k) {
  constexpr u32 xin = OFF_XF + (IT == 0 ? 0 : 2) * SZ_XF, stin = OFF_ST + (IT == 0 ? 0 : 2) * SZ_ST;
  constexpr u32 x1o = OFF_XF + (IT == 0 ? 1 : 3) * SZ_XF, st1o = OFF_ST + (IT == 0 ? 1 : 3) * SZ_ST;
  constexpr u32 ofo = OFF_OF + IT * SZ_OF, hfo = OFF_HF + IT * SZ_HF;
  constexpr u32 fgb = IT * FG_PER_IT;
  GAS u32* const flags = k.flags;
  const int tid = k.tid;
  {
    PHASE_IDS();
    const u32 sQ = OFF_WQ + p * 163840, sG = OFF_WG + B0 * 40960 + w * 5120;
    STAMP(0);
    if constexpr (IT == 0) {
      sfor<10>([&](auto I) { dma_q<0, 0, decltype(I)::value>(rs, l16, sQ, w, lds0); });
      const u32 token = 64 * m64 + 16 * w + t16;
      const u32 o = ((token >> 5) * 40 + 5 * c8) * 1024 + ((token & 31) + 32 * (g >> 1)) * 16 + (g & 1) * 8;
#pragma unroll
      for (int nb = 0; nb < 5; ++nb) ald2(k.xr[nb], rs, o, OFF_XF + nb * 1024);
    } else {
      if (w == 0) {
        const int grp = 2 * m128 - 1 + (lane >> 3);
        const bool act = lane < 24 && grp >= 0;
        poll_flags(flags, (FG_X2 + grp) * 32 + (lane & 7), act, lane);
      } else { WAITV(0); }
      CERT("Q0"); CERT("Q1"); CERT("Q2");
      BAR();
    }
    STAMP(1);
    u32x4 xf[40];
    const u32 xbase = xin + (4 * m128 + w) * 40960;
#define XLOAD(i) ald_a_sc1<((i) % 4) * 1024>(xf[i], rs, l16, xbase + ((i) / 4) * 4096)
    u32x2 hs0, hs1, sa0, sa1, sa2, sa3;
    const int rbh = m128 > 0 ? 4 * m128 - 1 : 0;
    const u32 hvo = (g >> 1) * 1024 + (g & 1) * 512 + (16 + t16) * 16;
    const int ht = 128 * m128 - 16 + t16;
    const u32 hso = ((2 * g) * 2048 + (ht < 0 ? 0 : ht)) * 8, sso = (4 * h * 2048 + 128 * m128 + 32 * w + tok) * 8;
    auto aux_load = [&](auto AC) {
      constexpr int a = decltype(AC)::value;
      if constexpr (a < 5) DMA_F("HALO", 0, "sc1 ", rs, hvo, xin + rbh * 40960 + (5 * w + a) * 2048, lds0 + L_HALO + (5 * w + a) * 1024);
      if constexpr (a == 5) ald2_sc1(sa0, rs, sso, stin);
      if constexpr (a == 6) ald2_sc1(sa1, rs, sso, stin + 16384);
      if constexpr (a == 7) ald2_sc1(sa2, rs, sso, stin + 32768);
      if constexpr (a == 8) ald2_sc1(sa3, rs, sso, stin + 49152);
      if constexpr (a == 9) ald2_sc1(hs0, rs, hso, stin);
      if constexpr (a == 10) ald2_sc1(hs1, rs, hso, stin + 16384);
    };
    if constexpr (IT == 0) sfor<11>([&](auto A) { aux_load(A); });
    sfor<XW>([&](auto I) { constexpr int i = decltype(I)::value; XLOAD(i); });
    using Q = QS<IT>;
    if constexpr (IT == 0) {
      WAITV(5 + 11 + XW); CERT("Q0");
      BAR();
    }
    constexpr int DQ = 4, NBQ = 6;
    u32x4 wb[NBQ];
    f32x16 acc0, acc1, acc2, acc3;
    f32x4 hacc, hacc4;
    u32x4 hw[3], hx[3];
    float rstd = 0.f, hrs = 0.f;
    u32 lb1 = l16 + 65536, lbh = l16 + L_HALO; asm volatile("" : "+v"(lb1), "+v"(lbh));
    const u32 hwbase = (w >> 1) * 1024 + (g >> 1) * 2048 + (g & 1) * 512 + (16 * (w & 1) + t16) * 16;
    const u32 hw4base = 81920 + (g >> 1) * 1024 + (g & 1) * 512 + t16 * 16 + w * 10240;
    const u32 kvrow = L_KST + (16 + 32 * w + tok) * 80 + h * 8;
    const int hn = 16 * w + 4 * g;
    const u32 haddr = L_KST + t16 * 80 + 2 * hn + (hn >= 40 ? (L_VST - L_KST - 80) : 0);
    const u32 haddr4 = L_VST + t16 * 80 + 2 * (64 + 4 * g - 40);
    const bool hzero = (m128 == 0);
    const u32 wo_o = OFF_WO + (c8 * 20 + 5 * w) * 5120;
#define LDSQ(F) (((F) / 40 % 3) * 40960 + ((F) % 40) * 1024)
#define RDW(off) (*(const LAS u32x4*)(lds + ((off) < 65536 ? l16 : lb1) + ((off) & 65535)))
    u32 gp0 = 0;
#define STG_OFF(n0) ((n0) < 40 ? 2 * (n0) : (n0) < 80 ? (L_VST - L_KST) + 2 * ((n0) - 40) : (L_QST - L_KST - 16 * 80) + 2 * ((n0) - 80))
#define GRAN(t, ACC, q, RS, STADDR, ZERO) do { \
      if constexpr ((t) == 0) gp0 = cvtpk(mul_s(ACC[4 * (q)], RS), mul_s(ACC[4 * (q) + 1], RS)); \
      if constexpr ((t) == 1) { u32x2 pk_ = {gp0, cvtpk(mul_s(ACC[4 * (q) + 2], RS), mul_s(ACC[4 * (q) + 3], RS))}; if (ZERO) pk_ = u32x2{0u, 0u}; *(LAS u32x2*)(lds + (STADDR)) = pk_; } \
    } while (0)
#define GRAN_MAIN(t, ACC, q, n0) GRAN(t, ACC, q, rstd, kvrow + STG_OFF(n0), false)
#pragma unroll
    for (int F = 0; F < DQ; ++F) wb[F % NBQ] = RDW(LDSQ(F));
    sfor<160>([&](auto FC) {
      constexpr int F = decltype(FC)::value, Fr = F + DQ;
      if constexpr (F == 20) QSTAMP(21);
      if constexpr (F == 40) QSTAMP(22);
      if constexpr (F == 60) QSTAMP(23);
      if constexpr (F == 100) QSTAMP(24);
      if constexpr (F == 120) QSTAMP(25);
      if constexpr (F == 140) QSTAMP(26);
      if constexpr (Q::x_at(F)) XLOAD((F >> 1) + XW);
      if constexpr (Q::aux_at(F) >= 0) aux_load(std::integral_constant<int, Q::aux_at(F)>{});
      if constexpr (Q::q1_at(F)) dma_q<1, 1, (F >> 1)>(rs, l16, sQ, w, lds0);
      if constexpr (Q::q2_at(F)) dma_q<2, 2, ((F - 40) >> 1)>(rs, l16, sQ, w, lds0);
      if constexpr (Fr == 80) {
        if constexpr (IT == 0) { WAITV(Q::upto(F) - Q::upto(59)); CERT("Q2"); }
        WAITL0(); BAR();
        STAMP(2);
      }
      if constexpr (Q::q3_at(F)) dma_q<3, 0, ((F - Q::Q3F0) >> 1)>(rs, l16, sQ, w, lds0);
      if constexpr (Q::wo_at(F) >= 0) {
        constexpr int i = Q::wo_at(F), s = i / 5, nb = i % 5;
        if constexpr (i < 24) ald_a<(i % 4) * 1024>(k.wo_r[s][nb], rs, l16, wo_o + (i / 4) * 4096); else ald_v<(i % 4) * 1024>(k.wo_r[s][nb], rs, l16, wo_o + (i / 4) * 4096);
      }
      if constexpr (Fr == 40) {
        if constexpr (IT == 0) { WAITV(Q::upto(F) - Q::upto(19)); CERT("Q1"); } else { WAITV(Q::upto(F) - Q::upto(9)); }
        CERT("HALO");
        BAR();
      }
      if constexpr (Fr == 120) { WAITV(Q::upto(F) - Q::upto(Q::Q3F0 + 8)); CERT("Q3a"); WAITL0(); BAR(); }
      if constexpr (Fr == 140) { WAITV(Q::upto(F) - Q::upto(Q::Q3F0 + 18)); CERT("Q3b"); BAR(); }
      if constexpr (Fr < 160) wb[Fr % NBQ] = RDW(LDSQ(Fr));
      if constexpr (F < 80) {
        constexpr int s = F >> 1;
        if constexpr ((F & 3) == 0) waitpin_a2<Q::xwait(s + 1) + (Q::upto(F) - Q::upto(F + 2))>(xf[s], xf[s + 1]);
        if constexpr (F == 0) mma32w_0(acc0, wb[0], xf[0], wb[1]);
        else if constexpr (F == 1) mma32_0(acc1, wb[1], xf[0]);
        else if constexpr (F & 1) mma32(acc1, wb[F % NBQ], xf[s]);
        else mma32w(acc0, wb[F % NBQ], xf[s], wb[(F + 1) % NBQ]);
        if constexpr (F == 60) {
          pin2(sa0); pin2(sa1); pin2(sa2); pin2(sa3);
          float nm;
          stats_finish(red32_add((u2f(sa0.x) + u2f(sa1.x)) + (u2f(sa2.x) + u2f(sa3.x))), red32_add((u2f(sa0.y) + u2f(sa1.y)) + (u2f(sa2.y) + u2f(sa3.y))), nm, rstd);
        }
        if constexpr (F == 64) {
          pin2(hs0); pin2(hs1);
          float nm;
          stats_finish(red16_32_add(u2f(hs0.x) + u2f(hs1.x)), red16_32_add(u2f(hs0.y) + u2f(hs1.y)), nm, hrs);
        }
        if constexpr (F == 68 && IT == 0) {
#pragma unroll
          for (int nb = 0; nb < 5; ++nb) pin2(k.xr[nb]);
        }
        if constexpr (F & 1) {
          if constexpr (s == 20) mma16_0(hacc, hw[0], hx[0]);
          if constexpr (s > 20 && s <= 39) mma16(hacc, hw[(s - 20) % 3], hx[(s - 20) % 3]);
          if constexpr (s >= 18 && s <= 37) {
            constexpr int sg = s - 18;
            hw[sg % 3] = *(const LAS u32x4*)(lds + hwbase + (sg < 10 ? sg * 4096 : 40960 + (sg - 10) * 4096));
            hx[sg % 3] = *(const LAS u32x4*)(lds + lbh + sg * 1024);
          }
        }
      } else if constexpr (F < 120) {
        constexpr int s = F - 80;
        if constexpr (s == 0) mma32w_0(acc2, wb[F % NBQ], xf[s], wb[(F + 1) % NBQ]); else if constexpr (s & 1) mma32(acc2, wb[F % NBQ], xf[s]); else mma32w(acc2, wb[F % NBQ], xf[s], wb[(F + 1) % NBQ]);
        if constexpr (s >= 1 && s <= 9 && (s & 1) == 1) {
          constexpr int j = s >> 1;
          hw[j % 3] = *(const LAS u32x4*)(lds + hw4base + j * 2048);
          hx[j % 3] = *(const LAS u32x4*)(lds + lbh + w * 5120 + j * 1024);
        }
        if constexpr (s == 5) mma16_0(hacc4, hw[0], hx[0]);
        if constexpr (s > 5 && s <= 13 && (s & 1) == 1) mma16(hacc4, hw[((s - 5) >> 1) % 3], hx[((s - 5) >> 1) % 3]);
        if constexpr (s == 17) *(LAS f32x4*)(lds + L_H4 + w * 1024 + l16) = hacc4;
        constexpr int gi = (s - 2) / 4, st = (s - 2) % 4;
        if constexpr (s >= 2 && gi < 8 && st < 2) {
          constexpr int b = gi >> 2, q = gi & 3, n0 = 32 * b + 8 * q;
          if constexpr (b == 0) GRAN_MAIN(st, acc0, q, n0); else GRAN_MAIN(st, acc1, q, n0);
        }
        if constexpr (s == 34 || s == 35) GRAN(s - 34, hacc, 0, hrs, haddr, hzero);
      } else {
        constexpr int s = F - 120;
        if constexpr (s == 0) mma32w_0(acc3, wb[F % NBQ], xf[s], wb[(F + 1) % NBQ]); else if constexpr (s & 1) mma32(acc3, wb[F % NBQ], xf[s]); else mma32w(acc3, wb[F % NBQ], xf[s], wb[(F + 1) % NBQ]);
        constexpr int gi = (s - 2) / 4, st = (s - 2) % 4;
        if constexpr (s >= 2 && gi < 4 && st < 2) GRAN_MAIN(st, acc2, gi, 64 + 8 * gi);
        if constexpr (s == 18) {
          const f32x4 p0 = *(const LAS f32x4*)(lds + L_H4 + l16), p1 = *(const LAS f32x4*)(lds + L_H4 + 1024 + l16);
          const f32x4 p2 = *(const LAS f32x4*)(lds + L_H4 + 2048 + l16), p3 = *(const LAS f32x4*)(lds + L_H4 + 3072 + l16);
          hacc4 = (p0 + p1) + (p2 + p3);
        }
        if constexpr (s == 24 || s == 25) GRAN(s - 24, hacc4, 0, hrs, haddr4, hzero);
      }
      SB();
    });
    STAMP(3);
    asm volatile("s_nop 11" : "+v"(acc3));
    GRAN_MAIN(0, acc3, 0, 96); GRAN_MAIN(1, acc3, 0, 96); GRAN_MAIN(0, acc3, 1, 104); GRAN_MAIN(1, acc3, 1, 104); GRAN_MAIN(0, acc3, 2, 112); GRAN_MAIN(1, acc3, 2, 112);
    WAITL0(); BAR();
    STAMP(4);
    {
      const int par = t16 & 1;
      const bool ca = (4 * g + par) > t16, cb = (4 * g + 2 + par) > t16;
      const unsigned long long mca = __ballot(ca), mcb = __ballot(cb), mpar = __ballot(par != 0);
      const bool neg = (m128 == 0) && (w == 0);
      const unsigned long long nga = neg ? mca : 0ull, ngb = neg ? mcb : 0ull;
      const u32 gq = g < 2 ? g : 2;
      const u32 mA = g < 3 ? ~0u : 0u, mB = g < 2 ? ~0u : 0u, mC = (g == 1 || g == 2) ? ~0u : 0u;
      const u32 pla = par ? 0x01000c0cu : 0x0c0c0100u, plb = par ? 0x03020c0cu : 0x0c0c0302u;
      const u32 sel0 = ca ? pla : 0x0c0c0c0cu, sel2 = ca ? 0x0c0c0c0cu : pla, sel1 = cb ? plb : 0x0c0c0c0cu, sel3 = cb ? 0x0c0c0c0cu : plb;
      constexpr float cexp = 0.22360679774997896f * 1.4426950408889634f;
      float cx = cexp; asm volatile("" : "+v"(cx));
      const u32 qa = L_QST + (32 * w + t16) * 80 + gq * 16, ka = L_KST + (32 * w + t16) * 80 + gq * 16;
      const u32 va = L_VST + (32 * w + 4 * g + (t16 >> 2)) * 80 + (t16 & 3) * 8;
      u32x4 qf[4], k0[4], k1[4];
      u32x4 v0[4], v1[4];
#pragma unroll
      for (int u = 0; u < 4; ++u) {
        const u32 o = (u >> 1) * 16 * 80 + (u & 1) * 32;
        qf[u] = *(const LAS u32x4*)(lds + qa + o);
        k0[u] = *(const LAS u32x4*)(lds + ka + o);
        k1[u] = *(const LAS u32x4*)(lds + ka + o + 16 * 80);
      }
      dma_g<0, 0>(rs, l16, sG, w, lds0);
#pragma unroll
      for (int u = 0; u < 4; ++u) {
        if (u & 1) { qf[u][0] &= mC; qf[u][1] &= mC; qf[u][2] &= mA; qf[u][3] &= mA; }
        else { qf[u][0] &= mA; qf[u][1] &= mA; qf[u][2] &= mB; qf[u][3] &= mB; }
      }
      f32x4 s0[4], s1[4];
      asm volatile("s_nop 1\n\t"
                   "v_mfma_f32_16x16x32_bf16 %0, %8, %16, 0\n\tv_mfma_f32_16x16x32_bf16 %1, %12, %16, 0\n\t"
                   "v_mfma_f32_16x16x32_bf16 %2, %9, %17, 0\n\tv_mfma_f32_16x16x32_bf16 %3, %13, %17, 0\n\t"
                   "v_mfma_f32_16x16x32_bf16 %4, %10, %18, 0\n\tv_mfma_f32_16x16x32_bf16 %5, %14, %18, 0\n\t"
                   "v_mfma_f32_16x16x32_bf16 %6, %11, %19, 0\n\tv_mfma_f32_16x16x32_bf16 %7, %15, %19, 0"
                   : "=&v"(s0[0]), "=&v"(s1[0]), "=&v"(s0[1]), "=&v"(s1[1]), "=&v"(s0[2]), "=&v"(s1[2]), "=&v"(s0[3]), "=&v"(s1[3])
                   : "v"(k0[0]), "v"(k0[1]), "v"(k0[2]), "v"(k0[3]), "v"(k1[0]), "v"(k1[1]), "v"(k1[2]), "v"(k1[3]), "v"(qf[0]), "v"(qf[1]), "v"(qf[2]), "v"(qf[3]));
#pragma unroll
      for (int u = 0; u < 4; ++u) {
        const u32 o = (u >> 1) * 16 * 80 + (u & 1) * 40;
        const u32x2 t0 = __builtin_bit_cast(u32x2, __builtin_amdgcn_ds_read_tr16_b64_v4i16((LAS s16x4*)(lds + va + o)));
        const u32x2 t1 = __builtin_bit_cast(u32x2, __builtin_amdgcn_ds_read_tr16_b64_v4i16((LAS s16x4*)(lds + va + o + 16 * 80)));
        const u32x2 t2 = __builtin_bit_cast(u32x2, __builtin_amdgcn_ds_read_tr16_b64_v4i16((LAS s16x4*)(lds + va + o + 32)));
        const u32x2 t3 = __builtin_bit_cast(u32x2, __builtin_amdgcn_ds_read_tr16_b64_v4i16((LAS s16x4*)(lds + va + o + 32 + 16 * 80)));
        v0[u] = u32x4{t0.x, t0.y, t1.x, t1.y}; v1[u] = u32x4{t2.x, t2.y, t3.x, t3.y};
      }
      dma_g<0, 1>(rs, l16, sG, w, lds0);
      asm volatile("s_nop 7" : "+v"(s0[0]), "+v"(s1[0]), "+v"(s0[1]), "+v"(s1[1]), "+v"(s0[2]), "+v"(s1[2]), "+v"(s0[3]), "+v"(s1[3]));
      float a[4], b[4], mx[4], sm[4];
#pragma unroll
      for (int u = 0; u < 4; ++u) {
        const float x00 = s0[u][0], x01 = s0[u][1], x02 = s0[u][2], x03 = s0[u][3], x10 = s1[u][0], x11 = s1[u][1], x12 = s1[u][2], x13 = s1[u][3];
        a[u] = sel_s(sel_s(x10, x11, mpar), sel_s(x00, x01, mpar), mca);
        b[u] = sel_s(sel_s(x12, x13, mpar), sel_s(x02, x03, mpar), mcb);
        if (u < 2) { a[u] = sel_s(a[u], -__builtin_inff(), nga); b[u] = sel_s(b[u], -__builtin_inff(), ngb); }
        mx[u] = max_s(a[u], b[u]);
      }
      dma_g<0, 2>(rs, l16, sG, w, lds0);
#pragma unroll
      for (int u = 0; u < 4; ++u) { auto r = __builtin_amdgcn_permlane16_swap(f2u(mx[u]), f2u(mx[u]), false, false); mx[u] = max_s(u2f(r[0]), u2f(r[1])); }
      dma_g<0, 3>(rs, l16, sG, w, lds0);
#pragma unroll
      for (int u = 0; u < 4; ++u) { auto r = __builtin_amdgcn_permlane32_swap(f2u(mx[u]), f2u(mx[u]), false, false); mx[u] = max_s(u2f(r[0]), u2f(r[1])); }
      dma_g<0, 4>(rs, l16, sG, w, lds0);
#pragma unroll
      for (int u = 0; u < 4; ++u) {
        const float t = -mul_s(mx[u], cx);
        a[u] = __builtin_amdgcn_exp2f(fma_s(a[u], cx, t)); b[u] = __builtin_amdgcn_exp2f(fma_s(b[u], cx, t));
        sm[u] = a[u] + b[u];
      }
      dma_g<1, 0>(rs, l16, sG, w, lds0);
#pragma unroll
      for (int u = 0; u < 4; ++u) { auto r = __builtin_amdgcn_permlane16_swap(f2u(sm[u]), f2u(sm[u]), false, false); sm[u] = u2f(r[0]) + u2f(r[1]); }
      dma_g<1, 1>(rs, l16, sG, w, lds0);
#pragma unroll
      for (int u = 0; u < 4; ++u) { auto r = __builtin_amdgcn_permlane32_swap(f2u(sm[u]), f2u(sm[u]), false, false); sm[u] = u2f(r[0]) + u2f(r[1]); }
      dma_g<1, 2>(rs, l16, sG, w, lds0);
      u32x4 pf[4];
#pragma unroll
      for (int u = 0; u < 4; ++u) {
        const float inv = __builtin_amdgcn_rcpf(sm[u]);
        const u32 pk = cvtpk(a[u] * inv, b[u] * inv);
        pf[u] = u32x4{__builtin_amdgcn_perm(pk, pk, sel0), __builtin_amdgcn_perm(pk, pk, sel1), __builtin_amdgcn_perm(pk, pk, sel2), __builtin_amdgcn_perm(pk, pk, sel3)};
      }
      dma_g<1, 3>(rs, l16, sG, w, lds0);
      f32x4 o0[4], o1[4];
      asm volatile("s_nop 1\n\t"
                   "v_mfma_f32_16x16x32_bf16 %0, %8, %16, 0\n\tv_mfma_f32_16x16x32_bf16 %1, %12, %16, 0\n\t"
                   "v_mfma_f32_16x16x32_bf16 %2, %9, %17, 0\n\tv_mfma_f32_16x16x32_bf16 %3, %13, %17, 0\n\t"
                   "v_mfma_f32_16x16x32_bf16 %4, %10, %18, 0\n\tv_mfma_f32_16x16x32_bf16 %5, %14, %18, 0\n\t"
                   "v_mfma_f32_16x16x32_bf16 %6, %11, %19, 0\n\tv_mfma_f32_16x16x32_bf16 %7, %15, %19, 0\n\ts_nop 7"
                   : "=&v"(o0[0]), "=&v"(o1[0]), "=&v"(o0[1]), "=&v"(o1[1]), "=&v"(o0[2]), "=&v"(o1[2]), "=&v"(o0[3]), "=&v"(o1[3])
                   : "v"(v0[0]), "v"(v0[1]), "v"(v0[2]), "v"(v0[3]), "v"(v1[0]), "v"(v1[1]), "v"(v1[2]), "v"(v1[3]), "v"(pf[0]), "v"(pf[1]), "v"(pf[2]), "v"(pf[3]));
      dma_g<1, 4>(rs, l16, sG, w, lds0);
      const u32 sg = 5 * p + g, sg4 = 5 * p + 4;
      const u32 oa = (sg >> 2) * 1024 + (t16 + 16 * (sg & 3)) * 16, oa4 = (sg4 >> 2) * 1024 + (t16 + 16 * (sg4 & 3)) * 16;
#pragma unroll
      for (int qb = 0; qb < 2; ++qb) {
        const u32 so = ofo + (8 * m128 + 2 * w + qb) * 20480;
        const f32x4 A0 = o0[2 * qb], B0_ = o0[2 * qb + 1], A1 = o1[2 * qb], B1 = o1[2 * qb + 1];
        stg_sc1(u32x4{cvtpk(A0[0], A0[1]), cvtpk(A0[2], A0[3]), cvtpk(B0_[0], B0_[1]), cvtpk(B0_[2], B0_[3])}, rs, oa, so);
        if (g == 0) stg_sc1(u32x4{cvtpk(A1[0], A1[1]), cvtpk(A1[2], A1[3]), cvtpk(B1[0], B1[1]), cvtpk(B1[2], B1[3])}, rs, oa4, so);
      }
    }
    SB();
    STAMP(5);
    WAITV(0); CERT("G0"); CERT("G1");
    WAITL0();
    BAR();
    if (tid == 0) __hip_atomic_store(flags + (fgb + FG_O + m128) * 32 + p, 1u, RLX_AGENT);
    STAMP(6); RSTAMP(0);
    sfor<5>([&](auto I) { dma_g<2, decltype(I)::value>(rs, l16, sG, w, lds0); });
    sfor<5>([&](auto I) { dma_g<3, decltype(I)::value>(rs, l16, sG, w, lds0); });
  }
  {
    PHASE_IDS();
    if (w == 0) poll_flags(flags, (fgb + FG_O + (m64 >> 1)) * 32 + (lane & 15), lane < 16, lane);
    BAR();
    STAMP(7); RSTAMP(1);
#pragma unroll
    for (int s = 0; s < 5; ++s)
#pragma unroll
      for (int nb = 0; nb < 5; ++nb) { if (s * 5 + nb < 24) pin_a(k.wo_r[s][nb]); else pin_v(k.wo_r[s][nb]); }
    u32x4 af[20];
    u32 tbo[4];
#pragma unroll
    for (int tbi = 0; tbi < 4; ++tbi) tbo[tbi] = ofo + ((4 * m64 + ((w + 1 + tbi) & 3)) * 20 + 5 * w) * 1024;
    constexpr int OW = 10;
#define OLOAD(q) ald_v_sc1<((q) % 5 % 4) * 1024>(af[q], rs, l16, tbo[(q) / 5] + ((q) % 5 / 4) * 4096)
    sfor<OW>([&](auto I) { constexpr int q = decltype(I)::value; OLOAD(q); });
    f32x4 acc[2][5];
    f32x4 part[3][5];
    sfor<20>([&](auto QC) {
      constexpr int q = decltype(QC)::value, tbi = q / 5, s = q % 5;
      if constexpr (q + OW < 20) OLOAD(q + OW);
      waitpin_v<(q < 10 ? OW : (19 - q))>(af[q]);
#pragma unroll
      for (int nb = 0; nb < 5; ++nb) acc[tbi & 1][nb] = mfma16(k.wo_r[s][nb], af[q], s == 0 ? f32x4{0.f, 0.f, 0.f, 0.f} : acc[tbi & 1][nb]);
      if constexpr (tbi > 0 && s < 3) {
        const u32 tbp = (w + tbi) & 3;
        *(LAS f32x4*)(lds + L_RED + ((tbp * 3 + (3 - tbi)) * 5 + 2 * s) * 1024 + l16) = acc[(tbi - 1) & 1][2 * s];
        if constexpr (2 * s + 1 < 5) *(LAS f32x4*)(lds + L_RED + ((tbp * 3 + (3 - tbi)) * 5 + 2 * s + 1) * 1024 + l16) = acc[(tbi - 1) & 1][2 * s + 1];
      }
      if constexpr (q == 18) {
        WAITL0(); BAR();
#pragma unroll
        for (int sl = 0; sl < 3; ++sl)
#pragma unroll
          for (int nb = 0; nb < 5; ++nb) part[sl][nb] = *(const LAS f32x4*)(lds + L_RED + ((w * 3 + sl) * 5 + nb) * 1024 + l16);
      }
      SB();
    });
    STAMP(8);
    float s1 = 0.f, s2 = 0.f;
#pragma unroll
    for (int nb = 0; nb < 5; ++nb) {
      const f32x4 v = (acc[1][nb] + part[0][nb]) + (part[1][nb] + part[2][nb]);
      const u32 a = cvtpk(bflo(k.xr[nb].x) + v[0], bfhi(k.xr[nb].x) + v[1]), b = cvtpk(bflo(k.xr[nb].y) + v[2], bfhi(k.xr[nb].y) + v[3]);
      k.xr[nb] = u32x2{a, b};
    }
    const u32 token = 64 * m64 + 16 * w + t16;
    const u32 xo = ((token >> 5) * 40 + 5 * c8) * 1024 + ((token & 31) + 32 * (g >> 1)) * 16;
#pragma unroll
    for (int pr = 0; pr < 3; ++pr) {
      const int A = 2 * pr, Bn = pr < 2 ? 2 * pr + 1 : 4;
      auto r0 = __builtin_amdgcn_permlane16_swap(k.xr[A].x, k.xr[Bn].x, false, false);
      auto r1 = __builtin_amdgcn_permlane16_swap(k.xr[A].y, k.xr[Bn].y, false, false);
      const u32x4 v = {r0[0], r1[0], r0[1], r1[1]};
      if (pr < 2) stg_sc1(v, rs, xo + (A + (g & 1)) * 1024, x1o);
      else if ((g & 1) == 0) stg_sc1(v, rs, xo + 4 * 1024, x1o);
    }
#pragma unroll
    for (int nb = 0; nb < 5; ++nb) {
      const float a = bflo(k.xr[nb].x), b = bfhi(k.xr[nb].x), c = bflo(k.xr[nb].y), d = bfhi(k.xr[nb].y);
      s1 += (a + b) + (c + d); s2 += (a * a + b * b) + (c * c + d * d);
    }
    s1 = red16_32_add(s1); s2 = red16_32_add(s2);
    if (g == 0) stg2_sc1(u32x2{f2u(s1), f2u(s2)}, rs, (c8 * 2048 + token) * 8, st1o);
    SB();
    STAMP(9);
    WAITV(0); CERT("G2"); CERT("G3");
    WAITL0();
    BAR();
    if (tid == 0) __hip_atomic_store(flags + (fgb + FG_X1 + m64) * 32 + c8, 1u, RLX_AGENT);
    STAMP(10); RSTAMP(2);
  }
  {
    PHASE_IDS();
    const int nblk = k.nblk;
    const int ds0 = w < 2 ? 14 * w : 28 + 13 * (w - 2);
    const u32 sG = OFF_WG + B0 * 40960 + w * 5120;
    sfor<5>([&](auto I) { dma_g<4, decltype(I)::value>(rs, l16, sG, w, lds0); });
    sfor<5>([&](auto I) { dma_g<5, decltype(I)::value>(rs, l16, sG, w, lds0); });
    sfor<5>([&](auto I) { dma_g<6, decltype(I)::value>(rs, l16, sG, w, lds0); });
    if (w == 0) poll_flags(flags, (fgb + FG_X1 + 2 * m128 + ((lane >> 3) & 1)) * 32 + (lane & 7), lane < 16, lane);
    BAR();
    STAMP(11); RSTAMP(3);
    u32x2 sa0, sa1, sa2, sa3;
    u32x4 xf[40];
    const u32 xbase = x1o + (4 * m128 + w) * 40960;
    const u32 sso = (4 * h * 2048 + 128 * m128 + 32 * w + tok) * 8;
    sfor<XW>([&](auto I) { constexpr int i = decltype(I)::value; XLOAD(i); });
    constexpr int NWD = GS::NWD;
    float r2 = 0.f;
    constexpr int DG = 4, NBG = 6;
    u32x4 wb[NBG];
    f32x16 ac[2][2];
    u32 lb1 = l16 + 65536, lb2 = l16 + 131072; asm volatile("" : "+v"(lb1), "+v"(lb2));
    const u32 hbase = (8 * m128 + 2 * w + (tok >> 4)) * 54 * 1024 + ((tok & 15) + 16 * h) * 16;
    const u32 wd_o = OFF_WD + (c8 * 54 + ds0) * 5120;
#define LDSG(F) ((((F) / 20) % 7) * 20480 + ((F) % 20) * 1024)
#define RDG(off) (*(const LAS u32x4*)(lds + ((off) < 65536 ? l16 : (off) < 131072 ? lb1 : lb2) + ((off) & 65535)))
    float hv0 = 0.f;
    u32 hp[4];
#define GU_STAGE(ACC, idx, t) do { \
      if constexpr ((t) < 8) { const float hv_ = mul_s(mul_s(ACC[t], r2), ACC[8 + (t)]); if constexpr ((t) & 1) hp[(t) >> 1] = cvtpk(hv0, hv_); else hv0 = hv_; } \
      if constexpr ((t) == 8) { const u32 fb_ = B0 + (idx); stg_sc1(u32x4{hp[0], hp[1], hp[2], hp[3]}, rs, hbase + (fb_ & 1) * 512, hfo + (fb_ >> 1) * 1024); } \
    } while (0)
    auto wd_load = [&](auto OC) {
      constexpr int i = decltype(OC)::value, s = i / 5, nb = i % 5;
      if constexpr (i < 24 || i >= 58) ald_a<(i % 4) * 1024>(k.wd_r[s][nb], rs, l16, wd_o + (i / 4) * 4096);
      else ald_v<(i % 4) * 1024>(k.wd_r[s][nb], rs, l16, wd_o + (i / 4) * 4096);
    };
#pragma unroll
    for (int F = 0; F < DG; ++F) wb[F % NBG] = RDG(LDSG(F));
    sfor<240>([&](auto FC) {
      constexpr int F = decltype(FC)::value, Fr = F + DG;
      if constexpr (GS::x_at(F)) XLOAD((F >> 1) + XW);
      if constexpr (GS::op(F) >= 0 && GS::op(F) < 1000) dma_g<GS::op(F) / 8, GS::op(F) % 8>(rs, l16, sG, w, lds0);
      if constexpr (GS::op(F) >= 1000 && GS::op(F) < 2000) wd_load(std::integral_constant<int, GS::op(F) - 1000>{});
      if constexpr (GS::op(F) == 3000) ald2_sc1(sa0, rs, sso, st1o);
      if constexpr (GS::op(F) == 3001) ald2_sc1(sa1, rs, sso, st1o + 16384);
      if constexpr (GS::op(F) == 3002) ald2_sc1(sa2, rs, sso, st1o + 32768);
      if constexpr (GS::op(F) == 3003) ald2_sc1(sa3, rs, sso, st1o + 49152);
      if constexpr (Fr == 40) { WAITL0(); BAR(); }
      if constexpr (Fr == 80) { CERT("G4"); CERT("G5"); CERT("G6"); WAITL0(); BAR(); STAMP(12); }
      if constexpr (Fr == 120) { GSTAMP(21); WAITV(GS::upto(F) - GS::upto(GS::slot(7 * 8 + 4))); CERT("G7"); WAITL0(); BAR(); GSTAMP(22); }
      if constexpr (Fr == 160) { GSTAMP(23); WAITV(GS::upto(F) - GS::upto(GS::slot(9 * 8 + 4))); CERT("G8"); CERT("G9"); WAITL0(); BAR(); GSTAMP(24); }
      if constexpr (Fr == 200) { GSTAMP(25); WAITV(GS::upto(F) - GS::upto(GS::slot(11 * 8 + 4))); CERT("G10"); CERT("G11"); BAR(); GSTAMP(26); }
      if constexpr (Fr < 240) wb[Fr % NBG] = RDG(LDSG(Fr));
      constexpr int ps = F / 80, s = (F % 80) >> 1, b = F & 1;
      if constexpr (ps == 0 && (F & 3) == 0) waitpin_a2<GS::xwait(s + 1) + (GS::upto(F) - GS::upto(F + 2))>(xf[s], xf[s + 1]);
      if constexpr (b == 0) { if constexpr (s == 0) mma32w_0(ac[ps & 1][0], wb[F % NBG], xf[0], wb[(F + 1) % NBG]); else mma32w(ac[ps & 1][0], wb[F % NBG], xf[s], wb[(F + 1) % NBG]); }
      else { if constexpr (s == 0) mma32_0(ac[ps & 1][1], wb[F % NBG], xf[0]); else mma32(ac[ps & 1][1], wb[F % NBG], xf[s]); }
      if constexpr (F == 60) {
        pin2(sa0); pin2(sa1); pin2(sa2); pin2(sa3);
        float rstd, nm;
        stats_finish(red32_add((u2f(sa0.x) + u2f(sa1.x)) + (u2f(sa2.x) + u2f(sa3.x))), red32_add((u2f(sa0.y) + u2f(sa1.y)) + (u2f(sa2.y) + u2f(sa3.y))), nm, rstd);
        r2 = rstd * rstd;
      }
      if constexpr (ps > 0 && b == 1 && (s & 1) == 0) {
        if constexpr (s >= 2 && s <= 18) GU_STAGE(ac[(ps - 1) & 1][0], 2 * (ps - 1), (s - 2) / 2);
        if constexpr (s >= 22 && s <= 38) GU_STAGE(ac[(ps - 1) & 1][1], 2 * (ps - 1) + 1, (s - 22) / 2);
      }
      SB();
    });
    asm volatile("s_nop 11" : "+v"(ac[0][0]), "+v"(ac[0][1]));
    STAMP(13);
    if (nblk == 7) {
      asm volatile("; BR7_BEGIN" ::: "memory");
      WAITV(GS::upto(239) - GS::upto(GS::slot(13 * 8 + 4))); CERT("G12"); CERT("G13"); BAR();
#pragma unroll
      for (int F = 240; F < 240 + DG; ++F) wb[F % NBG] = RDG(LDSG(F));
      sfor<40>([&](auto FC) {
        constexpr int F = 240 + decltype(FC)::value, Fr = F + DG;
        constexpr int s = F - 240;
        if constexpr (s >= 6 && (s & 3) == 2 && NWD + (s - 6) / 4 < 58) wd_load(std::integral_constant<int, NWD + (s - 6) / 4>{});
        if constexpr (Fr < 280) wb[Fr % NBG] = RDG(LDSG(Fr));
        if constexpr (s == 0) mma32w_0(ac[1][0], wb[F % NBG], xf[0], wb[(F + 1) % NBG]); else if constexpr (s & 1) mma32(ac[1][0], wb[F % NBG], xf[s]); else mma32w(ac[1][0], wb[F % NBG], xf[s], wb[(F + 1) % NBG]);
        if constexpr (s >= 3 && s <= 19 && (s & 1) == 1) GU_STAGE(ac[0][0], 4, (s - 3) / 2);
        if constexpr (s >= 21 && s <= 37 && (s & 1) == 1) GU_STAGE(ac[0][1], 5, (s - 21) / 2);
        SB();
      });
      asm volatile("s_nop 11" : "+v"(ac[1][0]));
      sfor<9>([&](auto T) { GU_STAGE(ac[1][0], 6, decltype(T)::value); });
      asm volatile("; BR7_ELSE" ::: "memory");
    } else {
      asm volatile("; BR7_ELSE" ::: "memory");
      sfor<9>([&](auto T) { GU_STAGE(ac[0][0], 4, decltype(T)::value); });
      sfor<9>([&](auto T) { GU_STAGE(ac[0][1], 5, decltype(T)::value); });
      sfor<8>([&](auto I) { wd_load(std::integral_constant<int, NWD + decltype(I)::value>{}); });
      asm volatile("; BR7_END" ::: "memory");
    }
    asm volatile("; BR7_END" ::: "memory");
    SB();
    STAMP(14);
    WAITV(0);
    WAITL0();
    BAR();
    if (tid == 0) __hip_atomic_store(flags + (fgb + FG_H + m128) * 32 + p, 1u, RLX_AGENT);
    {
      const u32 wd_o = OFF_WD + (c8 * 54 + ds0) * 5120;
      sfor<12>([&](auto I) { constexpr int i = 58 + decltype(I)::value; ald_a<(i % 4) * 1024>(k.wd_r[i / 5][i % 5], rs, l16, wd_o + (i / 4) * 4096); });
    }
    STAMP(15); RSTAMP(4);
  }
  {
    PHASE_IDS();
    const int ds0 = w < 2 ? 14 * w : 28 + 13 * (w - 2);
    const u32 sQ = OFF_WQ + p * 163840;
    STAMP(16);
    if (w == 0) poll_flags(flags, (fgb + FG_H + (m64 >> 1)) * 32 + (lane & 15), lane < 16, lane);
    else WAITV(0);
    BAR();
    STAMP(17); RSTAMP(5);
    sfor<70>([&](auto I) { constexpr int i = decltype(I)::value; if constexpr (i < 24 || i >= 58) pin_a(k.wd_r[i / 5][i % 5]); else pin_v(k.wd_r[i / 5][i % 5]); });
    constexpr int DH = 12;
    u32x4 af[DH];
    const bool s13 = w < 2;
    u32 tbo[4];
#pragma unroll
    for (int tbi = 0; tbi < 4; ++tbi) tbo[tbi] = hfo + ((4 * m64 + ((w + 1 + tbi) & 3)) * 54 + ds0) * 1024;
#define HLOAD(q) ald_a_sc1<((q) % 14 % 4) * 1024>(af[(q) % DH], rs, l16, tbo[(q) / 14] + ((q) % 14 / 4) * 4096)
    sfor<DH>([&](auto I) { constexpr int q = decltype(I)::value; HLOAD(q); });
    f32x4 acc[2][5];
    f32x4 part[3][5];
    sfor<56>([&](auto QC) {
      constexpr int q = decltype(QC)::value, tbi = q / 14, s = q % 14;
      constexpr int yf = (q + DH - 1 < 55 ? q + DH - 1 : 55) - q;
      constexpr int nd = q - 44;
      constexpr int yd = (IT == 0 && nd > 0) ? (nd + (nd < 8 ? nd : 8)) : 0;
      waitpin_a<yf + yd>(af[q % DH]);
      if constexpr (s == 0) {
#pragma unroll
        for (int nb = 0; nb < 5; ++nb) acc[tbi & 1][nb] = mfma16(k.wd_r[s][nb], af[q % DH], f32x4{0.f, 0.f, 0.f, 0.f});
      } else if (s < 13 || s13) {
#pragma unroll
        for (int nb = 0; nb < 5; ++nb) acc[tbi & 1][nb] = mfma16(k.wd_r[s][nb], af[q % DH], acc[tbi & 1][nb]);
      }
      if constexpr (tbi > 0 && s >= 1 && s < 6) {
        const u32 tbp = (w + tbi) & 3;
        *(LAS f32x4*)(lds + L_RED + ((tbp * 3 + (3 - tbi)) * 5 + (s - 1)) * 1024 + l16) = acc[(tbi - 1) & 1][s - 1];
      }
      if constexpr (q == 50) {
        WAITL0(); BAR();
#pragma unroll
        for (int sl = 0; sl < 3; ++sl)
#pragma unroll
          for (int nb = 0; nb < 5; ++nb) part[sl][nb] = *(const LAS f32x4*)(lds + L_RED + ((w * 3 + sl) * 5 + nb) * 1024 + l16);
      }
      SB();
      if constexpr (q + DH < 56) HLOAD(q + DH);
      if constexpr (IT == 0 && q >= 44) {
        constexpr int j0 = nd + (nd < 8 ? nd : 8);
        if constexpr (j0 < 10) dma_q<0, 0, j0>(rs, l16, sQ, w, lds0); else dma_q<1, 1, j0 - 10>(rs, l16, sQ, w, lds0);
        if constexpr (nd < 8) {
          constexpr int j1 = j0 + 1;
          if constexpr (j1 < 10) dma_q<0, 0, j1>(rs, l16, sQ, w, lds0); else dma_q<1, 1, j1 - 10>(rs, l16, sQ, w, lds0);
        }
      }
      SB();
    });
    STAMP(18);
#pragma unroll
    for (int nb = 0; nb < 5; ++nb) {
      const f32x4 v = (acc[1][nb] + part[0][nb]) + (part[1][nb] + part[2][nb]);
      const u32 a = cvtpk(bflo(k.xr[nb].x) + v[0], bfhi(k.xr[nb].x) + v[1]), b = cvtpk(bflo(k.xr[nb].y) + v[2], bfhi(k.xr[nb].y) + v[3]);
      k.xr[nb] = u32x2{a, b};
    }
    const u32 token = 64 * m64 + 16 * w + t16;
    STAMP(19);
    if constexpr (IT == 0) {
      float s1 = 0.f, s2 = 0.f;
      const u32 xo = ((token >> 5) * 40 + 5 * c8) * 1024 + ((token & 31) + 32 * (g >> 1)) * 16;
#pragma unroll
      for (int pr = 0; pr < 3; ++pr) {
        const int A = 2 * pr, Bn = pr < 2 ? 2 * pr + 1 : 4;
        auto r0 = __builtin_amdgcn_permlane16_swap(k.xr[A].x, k.xr[Bn].x, false, false);
        auto r1 = __builtin_amdgcn_permlane16_swap(k.xr[A].y, k.xr[Bn].y, false, false);
        const u32x4 v = {r0[0], r1[0], r0[1], r1[1]};
        if (pr < 2) stg_sc1(v, rs, xo + (A + (g & 1)) * 1024, OFF_XF + 2 * SZ_XF);
        else if ((g & 1) == 0) stg_sc1(v, rs, xo + 4 * 1024, OFF_XF + 2 * SZ_XF);
      }
#pragma unroll
      for (int nb = 0; nb < 5; ++nb) {
        const float a = bflo(k.xr[nb].x), b = bfhi(k.xr[nb].x), c = bflo(k.xr[nb].y), d = bfhi(k.xr[nb].y);
        s1 += (a + b) + (c + d); s2 += (a * a + b * b) + (c * c + d * d);
      }
      s1 = red16_32_add(s1); s2 = red16_32_add(s2);
      if (g == 0) stg2_sc1(u32x2{f2u(s1), f2u(s2)}, rs, (c8 * 2048 + token) * 8, OFF_ST + 2 * SZ_ST);
      SB();
      WAITV(0);
      WAITL0();
      BAR();
      if (tid == 0) __hip_atomic_store(flags + (FG_X2 + m64) * 32 + c8, 1u, RLX_AGENT);
      sfor<10>([&](auto I) { dma_q<2, 2, decltype(I)::value>(rs, l16, sQ, w, lds0); });
    } else {
      const u32 oo = token * 1280 + (80 * c8 + 8 * (g >> 1)) * 2;
#pragma unroll
      for (int pr = 0; pr < 3; ++pr) {
        const int A = 2 * pr, Bn = pr < 2 ? 2 * pr + 1 : 4;
        auto r0 = __builtin_amdgcn_permlane16_swap(k.xr[A].x, k.xr[Bn].x, false, false);
        auto r1 = __builtin_amdgcn_permlane16_swap(k.xr[A].y, k.xr[Bn].y, false, false);
        const u32x4 v = {r0[0], r1[0], r0[1], r1[1]};
        if (pr < 2) __builtin_amdgcn_raw_buffer_store_b128(v, k.rso, oo + (A + (g & 1)) * 32, 0, 0);
        else if ((g & 1) == 0) __builtin_amdgcn_raw_buffer_store_b128(v, k.rso, oo + 4 * 32, 0, 0);
      }
    }
    STAMP(20);
  }
}

__global__ __launch_bounds__(256, 1) void main_kernel(char* __restrict__ ws, u16* __restrict__ out) {
  extern __shared__ __attribute__((aligned(16))) char smem[];
  K k;
  k.lds = (LAS char*)smem;
  k.lds0 = (u32)(size_t)k.lds;
  k.tid = threadIdx.x; k.lane = k.tid & 63; k.w = __builtin_amdgcn_readfirstlane(k.tid >> 6);
  k.rs = __builtin_amdgcn_make_buffer_rsrc(ws, 0, WS_USED, 0x00020000);
  k.rso = __builtin_amdgcn_make_buffer_rsrc(out, 0, 2048 * 640 * 2, 0x00020000);
  k.flags = (GAS u32*)ws;
  const int bid = blockIdx.x, xc = bid & 7, j = bid >> 3, mg = xc >> 1, half = xc & 1;
  k.m128 = 4 * mg + (j >> 3); k.p = 8 * half + (j & 7);
  k.m64 = 8 * mg + (j >> 2); k.c8 = 4 * half + (j & 3);
  const int p7 = j & 7;
  k.B0 = 54 * half + (p7 < 6 ? 7 * p7 : 42 + 6 * (p7 - 6));
  k.nblk = p7 < 6 ? 7 : 6;
  iteration<0>(k);
  iteration<1>(k);
}

extern "C" void kernel_launch(void* const* d_in, const int* in_sizes, int n_in, void* d_out, int out_size, void* d_ws, size_t ws_size,
                              hipStream_t stream) {
  const u16* x0 = (const u16*)d_in[0];
  const u16* wqkv = (const u16*)d_in[1];
  const u16* wo = (const u16*)d_in[2];
  const u16* wg = (const u16*)d_in[4];
  const u16* wu = (const u16*)d_in[5];
  const u16* wd = (const u16*)d_in[6];
  (void)hipFuncSetAttribute((const void*)main_kernel, hipFuncAttributeMaxDynamicSharedMemorySize, LDS_BYTES);
  prep_kernel<<<PREP_UNITS, 512, 0, stream>>>(x0, wqkv, wo, wg, wu, wd, (char*)d_ws);
  main_kernel<<<256, 256, LDS_BYTES, stream>>>((char*)d_ws, (u16*)d_out);
}
```

```cpp
#include <hip/hip_runtime.h>
#include <stdint.h>
#include <utility>

#define LAS __attribute__((address_space(3)))
#define GAS __attribute__((address_space(1)))
#define DI __device__ __forceinline__
typedef unsigned u32;
typedef unsigned short u16;
typedef u32 u32x4 __attribute__((ext_vector_type(4)));
typedef u32 u32x2 __attribute__((ext_vector_type(2)));
typedef float f32x2 __attribute__((ext_vector_type(2)));
typedef float f32x4 __attribute__((ext_vector_type(4)));
typedef float f32x16 __attribute__((ext_vector_type(16)));
typedef short s16x8 __attribute__((ext_vector_type(8)));
typedef short s16x4 __attribute__((ext_vector_type(4)));
typedef __bf16 bf16x2 __attribute__((ext_vector_type(2)));
typedef __amdgpu_buffer_rsrc_t rsrc_t;

constexpr u32 FLAG_BYTES = 32768;
constexpr u32 OFF_WQ = 65536;
constexpr u32 OFF_WO = OFF_WQ + 16 * 163840;
constexpr u32 OFF_WG = OFF_WO + 8 * 20 * 5 * 1024;
constexpr u32 OFF_WD = OFF_WG + 108 * 40960;
constexpr u32 OFF_CSQ = OFF_WD + 8 * 54 * 5 * 1024;
constexpr u32 OFF_CSG = OFF_CSQ + 16384;
constexpr u32 SZ_XF = 64 * 40 * 1024;
constexpr u32 OFF_XF = OFF_CSG + 28672;
constexpr u32 SZ_ST = 8 * 2048 * 8;
constexpr u32 OFF_ST = OFF_XF + 4 * SZ_XF;
constexpr u32 SZ_OF = 128 * 20 * 1024;
constexpr u32 OFF_OF = OFF_ST + 4 * SZ_ST;
constexpr u32 SZ_HF = 128 * 54 * 1024;
constexpr u32 OFF_HF = OFF_OF + 2 * SZ_OF;
constexpr u32 WS_USED = OFF_HF + 2 * SZ_HF;
constexpr u32 FG_O = 0, FG_X1 = 16, FG_H = 48, FG_X2 = 64, FG_PER_IT = 96;
constexpr u32 FG_TMO = 2 * FG_PER_IT;

constexpr u32 L_HALO = 122880;
constexpr u32 L_KST = 40960;
constexpr u32 L_VST = L_KST + 144 * 80;
constexpr u32 L_QST = L_VST + 144 * 80;
constexpr u32 L_RED = 100352;
constexpr u32 L_H4 = 143360;
constexpr u32 LDS_BYTES = 163840;

constexpr u32 AUX_SC1 = 16;
#define RLX_AGENT __ATOMIC_RELAXED, __HIP_MEMORY_SCOPE_AGENT
#define SB() __builtin_amdgcn_sched_barrier(0)
#define MEMFENCE() asm volatile("" ::: "memory")
#define WAITV(n) asm volatile("s_waitcnt vmcnt(%0)" ::"i"(n) : "memory")
#define WAITL0() asm volatile("s_waitcnt lgkmcnt(0)" ::: "memory")
#define BAR() do { MEMFENCE(); __builtin_amdgcn_s_barrier(); MEMFENCE(); } while (0)
#define CERT(tag) asm volatile("; CERT " tag ::: "memory")

template <int... I, class Fn> DI void sfor_impl(std::integer_sequence<int, I...>, Fn&& f) { (f(std::integral_constant<int, I>{}), ...); }
template <int N, class Fn> DI void sfor(Fn&& f) { sfor_impl(std::make_integer_sequence<int, N>{}, f); }
DI f32x16 mfma32(u32x4 a, u32x4 b, f32x16 c) { return __builtin_amdgcn_mfma_f32_32x32x16_bf16(__builtin_bit_cast(s16x8, a), __builtin_bit_cast(s16x8, b), c, 0, 0, 0); }
DI f32x4 mfma16(u32x4 a, u32x4 b, f32x4 c) { return __builtin_amdgcn_mfma_f32_16x16x32_bf16(__builtin_bit_cast(s16x8, a), __builtin_bit_cast(s16x8, b), c, 0, 0, 0); }
DI u32 cvtpk(float lo, float hi) { f32x2 v = {lo, hi}; bf16x2 b = __builtin_convertvector(v, bf16x2); return __builtin_bit_cast(u32, b); }
DI float u2f(u32 v) { return __builtin_bit_cast(float, v); }
DI u32 f2u(float v) { return __builtin_bit_cast(u32, v); }
DI float bflo(u32 v) { return __builtin_bit_cast(float, v << 16); }
DI float bfhi(u32 v) { return __builtin_bit_cast(float, v & 0xffff0000u); }
DI float fma_s(float a, float b, float c) { float r; asm("v_fma_f32 %0, %1, %2, %3" : "=v"(r) : "v"(a), "v"(b), "v"(c)); return r; }
DI float mul_s(float a, float b) { float r; asm("v_mul_f32_e32 %0, %1, %2" : "=v"(r) : "v"(a), "v"(b)); return r; }
DI float add_s(float a, float b) { float r; asm("v_add_f32_e32 %0, %1, %2" : "=v"(r) : "v"(a), "v"(b)); return r; }
DI float max_s(float a, float b) { float r; asm("v_max_f32_e32 %0, %1, %2" : "=v"(r) : "v"(a), "v"(b)); return r; }
DI float sel_s(float f, float t, unsigned long long m) { float r; asm("v_cndmask_b32_e64 %0, %1, %2, %3" : "=v"(r) : "v"(f), "v"(t), "s"(m)); return r; }
DI float red16_32_add(float v) {
  auto a = __builtin_amdgcn_permlane16_swap(f2u(v), f2u(v), false, false);
  v = u2f(a[0]) + u2f(a[1]);
  auto b = __builtin_amdgcn_permlane32_swap(f2u(v), f2u(v), false, false);
  return u2f(b[0]) + u2f(b[1]);
}
DI float red16_32_max(float v) {
  auto a = __builtin_amdgcn_permlane16_swap(f2u(v), f2u(v), false, false);
  v = __builtin_fmaxf(u2f(a[0]), u2f(a[1]));
  auto b = __builtin_amdgcn_permlane32_swap(f2u(v), f2u(v), false, false);
  return __builtin_fmaxf(u2f(b[0]), u2f(b[1]));
}
DI float red32_add(float v) {
  auto b = __builtin_amdgcn_permlane32_swap(f2u(v), f2u(v), false, false);
  return u2f(b[0]) + u2f(b[1]);
}
DI void stg_sc1(u32x4 v, rsrc_t rs, u32 voff, u32 soff) { __builtin_amdgcn_raw_buffer_store_b128(v, rs, voff, soff, AUX_SC1); }
DI void stg2_sc1(u32x2 v, rsrc_t rs, u32 voff, u32 soff) { __builtin_amdgcn_raw_buffer_store_b64(v, rs, voff, soff, AUX_SC1); }

template <int IMM> DI void ald_v_sc1(u32x4& d, rsrc_t rs, u32 voff, u32 soff) {
  asm volatile("buffer_load_dwordx4 %0, %1, %2, %3 offen offset:%4 sc1" : "=v"(d) : "v"(voff), "s"(rs), "s"(soff), "i"(IMM) : "memory");
}
template <int IMM> DI void ald_a(u32x4& d, rsrc_t rs, u32 voff, u32 soff) {
  asm volatile("buffer_load_dwordx4 %0, %1, %2, %3 offen offset:%4" : "=a"(d) : "v"(voff), "s"(rs), "s"(soff), "i"(IMM) : "memory");
}
template <int IMM> DI void ald_v(u32x4& d, rsrc_t rs, u32 voff, u32 soff) {
  asm volatile("buffer_load_dwordx4 %0, %1, %2, %3 offen offset:%4" : "=v"(d) : "v"(voff), "s"(rs), "s"(soff), "i"(IMM) : "memory");
}
DI void ald2_sc1(u32x2& d, rsrc_t rs, u32 voff, u32 soff) {
  asm volatile("buffer_load_dwordx2 %0, %1, %2, %3 offen sc1" : "=v"(d) : "v"(voff), "s"(rs), "s"(soff) : "memory");
}
DI void ald2(u32x2& d, rsrc_t rs, u32 voff, u32 soff) {
  asm volatile("buffer_load_dwordx2 %0, %1, %2, %3 offen" : "=v"(d) : "v"(voff), "s"(rs), "s"(soff) : "memory");
}
DI void ald1(u32& d, rsrc_t rs, u32 voff, u32 soff) {
  asm volatile("buffer_load_dword %0, %1, %2, %3 offen" : "=v"(d) : "v"(voff), "s"(rs), "s"(soff) : "memory");
}
template <int IMM> DI void ald_a_sc1(u32x4& d, rsrc_t rs, u32 voff, u32 soff) {
  asm volatile("buffer_load_dwordx4 %0, %1, %2, %3 offen offset:%4 sc1" : "=a"(d) : "v"(voff), "s"(rs), "s"(soff), "i"(IMM) : "memory");
}
template <int N> DI void waitpin_a(u32x4& r) { asm volatile("s_waitcnt vmcnt(%1)" : "+a"(r) : "i"(N) : "memory"); }
template <int N> DI void waitpin_a2(u32x4& r0, u32x4& r1) { asm volatile("s_waitcnt vmcnt(%2)" : "+a"(r0), "+a"(r1) : "i"(N) : "memory"); }
DI void mma32(f32x16& acc, const u32x4& wv, const u32x4& xa) { asm volatile("v_mfma_f32_32x32x16_bf16 %0, %1, %2, %0" : "+v"(acc) : "v"(wv), "a"(xa)); }
DI void mma32_0(f32x16& acc, const u32x4& wv, const u32x4& xa) { asm volatile("v_mfma_f32_32x32x16_bf16 %0, %1, %2, 0" : "=&v"(acc) : "v"(wv), "a"(xa)); }
DI void mma32w(f32x16& acc, const u32x4& wv, const u32x4& xa, const u32x4& wn) { asm volatile("v_mfma_f32_32x32x16_bf16 %0, %1, %2, %0" : "+v"(acc) : "v"(wv), "a"(xa), "v"(wn)); }
DI void mma32w_0(f32x16& acc, const u32x4& wv, const u32x4& xa, const u32x4& wn) { asm volatile("v_mfma_f32_32x32x16_bf16 %0, %1, %2, 0" : "=&v"(acc) : "v"(wv), "a"(xa), "v"(wn)); }
DI void mma16(f32x4& acc, const u32x4& av, const u32x4& bv) { asm volatile("v_mfma_f32_16x16x32_bf16 %0, %1, %2, %0" : "+v"(acc) : "v"(av), "v"(bv)); }
DI void mma16_0(f32x4& acc, const u32x4& av, const u32x4& bv) { asm volatile("v_mfma_f32_16x16x32_bf16 %0, %1, %2, 0" : "=&v"(acc) : "v"(av), "v"(bv)); }
DI void pin_vv(u32x4& a, u32x4& b) { asm volatile("" : "+v"(a), "+v"(b)); }
template <int N> DI void waitpin_v(u32x4& r) { asm volatile("s_waitcnt vmcnt(%1)" : "+v"(r) : "i"(N) : "memory"); }
DI void pin_v(u32x4& r) { asm volatile("" : "+v"(r)::"memory"); }
DI void pin_a(u32x4& r) { asm volatile("" : "+a"(r)::"memory"); }
DI void pin2(u32x2& r) { asm volatile("" : "+v"(r)::"memory"); }
DI void pin1(u32& r) { asm volatile("" : "+v"(r)::"memory"); }
#define DMA_F(TAG, IMM, SC, rs, voff, soff, m0v) \
  asm volatile("s_mov_b32 m0, %2\n\ts_nop 0\n\tbuffer_load_dwordx4 %0, %1, %3 offen offset:%4 " SC "lds ; DMA " TAG ::"v"(voff), "s"(rs), "s"(m0v), "s"(soff), "i"(IMM) : "memory")
#define DMA_N(TAG, IMM, SC, rs, voff, soff) \
  asm volatile("buffer_load_dwordx4 %0, %1, %2 offen offset:%3 " SC "lds ; DMA " TAG ::"v"(voff), "s"(rs), "s"(soff), "i"(IMM) : "memory")

constexpr int PREP_UNITS = 84 + 20 + 60 + 64;
__global__ __launch_bounds__(512) void prep_kernel(const u16* __restrict__ x0, const u16* __restrict__ wqkv, const u16* __restrict__ wo,
                                                   const u16* __restrict__ wg, const u16* __restrict__ wu, const u16* __restrict__ wd, char* __restrict__ ws) {
  __shared__ __attribute__((aligned(16))) char lds[81920 + 1024];
  const int tid = threadIdx.x, l = tid & 63, w = __builtin_amdgcn_readfirstlane(tid >> 6);
  const int u = blockIdx.x;
  if (u == PREP_UNITS - 1) {
    for (int i = tid; i < (int)(FLAG_BYTES / 16); i += 512) ((u32x4*)ws)[i] = u32x4{0, 0, 0, 0};
  }
  const int gam = l >> 4, q = (l & 15) >> 2, pp = l & 3;
  if (u < 84) {
    const int type = u < 30 ? 0 : u < 57 ? 1 : 2, cg = u < 30 ? u : u < 57 ? u - 30 : u - 57;
    const u16* src = type == 0 ? wqkv : type == 1 ? wg : wu;
    const int pitch = type == 0 ? 1920 : 1728;
#pragma unroll
    for (int i = 0; i < 10; ++i) {
      const int r = 8 * i + w;
      const u16* g = src + (size_t)(8 * r + (l >> 3)) * pitch + 64 * cg + (l & 7) * 8;
      __builtin_amdgcn_global_load_lds((const GAS u32*)g, (LAS u32*)(lds + r * 1024), 16, 0, 0);
    }
    const int c = 64 * cg + l;
    u32 dbase; int dstep;
    if (type == 0) {
      const int sec = c / 640, cc = c % 640, p = cc / 40, r40 = cc % 40;
      const int n = (sec == 1 ? 0 : sec == 2 ? 40 : 80) + r40;
      const int b = n >> 5, nl = n & 31;
      dbase = OFF_WQ + p * 163840 + (b < 2 ? b * 1024 : 81920 + (b - 2) * 40960) + nl * 16;
      dstep = b < 2 ? 2048 : 1024;
    } else {
      const int fb = c >> 4, nl = (c & 15) + 16 * (type - 1);
      const int half = fb / 54, rem = fb % 54;
      const int idx = rem < 42 ? rem % 7 : (rem - 42) % 6, B0 = 54 * half + rem - idx;
      const bool paired = idx < 6;
      dbase = OFF_WG + (paired ? (B0 + (idx & ~1)) * 40960 + (idx & 1) * 1024 : (B0 + 6) * 40960) + nl * 16;
      dstep = paired ? 2048 : 1024;
    }
    __syncthreads();
    u32x4 sl[10];
    float csum = 0.f;
#pragma unroll
    for (int i = 0; i < 10; ++i) {
      const int o = 8 * i + w;
      const u32x2 a = __builtin_bit_cast(u32x2, __builtin_amdgcn_ds_read_tr16_b64_v4i16((LAS s16x4*)(lds + (8 * o + q) * 128 + gam * 32 + pp * 8)));
      const u32x2 b2 = __builtin_bit_cast(u32x2, __builtin_amdgcn_ds_read_tr16_b64_v4i16((LAS s16x4*)(lds + (8 * o + 4 + q) * 128 + gam * 32 + pp * 8)));
      sl[i] = u32x4{a.x, a.y, b2.x, b2.y};
    }
#pragma unroll
    for (int i = 0; i < 10; ++i)
#pragma unroll
      for (int j = 0; j < 4; ++j) csum += bflo(sl[i][j]) + bfhi(sl[i][j]);
    __syncthreads();
    float* red = (float*)lds;
    red[w * 64 + l] = csum;
    __syncthreads();
    const float mean = (((red[l] + red[64 + l]) + (red[128 + l] + red[192 + l])) + ((red[256 + l] + red[320 + l]) + (red[384 + l] + red[448 + l]))) * (1.0f / 640.0f);
#pragma unroll
    for (int i = 0; i < 10; ++i) {
      const int o = 8 * i + w;
      u32x4 v;
#pragma unroll
      for (int j = 0; j < 4; ++j) v[j] = cvtpk(bflo(sl[i][j]) - mean, bfhi(sl[i][j]) - mean);
      *(u32x4*)(ws + dbase + (o >> 1) * dstep + (o & 1) * 512) = v;
    }
  } else if (u < 164) {
    const bool iswo = u < 104;
    const int v = iswo ? u - 84 : u - 104, cg = iswo ? v >> 1 : v / 6, kk = iswo ? v & 1 : v % 6;
    const u16* src = iswo ? wo : wd;
    const int nrows = iswo ? 320 : 288, row0 = kk * nrows, noct = nrows / 8;
#pragma unroll
    for (int i = 0; i < 5; ++i) {
      const int r = 8 * i + w;
      if (r < noct) {
        const u16* g = src + (size_t)(row0 + 8 * r + (l >> 3)) * 640 + 64 * cg + (l & 7) * 8;
        __builtin_amdgcn_global_load_lds((const GAS u32*)g, (LAS u32*)(lds + r * 1024), 16, 0, 0);
      }
    }
    const int c = 64 * cg + l, c8 = c / 80, nb = (c % 80) >> 4, n = c & 15;
    const u32 dbase = (iswo ? OFF_WO + c8 * 20 * 5120 : OFF_WD + c8 * 54 * 5120) + nb * 1024 + n * 16;
    __syncthreads();
#pragma unroll
    for (int i = 0; i < 5; ++i) {
      const int o = 8 * i + w;
      if (o < noct) {
        int r0, r1; u32 dst;
        if (iswo) {
          const int pl = o / 5, ii = o % 5, sg = 40 * kk + o;
          r0 = 40 * pl + 4 * ii + q; r1 = r0 + 20;
          dst = dbase + (sg >> 2) * 5120 + (sg & 3) * 256;
        } else {
          const int s_ = o >> 2, g = o & 3;
          r0 = 32 * s_ + 16 * (g >> 1) + 4 * (g & 1) + q; r1 = r0 + 8;
          dst = dbase + (9 * kk + s_) * 5120 + g * 256;
        }
        const u32x2 a = __builtin_bit_cast(u32x2, __builtin_amdgcn_ds_read_tr16_b64_v4i16((LAS s16x4*)(lds + r0 * 128 + gam * 32 + pp * 8)));
        const u32x2 b2 = __builtin_bit_cast(u32x2, __builtin_amdgcn_ds_read_tr16_b64_v4i16((LAS s16x4*)(lds + r1 * 128 + gam * 32 + pp * 8)));
        *(u32x4*)(ws + dst) = u32x4{a.x, a.y, b2.x, b2.y};
      }
    }
  } else {
    const int rb = u - 164;
#pragma unroll
    for (int i = 0; i < 5; ++i) {
      const int cidx = 64 * (8 * i + w) + l, row = cidx / 80, cc = cidx % 80;
      const u16* g = x0 + (size_t)(32 * rb + row) * 640 + ((cc ^ (row & 7)) * 8);
      __builtin_amdgcn_global_load_lds((const GAS u32*)g, (LAS u32*)(lds + (8 * i + w) * 1024), 16, 0, 0);
    }
    __syncthreads();
    const int tok = l & 31, h = l >> 5;
    float s1 = 0.f, s2 = 0.f;
#pragma unroll
    for (int i = 0; i < 5; ++i) {
      const int s = 5 * w + i, cc = 2 * s + h;
      const u32x4 v = *(const u32x4*)(lds + tok * 1280 + ((cc ^ (tok & 7)) * 16));
      *(u32x4*)(ws + OFF_XF + (rb * 40 + s) * 1024 + l * 16) = v;
#pragma unroll
      for (int j = 0; j < 4; ++j) { float a = bflo(v[j]), b = bfhi(v[j]); s1 += a + b; s2 += a * a + b * b; }
    }
    s1 = red32_add(s1); s2 = red32_add(s2);
    if (l < 32) {
      f32x2* st = (f32x2*)(ws + OFF_ST);
      st[w * 2048 + 32 * rb + tok] = f32x2{s1, s2};
    }
  }
}

#define STAMP(i) do {} while (0)
#define RSTAMP(i) do {} while (0)
#define GSTAMP(i) STAMP(i)
#define QSTAMP(i) do {} while (0)
constexpr u32 OFF_STAMP = WS_USED + 4096;
constexpr u32 kSpinCap = 1u << 16;

struct K {
  rsrc_t rs, rso;
  LAS char* lds;
  u32 lds0;
  GAS u32* flags;
  int lane, w, m128, p, m64, c8, B0, nblk, tid, it;
  u32x2 xr[5];
  u32x4 wo_r[5][5];
  u32x4 wd_r[14][5];
};

#define PHASE_IDS() int lane = k.lane; asm volatile("" : "+v"(lane)); \
  int it = k.it; asm volatile("" : "+s"(it)); \
  const u32 xin = OFF_XF + it * 2 * SZ_XF, stin = OFF_ST + it * 2 * SZ_ST, x1o = xin + SZ_XF, st1o = stin + SZ_ST; \
  const u32 ofo = OFF_OF + it * SZ_OF, hfo = OFF_HF + it * SZ_HF, fgb = it * FG_PER_IT; \
  (void)xin; (void)stin; (void)x1o; (void)st1o; (void)ofo; (void)hfo; (void)fgb; \
  int w = k.w, m128 = k.m128, p = k.p, m64 = k.m64, c8 = k.c8, B0 = k.B0; \
  asm volatile("" : "+s"(w), "+s"(m128), "+s"(p), "+s"(m64), "+s"(c8), "+s"(B0)); \
  const u32 l16 = lane * 16; const int tok = lane & 31, h = lane >> 5, t16 = lane & 15, g = lane >> 4; \
  const rsrc_t rs = k.rs; LAS char* const lds = k.lds; const u32 lds0 = k.lds0; \
  (void)tok; (void)h; (void)t16; (void)g; (void)l16; (void)m128; (void)p; (void)m64; (void)c8; (void)B0; (void)lds; (void)lds0; (void)rs

DI void poll_flags(GAS u32* flags, u32 fo, bool act, int lane) {
  for (u32 spins = 0;;) {
    u32 v = act ? __hip_atomic_load(flags + fo, RLX_AGENT) : 1u;
    if (__all(v == 1u)) break;
    __builtin_amdgcn_s_sleep(1);
    if (++spins > kSpinCap) {
      if (lane == 0) __hip_atomic_store(flags + FG_TMO * 32, 1u, RLX_AGENT);
      break;
    }
    if ((spins & 255u) == 0u && __builtin_amdgcn_readfirstlane(__hip_atomic_load(flags + FG_TMO * 32, RLX_AGENT)) != 0u) break;
  }
}
DI void stats_finish(float s1, float s2, float& nmean, float& rstd) {
  const float mean = s1 * (1.0f / 640.0f);
  const float var = __builtin_fmaxf(s2 * (1.0f / 640.0f) - mean * mean, 0.f);
  nmean = -mean; rstd = __builtin_amdgcn_rsqf(var + 1e-5f);
}

template <int C, int SLOT, int I> DI void dma_q(rsrc_t rs, u32 l16, u32 sQ, int w, u32 lds0) {
#define Q_EMIT(TAG, FIRST, IMM, SO, MO) do { if constexpr (FIRST) DMA_F(TAG, IMM, "", rs, l16, SO, MO); else DMA_N(TAG, IMM, "", rs, l16, SO); } while (0)
  if constexpr (C < 2) {
    constexpr int grp = I / 4, imm = (I % 4) * 1024;
    const u32 so = sQ + w * 10240 + C * 40960 + grp * 4096, mo = lds0 + SLOT * 40960 + w * 10240 + grp * 4096;
    if constexpr (C == 0) Q_EMIT("Q0", I % 4 == 0, imm, so, mo); else Q_EMIT("Q1", I % 4 == 0, imm, so, mo);
  } else {
    constexpr int hf = I / 5, ii = I % 5, grp = ii / 4, imm = (ii % 4) * 1024;
    const u32 so = sQ + w * 5120 + 81920 + (C - 2) * 40960 + hf * 20480 + grp * 4096, mo = lds0 + SLOT * 40960 + w * 5120 + hf * 20480 + grp * 4096;
    if constexpr (C == 2) Q_EMIT("Q2", ii % 4 == 0, imm, so, mo);
    else if constexpr (hf == 0) Q_EMIT("Q3a", ii % 4 == 0, imm, so, mo);
    else Q_EMIT("Q3b", ii % 4 == 0, imm, so, mo);
  }
#undef Q_EMIT
}
template <int HH, int I> DI void dma_g(rsrc_t rs, u32 l16, u32 sG, int w, u32 lds0) {
  constexpr int grp = I / 4, imm = (I % 4) * 1024;
  const u32 so = sG + HH * 20480 + grp * 4096, mo = lds0 + w * 5120 + (HH % 7) * 20480 + grp * 4096;
#define G_CASE(n) if constexpr (HH == n) { if constexpr (I % 4 == 0) DMA_F("G" #n, imm, "", rs, l16, so, mo); else DMA_N("G" #n, imm, "", rs, l16, so); }
  G_CASE(0) G_CASE(1) G_CASE(2) G_CASE(3) G_CASE(4) G_CASE(5) G_CASE(6) G_CASE(7) G_CASE(8) G_CASE(9) G_CASE(10) G_CASE(11) G_CASE(12) G_CASE(13)
#undef G_CASE
}

constexpr int XW = 10;
struct QS {
  static constexpr int Q3F0 = 78;
  static constexpr bool x_at(int f) { return f < 80 && (f & 1) == 0 && (f >> 1) + XW < 40; }
  static constexpr int aux_at(int f) { return (f < 22 && (f & 1) == 1) ? f >> 1 : -1; }
  static constexpr bool q3_at(int f) { return f >= Q3F0 && f < Q3F0 + 20 && ((f - Q3F0) & 1) == 0; }
  static constexpr int wo_at(int f) {
    if (f >= 60 && f < 76 && (f & 1) == 0) return (f - 60) / 2;
    if (f >= 100 && f < 151 && (f - 100) % 3 == 0) return 8 + (f - 100) / 3;
    return -1;
  }
  static constexpr int upto(int F) { int n = 0; for (int f = 0; f <= F; ++f) n += x_at(f) + (aux_at(f) >= 0) + q3_at(f) + (wo_at(f) >= 0); return n; }
  static constexpr int xidx(int i) { return i < XW ? i : XW + upto(2 * (i - XW)) - 1; }
  static constexpr int xwait(int s) { return XW + upto(2 * s) - 1 - xidx(s); }
};
struct GS {
  static constexpr int NWD = 50;
  static constexpr bool x_at(int f) { return f < 80 && (f & 1) == 0 && (f >> 1) + XW < 40; }
  static constexpr int op(int F) {
    if (F < 8 && (F & 1)) return 3000 + (F >> 1);
    if (F & 1) return -1;
    if (F >= 62 && F < 82) return (7 + (F - 62) / 10) * 8 + ((F - 62) % 10) / 2;
    if (F >= 82 && F < 102) return (9 + (F - 82) / 10) * 8 + ((F - 82) % 10) / 2;
    if (F >= 102 && F < 118) return 1000 + (F - 102) / 2;
    if (F >= 118 && F < 138) return (11 + (F - 118) / 10) * 8 + ((F - 118) % 10) / 2;
    if (F >= 138 && F < 158) return 1008 + (F - 138) / 2;
    if (F >= 158 && F < 168) return 13 * 8 + (F - 158) / 2;
    if (F >= 168 && F < 240 && 18 + (F - 168) / 2 < NWD) return 1018 + (F - 168) / 2;
    return -1;
  }
  static constexpr bool st_at(int F) { const int ps = F / 80, r = F % 80; return F < 240 && ps > 0 && (r == 37 || r == 77); }
  static constexpr int upto(int F) { int n = 0; for (int f = 0; f <= F; ++f) n += x_at(f) + (op(f) >= 0) + st_at(f); return n; }
  static constexpr int slot(int o) { for (int f = 0; f < 300; ++f) if (op(f) == o) return f; return -1; }
  static constexpr int xidx(int i) { return i < XW ? i : XW + upto(2 * (i - XW)) - 1; }
  static constexpr int xwait(int s) { return XW + upto(2 * s) - 1 - xidx(s); }
};
static_assert(18 + (240 - 168) / 2 >= GS::NWD, "all prefetched Wd fragments need a slot");

DI void iteration(K& k) {
  GAS u32* const flags = k.flags;
  const int tid = k.tid;
  {
    PHASE_IDS();
    const u32 sQ = OFF_WQ + p * 163840, sG = OFF_WG + B0 * 40960 + w * 5120;
    STAMP(0);
    if (it > 0 && w == 0) {
      const int grp = 2 * m128 - 1 + (lane >> 3);
      const bool act = lane < 24 && grp >= 0;
      poll_flags(flags, (FG_X2 + grp) * 32 + (lane & 7), act, lane);
    }
    WAITV(0); CERT("Q0"); CERT("Q1"); CERT("Q2");
    BAR();
    STAMP(1);
    u32x4 xf[40];
    const u32 xbase = xin + (4 * m128 + w) * 40960;
#define XLOAD(i) ald_a_sc1<((i) % 4) * 1024>(xf[i], rs, l16, xbase + ((i) / 4) * 4096)
    u32x2 hs0, hs1, sa0, sa1, sa2, sa3;
    const int rbh = m128 > 0 ? 4 * m128 - 1 : 0;
    const u32 hvo = (g >> 1) * 1024 + (g & 1) * 512 + (16 + t16) * 16;
    const int ht = 128 * m128 - 16 + t16;
    const u32 hso = ((2 * g) * 2048 + (ht < 0 ? 0 : ht)) * 8, sso = (4 * h * 2048 + 128 * m128 + 32 * w + tok) * 8;
    auto aux_load = [&](auto AC) {
      constexpr int a = decltype(AC)::value;
      if constexpr (a < 5) DMA_F("HALO", 0, "sc1 ", rs, hvo, xin + rbh * 40960 + (5 * w + a) * 2048, lds0 + L_HALO + (5 * w + a) * 1024);
      if constexpr (a == 5) ald2_sc1(sa0, rs, sso, stin);
      if constexpr (a == 6) ald2_sc1(sa1, rs, sso, stin + 16384);
      if constexpr (a == 7) ald2_sc1(sa2, rs, sso, stin + 32768);
      if constexpr (a == 8) ald2_sc1(sa3, rs, sso, stin + 49152);
      if constexpr (a == 9) ald2_sc1(hs0, rs, hso, stin);
      if constexpr (a == 10) ald2_sc1(hs1, rs, hso, stin + 16384);
    };
    sfor<XW>([&](auto I) { constexpr int i = decltype(I)::value; XLOAD(i); });
    using Q = QS;
    constexpr int DQ = 4, NBQ = 6;
    u32x4 wb[NBQ];
    f32x16 acc0, acc1, acc2, acc3;
    f32x4 hacc, hacc4;
    u32x4 hw[3], hx[3];
    float rstd = 0.f, hrs = 0.f;
    u32 lb1 = l16 + 65536, lbh = l16 + L_HALO; asm volatile("" : "+v"(lb1), "+v"(lbh));
    const u32 hwbase = (w >> 1) * 1024 + (g >> 1) * 2048 + (g & 1) * 512 + (16 * (w & 1) + t16) * 16;
    const u32 hw4base = 81920 + (g >> 1) * 1024 + (g & 1) * 512 + t16 * 16 + w * 10240;
    const u32 kvrow = L_KST + (16 + 32 * w + tok) * 80 + h * 8;
    const int hn = 16 * w + 4 * g;
    const u32 haddr = L_KST + t16 * 80 + 2 * hn + (hn >= 40 ? (L_VST - L_KST - 80) : 0);
    const u32 haddr4 = L_VST + t16 * 80 + 2 * (64 + 4 * g - 40);
    const bool hzero = (m128 == 0);
    const u32 wo_o = OFF_WO + (c8 * 20 + 5 * w) * 5120;
#define LDSQ(F) (((F) / 40 % 3) * 40960 + ((F) % 40) * 1024)
#define RDW(off) (*(const LAS u32x4*)(lds + ((off) < 65536 ? l16 : lb1) + ((off) & 65535)))
    u32 gp0 = 0;
#define STG_OFF(n0) ((n0) < 40 ? 2 * (n0) : (n0) < 80 ? (L_VST - L_KST) + 2 * ((n0) - 40) : (L_QST - L_KST - 16 * 80) + 2 * ((n0) - 80))
#define GRAN(t, ACC, q, RS, STADDR, ZERO) do { \
      if constexpr ((t) == 0) gp0 = cvtpk(mul_s(ACC[4 * (q)], RS), mul_s(ACC[4 * (q) + 1], RS)); \
      if constexpr ((t) == 1) { u32x2 pk_ = {gp0, cvtpk(mul_s(ACC[4 * (q) + 2], RS), mul_s(ACC[4 * (q) + 3], RS))}; if (ZERO) pk_ = u32x2{0u, 0u}; *(LAS u32x2*)(lds + (STADDR)) = pk_; } \
    } while (0)
#define GRAN_MAIN(t, ACC, q, n0) GRAN(t, ACC, q, rstd, kvrow + STG_OFF(n0), false)
#pragma unroll
    for (int F = 0; F < DQ; ++F) wb[F % NBQ] = RDW(LDSQ(F));
    sfor<160>([&](auto FC) {
      constexpr int F = decltype(FC)::value, Fr = F + DQ;
      if constexpr (F == 20) QSTAMP(21);
      if constexpr (F == 40) QSTAMP(22);
      if constexpr (F == 60) QSTAMP(23);
      if constexpr (F == 100) QSTAMP(24);
      if constexpr (F == 120) QSTAMP(25);
      if constexpr (F == 140) QSTAMP(26);
      if constexpr (Q::x_at(F)) XLOAD((F >> 1) + XW);
      if constexpr (Q::aux_at(F) >= 0) aux_load(std::integral_constant<int, Q::aux_at(F)>{});
      if constexpr (Fr == 80) {
        WAITL0(); BAR();
        STAMP(2);
      }
      if constexpr (Q::q3_at(F)) dma_q<3, 0, ((F - Q::Q3F0) >> 1)>(rs, l16, sQ, w, lds0);
      if constexpr (Q::wo_at(F) >= 0) {
        constexpr int i = Q::wo_at(F), s = i / 5, nb = i % 5;
        if constexpr (i < 24) ald_a<(i % 4) * 1024>(k.wo_r[s][nb], rs, l16, wo_o + (i / 4) * 4096); else ald_v<(i % 4) * 1024>(k.wo_r[s][nb], rs, l16, wo_o + (i / 4) * 4096);
      }
      if constexpr (Fr == 40) {
        WAITV(Q::upto(F) - Q::upto(9));
        CERT("HALO");
        BAR();
      }
      if constexpr (Fr == 120) { WAITV(Q::upto(F) - Q::upto(Q::Q3F0 + 8)); CERT("Q3a"); WAITL0(); BAR(); }
      if constexpr (Fr == 140) { WAITV(Q::upto(F) - Q::upto(Q::Q3F0 + 18)); CERT("Q3b"); BAR(); }
      if constexpr (Fr < 160) wb[Fr % NBQ] = RDW(LDSQ(Fr));
      if constexpr (F < 80) {
        constexpr int s = F >> 1;
        if constexpr ((F & 3) == 0) waitpin_a2<Q::xwait(s + 1) + (Q::upto(F) - Q::upto(F + 2))>(xf[s], xf[s + 1]);
        if constexpr (F == 0) mma32w_0(acc0, wb[0], xf[0], wb[1]);
        else if constexpr (F == 1) mma32_0(acc1, wb[1], xf[0]);
        else if constexpr (F & 1) mma32(acc1, wb[F % NBQ], xf[s]);
        else mma32w(acc0, wb[F % NBQ], xf[s], wb[(F + 1) % NBQ]);
        if constexpr (F == 60) {
          pin2(sa0); pin2(sa1); pin2(sa2); pin2(sa3);
          float nm;
          stats_finish(red32_add((u2f(sa0.x) + u2f(sa1.x)) + (u2f(sa2.x) + u2f(sa3.x))), red32_add((u2f(sa0.y) + u2f(sa1.y)) + (u2f(sa2.y) + u2f(sa3.y))), nm, rstd);
        }
        if constexpr (F == 64) {
          pin2(hs0); pin2(hs1);
          float nm;
          stats_finish(red16_32_add(u2f(hs0.x) + u2f(hs1.x)), red16_32_add(u2f(hs0.y) + u2f(hs1.y)), nm, hrs);
        }
        if constexpr (F == 68) {
#pragma unroll
          for (int nb = 0; nb < 5; ++nb) pin2(k.xr[nb]);
        }
        if constexpr (F & 1) {
          if constexpr (s == 20) mma16_0(hacc, hw[0], hx[0]);
          if constexpr (s > 20 && s <= 39) mma16(hacc, hw[(s - 20) % 3], hx[(s - 20) % 3]);
          if constexpr (s >= 18 && s <= 37) {
            constexpr int sg = s - 18;
            hw[sg % 3] = *(const LAS u32x4*)(lds + hwbase + (sg < 10 ? sg * 4096 : 40960 + (sg - 10) * 4096));
            hx[sg % 3] = *(const LAS u32x4*)(lds + lbh + sg * 1024);
          }
        }
      } else if constexpr (F < 120) {
        constexpr int s = F - 80;
        if constexpr (s == 0) mma32w_0(acc2, wb[F % NBQ], xf[s], wb[(F + 1) % NBQ]); else if constexpr (s & 1) mma32(acc2, wb[F % NBQ], xf[s]); else mma32w(acc2, wb[F % NBQ], xf[s], wb[(F + 1) % NBQ]);
        if constexpr (s >= 1 && s <= 9 && (s & 1) == 1) {
          constexpr int j = s >> 1;
          hw[j % 3] = *(const LAS u32x4*)(lds + hw4base + j * 2048);
          hx[j % 3] = *(const LAS u32x4*)(lds + lbh + w * 5120 + j * 1024);
        }
        if constexpr (s == 5) mma16_0(hacc4, hw[0], hx[0]);
        if constexpr (s > 5 && s <= 13 && (s & 1) == 1) mma16(hacc4, hw[((s - 5) >> 1) % 3], hx[((s - 5) >> 1) % 3]);
        if constexpr (s == 17) *(LAS f32x4*)(lds + L_H4 + w * 1024 + l16) = hacc4;
        constexpr int gi = (s - 2) / 4, st = (s - 2) % 4;
        if constexpr (s >= 2 && gi < 8 && st < 2) {
          constexpr int b = gi >> 2, q = gi & 3, n0 = 32 * b + 8 * q;
          if constexpr (b == 0) GRAN_MAIN(st, acc0, q, n0); else GRAN_MAIN(st, acc1, q, n0);
        }
        if constexpr (s == 34 || s == 35) GRAN(s - 34, hacc, 0, hrs, haddr, hzero);
      } else {
        constexpr int s = F - 120;
        if constexpr (s == 0) mma32w_0(acc3, wb[F % NBQ], xf[s], wb[(F + 1) % NBQ]); else if constexpr (s & 1) mma32(acc3, wb[F % NBQ], xf[s]); else mma32w(acc3, wb[F % NBQ], xf[s], wb[(F + 1) % NBQ]);
        constexpr int gi = (s - 2) / 4, st = (s - 2) % 4;
        if constexpr (s >= 2 && gi < 4 && st < 2) GRAN_MAIN(st, acc2, gi, 64 + 8 * gi);
        if constexpr (s == 18) {
          const f32x4 p0 = *(const LAS f32x4*)(lds + L_H4 + l16), p1 = *(const LAS f32x4*)(lds + L_H4 + 1024 + l16);
          const f32x4 p2 = *(const LAS f32x4*)(lds + L_H4 + 2048 + l16), p3 = *(const LAS f32x4*)(lds + L_H4 + 3072 + l16);
          hacc4 = (p0 + p1) + (p2 + p3);
        }
        if constexpr (s == 24 || s == 25) GRAN(s - 24, hacc4, 0, hrs, haddr4, hzero);
      }
      SB();
    });
    STAMP(3);
    asm volatile("s_nop 11" : "+v"(acc3));
    GRAN_MAIN(0, acc3, 0, 96); GRAN_MAIN(1, acc3, 0, 96); GRAN_MAIN(0, acc3, 1, 104); GRAN_MAIN(1, acc3, 1, 104); GRAN_MAIN(0, acc3, 2, 112); GRAN_MAIN(1, acc3, 2, 112);
    WAITL0(); BAR();
    STAMP(4);
    {
      const int par = t16 & 1;
      const bool ca = (4 * g + par) > t16, cb = (4 * g + 2 + par) > t16;
      const unsigned long long mca = __ballot(ca), mcb = __ballot(cb), mpar = __ballot(par != 0);
      const bool neg = (m128 == 0) && (w == 0);
      const unsigned long long nga = neg ? mca : 0ull, ngb = neg ? mcb : 0ull;
      const u32 gq = g < 2 ? g : 2;
      const u32 mA = g < 3 ? ~0u : 0u, mB = g < 2 ? ~0u : 0u, mC = (g == 1 || g == 2) ? ~0u : 0u;
      const u32 pla = par ? 0x01000c0cu : 0x0c0c0100u, plb = par ? 0x03020c0cu : 0x0c0c0302u;
      const u32 sel0 = ca ? pla : 0x0c0c0c0cu, sel2 = ca ? 0x0c0c0c0cu : pla, sel1 = cb ? plb : 0x0c0c0c0cu, sel3 = cb ? 0x0c0c0c0cu : plb;
      constexpr float cexp = 0.22360679774997896f * 1.4426950408889634f;
      float cx = cexp; asm volatile("" : "+v"(cx));
      const u32 qa = L_QST + (32 * w + t16) * 80 + gq * 16, ka = L_KST + (32 * w + t16) * 80 + gq * 16;
      const u32 va = L_VST + (32 * w + 4 * g + (t16 >> 2)) * 80 + (t16 & 3) * 8;
      u32x4 qf[4], k0[4], k1[4];
      u32x4 v0[4], v1[4];
#pragma unroll
      for (int u = 0; u < 4; ++u) {
        const u32 o = (u >> 1) * 16 * 80 + (u & 1) * 32;
        qf[u] = *(const LAS u32x4*)(lds + qa + o);
        k0[u] = *(const LAS u32x4*)(lds + ka + o);
        k1[u] = *(const LAS u32x4*)(lds + ka + o + 16 * 80);
      }
      dma_g<0, 0>(rs, l16, sG, w, lds0);
#pragma unroll
      for (int u = 0; u < 4; ++u) {
        if (u & 1) { qf[u][0] &= mC; qf[u][1] &= mC; qf[u][2] &= mA; qf[u][3] &= mA; }
        else { qf[u][0] &= mA; qf[u][1] &= mA; qf[u][2] &= mB; qf[u][3] &= mB; }
      }
      f32x4 s0[4], s1[4];
      asm volatile("s_nop 1\n\t"
                   "v_mfma_f32_16x16x32_bf16 %0, %8, %16, 0\n\tv_mfma_f32_16x16x32_bf16 %1, %12, %16, 0\n\t"
                   "v_mfma_f32_16x16x32_bf16 %2, %9, %17, 0\n\tv_mfma_f32_16x16x32_bf16 %3, %13, %17, 0\n\t"
                   "v_mfma_f32_16x16x32_bf16 %4, %10, %18, 0\n\tv_mfma_f32_16x16x32_bf16 %5, %14, %18, 0\n\t"
                   "v_mfma_f32_16x16x32_bf16 %6, %11, %19, 0\n\tv_mfma_f32_16x16x32_bf16 %7, %15, %19, 0"
                   : "=&v"(s0[0]), "=&v"(s1[0]), "=&v"(s0[1]), "=&v"(s1[1]), "=&v"(s0[2]), "=&v"(s1[2]), "=&v"(s0[3]), "=&v"(s1[3])
                   : "v"(k0[0]), "v"(k0[1]), "v"(k0[2]), "v"(k0[3]), "v"(k1[0]), "v"(k1[1]), "v"(k1[2]), "v"(k1[3]), "v"(qf[0]), "v"(qf[1]), "v"(qf[2]), "v"(qf[3]));
#pragma unroll
      for (int u = 0; u < 4; ++u) {
        const u32 o = (u >> 1) * 16 * 80 + (u & 1) * 40;
        const u32x2 t0 = __builtin_bit_cast(u32x2, __builtin_amdgcn_ds_read_tr16_b64_v4i16((LAS s16x4*)(lds + va + o)));
        const u32x2 t1 = __builtin_bit_cast(u32x2, __builtin_amdgcn_ds_read_tr16_b64_v4i16((LAS s16x4*)(lds + va + o + 16 * 80)));
        const u32x2 t2 = __builtin_bit_cast(u32x2, __builtin_amdgcn_ds_read_tr16_b64_v4i16((LAS s16x4*)(lds + va + o + 32)));
        const u32x2 t3 = __builtin_bit_cast(u32x2, __builtin_amdgcn_ds_read_tr16_b64_v4i16((LAS s16x4*)(lds + va + o + 32 + 16 * 80)));
        v0[u] = u32x4{t0.x, t0.y, t1.x, t1.y}; v1[u] = u32x4{t2.x, t2.y, t3.x, t3.y};
      }
      dma_g<0, 1>(rs, l16, sG, w, lds0);
      asm volatile("s_nop 7" : "+v"(s0[0]), "+v"(s1[0]), "+v"(s0[1]), "+v"(s1[1]), "+v"(s0[2]), "+v"(s1[2]), "+v"(s0[3]), "+v"(s1[3]));
      float a[4], b[4], mx[4], sm[4];
#pragma unroll
      for (int u = 0; u < 4; ++u) {
        const float x00 = s0[u][0], x01 = s0[u][1], x02 = s0[u][2], x03 = s0[u][3], x10 = s1[u][0], x11 = s1[u][1], x12 = s1[u][2], x13 = s1[u][3];
        a[u] = sel_s(sel_s(x10, x11, mpar), sel_s(x00, x01, mpar), mca);
        b[u] = sel_s(sel_s(x12, x13, mpar), sel_s(x02, x03, mpar), mcb);
        if (u < 2) { a[u] = sel_s(a[u], -__builtin_inff(), nga); b[u] = sel_s(b[u], -__builtin_inff(), ngb); }
        mx[u] = max_s(a[u], b[u]);
      }
      dma_g<0, 2>(rs, l16, sG, w, lds0);
#pragma unroll
      for (int u = 0; u < 4; ++u) { auto r = __builtin_amdgcn_permlane16_swap(f2u(mx[u]), f2u(mx[u]), false, false); mx[u] = max_s(u2f(r[0]), u2f(r[1])); }
      dma_g<0, 3>(rs, l16, sG, w, lds0);
#pragma unroll
      for (int u = 0; u < 4; ++u) { auto r = __builtin_amdgcn_permlane32_swap(f2u(mx[u]), f2u(mx[u]), false, false); mx[u] = max_s(u2f(r[0]), u2f(r[1])); }
      dma_g<0, 4>(rs, l16, sG, w, lds0);
#pragma unroll
      for (int u = 0; u < 4; ++u) {
        const float t = -mul_s(mx[u], cx);
        a[u] = __builtin_amdgcn_exp2f(fma_s(a[u], cx, t)); b[u] = __builtin_amdgcn_exp2f(fma_s(b[u], cx, t));
        sm[u] = a[u] + b[u];
      }
      dma_g<1, 0>(rs, l16, sG, w, lds0);
#pragma unroll
      for (int u = 0; u < 4; ++u) { auto r = __builtin_amdgcn_permlane16_swap(f2u(sm[u]), f2u(sm[u]), false, false); sm[u] = u2f(r[0]) + u2f(r[1]); }
      dma_g<1, 1>(rs, l16, sG, w, lds0);
#pragma unroll
      for (int u = 0; u < 4; ++u) { auto r = __builtin_amdgcn_permlane32_swap(f2u(sm[u]), f2u(sm[u]), false, false); sm[u] = u2f(r[0]) + u2f(r[1]); }
      dma_g<1, 2>(rs, l16, sG, w, lds0);
      u32x4 pf[4];
#pragma unroll
      for (int u = 0; u < 4; ++u) {
        const float inv = __builtin_amdgcn_rcpf(sm[u]);
        const u32 pk = cvtpk(a[u] * inv, b[u] * inv);
        pf[u] = u32x4{__builtin_amdgcn_perm(pk, pk, sel0), __builtin_amdgcn_perm(pk, pk, sel1), __builtin_amdgcn_perm(pk, pk, sel2), __builtin_amdgcn_perm(pk, pk, sel3)};
      }
      dma_g<1, 3>(rs, l16, sG, w, lds0);
      f32x4 o0[4], o1[4];
      asm volatile("s_nop 1\n\t"
                   "v_mfma_f32_16x16x32_bf16 %0, %8, %16, 0\n\tv_mfma_f32_16x16x32_bf16 %1, %12, %16, 0\n\t"
                   "v_mfma_f32_16x16x32_bf16 %2, %9, %17, 0\n\tv_mfma_f32_16x16x32_bf16 %3, %13, %17, 0\n\t"
                   "v_mfma_f32_16x16x32_bf16 %4, %10, %18, 0\n\tv_mfma_f32_16x16x32_bf16 %5, %14, %18, 0\n\t"
                   "v_mfma_f32_16x16x32_bf16 %6, %11, %19, 0\n\tv_mfma_f32_16x16x32_bf16 %7, %15, %19, 0\n\ts_nop 7"
                   : "=&v"(o0[0]), "=&v"(o1[0]), "=&v"(o0[1]), "=&v"(o1[1]), "=&v"(o0[2]), "=&v"(o1[2]), "=&v"(o0[3]), "=&v"(o1[3])
                   : "v"(v0[0]), "v"(v0[1]), "v"(v0[2]), "v"(v0[3]), "v"(v1[0]), "v"(v1[1]), "v"(v1[2]), "v"(v1[3]), "v"(pf[0]), "v"(pf[1]), "v"(pf[2]), "v"(pf[3]));
      dma_g<1, 4>(rs, l16, sG, w, lds0);
      const u32 sg = 5 * p + g, sg4 = 5 * p + 4;
      const u32 oa = (sg >> 2) * 1024 + (t16 + 16 * (sg & 3)) * 16, oa4 = (sg4 >> 2) * 1024 + (t16 + 16 * (sg4 & 3)) * 16;
#pragma unroll
      for (int qb = 0; qb < 2; ++qb) {
        const u32 so = ofo + (8 * m128 + 2 * w + qb) * 20480;
        const f32x4 A0 = o0[2 * qb], B0_ = o0[2 * qb + 1], A1 = o1[2 * qb], B1 = o1[2 * qb + 1];
        stg_sc1(u32x4{cvtpk(A0[0], A0[1]), cvtpk(A0[2], A0[3]), cvtpk(B0_[0], B0_[1]), cvtpk(B0_[2], B0_[3])}, rs, oa, so);
        if (g == 0) stg_sc1(u32x4{cvtpk(A1[0], A1[1]), cvtpk(A1[2], A1[3]), cvtpk(B1[0], B1[1]), cvtpk(B1[2], B1[3])}, rs, oa4, so);
      }
    }
    SB();
    STAMP(5);
    WAITV(0); CERT("G0"); CERT("G1");
    WAITL0();
    BAR();
    if (tid == 0) __hip_atomic_store(flags + (fgb + FG_O + m128) * 32 + p, 1u, RLX_AGENT);
    STAMP(6); RSTAMP(0);
    sfor<5>([&](auto I) { dma_g<2, decltype(I)::value>(rs, l16, sG, w, lds0); });
    sfor<5>([&](auto I) { dma_g<3, decltype(I)::value>(rs, l16, sG, w, lds0); });
  }
  {
    PHASE_IDS();
    if (w == 0) poll_flags(flags, (fgb + FG_O + (m64 >> 1)) * 32 + (lane & 15), lane < 16, lane);
    BAR();
    STAMP(7); RSTAMP(1);
#pragma unroll
    for (int s = 0; s < 5; ++s)
#pragma unroll
      for (int nb = 0; nb < 5; ++nb) { if (s * 5 + nb < 24) pin_a(k.wo_r[s][nb]); else pin_v(k.wo_r[s][nb]); }
    u32x4 af[20];
    u32 tbo[4];
#pragma unroll
    for (int tbi = 0; tbi < 4; ++tbi) tbo[tbi] = ofo + ((4 * m64 + ((w + 1 + tbi) & 3)) * 20 + 5 * w) * 1024;
    constexpr int OW = 10;
#define OLOAD(q) ald_v_sc1<((q) % 5 % 4) * 1024>(af[q], rs, l16, tbo[(q) / 5] + ((q) % 5 / 4) * 4096)
    sfor<OW>([&](auto I) { constexpr int q = decltype(I)::value; OLOAD(q); });
    f32x4 acc[2][5];
    f32x4 part[3][5];
    sfor<20>([&](auto QC) {
      constexpr int q = decltype(QC)::value, tbi = q / 5, s = q % 5;
      if constexpr (q + OW < 20) OLOAD(q + OW);
      waitpin_v<(q < 10 ? OW : (19 - q))>(af[q]);
#pragma unroll
      for (int nb = 0; nb < 5; ++nb) acc[tbi & 1][nb] = mfma16(k.wo_r[s][nb], af[q], s == 0 ? f32x4{0.f, 0.f, 0.f, 0.f} : acc[tbi & 1][nb]);
      if constexpr (tbi > 0 && s < 3) {
        const u32 tbp = (w + tbi) & 3;
        *(LAS f32x4*)(lds + L_RED + ((tbp * 3 + (3 - tbi)) * 5 + 2 * s) * 1024 + l16) = acc[(tbi - 1) & 1][2 * s];
        if constexpr (2 * s + 1 < 5) *(LAS f32x4*)(lds + L_RED + ((tbp * 3 + (3 - tbi)) * 5 + 2 * s + 1) * 1024 + l16) = acc[(tbi - 1) & 1][2 * s + 1];
      }
      if constexpr (q == 18) {
        WAITL0(); BAR();
#pragma unroll
        for (int sl = 0; sl < 3; ++sl)
#pragma unroll
          for (int nb = 0; nb < 5; ++nb) part[sl][nb] = *(const LAS f32x4*)(lds + L_RED + ((w * 3 + sl) * 5 + nb) * 1024 + l16);
      }
      SB();
    });
    STAMP(8);
    float s1 = 0.f, s2 = 0.f;
#pragma unroll
    for (int nb = 0; nb < 5; ++nb) {
      const f32x4 v = (acc[1][nb] + part[0][nb]) + (part[1][nb] + part[2][nb]);
      const u32 a = cvtpk(bflo(k.xr[nb].x) + v[0], bfhi(k.xr[nb].x) + v[1]), b = cvtpk(bflo(k.xr[nb].y) + v[2], bfhi(k.xr[nb].y) + v[3]);
      k.xr[nb] = u32x2{a, b};
    }
    const u32 token = 64 * m64 + 16 * w + t16;
    const u32 xo = ((token >> 5) * 40 + 5 * c8) * 1024 + ((token & 31) + 32 * (g >> 1)) * 16;
#pragma unroll
    for (int pr = 0; pr < 3; ++pr) {
      const int A = 2 * pr, Bn = pr < 2 ? 2 * pr + 1 : 4;
      auto r0 = __builtin_amdgcn_permlane16_swap(k.xr[A].x, k.xr[Bn].x, false, false);
      auto r1 = __builtin_amdgcn_permlane16_swap(k.xr[A].y, k.xr[Bn].y, false, false);
      const u32x4 v = {r0[0], r1[0], r0[1], r1[1]};
      if (pr < 2) stg_sc1(v, rs, xo + (A + (g & 1)) * 1024, x1o);
      else if ((g & 1) == 0) stg_sc1(v, rs, xo + 4 * 1024, x1o);
    }
#pragma unroll
    for (int nb = 0; nb < 5; ++nb) {
      const float a = bflo(k.xr[nb].x), b = bfhi(k.xr[nb].x), c = bflo(k.xr[nb].y), d = bfhi(k.xr[nb].y);
      s1 += (a + b) + (c + d); s2 += (a * a + b * b) + (c * c + d * d);
    }
    s1 = red16_32_add(s1); s2 = red16_32_add(s2);
    if (g == 0) stg2_sc1(u32x2{f2u(s1), f2u(s2)}, rs, (c8 * 2048 + token) * 8, st1o);
    SB();
    STAMP(9);
    WAITV(0); CERT("G2"); CERT("G3");
    WAITL0();
    BAR();
    if (tid == 0) __hip_atomic_store(flags + (fgb + FG_X1 + m64) * 32 + c8, 1u, RLX_AGENT);
    STAMP(10); RSTAMP(2);
  }
  {
    PHASE_IDS();
    const int nblk = k.nblk;
    const int ds0 = w < 2 ? 14 * w : 28 + 13 * (w - 2);
    const u32 sG = OFF_WG + B0 * 40960 + w * 5120;
    sfor<5>([&](auto I) { dma_g<4, decltype(I)::value>(rs, l16, sG, w, lds0); });
    sfor<5>([&](auto I) { dma_g<5, decltype(I)::value>(rs, l16, sG, w, lds0); });
    sfor<5>([&](auto I) { dma_g<6, decltype(I)::value>(rs, l16, sG, w, lds0); });
    if (w == 0) poll_flags(flags, (fgb + FG_X1 + 2 * m128 + ((lane >> 3) & 1)) * 32 + (lane & 7), lane < 16, lane);
    BAR();
    STAMP(11); RSTAMP(3);
    u32x2 sa0, sa1, sa2, sa3;
    u32x4 xf[40];
    const u32 xbase = x1o + (4 * m128 + w) * 40960;
    const u32 sso = (4 * h * 2048 + 128 * m128 + 32 * w + tok) * 8;
    sfor<XW>([&](auto I) { constexpr int i = decltype(I)::value; XLOAD(i); });
    constexpr int NWD = GS::NWD;
    float r2 = 0.f;
    constexpr int DG = 4, NBG = 6;
    u32x4 wb[NBG];
    f32x16 ac[2][2];
    u32 lb1 = l16 + 65536, lb2 = l16 + 131072; asm volatile("" : "+v"(lb1), "+v"(lb2));
    const u32 hbase = (8 * m128 + 2 * w + (tok >> 4)) * 54 * 1024 + ((tok & 15) + 16 * h) * 16;
    const u32 wd_o = OFF_WD + (c8 * 54 + ds0) * 5120;
#define LDSG(F) ((((F) / 20) % 7) * 20480 + ((F) % 20) * 1024)
#define RDG(off) (*(const LAS u32x4*)(lds + ((off) < 65536 ? l16 : (off) < 131072 ? lb1 : lb2) + ((off) & 65535)))
    float hv0 = 0.f;
    u32 hp[4];
#define GU_STAGE(ACC, idx, t) do { \
      if constexpr ((t) < 8) { const float hv_ = mul_s(mul_s(ACC[t], r2), ACC[8 + (t)]); if constexpr ((t) & 1) hp[(t) >> 1] = cvtpk(hv0, hv_); else hv0 = hv_; } \
      if constexpr ((t) == 8) { const u32 fb_ = B0 + (idx); stg_sc1(u32x4{hp[0], hp[1], hp[2], hp[3]}, rs, hbase + (fb_ & 1) * 512, hfo + (fb_ >> 1) * 1024); } \
    } while (0)
    auto wd_load = [&](auto OC) {
      constexpr int i = decltype(OC)::value, s = i / 5, nb = i % 5;
      if constexpr (i < 24 || i >= 58) ald_a<(i % 4) * 1024>(k.wd_r[s][nb], rs, l16, wd_o + (i / 4) * 4096);
      else ald_v<(i % 4) * 1024>(k.wd_r[s][nb], rs, l16, wd_o + (i / 4) * 4096);
    };
#pragma unroll
    for (int F = 0; F < DG; ++F) wb[F % NBG] = RDG(LDSG(F));
    sfor<240>([&](auto FC) {
      constexpr int F = decltype(FC)::value, Fr = F + DG;
      if constexpr (GS::x_at(F)) XLOAD((F >> 1) + XW);
      if constexpr (GS::op(F) >= 0 && GS::op(F) < 1000) dma_g<GS::op(F) / 8, GS::op(F) % 8>(rs, l16, sG, w, lds0);
      if constexpr (GS::op(F) >= 1000 && GS::op(F) < 2000) wd_load(std::integral_constant<int, GS::op(F) - 1000>{});
      if constexpr (GS::op(F) == 3000) ald2_sc1(sa0, rs, sso, st1o);
      if constexpr (GS::op(F) == 3001) ald2_sc1(sa1, rs, sso, st1o + 16384);
      if constexpr (GS::op(F) == 3002) ald2_sc1(sa2, rs, sso, st1o + 32768);
      if constexpr (GS::op(F) == 3003) ald2_sc1(sa3, rs, sso, st1o + 49152);
      if constexpr (Fr == 40) { WAITL0(); BAR(); }
      if constexpr (Fr == 80) { CERT("G4"); CERT("G5"); CERT("G6"); WAITL0(); BAR(); STAMP(12); }
      if constexpr (Fr == 120) { GSTAMP(21); WAITV(GS::upto(F) - GS::upto(GS::slot(7 * 8 + 4))); CERT("G7"); WAITL0(); BAR(); GSTAMP(22); }
      if constexpr (Fr == 160) { GSTAMP(23); WAITV(GS::upto(F) - GS::upto(GS::slot(9 * 8 + 4))); CERT("G8"); CERT("G9"); WAITL0(); BAR(); GSTAMP(24); }
      if constexpr (Fr == 200) { GSTAMP(25); WAITV(GS::upto(F) - GS::upto(GS::slot(11 * 8 + 4))); CERT("G10"); CERT("G11"); BAR(); GSTAMP(26); }
      if constexpr (Fr < 240) wb[Fr % NBG] = RDG(LDSG(Fr));
      constexpr int ps = F / 80, s = (F % 80) >> 1, b = F & 1;
      if constexpr (ps == 0 && (F & 3) == 0) waitpin_a2<GS::xwait(s + 1) + (GS::upto(F) - GS::upto(F + 2))>(xf[s], xf[s + 1]);
      if constexpr (b == 0) { if constexpr (s == 0) mma32w_0(ac[ps & 1][0], wb[F % NBG], xf[0], wb[(F + 1) % NBG]); else mma32w(ac[ps & 1][0], wb[F % NBG], xf[s], wb[(F + 1) % NBG]); }
      else { if constexpr (s == 0) mma32_0(ac[ps & 1][1], wb[F % NBG], xf[0]); else mma32(ac[ps & 1][1], wb[F % NBG], xf[s]); }
      if constexpr (F == 60) {
        pin2(sa0); pin2(sa1); pin2(sa2); pin2(sa3);
        float rstd, nm;
        stats_finish(red32_add((u2f(sa0.x) + u2f(sa1.x)) + (u2f(sa2.x) + u2f(sa3.x))), red32_add((u2f(sa0.y) + u2f(sa1.y)) + (u2f(sa2.y) + u2f(sa3.y))), nm, rstd);
        r2 = rstd * rstd;
      }
      if constexpr (ps > 0 && b == 1 && (s & 1) == 0) {
        if constexpr (s >= 2 && s <= 18) GU_STAGE(ac[(ps - 1) & 1][0], 2 * (ps - 1), (s - 2) / 2);
        if constexpr (s >= 22 && s <= 38) GU_STAGE(ac[(ps - 1) & 1][1], 2 * (ps - 1) + 1, (s - 22) / 2);
      }
      SB();
    });
    asm volatile("s_nop 11" : "+v"(ac[0][0]), "+v"(ac[0][1]));
    STAMP(13);
    if (nblk == 7) {
      asm volatile("; BR7_BEGIN" ::: "memory");
      WAITV(GS::upto(239) - GS::upto(GS::slot(13 * 8 + 4))); CERT("G12"); CERT("G13"); BAR();
#pragma unroll
      for (int F = 240; F < 240 + DG; ++F) wb[F % NBG] = RDG(LDSG(F));
      sfor<40>([&](auto FC) {
        constexpr int F = 240 + decltype(FC)::value, Fr = F + DG;
        constexpr int s = F - 240;
        if constexpr (s >= 6 && (s & 3) == 2 && NWD + (s - 6) / 4 < 58) wd_load(std::integral_constant<int, NWD + (s - 6) / 4>{});
        if constexpr (Fr < 280) wb[Fr % NBG] = RDG(LDSG(Fr));
        if constexpr (s == 0) mma32w_0(ac[1][0], wb[F % NBG], xf[0], wb[(F + 1) % NBG]); else if constexpr (s & 1) mma32(ac[1][0], wb[F % NBG], xf[s]); else mma32w(ac[1][0], wb[F % NBG], xf[s], wb[(F + 1) % NBG]);
        if constexpr (s >= 3 && s <= 19 && (s & 1) == 1) GU_STAGE(ac[0][0], 4, (s - 3) / 2);
        if constexpr (s >= 21 && s <= 37 && (s & 1) == 1) GU_STAGE(ac[0][1], 5, (s - 21) / 2);
        SB();
      });
      asm volatile("s_nop 11" : "+v"(ac[1][0]));
      sfor<9>([&](auto T) { GU_STAGE(ac[1][0], 6, decltype(T)::value); });
      asm volatile("; BR7_ELSE" ::: "memory");
    } else {
      asm volatile("; BR7_ELSE" ::: "memory");
      sfor<9>([&](auto T) { GU_STAGE(ac[0][0], 4, decltype(T)::value); });
      sfor<9>([&](auto T) { GU_STAGE(ac[0][1], 5, decltype(T)::value); });
      sfor<8>([&](auto I) { wd_load(std::integral_constant<int, NWD + decltype(I)::value>{}); });
      asm volatile("; BR7_END" ::: "memory");
    }
    asm volatile("; BR7_END" ::: "memory");
    SB();
    STAMP(14);
    WAITV(0);
    WAITL0();
    BAR();
    if (tid == 0) __hip_atomic_store(flags + (fgb + FG_H + m128) * 32 + p, 1u, RLX_AGENT);
    {
      const u32 wd_o = OFF_WD + (c8 * 54 + ds0) * 5120;
      sfor<12>([&](auto I) { constexpr int i = 58 + decltype(I)::value; ald_a<(i % 4) * 1024>(k.wd_r[i / 5][i % 5], rs, l16, wd_o + (i / 4) * 4096); });
    }
    STAMP(15); RSTAMP(4);
  }
  {
    PHASE_IDS();
    const int ds0 = w < 2 ? 14 * w : 28 + 13 * (w - 2);
    const u32 sQ = OFF_WQ + p * 163840;
    STAMP(16);
    if (w == 0) poll_flags(flags, (fgb + FG_H + (m64 >> 1)) * 32 + (lane & 15), lane < 16, lane);
    else WAITV(0);
    BAR();
    STAMP(17); RSTAMP(5);
    sfor<70>([&](auto I) { constexpr int i = decltype(I)::value; if constexpr (i < 24 || i >= 58) pin_a(k.wd_r[i / 5][i % 5]); else pin_v(k.wd_r[i / 5][i % 5]); });
    constexpr int DH = 12;
    u32x4 af[DH];
    const bool s13 = w < 2;
    u32 tbo[4];
#pragma unroll
    for (int tbi = 0; tbi < 4; ++tbi) tbo[tbi] = hfo + ((4 * m64 + ((w + 1 + tbi) & 3)) * 54 + ds0) * 1024;
#define HLOAD(q) ald_a_sc1<((q) % 14 % 4) * 1024>(af[(q) % DH], rs, l16, tbo[(q) / 14] + ((q) % 14 / 4) * 4096)
    sfor<DH>([&](auto I) { constexpr int q = decltype(I)::value; HLOAD(q); });
    f32x4 acc[2][5];
    f32x4 part[3][5];
    sfor<56>([&](auto QC) {
      constexpr int q = decltype(QC)::value, tbi = q / 14, s = q % 14;
      constexpr int yf = (q + DH - 1 < 55 ? q + DH - 1 : 55) - q;
      constexpr int nd = q - 44;
      constexpr int yd = (nd > 0) ? (nd + (nd < 8 ? nd : 8)) : 0;
      waitpin_a<yf + yd>(af[q % DH]);
      if constexpr (s == 0) {
#pragma unroll
        for (int nb = 0; nb < 5; ++nb) acc[tbi & 1][nb] = mfma16(k.wd_r[s][nb], af[q % DH], f32x4{0.f, 0.f, 0.f, 0.f});
      } else if (s < 13 || s13) {
#pragma unroll
        for (int nb = 0; nb < 5; ++nb) acc[tbi & 1][nb] = mfma16(k.wd_r[s][nb], af[q % DH], acc[tbi & 1][nb]);
      }
      if constexpr (tbi > 0 && s >= 1 && s < 6) {
        const u32 tbp = (w + tbi) & 3;
        *(LAS f32x4*)(lds + L_RED + ((tbp * 3 + (3 - tbi)) * 5 + (s - 1)) * 1024 + l16) = acc[(tbi - 1) & 1][s - 1];
      }
      if constexpr (q == 50) {
        WAITL0(); BAR();
#pragma unroll
        for (int sl = 0; sl < 3; ++sl)
#pragma unroll
          for (int nb = 0; nb < 5; ++nb) part[sl][nb] = *(const LAS f32x4*)(lds + L_RED + ((w * 3 + sl) * 5 + nb) * 1024 + l16);
      }
      SB();
      if constexpr (q + DH < 56) HLOAD(q + DH);
      if constexpr (q >= 44) {
        constexpr int j0 = nd + (nd < 8 ? nd : 8);
        if constexpr (j0 < 10) dma_q<0, 0, j0>(rs, l16, sQ, w, lds0); else dma_q<1, 1, j0 - 10>(rs, l16, sQ, w, lds0);
        if constexpr (nd < 8) {
          constexpr int j1 = j0 + 1;
          if constexpr (j1 < 10) dma_q<0, 0, j1>(rs, l16, sQ, w, lds0); else dma_q<1, 1, j1 - 10>(rs, l16, sQ, w, lds0);
        }
      }
      SB();
    });
    STAMP(18);
#pragma unroll
    for (int nb = 0; nb < 5; ++nb) {
      const f32x4 v = (acc[1][nb] + part[0][nb]) + (part[1][nb] + part[2][nb]);
      const u32 a = cvtpk(bflo(k.xr[nb].x) + v[0], bfhi(k.xr[nb].x) + v[1]), b = cvtpk(bflo(k.xr[nb].y) + v[2], bfhi(k.xr[nb].y) + v[3]);
      k.xr[nb] = u32x2{a, b};
    }
    const u32 token = 64 * m64 + 16 * w + t16;
    STAMP(19);
    if (it == 0) {
      float s1 = 0.f, s2 = 0.f;
      const u32 xo = ((token >> 5) * 40 + 5 * c8) * 1024 + ((token & 31) + 32 * (g >> 1)) * 16;
#pragma unroll
      for (int pr = 0; pr < 3; ++pr) {
        const int A = 2 * pr, Bn = pr < 2 ? 2 * pr + 1 : 4;
        auto r0 = __builtin_amdgcn_permlane16_swap(k.xr[A].x, k.xr[Bn].x, false, false);
        auto r1 = __builtin_amdgcn_permlane16_swap(k.xr[A].y, k.xr[Bn].y, false, false);
        const u32x4 v = {r0[0], r1[0], r0[1], r1[1]};
        if (pr < 2) stg_sc1(v, rs, xo + (A + (g & 1)) * 1024, OFF_XF + 2 * SZ_XF);
        else if ((g & 1) == 0) stg_sc1(v, rs, xo + 4 * 1024, OFF_XF + 2 * SZ_XF);
      }
#pragma unroll
      for (int nb = 0; nb < 5; ++nb) {
        const float a = bflo(k.xr[nb].x), b = bfhi(k.xr[nb].x), c = bflo(k.xr[nb].y), d = bfhi(k.xr[nb].y);
        s1 += (a + b) + (c + d); s2 += (a * a + b * b) + (c * c + d * d);
      }
      s1 = red16_32_add(s1); s2 = red16_32_add(s2);
      if (g == 0) stg2_sc1(u32x2{f2u(s1), f2u(s2)}, rs, (c8 * 2048 + token) * 8, OFF_ST + 2 * SZ_ST);
      SB();
      WAITV(0);
      WAITL0();
      BAR();
      if (tid == 0) __hip_atomic_store(flags + (FG_X2 + m64) * 32 + c8, 1u, RLX_AGENT);
      sfor<10>([&](auto I) { dma_q<2, 2, decltype(I)::value>(rs, l16, sQ, w, lds0); });
    } else {
      const u32 oo = token * 1280 + (80 * c8 + 8 * (g >> 1)) * 2;
#pragma unroll
      for (int pr = 0; pr < 3; ++pr) {
        const int A = 2 * pr, Bn = pr < 2 ? 2 * pr + 1 : 4;
        auto r0 = __builtin_amdgcn_permlane16_swap(k.xr[A].x, k.xr[Bn].x, false, false);
        auto r1 = __builtin_amdgcn_permlane16_swap(k.xr[A].y, k.xr[Bn].y, false, false);
        const u32x4 v = {r0[0], r1[0], r0[1], r1[1]};
        if (pr < 2) __builtin_amdgcn_raw_buffer_store_b128(v, k.rso, oo + (A + (g & 1)) * 32, 0, 0);
        else if ((g & 1) == 0) __builtin_amdgcn_raw_buffer_store_b128(v, k.rso, oo + 4 * 32, 0, 0);
      }
    }
    STAMP(20);
  }
}

__global__ __launch_bounds__(256, 1) void main_kernel(char* __restrict__ ws, u16* __restrict__ out) {
  extern __shared__ __attribute__((aligned(16))) char smem[];
  K k;
  k.lds = (LAS char*)smem;
  k.lds0 = (u32)(size_t)k.lds;
  k.tid = threadIdx.x; k.lane = k.tid & 63; k.w = __builtin_amdgcn_readfirstlane(k.tid >> 6);
  k.rs = __builtin_amdgcn_make_buffer_rsrc(ws, 0, WS_USED, 0x00020000);
  k.rso = __builtin_amdgcn_make_buffer_rsrc(out, 0, 2048 * 640 * 2, 0x00020000);
  k.flags = (GAS u32*)ws;
  const int bid = blockIdx.x, xc = bid & 7, j = bid >> 3, mg = xc >> 1, half = xc & 1;
  k.m128 = 4 * mg + (j >> 3); k.p = 8 * half + (j & 7);
  k.m64 = 8 * mg + (j >> 2); k.c8 = 4 * half + (j & 3);
  const int p7 = j & 7;
  k.B0 = 54 * half + (p7 < 6 ? 7 * p7 : 42 + 6 * (p7 - 6));
  k.nblk = p7 < 6 ? 7 : 6;
  {
    PHASE_IDS();
    const u32 sQ = OFF_WQ + p * 163840;
    sfor<10>([&](auto I) { dma_q<0, 0, decltype(I)::value>(rs, l16, sQ, w, lds0); });
    sfor<10>([&](auto I) { dma_q<1, 1, decltype(I)::value>(rs, l16, sQ, w, lds0); });
    sfor<10>([&](auto I) { dma_q<2, 2, decltype(I)::value>(rs, l16, sQ, w, lds0); });
    const u32 token = 64 * m64 + 16 * w + t16;
    const u32 o = ((token >> 5) * 40 + 5 * c8) * 1024 + ((token & 31) + 32 * (g >> 1)) * 16 + (g & 1) * 8;
#pragma unroll
    for (int nb = 0; nb < 5; ++nb) ald2(k.xr[nb], rs, o, OFF_XF + nb * 1024);
  }
#pragma nounroll
  for (int it = 0; it < 2; ++it) { k.it = it; iteration(k); }
}

extern "C" void kernel_launch(void* const* d_in, const int* in_sizes, int n_in, void* d_out, int out_size, void* d_ws, size_t ws_size,
                              hipStream_t stream) {
  const u16* x0 = (const u16*)d_in[0];
  const u16* wqkv = (const u16*)d_in[1];
  const u16* wo = (const u16*)d_in[2];
  const u16* wg = (const u16*)d_in[4];
  const u16* wu = (const u16*)d_in[5];
  const u16* wd = (const u16*)d_in[6];
  (void)hipFuncSetAttribute((const void*)main_kernel, hipFuncAttributeMaxDynamicSharedMemorySize, LDS_BYTES);
  prep_kernel<<<PREP_UNITS, 512, 0, stream>>>(x0, wqkv, wo, wg, wu, wd, (char*)d_ws);
  main_kernel<<<256, 256, LDS_BYTES, stream>>>((char*)d_ws, (u16*)d_out);
}
```

```cpp
#include <hip/hip_runtime.h>
#include <stdint.h>
#include <utility>

#define LAS __attribute__((address_space(3)))
#define GAS __attribute__((address_space(1)))
#define DI __device__ __forceinline__
typedef unsigned u32;
typedef unsigned short u16;
typedef u32 u32x4 __attribute__((ext_vector_type(4)));
typedef u32 u32x2 __attribute__((ext_vector_type(2)));
typedef float f32x2 __attribute__((ext_vector_type(2)));
typedef float f32x4 __attribute__((ext_vector_type(4)));
typedef float f32x16 __attribute__((ext_vector_type(16)));
typedef short s16x8 __attribute__((ext_vector_type(8)));
typedef short s16x4 __attribute__((ext_vector_type(4)));
typedef __bf16 bf16x2 __attribute__((ext_vector_type(2)));
typedef __amdgpu_buffer_rsrc_t rsrc_t;

constexpr u32 FLAG_BYTES = 32768;
constexpr u32 OFF_WQ = 65536;
constexpr u32 OFF_WO = OFF_WQ + 16 * 163840;
constexpr u32 OFF_WG = OFF_WO + 8 * 20 * 5 * 1024;
constexpr u32 OFF_WD = OFF_WG + 108 * 40960;
constexpr u32 OFF_CSQ = OFF_WD + 8 * 54 * 5 * 1024;
constexpr u32 OFF_CSG = OFF_CSQ + 16384;
constexpr u32 SZ_XF = 64 * 40 * 1024;
constexpr u32 OFF_XF = OFF_CSG + 28672;
constexpr u32 SZ_ST = 8 * 2048 * 8;
constexpr u32 OFF_ST = OFF_XF + 4 * SZ_XF;
constexpr u32 SZ_OF = 128 * 20 * 1024;
constexpr u32 OFF_OF = OFF_ST + 4 * SZ_ST;
constexpr u32 SZ_HF = 128 * 54 * 1024;
constexpr u32 OFF_HF = OFF_OF + 2 * SZ_OF;
constexpr u32 WS_USED = OFF_HF + 2 * SZ_HF;
constexpr u32 FG_O = 0, FG_X1 = 16, FG_H = 48, FG_X2 = 64, FG_PER_IT = 96;
constexpr u32 FG_TMO = 2 * FG_PER_IT;
constexpr u32 FG_EPOCH = FG_TMO + 1;
constexpr u32 FW_PF = 8192;

constexpr u32 L_HALO = 122880;
constexpr u32 L_KST = 40960;
constexpr u32 L_VST = L_KST + 144 * 80;
constexpr u32 L_QST = L_VST + 144 * 80;
constexpr u32 L_RED = 100352;
constexpr u32 L_H4 = 143360;
constexpr u32 LDS_BYTES = 163840;

constexpr u32 AUX_SC1 = 16;
#define RLX_AGENT __ATOMIC_RELAXED, __HIP_MEMORY_SCOPE_AGENT
#define SB() __builtin_amdgcn_sched_barrier(0)
#define MEMFENCE() asm volatile("" ::: "memory")
#define WAITV(n) asm volatile("s_waitcnt vmcnt(%0)" ::"i"(n) : "memory")
#define WAITL0() asm volatile("s_waitcnt lgkmcnt(0)" ::: "memory")
#define BAR() do { MEMFENCE(); __builtin_amdgcn_s_barrier(); MEMFENCE(); } while (0)
#define CERT(tag) asm volatile("; CERT " tag ::: "memory")

template <int... I, class Fn> DI void sfor_impl(std::integer_sequence<int, I...>, Fn&& f) { (f(std::integral_constant<int, I>{}), ...); }
template <int N, class Fn> DI void sfor(Fn&& f) { sfor_impl(std::make_integer_sequence<int, N>{}, f); }
DI f32x16 mfma32(u32x4 a, u32x4 b, f32x16 c) { return __builtin_amdgcn_mfma_f32_32x32x16_bf16(__builtin_bit_cast(s16x8, a), __builtin_bit_cast(s16x8, b), c, 0, 0, 0); }
DI f32x4 mfma16(u32x4 a, u32x4 b, f32x4 c) { return __builtin_amdgcn_mfma_f32_16x16x32_bf16(__builtin_bit_cast(s16x8, a), __builtin_bit_cast(s16x8, b), c, 0, 0, 0); }
DI u32 cvtpk(float lo, float hi) { f32x2 v = {lo, hi}; bf16x2 b = __builtin_convertvector(v, bf16x2); return __builtin_bit_cast(u32, b); }
DI float u2f(u32 v) { return __builtin_bit_cast(float, v); }
DI u32 f2u(float v) { return __builtin_bit_cast(u32, v); }
DI float bflo(u32 v) { return __builtin_bit_cast(float, v << 16); }
DI float bfhi(u32 v) { return __builtin_bit_cast(float, v & 0xffff0000u); }
DI float fma_s(float a, float b, float c) { float r; asm("v_fma_f32 %0, %1, %2, %3" : "=v"(r) : "v"(a), "v"(b), "v"(c)); return r; }
DI float mul_s(float a, float b) { float r; asm("v_mul_f32_e32 %0, %1, %2" : "=v"(r) : "v"(a), "v"(b)); return r; }
DI float add_s(float a, float b) { float r; asm("v_add_f32_e32 %0, %1, %2" : "=v"(r) : "v"(a), "v"(b)); return r; }
DI float max_s(float a, float b) { float r; asm("v_max_f32_e32 %0, %1, %2" : "=v"(r) : "v"(a), "v"(b)); return r; }
DI float sel_s(float f, float t, unsigned long long m) { float r; asm("v_cndmask_b32_e64 %0, %1, %2, %3" : "=v"(r) : "v"(f), "v"(t), "s"(m)); return r; }
DI float red16_32_add(float v) {
  auto a = __builtin_amdgcn_permlane16_swap(f2u(v), f2u(v), false, false);
  v = u2f(a[0]) + u2f(a[1]);
  auto b = __builtin_amdgcn_permlane32_swap(f2u(v), f2u(v), false, false);
  return u2f(b[0]) + u2f(b[1]);
}
DI float red16_32_max(float v) {
  auto a = __builtin_amdgcn_permlane16_swap(f2u(v), f2u(v), false, false);
  v = __builtin_fmaxf(u2f(a[0]), u2f(a[1]));
  auto b = __builtin_amdgcn_permlane32_swap(f2u(v), f2u(v), false, false);
  return __builtin_fmaxf(u2f(b[0]), u2f(b[1]));
}
DI float red32_add(float v) {
  auto b = __builtin_amdgcn_permlane32_swap(f2u(v), f2u(v), false, false);
  return u2f(b[0]) + u2f(b[1]);
}
DI void stg_sc1(u32x4 v, rsrc_t rs, u32 voff, u32 soff) { __builtin_amdgcn_raw_buffer_store_b128(v, rs, voff, soff, AUX_SC1); }
DI void stg2_sc1(u32x2 v, rsrc_t rs, u32 voff, u32 soff) { __builtin_amdgcn_raw_buffer_store_b64(v, rs, voff, soff, AUX_SC1); }

template <int IMM> DI void ald_v_sc1(u32x4& d, rsrc_t rs, u32 voff, u32 soff) {
  asm volatile("buffer_load_dwordx4 %0, %1, %2, %3 offen offset:%4 sc1" : "=v"(d) : "v"(voff), "s"(rs), "s"(soff), "i"(IMM) : "memory");
}
template <int IMM> DI void ald_a(u32x4& d, rsrc_t rs, u32 voff, u32 soff) {
  asm volatile("buffer_load_dwordx4 %0, %1, %2, %3 offen offset:%4 sc1" : "=a"(d) : "v"(voff), "s"(rs), "s"(soff), "i"(IMM) : "memory");
}
template <int IMM> DI void ald_v(u32x4& d, rsrc_t rs, u32 voff, u32 soff) {
  asm volatile("buffer_load_dwordx4 %0, %1, %2, %3 offen offset:%4 sc1" : "=v"(d) : "v"(voff), "s"(rs), "s"(soff), "i"(IMM) : "memory");
}
DI void ald2_sc1(u32x2& d, rsrc_t rs, u32 voff, u32 soff) {
  asm volatile("buffer_load_dwordx2 %0, %1, %2, %3 offen sc1" : "=v"(d) : "v"(voff), "s"(rs), "s"(soff) : "memory");
}
DI void ald2(u32x2& d, rsrc_t rs, u32 voff, u32 soff) {
  asm volatile("buffer_load_dwordx2 %0, %1, %2, %3 offen sc1" : "=v"(d) : "v"(voff), "s"(rs), "s"(soff) : "memory");
}
DI void ald1(u32& d, rsrc_t rs, u32 voff, u32 soff) {
  asm volatile("buffer_load_dword %0, %1, %2, %3 offen" : "=v"(d) : "v"(voff), "s"(rs), "s"(soff) : "memory");
}
template <int IMM> DI void ald_a_sc1(u32x4& d, rsrc_t rs, u32 voff, u32 soff) {
  asm volatile("buffer_load_dwordx4 %0, %1, %2, %3 offen offset:%4 sc1" : "=a"(d) : "v"(voff), "s"(rs), "s"(soff), "i"(IMM) : "memory");
}
template <int N> DI void waitpin_a(u32x4& r) { asm volatile("s_waitcnt vmcnt(%1)" : "+a"(r) : "i"(N) : "memory"); }
template <int N> DI void waitpin_a2(u32x4& r0, u32x4& r1) { asm volatile("s_waitcnt vmcnt(%2)" : "+a"(r0), "+a"(r1) : "i"(N) : "memory"); }
DI void mma32(f32x16& acc, const u32x4& wv, const u32x4& xa) { asm volatile("v_mfma_f32_32x32x16_bf16 %0, %1, %2, %0" : "+v"(acc) : "v"(wv), "a"(xa)); }
DI void mma32_0(f32x16& acc, const u32x4& wv, const u32x4& xa) { asm volatile("v_mfma_f32_32x32x16_bf16 %0, %1, %2, 0" : "=&v"(acc) : "v"(wv), "a"(xa)); }
DI void mma32w(f32x16& acc, const u32x4& wv, const u32x4& xa, const u32x4& wn) { asm volatile("v_mfma_f32_32x32x16_bf16 %0, %1, %2, %0" : "+v"(acc) : "v"(wv), "a"(xa), "v"(wn)); }
DI void mma32w_0(f32x16& acc, const u32x4& wv, const u32x4& xa, const u32x4& wn) { asm volatile("v_mfma_f32_32x32x16_bf16 %0, %1, %2, 0" : "=&v"(acc) : "v"(wv), "a"(xa), "v"(wn)); }
DI void mma16(f32x4& acc, const u32x4& av, const u32x4& bv) { asm volatile("v_mfma_f32_16x16x32_bf16 %0, %1, %2, %0" : "+v"(acc) : "v"(av), "v"(bv)); }
DI void mma16_0(f32x4& acc, const u32x4& av, const u32x4& bv) { asm volatile("v_mfma_f32_16x16x32_bf16 %0, %1, %2, 0" : "=&v"(acc) : "v"(av), "v"(bv)); }
DI void pin_vv(u32x4& a, u32x4& b) { asm volatile("" : "+v"(a), "+v"(b)); }
template <int N> DI void waitpin_v(u32x4& r) { asm volatile("s_waitcnt vmcnt(%1)" : "+v"(r) : "i"(N) : "memory"); }
DI void pin_v(u32x4& r) { asm volatile("" : "+v"(r)::"memory"); }
DI void pin_a(u32x4& r) { asm volatile("" : "+a"(r)::"memory"); }
DI void pin2(u32x2& r) { asm volatile("" : "+v"(r)::"memory"); }
DI void pin1(u32& r) { asm volatile("" : "+v"(r)::"memory"); }
#define DMA_F(TAG, IMM, SC, rs, voff, soff, m0v) \
  asm volatile("s_mov_b32 m0, %2\n\ts_nop 0\n\tbuffer_load_dwordx4 %0, %1, %3 offen offset:%4 " SC "lds ; DMA " TAG ::"v"(voff), "s"(rs), "s"(m0v), "s"(soff), "i"(IMM) : "memory")
#define DMA_N(TAG, IMM, SC, rs, voff, soff) \
  asm volatile("buffer_load_dwordx4 %0, %1, %2 offen offset:%3 " SC "lds ; DMA " TAG ::"v"(voff), "s"(rs), "s"(soff), "i"(IMM) : "memory")

#define STAMP(i) do {} while (0)
#define RSTAMP(i) do {} while (0)
#define SSTAMP(i) do {} while (0)
#define GSTAMP(i) STAMP(i)
#define QSTAMP(i) do {} while (0)
constexpr u32 OFF_STAMP = WS_USED + 4096;
constexpr u32 kSpinCap = 1u << 16;

struct K {
  rsrc_t rs, rso;
  LAS char* lds;
  u32 lds0;
  GAS u32* flags;
  int lane, w, m128, p, m64, c8, B0, nblk, tid, it;
  u32 tag;
  u32x2 xr[5];
  u32x4 wo_r[5][5];
  u32x4 wd_r[14][5];
};

#define PHASE_IDS() int lane = k.lane; asm volatile("" : "+v"(lane)); \
  int it = k.it; asm volatile("" : "+s"(it)); \
  const u32 xin = OFF_XF + it * 2 * SZ_XF, stin = OFF_ST + it * 2 * SZ_ST, x1o = xin + SZ_XF, st1o = stin + SZ_ST; \
  const u32 ofo = OFF_OF + it * SZ_OF, hfo = OFF_HF + it * SZ_HF, fgb = it * FG_PER_IT; \
  (void)xin; (void)stin; (void)x1o; (void)st1o; (void)ofo; (void)hfo; (void)fgb; \
  int w = k.w, m128 = k.m128, p = k.p, m64 = k.m64, c8 = k.c8, B0 = k.B0; \
  asm volatile("" : "+s"(w), "+s"(m128), "+s"(p), "+s"(m64), "+s"(c8), "+s"(B0)); \
  const u32 l16 = lane * 16; const int tok = lane & 31, h = lane >> 5, t16 = lane & 15, g = lane >> 4; \
  const rsrc_t rs = k.rs; LAS char* const lds = k.lds; const u32 lds0 = k.lds0; \
  (void)tok; (void)h; (void)t16; (void)g; (void)l16; (void)m128; (void)p; (void)m64; (void)c8; (void)B0; (void)lds; (void)lds0; (void)rs

DI void poll_flags(GAS u32* flags, u32 fo, bool act, int lane, u32 tag) {
  for (u32 spins = 0;;) {
    u32 v = act ? __hip_atomic_load(flags + fo, RLX_AGENT) : tag;
    if (__all(v == tag)) break;
    __builtin_amdgcn_s_sleep(1);
    if (++spins > kSpinCap) {
      if (lane == 0) __hip_atomic_store(flags + FG_TMO * 32, tag, RLX_AGENT);
      break;
    }
    if ((spins & 255u) == 0u && (u32)__builtin_amdgcn_readfirstlane(__hip_atomic_load(flags + FG_TMO * 32, RLX_AGENT)) == tag) break;
  }
}
DI void stats_finish(float s1, float s2, float& nmean, float& rstd) {
  const float mean = s1 * (1.0f / 640.0f);
  const float var = __builtin_fmaxf(s2 * (1.0f / 640.0f) - mean * mean, 0.f);
  nmean = -mean; rstd = __builtin_amdgcn_rsqf(var + 1e-5f);
}

template <int C, int SLOT, int I> DI void dma_q(rsrc_t rs, u32 l16, u32 sQ, int w, u32 lds0) {
#define Q_EMIT(TAG, FIRST, IMM, SO, MO) do { if constexpr (FIRST) DMA_F(TAG, IMM, "sc1 ", rs, l16, SO, MO); else DMA_N(TAG, IMM, "sc1 ", rs, l16, SO); } while (0)
  if constexpr (C < 2) {
    constexpr int grp = I / 4, imm = (I % 4) * 1024;
    const u32 so = sQ + w * 10240 + C * 40960 + grp * 4096, mo = lds0 + SLOT * 40960 + w * 10240 + grp * 4096;
    if constexpr (C == 0) Q_EMIT("Q0", I % 4 == 0, imm, so, mo); else Q_EMIT("Q1", I % 4 == 0, imm, so, mo);
  } else {
    constexpr int hf = I / 5, ii = I % 5, grp = ii / 4, imm = (ii % 4) * 1024;
    const u32 so = sQ + w * 5120 + 81920 + (C - 2) * 40960 + hf * 20480 + grp * 4096, mo = lds0 + SLOT * 40960 + w * 5120 + hf * 20480 + grp * 4096;
    if constexpr (C == 2) Q_EMIT("Q2", ii % 4 == 0, imm, so, mo);
    else if constexpr (hf == 0) Q_EMIT("Q3a", ii % 4 == 0, imm, so, mo);
    else Q_EMIT("Q3b", ii % 4 == 0, imm, so, mo);
  }
#undef Q_EMIT
}
template <int HH, int I> DI void dma_g(rsrc_t rs, u32 l16, u32 sG, int w, u32 lds0) {
  constexpr int grp = I / 4, imm = (I % 4) * 1024;
  const u32 so = sG + HH * 20480 + grp * 4096, mo = lds0 + w * 5120 + (HH % 7) * 20480 + grp * 4096;
#define G_CASE(n) if constexpr (HH == n) { if constexpr (I % 4 == 0) DMA_F("G" #n, imm, "sc1 ", rs, l16, so, mo); else DMA_N("G" #n, imm, "sc1 ", rs, l16, so); }
  G_CASE(0) G_CASE(1) G_CASE(2) G_CASE(3) G_CASE(4) G_CASE(5) G_CASE(6) G_CASE(7) G_CASE(8) G_CASE(9) G_CASE(10) G_CASE(11) G_CASE(12) G_CASE(13)
#undef G_CASE
}

constexpr int XW = 10;
struct QS {
  static constexpr int Q3F0 = 78;
  static constexpr bool x_at(int f) { return f < 80 && (f & 1) == 0 && (f >> 1) + XW < 40; }
  static constexpr int aux_at(int f) { return (f < 22 && (f & 1) == 1) ? f >> 1 : -1; }
  static constexpr bool q3_at(int f) { return f >= Q3F0 && f < Q3F0 + 20 && ((f - Q3F0) & 1) == 0; }
  static constexpr int wo_at(int f) {
    if (f >= 60 && f < 76 && (f & 1) == 0) return (f - 60) / 2;
    if (f >= 100 && f < 151 && (f - 100) % 3 == 0) return 8 + (f - 100) / 3;
    return -1;
  }
  static constexpr int upto(int F) { int n = 0; for (int f = 0; f <= F; ++f) n += x_at(f) + (aux_at(f) >= 0) + q3_at(f) + (wo_at(f) >= 0); return n; }
  static constexpr int xidx(int i) { return i < XW ? i : XW + upto(2 * (i - XW)) - 1; }
  static constexpr int xwait(int s) { return XW + upto(2 * s) - 1 - xidx(s); }
};
struct GS {
  static constexpr int NWD = 50;
  static constexpr bool x_at(int f) { return f < 80 && (f & 1) == 0 && (f >> 1) + XW < 40; }
  static constexpr int op(int F) {
    if (F < 8 && (F & 1)) return 3000 + (F >> 1);
    if (F & 1) return -1;
    if (F >= 62 && F < 82) return (7 + (F - 62) / 10) * 8 + ((F - 62) % 10) / 2;
    if (F >= 82 && F < 102) return (9 + (F - 82) / 10) * 8 + ((F - 82) % 10) / 2;
    if (F >= 102 && F < 118) return 1000 + (F - 102) / 2;
    if (F >= 118 && F < 138) return (11 + (F - 118) / 10) * 8 + ((F - 118) % 10) / 2;
    if (F >= 138 && F < 158) return 1008 + (F - 138) / 2;
    if (F >= 158 && F < 168) return 13 * 8 + (F - 158) / 2;
    if (F >= 168 && F < 240 && 18 + (F - 168) / 2 < NWD) return 1018 + (F - 168) / 2;
    return -1;
  }
  static constexpr bool st_at(int F) { const int ps = F / 80, r = F % 80; return F < 240 && ps > 0 && (r == 37 || r == 77); }
  static constexpr int upto(int F) { int n = 0; for (int f = 0; f <= F; ++f) n += x_at(f) + (op(f) >= 0) + st_at(f); return n; }
  static constexpr int slot(int o) { for (int f = 0; f < 300; ++f) if (op(f) == o) return f; return -1; }
  static constexpr int xidx(int i) { return i < XW ? i : XW + upto(2 * (i - XW)) - 1; }
  static constexpr int xwait(int s) { return XW + upto(2 * s) - 1 - xidx(s); }
};
static_assert(18 + (240 - 168) / 2 >= GS::NWD, "all prefetched Wd fragments need a slot");

DI void iteration(K& k) {
  GAS u32* const flags = k.flags;
  const int tid = k.tid;
  {
    PHASE_IDS();
    const u32 sQ = OFF_WQ + p * 163840, sG = OFF_WG + B0 * 40960 + w * 5120;
    STAMP(0);
    if (it > 0 && w == 0) {
      const int grp = 2 * m128 - 1 + (lane >> 3);
      const bool act = lane < 24 && grp >= 0;
      poll_flags(flags, (FG_X2 + grp) * 32 + (lane & 7), act, lane, k.tag);
    }
    WAITV(0); CERT("Q0"); CERT("Q1"); CERT("Q2");
    BAR();
    STAMP(1);
    u32x4 xf[40];
    const u32 xbase = xin + (4 * m128 + w) * 40960;
#define XLOAD(i) ald_a_sc1<((i) % 4) * 1024>(xf[i], rs, l16, xbase + ((i) / 4) * 4096)
    u32x2 hs0, hs1, sa0, sa1, sa2, sa3;
    const int rbh = m128 > 0 ? 4 * m128 - 1 : 0;
    const u32 hvo = (g >> 1) * 1024 + (g & 1) * 512 + (16 + t16) * 16;
    const int ht = 128 * m128 - 16 + t16;
    const u32 hso = ((2 * g) * 2048 + (ht < 0 ? 0 : ht)) * 8, sso = (4 * h * 2048 + 128 * m128 + 32 * w + tok) * 8;
    auto aux_load = [&](auto AC) {
      constexpr int a = decltype(AC)::value;
      if constexpr (a < 5) DMA_F("HALO", 0, "sc1 ", rs, hvo, xin + rbh * 40960 + (5 * w + a) * 2048, lds0 + L_HALO + (5 * w + a) * 1024);
      if constexpr (a == 5) ald2_sc1(sa0, rs, sso, stin);
      if constexpr (a == 6) ald2_sc1(sa1, rs, sso, stin + 16384);
      if constexpr (a == 7) ald2_sc1(sa2, rs, sso, stin + 32768);
      if constexpr (a == 8) ald2_sc1(sa3, rs, sso, stin + 49152);
      if constexpr (a == 9) ald2_sc1(hs0, rs, hso, stin);
      if constexpr (a == 10) ald2_sc1(hs1, rs, hso, stin + 16384);
    };
    sfor<XW>([&](auto I) { constexpr int i = decltype(I)::value; XLOAD(i); });
    using Q = QS;
    constexpr int DQ = 4, NBQ = 6;
    u32x4 wb[NBQ];
    f32x16 acc0, acc1, acc2, acc3;
    f32x4 hacc, hacc4;
    u32x4 hw[3], hx[3];
    float rstd = 0.f, hrs = 0.f;
    u32 lb1 = l16 + 65536, lbh = l16 + L_HALO; asm volatile("" : "+v"(lb1), "+v"(lbh));
    const u32 hwbase = (w >> 1) * 1024 + (g >> 1) * 2048 + (g & 1) * 512 + (16 * (w & 1) + t16) * 16;
    const u32 hw4base = 81920 + (g >> 1) * 1024 + (g & 1) * 512 + t16 * 16 + w * 10240;
    const u32 kvrow = L_KST + (16 + 32 * w + tok) * 80 + h * 8;
    const int hn = 16 * w + 4 * g;
    const u32 haddr = L_KST + t16 * 80 + 2 * hn + (hn >= 40 ? (L_VST - L_KST - 80) : 0);
    const u32 haddr4 = L_VST + t16 * 80 + 2 * (64 + 4 * g - 40);
    const bool hzero = (m128 == 0);
    const u32 wo_o = OFF_WO + (c8 * 20 + 5 * w) * 5120;
#define LDSQ(F) (((F) / 40 % 3) * 40960 + ((F) % 40) * 1024)
#define RDW(off) (*(const LAS u32x4*)(lds + ((off) < 65536 ? l16 : lb1) + ((off) & 65535)))
    u32 gp0 = 0;
#define STG_OFF(n0) ((n0) < 40 ? 2 * (n0) : (n0) < 80 ? (L_VST - L_KST) + 2 * ((n0) - 40) : (L_QST - L_KST - 16 * 80) + 2 * ((n0) - 80))
#define GRAN(t, ACC, q, RS, STADDR, ZERO) do { \
      if constexpr ((t) == 0) gp0 = cvtpk(mul_s(ACC[4 * (q)], RS), mul_s(ACC[4 * (q) + 1], RS)); \
      if constexpr ((t) == 1) { u32x2 pk_ = {gp0, cvtpk(mul_s(ACC[4 * (q) + 2], RS), mul_s(ACC[4 * (q) + 3], RS))}; if (ZERO) pk_ = u32x2{0u, 0u}; *(LAS u32x2*)(lds + (STADDR)) = pk_; } \
    } while (0)
#define GRAN_MAIN(t, ACC, q, n0) GRAN(t, ACC, q, rstd, kvrow + STG_OFF(n0), false)
#pragma unroll
    for (int F = 0; F < DQ; ++F) wb[F % NBQ] = RDW(LDSQ(F));
    sfor<160>([&](auto FC) {
      constexpr int F = decltype(FC)::value, Fr = F + DQ;
      if constexpr (F == 20) QSTAMP(21);
      if constexpr (F == 40) QSTAMP(22);
      if constexpr (F == 60) QSTAMP(23);
      if constexpr (F == 100) QSTAMP(24);
      if constexpr (F == 120) QSTAMP(25);
      if constexpr (F == 140) QSTAMP(26);
      if constexpr (Q::x_at(F)) XLOAD((F >> 1) + XW);
      if constexpr (Q::aux_at(F) >= 0) aux_load(std::integral_constant<int, Q::aux_at(F)>{});
      if constexpr (Fr == 80) {
        WAITL0(); BAR();
        STAMP(2);
      }
      if constexpr (Q::q3_at(F)) dma_q<3, 0, ((F - Q::Q3F0) >> 1)>(rs, l16, sQ, w, lds0);
      if constexpr (Q::wo_at(F) >= 0) {
        constexpr int i = Q::wo_at(F), s = i / 5, nb = i % 5;
        if constexpr (i < 24) ald_a<(i % 4) * 1024>(k.wo_r[s][nb], rs, l16, wo_o + (i / 4) * 4096); else ald_v<(i % 4) * 1024>(k.wo_r[s][nb], rs, l16, wo_o + (i / 4) * 4096);
      }
      if constexpr (Fr == 40) {
        WAITV(Q::upto(F) - Q::upto(9));
        CERT("HALO");
        BAR();
      }
      if constexpr (Fr == 120) { WAITV(Q::upto(F) - Q::upto(Q::Q3F0 + 8)); CERT("Q3a"); WAITL0(); BAR(); }
      if constexpr (Fr == 140) { WAITV(Q::upto(F) - Q::upto(Q::Q3F0 + 18)); CERT("Q3b"); BAR(); }
      if constexpr (Fr < 160) wb[Fr % NBQ] = RDW(LDSQ(Fr));
      if constexpr (F < 80) {
        constexpr int s = F >> 1;
        if constexpr ((F & 3) == 0) waitpin_a2<Q::xwait(s + 1) + (Q::upto(F) - Q::upto(F + 2))>(xf[s], xf[s + 1]);
        if constexpr (F == 0) mma32w_0(acc0, wb[0], xf[0], wb[1]);
        else if constexpr (F == 1) mma32_0(acc1, wb[1], xf[0]);
        else if constexpr (F & 1) mma32(acc1, wb[F % NBQ], xf[s]);
        else mma32w(acc0, wb[F % NBQ], xf[s], wb[(F + 1) % NBQ]);
        if constexpr (F == 60) {
          pin2(sa0); pin2(sa1); pin2(sa2); pin2(sa3);
          float nm;
          stats_finish(red32_add((u2f(sa0.x) + u2f(sa1.x)) + (u2f(sa2.x) + u2f(sa3.x))), red32_add((u2f(sa0.y) + u2f(sa1.y)) + (u2f(sa2.y) + u2f(sa3.y))), nm, rstd);
        }
        if constexpr (F == 64) {
          pin2(hs0); pin2(hs1);
          float nm;
          stats_finish(red16_32_add(u2f(hs0.x) + u2f(hs1.x)), red16_32_add(u2f(hs0.y) + u2f(hs1.y)), nm, hrs);
        }
        if constexpr (F == 68) {
#pragma unroll
          for (int nb = 0; nb < 5; ++nb) pin2(k.xr[nb]);
        }
        if constexpr (F & 1) {
          if constexpr (s == 20) mma16_0(hacc, hw[0], hx[0]);
          if constexpr (s > 20 && s <= 39) mma16(hacc, hw[(s - 20) % 3], hx[(s - 20) % 3]);
          if constexpr (s >= 18 && s <= 37) {
            constexpr int sg = s - 18;
            hw[sg % 3] = *(const LAS u32x4*)(lds + hwbase + (sg < 10 ? sg * 4096 : 40960 + (sg - 10) * 4096));
            hx[sg % 3] = *(const LAS u32x4*)(lds + lbh + sg * 1024);
          }
        }
      } else if constexpr (F < 120) {
        constexpr int s = F - 80;
        if constexpr (s == 0) mma32w_0(acc2, wb[F % NBQ], xf[s], wb[(F + 1) % NBQ]); else if constexpr (s & 1) mma32(acc2, wb[F % NBQ], xf[s]); else mma32w(acc2, wb[F % NBQ], xf[s], wb[(F + 1) % NBQ]);
        if constexpr (s >= 1 && s <= 9 && (s & 1) == 1) {
          constexpr int j = s >> 1;
          hw[j % 3] = *(const LAS u32x4*)(lds + hw4base + j * 2048);
          hx[j % 3] = *(const LAS u32x4*)(lds + lbh + w * 5120 + j * 1024);
        }
        if constexpr (s == 5) mma16_0(hacc4, hw[0], hx[0]);
        if constexpr (s > 5 && s <= 13 && (s & 1) == 1) mma16(hacc4, hw[((s - 5) >> 1) % 3], hx[((s - 5) >> 1) % 3]);
        if constexpr (s == 17) *(LAS f32x4*)(lds + L_H4 + w * 1024 + l16) = hacc4;
        constexpr int gi = (s - 2) / 4, st = (s - 2) % 4;
        if constexpr (s >= 2 && gi < 8 && st < 2) {
          constexpr int b = gi >> 2, q = gi & 3, n0 = 32 * b + 8 * q;
          if constexpr (b == 0) GRAN_MAIN(st, acc0, q, n0); else GRAN_MAIN(st, acc1, q, n0);
        }
        if constexpr (s == 34 || s == 35) GRAN(s - 34, hacc, 0, hrs, haddr, hzero);
      } else {
        constexpr int s = F - 120;
        if constexpr (s == 0) mma32w_0(acc3, wb[F % NBQ], xf[s], wb[(F + 1) % NBQ]); else if constexpr (s & 1) mma32(acc3, wb[F % NBQ], xf[s]); else mma32w(acc3, wb[F % NBQ], xf[s], wb[(F + 1) % NBQ]);
        constexpr int gi = (s - 2) / 4, st = (s - 2) % 4;
        if constexpr (s >= 2 && gi < 4 && st < 2) GRAN_MAIN(st, acc2, gi, 64 + 8 * gi);
        if constexpr (s == 18) {
          const f32x4 p0 = *(const LAS f32x4*)(lds + L_H4 + l16), p1 = *(const LAS f32x4*)(lds + L_H4 + 1024 + l16);
          const f32x4 p2 = *(const LAS f32x4*)(lds + L_H4 + 2048 + l16), p3 = *(const LAS f32x4*)(lds + L_H4 + 3072 + l16);
          hacc4 = (p0 + p1) + (p2 + p3);
        }
        if constexpr (s == 24 || s == 25) GRAN(s - 24, hacc4, 0, hrs, haddr4, hzero);
      }
      SB();
    });
    STAMP(3);
    asm volatile("s_nop 11" : "+v"(acc3));
    GRAN_MAIN(0, acc3, 0, 96); GRAN_MAIN(1, acc3, 0, 96); GRAN_MAIN(0, acc3, 1, 104); GRAN_MAIN(1, acc3, 1, 104); GRAN_MAIN(0, acc3, 2, 112); GRAN_MAIN(1, acc3, 2, 112);
    WAITL0(); BAR();
    STAMP(4);
    {
      const int par = t16 & 1;
      const bool ca = (4 * g + par) > t16, cb = (4 * g + 2 + par) > t16;
      const unsigned long long mca = __ballot(ca), mcb = __ballot(cb), mpar = __ballot(par != 0);
      const bool neg = (m128 == 0) && (w == 0);
      const unsigned long long nga = neg ? mca : 0ull, ngb = neg ? mcb : 0ull;
      const u32 gq = g < 2 ? g : 2;
      const u32 mA = g < 3 ? ~0u : 0u, mB = g < 2 ? ~0u : 0u, mC = (g == 1 || g == 2) ? ~0u : 0u;
      const u32 pla = par ? 0x01000c0cu : 0x0c0c0100u, plb = par ? 0x03020c0cu : 0x0c0c0302u;
      const u32 sel0 = ca ? pla : 0x0c0c0c0cu, sel2 = ca ? 0x0c0c0c0cu : pla, sel1 = cb ? plb : 0x0c0c0c0cu, sel3 = cb ? 0x0c0c0c0cu : plb;
      constexpr float cexp = 0.22360679774997896f * 1.4426950408889634f;
      float cx = cexp; asm volatile("" : "+v"(cx));
      const u32 qa = L_QST + (32 * w + t16) * 80 + gq * 16, ka = L_KST + (32 * w + t16) * 80 + gq * 16;
      const u32 va = L_VST + (32 * w + 4 * g + (t16 >> 2)) * 80 + (t16 & 3) * 8;
      u32x4 qf[4], k0[4], k1[4];
      u32x4 v0[4], v1[4];
#pragma unroll
      for (int u = 0; u < 4; ++u) {
        const u32 o = (u >> 1) * 16 * 80 + (u & 1) * 32;
        qf[u] = *(const LAS u32x4*)(lds + qa + o);
        k0[u] = *(const LAS u32x4*)(lds + ka + o);
        k1[u] = *(const LAS u32x4*)(lds + ka + o + 16 * 80);
      }
      dma_g<0, 0>(rs, l16, sG, w, lds0);
#pragma unroll
      for (int u = 0; u < 4; ++u) {
        if (u & 1) { qf[u][0] &= mC; qf[u][1] &= mC; qf[u][2] &= mA; qf[u][3] &= mA; }
        else { qf[u][0] &= mA; qf[u][1] &= mA; qf[u][2] &= mB; qf[u][3] &= mB; }
      }
      f32x4 s0[4], s1[4];
      asm volatile("s_nop 1\n\t"
                   "v_mfma_f32_16x16x32_bf16 %0, %8, %16, 0\n\tv_mfma_f32_16x16x32_bf16 %1, %12, %16, 0\n\t"
                   "v_mfma_f32_16x16x32_bf16 %2, %9, %17, 0\n\tv_mfma_f32_16x16x32_bf16 %3, %13, %17, 0\n\t"
                   "v_mfma_f32_16x16x32_bf16 %4, %10, %18, 0\n\tv_mfma_f32_16x16x32_bf16 %5, %14, %18, 0\n\t"
                   "v_mfma_f32_16x16x32_bf16 %6, %11, %19, 0\n\tv_mfma_f32_16x16x32_bf16 %7, %15, %19, 0"
                   : "=&v"(s0[0]), "=&v"(s1[0]), "=&v"(s0[1]), "=&v"(s1[1]), "=&v"(s0[2]), "=&v"(s1[2]), "=&v"(s0[3]), "=&v"(s1[3])
                   : "v"(k0[0]), "v"(k0[1]), "v"(k0[2]), "v"(k0[3]), "v"(k1[0]), "v"(k1[1]), "v"(k1[2]), "v"(k1[3]), "v"(qf[0]), "v"(qf[1]), "v"(qf[2]), "v"(qf[3]));
#pragma unroll
      for (int u = 0; u < 4; ++u) {
        const u32 o = (u >> 1) * 16 * 80 + (u & 1) * 40;
        const u32x2 t0 = __builtin_bit_cast(u32x2, __builtin_amdgcn_ds_read_tr16_b64_v4i16((LAS s16x4*)(lds + va + o)));
        const u32x2 t1 = __builtin_bit_cast(u32x2, __builtin_amdgcn_ds_read_tr16_b64_v4i16((LAS s16x4*)(lds + va + o + 16 * 80)));
        const u32x2 t2 = __builtin_bit_cast(u32x2, __builtin_amdgcn_ds_read_tr16_b64_v4i16((LAS s16x4*)(lds + va + o + 32)));
        const u32x2 t3 = __builtin_bit_cast(u32x2, __builtin_amdgcn_ds_read_tr16_b64_v4i16((LAS s16x4*)(lds + va + o + 32 + 16 * 80)));
        v0[u] = u32x4{t0.x, t0.y, t1.x, t1.y}; v1[u] = u32x4{t2.x, t2.y, t3.x, t3.y};
      }
      dma_g<0, 1>(rs, l16, sG, w, lds0);
      asm volatile("s_nop 7" : "+v"(s0[0]), "+v"(s1[0]), "+v"(s0[1]), "+v"(s1[1]), "+v"(s0[2]), "+v"(s1[2]), "+v"(s0[3]), "+v"(s1[3]));
      float a[4], b[4], mx[4], sm[4];
#pragma unroll
      for (int u = 0; u < 4; ++u) {
        const float x00 = s0[u][0], x01 = s0[u][1], x02 = s0[u][2], x03 = s0[u][3], x10 = s1[u][0], x11 = s1[u][1], x12 = s1[u][2], x13 = s1[u][3];
        a[u] = sel_s(sel_s(x10, x11, mpar), sel_s(x00, x01, mpar), mca);
        b[u] = sel_s(sel_s(x12, x13, mpar), sel_s(x02, x03, mpar), mcb);
        if (u < 2) { a[u] = sel_s(a[u], -__builtin_inff(), nga); b[u] = sel_s(b[u], -__builtin_inff(), ngb); }
        mx[u] = max_s(a[u], b[u]);
      }
      dma_g<0, 2>(rs, l16, sG, w, lds0);
#pragma unroll
      for (int u = 0; u < 4; ++u) { auto r = __builtin_amdgcn_permlane16_swap(f2u(mx[u]), f2u(mx[u]), false, false); mx[u] = max_s(u2f(r[0]), u2f(r[1])); }
      dma_g<0, 3>(rs, l16, sG, w, lds0);
#pragma unroll
      for (int u = 0; u < 4; ++u) { auto r = __builtin_amdgcn_permlane32_swap(f2u(mx[u]), f2u(mx[u]), false, false); mx[u] = max_s(u2f(r[0]), u2f(r[1])); }
      dma_g<0, 4>(rs, l16, sG, w, lds0);
#pragma unroll
      for (int u = 0; u < 4; ++u) {
        const float t = -mul_s(mx[u], cx);
        a[u] = __builtin_amdgcn_exp2f(fma_s(a[u], cx, t)); b[u] = __builtin_amdgcn_exp2f(fma_s(b[u], cx, t));
        sm[u] = a[u] + b[u];
      }
      dma_g<1, 0>(rs, l16, sG, w, lds0);
#pragma unroll
      for (int u = 0; u < 4; ++u) { auto r = __builtin_amdgcn_permlane16_swap(f2u(sm[u]), f2u(sm[u]), false, false); sm[u] = u2f(r[0]) + u2f(r[1]); }
      dma_g<1, 1>(rs, l16, sG, w, lds0);
#pragma unroll
      for (int u = 0; u < 4; ++u) { auto r = __builtin_amdgcn_permlane32_swap(f2u(sm[u]), f2u(sm[u]), false, false); sm[u] = u2f(r[0]) + u2f(r[1]); }
      dma_g<1, 2>(rs, l16, sG, w, lds0);
      u32x4 pf[4];
#pragma unroll
      for (int u = 0; u < 4; ++u) {
        const float inv = __builtin_amdgcn_rcpf(sm[u]);
        const u32 pk = cvtpk(a[u] * inv, b[u] * inv);
        pf[u] = u32x4{__builtin_amdgcn_perm(pk, pk, sel0), __builtin_amdgcn_perm(pk, pk, sel1), __builtin_amdgcn_perm(pk, pk, sel2), __builtin_amdgcn_perm(pk, pk, sel3)};
      }
      dma_g<1, 3>(rs, l16, sG, w, lds0);
      f32x4 o0[4], o1[4];
      asm volatile("s_nop 1\n\t"
                   "v_mfma_f32_16x16x32_bf16 %0, %8, %16, 0\n\tv_mfma_f32_16x16x32_bf16 %1, %12, %16, 0\n\t"
                   "v_mfma_f32_16x16x32_bf16 %2, %9, %17, 0\n\tv_mfma_f32_16x16x32_bf16 %3, %13, %17, 0\n\t"
                   "v_mfma_f32_16x16x32_bf16 %4, %10, %18, 0\n\tv_mfma_f32_16x16x32_bf16 %5, %14, %18, 0\n\t"
                   "v_mfma_f32_16x16x32_bf16 %6, %11, %19, 0\n\tv_mfma_f32_16x16x32_bf16 %7, %15, %19, 0\n\ts_nop 7"
                   : "=&v"(o0[0]), "=&v"(o1[0]), "=&v"(o0[1]), "=&v"(o1[1]), "=&v"(o0[2]), "=&v"(o1[2]), "=&v"(o0[3]), "=&v"(o1[3])
                   : "v"(v0[0]), "v"(v0[1]), "v"(v0[2]), "v"(v0[3]), "v"(v1[0]), "v"(v1[1]), "v"(v1[2]), "v"(v1[3]), "v"(pf[0]), "v"(pf[1]), "v"(pf[2]), "v"(pf[3]));
      dma_g<1, 4>(rs, l16, sG, w, lds0);
      const u32 sg = 5 * p + g, sg4 = 5 * p + 4;
      const u32 oa = (sg >> 2) * 1024 + (t16 + 16 * (sg & 3)) * 16, oa4 = (sg4 >> 2) * 1024 + (t16 + 16 * (sg4 & 3)) * 16;
#pragma unroll
      for (int qb = 0; qb < 2; ++qb) {
        const u32 so = ofo + (8 * m128 + 2 * w + qb) * 20480;
        const f32x4 A0 = o0[2 * qb], B0_ = o0[2 * qb + 1], A1 = o1[2 * qb], B1 = o1[2 * qb + 1];
        stg_sc1(u32x4{cvtpk(A0[0], A0[1]), cvtpk(A0[2], A0[3]), cvtpk(B0_[0], B0_[1]), cvtpk(B0_[2], B0_[3])}, rs, oa, so);
        if (g == 0) stg_sc1(u32x4{cvtpk(A1[0], A1[1]), cvtpk(A1[2], A1[3]), cvtpk(B1[0], B1[1]), cvtpk(B1[2], B1[3])}, rs, oa4, so);
      }
    }
    SB();
    STAMP(5);
    WAITV(0); CERT("G0"); CERT("G1");
    WAITL0();
    BAR();
    if (tid == 0) __hip_atomic_store(flags + (fgb + FG_O + m128) * 32 + p, k.tag, RLX_AGENT);
    STAMP(6); RSTAMP(0);
    sfor<5>([&](auto I) { dma_g<2, decltype(I)::value>(rs, l16, sG, w, lds0); });
    sfor<5>([&](auto I) { dma_g<3, decltype(I)::value>(rs, l16, sG, w, lds0); });
  }
  {
    PHASE_IDS();
    if (w == 0) poll_flags(flags, (fgb + FG_O + (m64 >> 1)) * 32 + (lane & 15), lane < 16, lane, k.tag);
    BAR();
    STAMP(7); RSTAMP(1);
#pragma unroll
    for (int s = 0; s < 5; ++s)
#pragma unroll
      for (int nb = 0; nb < 5; ++nb) { if (s * 5 + nb < 24) pin_a(k.wo_r[s][nb]); else pin_v(k.wo_r[s][nb]); }
    u32x4 af[20];
    u32 tbo[4];
#pragma unroll
    for (int tbi = 0; tbi < 4; ++tbi) tbo[tbi] = ofo + ((4 * m64 + ((w + 1 + tbi) & 3)) * 20 + 5 * w) * 1024;
    constexpr int OW = 10;
#define OLOAD(q) ald_v_sc1<((q) % 5 % 4) * 1024>(af[q], rs, l16, tbo[(q) / 5] + ((q) % 5 / 4) * 4096)
    sfor<OW>([&](auto I) { constexpr int q = decltype(I)::value; OLOAD(q); });
    f32x4 acc[2][5];
    f32x4 part[3][5];
    sfor<20>([&](auto QC) {
      constexpr int q = decltype(QC)::value, tbi = q / 5, s = q % 5;
      if constexpr (q + OW < 20) OLOAD(q + OW);
      waitpin_v<(q < 10 ? OW : (19 - q))>(af[q]);
#pragma unroll
      for (int nb = 0; nb < 5; ++nb) acc[tbi & 1][nb] = mfma16(k.wo_r[s][nb], af[q], s == 0 ? f32x4{0.f, 0.f, 0.f, 0.f} : acc[tbi & 1][nb]);
      if constexpr (tbi > 0 && s < 3) {
        const u32 tbp = (w + tbi) & 3;
        *(LAS f32x4*)(lds + L_RED + ((tbp * 3 + (3 - tbi)) * 5 + 2 * s) * 1024 + l16) = acc[(tbi - 1) & 1][2 * s];
        if constexpr (2 * s + 1 < 5) *(LAS f32x4*)(lds + L_RED + ((tbp * 3 + (3 - tbi)) * 5 + 2 * s + 1) * 1024 + l16) = acc[(tbi - 1) & 1][2 * s + 1];
      }
      if constexpr (q == 18) {
        WAITL0(); BAR();
#pragma unroll
        for (int sl = 0; sl < 3; ++sl)
#pragma unroll
          for (int nb = 0; nb < 5; ++nb) part[sl][nb] = *(const LAS f32x4*)(lds + L_RED + ((w * 3 + sl) * 5 + nb) * 1024 + l16);
      }
      SB();
    });
    STAMP(8);
    float s1 = 0.f, s2 = 0.f;
#pragma unroll
    for (int nb = 0; nb < 5; ++nb) {
      const f32x4 v = (acc[1][nb] + part[0][nb]) + (part[1][nb] + part[2][nb]);
      const u32 a = cvtpk(bflo(k.xr[nb].x) + v[0], bfhi(k.xr[nb].x) + v[1]), b = cvtpk(bflo(k.xr[nb].y) + v[2], bfhi(k.xr[nb].y) + v[3]);
      k.xr[nb] = u32x2{a, b};
    }
    const u32 token = 64 * m64 + 16 * w + t16;
    const u32 xo = ((token >> 5) * 40 + 5 * c8) * 1024 + ((token & 31) + 32 * (g >> 1)) * 16;
#pragma unroll
    for (int pr = 0; pr < 3; ++pr) {
      const int A = 2 * pr, Bn = pr < 2 ? 2 * pr + 1 : 4;
      auto r0 = __builtin_amdgcn_permlane16_swap(k.xr[A].x, k.xr[Bn].x, false, false);
      auto r1 = __builtin_amdgcn_permlane16_swap(k.xr[A].y, k.xr[Bn].y, false, false);
      const u32x4 v = {r0[0], r1[0], r0[1], r1[1]};
      if (pr < 2) stg_sc1(v, rs, xo + (A + (g & 1)) * 1024, x1o);
      else if ((g & 1) == 0) stg_sc1(v, rs, xo + 4 * 1024, x1o);
    }
#pragma unroll
    for (int nb = 0; nb < 5; ++nb) {
      const float a = bflo(k.xr[nb].x), b = bfhi(k.xr[nb].x), c = bflo(k.xr[nb].y), d = bfhi(k.xr[nb].y);
      s1 += (a + b) + (c + d); s2 += (a * a + b * b) + (c * c + d * d);
    }
    s1 = red16_32_add(s1); s2 = red16_32_add(s2);
    if (g == 0) stg2_sc1(u32x2{f2u(s1), f2u(s2)}, rs, (c8 * 2048 + token) * 8, st1o);
    SB();
    STAMP(9);
    WAITV(0); CERT("G2"); CERT("G3");
    WAITL0();
    BAR();
    if (tid == 0) __hip_atomic_store(flags + (fgb + FG_X1 + m64) * 32 + c8, k.tag, RLX_AGENT);
    STAMP(10); RSTAMP(2);
  }
  {
    PHASE_IDS();
    const int nblk = k.nblk;
    const int ds0 = w < 2 ? 14 * w : 28 + 13 * (w - 2);
    const u32 sG = OFF_WG + B0 * 40960 + w * 5120;
    sfor<5>([&](auto I) { dma_g<4, decltype(I)::value>(rs, l16, sG, w, lds0); });
    sfor<5>([&](auto I) { dma_g<5, decltype(I)::value>(rs, l16, sG, w, lds0); });
    sfor<5>([&](auto I) { dma_g<6, decltype(I)::value>(rs, l16, sG, w, lds0); });
    if (w == 0) poll_flags(flags, (fgb + FG_X1 + 2 * m128 + ((lane >> 3) & 1)) * 32 + (lane & 7), lane < 16, lane, k.tag);
    BAR();
    STAMP(11); RSTAMP(3);
    u32x2 sa0, sa1, sa2, sa3;
    u32x4 xf[40];
    const u32 xbase = x1o + (4 * m128 + w) * 40960;
    const u32 sso = (4 * h * 2048 + 128 * m128 + 32 * w + tok) * 8;
    sfor<XW>([&](auto I) { constexpr int i = decltype(I)::value; XLOAD(i); });
    constexpr int NWD = GS::NWD;
    float r2 = 0.f;
    constexpr int DG = 4, NBG = 6;
    u32x4 wb[NBG];
    f32x16 ac[2][2];
    u32 lb1 = l16 + 65536, lb2 = l16 + 131072; asm volatile("" : "+v"(lb1), "+v"(lb2));
    const u32 hbase = (8 * m128 + 2 * w + (tok >> 4)) * 54 * 1024 + ((tok & 15) + 16 * h) * 16;
    const u32 wd_o = OFF_WD + (c8 * 54 + ds0) * 5120;
#define LDSG(F) ((((F) / 20) % 7) * 20480 + ((F) % 20) * 1024)
#define RDG(off) (*(const LAS u32x4*)(lds + ((off) < 65536 ? l16 : (off) < 131072 ? lb1 : lb2) + ((off) & 65535)))
    float hv0 = 0.f;
    u32 hp[4];
#define GU_STAGE(ACC, idx, t) do { \
      if constexpr ((t) < 8) { const float hv_ = mul_s(mul_s(ACC[t], r2), ACC[8 + (t)]); if constexpr ((t) & 1) hp[(t) >> 1] = cvtpk(hv0, hv_); else hv0 = hv_; } \
      if constexpr ((t) == 8) { const u32 fb_ = B0 + (idx); stg_sc1(u32x4{hp[0], hp[1], hp[2], hp[3]}, rs, hbase + (fb_ & 1) * 512, hfo + (fb_ >> 1) * 1024); } \
    } while (0)
    auto wd_load = [&](auto OC) {
      constexpr int i = decltype(OC)::value, s = i / 5, nb = i % 5;
      if constexpr (i < 24 || i >= 58) ald_a<(i % 4) * 1024>(k.wd_r[s][nb], rs, l16, wd_o + (i / 4) * 4096);
      else ald_v<(i % 4) * 1024>(k.wd_r[s][nb], rs, l16, wd_o + (i / 4) * 4096);
    };
#pragma unroll
    for (int F = 0; F < DG; ++F) wb[F % NBG] = RDG(LDSG(F));
    sfor<240>([&](auto FC) {
      constexpr int F = decltype(FC)::value, Fr = F + DG;
      if constexpr (GS::x_at(F)) XLOAD((F >> 1) + XW);
      if constexpr (GS::op(F) >= 0 && GS::op(F) < 1000) dma_g<GS::op(F) / 8, GS::op(F) % 8>(rs, l16, sG, w, lds0);
      if constexpr (GS::op(F) >= 1000 && GS::op(F) < 2000) wd_load(std::integral_constant<int, GS::op(F) - 1000>{});
      if constexpr (GS::op(F) == 3000) ald2_sc1(sa0, rs, sso, st1o);
      if constexpr (GS::op(F) == 3001) ald2_sc1(sa1, rs, sso, st1o + 16384);
      if constexpr (GS::op(F) == 3002) ald2_sc1(sa2, rs, sso, st1o + 32768);
      if constexpr (GS::op(F) == 3003) ald2_sc1(sa3, rs, sso, st1o + 49152);
      if constexpr (Fr == 40) { WAITL0(); BAR(); }
      if constexpr (Fr == 80) { CERT("G4"); CERT("G5"); CERT("G6"); WAITL0(); BAR(); STAMP(12); }
      if constexpr (Fr == 120) { GSTAMP(21); WAITV(GS::upto(F) - GS::upto(GS::slot(7 * 8 + 4))); CERT("G7"); WAITL0(); BAR(); GSTAMP(22); }
      if constexpr (Fr == 160) { GSTAMP(23); WAITV(GS::upto(F) - GS::upto(GS::slot(9 * 8 + 4))); CERT("G8"); CERT("G9"); WAITL0(); BAR(); GSTAMP(24); }
      if constexpr (Fr == 200) { GSTAMP(25); WAITV(GS::upto(F) - GS::upto(GS::slot(11 * 8 + 4))); CERT("G10"); CERT("G11"); BAR(); GSTAMP(26); }
      if constexpr (Fr < 240) wb[Fr % NBG] = RDG(LDSG(Fr));
      constexpr int ps = F / 80, s = (F % 80) >> 1, b = F & 1;
      if constexpr (ps == 0 && (F & 3) == 0) waitpin_a2<GS::xwait(s + 1) + (GS::upto(F) - GS::upto(F + 2))>(xf[s], xf[s + 1]);
      if constexpr (b == 0) { if constexpr (s == 0) mma32w_0(ac[ps & 1][0], wb[F % NBG], xf[0], wb[(F + 1) % NBG]); else mma32w(ac[ps & 1][0], wb[F % NBG], xf[s], wb[(F + 1) % NBG]); }
      else { if constexpr (s == 0) mma32_0(ac[ps & 1][1], wb[F % NBG], xf[0]); else mma32(ac[ps & 1][1], wb[F % NBG], xf[s]); }
      if constexpr (F == 60) {
        pin2(sa0); pin2(sa1); pin2(sa2); pin2(sa3);
        float rstd, nm;
        stats_finish(red32_add((u2f(sa0.x) + u2f(sa1.x)) + (u2f(sa2.x) + u2f(sa3.x))), red32_add((u2f(sa0.y) + u2f(sa1.y)) + (u2f(sa2.y) + u2f(sa3.y))), nm, rstd);
        r2 = rstd * rstd;
      }
      if constexpr (ps > 0 && b == 1 && (s & 1) == 0) {
        if constexpr (s >= 2 && s <= 18) GU_STAGE(ac[(ps - 1) & 1][0], 2 * (ps - 1), (s - 2) / 2);
        if constexpr (s >= 22 && s <= 38) GU_STAGE(ac[(ps - 1) & 1][1], 2 * (ps - 1) + 1, (s - 22) / 2);
      }
      SB();
    });
    asm volatile("s_nop 11" : "+v"(ac[0][0]), "+v"(ac[0][1]));
    STAMP(13);
    if (nblk == 7) {
      asm volatile("; BR7_BEGIN" ::: "memory");
      WAITV(GS::upto(239) - GS::upto(GS::slot(13 * 8 + 4))); CERT("G12"); CERT("G13"); BAR();
#pragma unroll
      for (int F = 240; F < 240 + DG; ++F) wb[F % NBG] = RDG(LDSG(F));
      sfor<40>([&](auto FC) {
        constexpr int F = 240 + decltype(FC)::value, Fr = F + DG;
        constexpr int s = F - 240;
        if constexpr (s >= 6 && (s & 3) == 2 && NWD + (s - 6) / 4 < 58) wd_load(std::integral_constant<int, NWD + (s - 6) / 4>{});
        if constexpr (Fr < 280) wb[Fr % NBG] = RDG(LDSG(Fr));
        if constexpr (s == 0) mma32w_0(ac[1][0], wb[F % NBG], xf[0], wb[(F + 1) % NBG]); else if constexpr (s & 1) mma32(ac[1][0], wb[F % NBG], xf[s]); else mma32w(ac[1][0], wb[F % NBG], xf[s], wb[(F + 1) % NBG]);
        if constexpr (s >= 3 && s <= 19 && (s & 1) == 1) GU_STAGE(ac[0][0], 4, (s - 3) / 2);
        if constexpr (s >= 21 && s <= 37 && (s & 1) == 1) GU_STAGE(ac[0][1], 5, (s - 21) / 2);
        SB();
      });
      asm volatile("s_nop 11" : "+v"(ac[1][0]));
      sfor<9>([&](auto T) { GU_STAGE(ac[1][0], 6, decltype(T)::value); });
      asm volatile("; BR7_ELSE" ::: "memory");
    } else {
      asm volatile("; BR7_ELSE" ::: "memory");
      sfor<9>([&](auto T) { GU_STAGE(ac[0][0], 4, decltype(T)::value); });
      sfor<9>([&](auto T) { GU_STAGE(ac[0][1], 5, decltype(T)::value); });
      sfor<8>([&](auto I) { wd_load(std::integral_constant<int, NWD + decltype(I)::value>{}); });
      asm volatile("; BR7_END" ::: "memory");
    }
    asm volatile("; BR7_END" ::: "memory");
    SB();
    STAMP(14);
    WAITV(0);
    WAITL0();
    BAR();
    if (tid == 0) __hip_atomic_store(flags + (fgb + FG_H + m128) * 32 + p, k.tag, RLX_AGENT);
    {
      const u32 wd_o = OFF_WD + (c8 * 54 + ds0) * 5120;
      sfor<12>([&](auto I) { constexpr int i = 58 + decltype(I)::value; ald_a<(i % 4) * 1024>(k.wd_r[i / 5][i % 5], rs, l16, wd_o + (i / 4) * 4096); });
    }
    STAMP(15); RSTAMP(4);
  }
  {
    PHASE_IDS();
    const int ds0 = w < 2 ? 14 * w : 28 + 13 * (w - 2);
    const u32 sQ = OFF_WQ + p * 163840;
    STAMP(16);
    if (w == 0) poll_flags(flags, (fgb + FG_H + (m64 >> 1)) * 32 + (lane & 15), lane < 16, lane, k.tag);
    else WAITV(0);
    BAR();
    STAMP(17); RSTAMP(5);
    sfor<70>([&](auto I) { constexpr int i = decltype(I)::value; if constexpr (i < 24 || i >= 58) pin_a(k.wd_r[i / 5][i % 5]); else pin_v(k.wd_r[i / 5][i % 5]); });
    constexpr int DH = 12;
    u32x4 af[DH];
    const bool s13 = w < 2;
    u32 tbo[4];
#pragma unroll
    for (int tbi = 0; tbi < 4; ++tbi) tbo[tbi] = hfo + ((4 * m64 + ((w + 1 + tbi) & 3)) * 54 + ds0) * 1024;
#define HLOAD(q) ald_a_sc1<((q) % 14 % 4) * 1024>(af[(q) % DH], rs, l16, tbo[(q) / 14] + ((q) % 14 / 4) * 4096)
    sfor<DH>([&](auto I) { constexpr int q = decltype(I)::value; HLOAD(q); });
    f32x4 acc[2][5];
    f32x4 part[3][5];
    sfor<56>([&](auto QC) {
      constexpr int q = decltype(QC)::value, tbi = q / 14, s = q % 14;
      constexpr int yf = (q + DH - 1 < 55 ? q + DH - 1 : 55) - q;
      constexpr int nd = q - 44;
      constexpr int yd = (nd > 0) ? (nd + (nd < 8 ? nd : 8)) : 0;
      waitpin_a<yf + yd>(af[q % DH]);
      if constexpr (s == 0) {
#pragma unroll
        for (int nb = 0; nb < 5; ++nb) acc[tbi & 1][nb] = mfma16(k.wd_r[s][nb], af[q % DH], f32x4{0.f, 0.f, 0.f, 0.f});
      } else if (s < 13 || s13) {
#pragma unroll
        for (int nb = 0; nb < 5; ++nb) acc[tbi & 1][nb] = mfma16(k.wd_r[s][nb], af[q % DH], acc[tbi & 1][nb]);
      }
      if constexpr (tbi > 0 && s >= 1 && s < 6) {
        const u32 tbp = (w + tbi) & 3;
        *(LAS f32x4*)(lds + L_RED + ((tbp * 3 + (3 - tbi)) * 5 + (s - 1)) * 1024 + l16) = acc[(tbi - 1) & 1][s - 1];
      }
      if constexpr (q == 50) {
        WAITL0(); BAR();
#pragma unroll
        for (int sl = 0; sl < 3; ++sl)
#pragma unroll
          for (int nb = 0; nb < 5; ++nb) part[sl][nb] = *(const LAS f32x4*)(lds + L_RED + ((w * 3 + sl) * 5 + nb) * 1024 + l16);
      }
      SB();
      if constexpr (q + DH < 56) HLOAD(q + DH);
      if constexpr (q >= 44) {
        constexpr int j0 = nd + (nd < 8 ? nd : 8);
        if constexpr (j0 < 10) dma_q<0, 0, j0>(rs, l16, sQ, w, lds0); else dma_q<1, 1, j0 - 10>(rs, l16, sQ, w, lds0);
        if constexpr (nd < 8) {
          constexpr int j1 = j0 + 1;
          if constexpr (j1 < 10) dma_q<0, 0, j1>(rs, l16, sQ, w, lds0); else dma_q<1, 1, j1 - 10>(rs, l16, sQ, w, lds0);
        }
      }
      SB();
    });
    STAMP(18);
#pragma unroll
    for (int nb = 0; nb < 5; ++nb) {
      const f32x4 v = (acc[1][nb] + part[0][nb]) + (part[1][nb] + part[2][nb]);
      const u32 a = cvtpk(bflo(k.xr[nb].x) + v[0], bfhi(k.xr[nb].x) + v[1]), b = cvtpk(bflo(k.xr[nb].y) + v[2], bfhi(k.xr[nb].y) + v[3]);
      k.xr[nb] = u32x2{a, b};
    }
    const u32 token = 64 * m64 + 16 * w + t16;
    STAMP(19);
    if (it == 0) {
      float s1 = 0.f, s2 = 0.f;
      const u32 xo = ((token >> 5) * 40 + 5 * c8) * 1024 + ((token & 31) + 32 * (g >> 1)) * 16;
#pragma unroll
      for (int pr = 0; pr < 3; ++pr) {
        const int A = 2 * pr, Bn = pr < 2 ? 2 * pr + 1 : 4;
        auto r0 = __builtin_amdgcn_permlane16_swap(k.xr[A].x, k.xr[Bn].x, false, false);
        auto r1 = __builtin_amdgcn_permlane16_swap(k.xr[A].y, k.xr[Bn].y, false, false);
        const u32x4 v = {r0[0], r1[0], r0[1], r1[1]};
        if (pr < 2) stg_sc1(v, rs, xo + (A + (g & 1)) * 1024, OFF_XF + 2 * SZ_XF);
        else if ((g & 1) == 0) stg_sc1(v, rs, xo + 4 * 1024, OFF_XF + 2 * SZ_XF);
      }
#pragma unroll
      for (int nb = 0; nb < 5; ++nb) {
        const float a = bflo(k.xr[nb].x), b = bfhi(k.xr[nb].x), c = bflo(k.xr[nb].y), d = bfhi(k.xr[nb].y);
        s1 += (a + b) + (c + d); s2 += (a * a + b * b) + (c * c + d * d);
      }
      s1 = red16_32_add(s1); s2 = red16_32_add(s2);
      if (g == 0) stg2_sc1(u32x2{f2u(s1), f2u(s2)}, rs, (c8 * 2048 + token) * 8, OFF_ST + 2 * SZ_ST);
      SB();
      WAITV(0);
      WAITL0();
      BAR();
      if (tid == 0) __hip_atomic_store(flags + (FG_X2 + m64) * 32 + c8, k.tag, RLX_AGENT);
      sfor<10>([&](auto I) { dma_q<2, 2, decltype(I)::value>(rs, l16, sQ, w, lds0); });
    } else {
      const u32 oo = token * 1280 + (80 * c8 + 8 * (g >> 1)) * 2;
#pragma unroll
      for (int pr = 0; pr < 3; ++pr) {
        const int A = 2 * pr, Bn = pr < 2 ? 2 * pr + 1 : 4;
        auto r0 = __builtin_amdgcn_permlane16_swap(k.xr[A].x, k.xr[Bn].x, false, false);
        auto r1 = __builtin_amdgcn_permlane16_swap(k.xr[A].y, k.xr[Bn].y, false, false);
        const u32x4 v = {r0[0], r1[0], r0[1], r1[1]};
        if (pr < 2) __builtin_amdgcn_raw_buffer_store_b128(v, k.rso, oo + (A + (g & 1)) * 32, 0, 0);
        else if ((g & 1) == 0) __builtin_amdgcn_raw_buffer_store_b128(v, k.rso, oo + 4 * 32, 0, 0);
      }
    }
    STAMP(20);
  }
}

DI void stw_sc1(u32x4 v, rsrc_t rs, u32 off) { __builtin_amdgcn_raw_buffer_store_b128(v, rs, off, 0, AUX_SC1); }
DI float dot2ones(u32 v, float c) { return __builtin_amdgcn_fdot2_f32_bf16(__builtin_bit_cast(bf16x2, v), __builtin_bit_cast(bf16x2, 0x3f803f80u), c, false); }
DI void setup(K& k, int u, const u16* x0, const u16* wqkv, const u16* wo, const u16* wg, const u16* wu, const u16* wd) {
  const int l = k.lane, w = k.w;
  const rsrc_t rs = k.rs;
  LAS char* const lds = k.lds;
  const u32 lds0 = k.lds0;
  const int type = u < 60 ? 0 : u < 124 ? 3 : u < 178 ? 1 : u < 232 ? 2 : u < 252 ? 4 : 5;
  const int idx = u < 60 ? u : u < 124 ? u - 60 : u < 178 ? u - 124 : u < 232 ? u - 178 : u - 232;
  const u16* src = type == 0 ? wqkv : type == 1 ? wg : type == 2 ? wu : type == 3 ? x0 : wo;
  const u32 pitch = type == 0 ? 3840 : type <= 2 ? 3456 : 1280;
  const u32 nrec = type == 0 ? 640 * 3840 : type <= 2 ? 640 * 3456 : type == 3 ? 2048 * 1280 : type == 4 ? 640 * 1280 : 0;
  const rsrc_t rsrcA = __builtin_amdgcn_make_buffer_rsrc((void*)src, 0, nrec, 0x00020000);
  const rsrc_t rsrcD = __builtin_amdgcn_make_buffer_rsrc((void*)wd, 0, 1728 * 1280, 0x00020000);
  {
    const u32 voW = (16 * w + (l >> 2)) * pitch + idx * 64 + (l & 3) * 16;
    const u32 voO = ((idx & 1) * 320 + 8 * w + (l >> 3)) * 1280 + (idx >> 1) * 128 + (l & 7) * 16;
    const u32 vo = type == 4 ? voO : voW, step = type == 4 ? 32 * 1280 : 64 * pitch;
#pragma unroll
    for (int j = 0; j < 10; ++j) {
      const u32 cidx = 64 * (4 * j + w) + l, row = (cidx * 52429u) >> 22, cc = cidx - row * 80;
      const u32 vx = (32 * idx + row) * 1280 + ((cc ^ (row & 7)) * 16);
      const u32 v = type == 3 ? vx : vo + j * step;
      DMA_F("SA", 0, "", rsrcA, v, 0u, lds0 + (4 * j + w) * 1024);
    }
#pragma unroll
    for (int t = 0; t < 3; ++t) {
      const u32 d = u + 256 * t;
      const u32 v = d < 540 ? ((d / 10) * 32 + 8 * w + (l >> 3)) * 1280 + (d % 10) * 128 + (l & 7) * 16 : 0x7fffffffu;
      DMA_F("SB", 0, "", rsrcD, v, 0u, lds0 + 40960 + t * 4096 + w * 1024);
    }
  }
  u32 ep;
  asm volatile("global_load_dword %0, %1, off sc1" : "=v"(ep) : "v"(k.flags + FG_EPOCH * 32) : "memory");
  SSTAMP(21);
  WAITV(0); CERT("SA"); CERT("SB");
  asm volatile("" : "+v"(ep));
  BAR();
  SSTAMP(22);
  k.tag = __builtin_amdgcn_readfirstlane(ep * 2654435761u + 0x9E3779B9u);
  const int gam = l >> 4, q = (l & 15) >> 2, pp = l & 3;
#pragma unroll
  for (int t = 0; t < 3; ++t) {
    const u32 d = u + 256 * t;
    if (d < 540) {
      const u32 cg = d % 10, s_ = d / 10, c = 64 * cg + l, c8 = c / 80, nb = (c % 80) >> 4, n = c & 15;
      const u32 r0 = 16 * (w >> 1) + 4 * (w & 1) + q;
      const u32x2 a = __builtin_bit_cast(u32x2, __builtin_amdgcn_ds_read_tr16_b64_v4i16((LAS s16x4*)(lds + 40960 + t * 4096 + r0 * 128 + gam * 32 + pp * 8)));
      const u32x2 b2 = __builtin_bit_cast(u32x2, __builtin_amdgcn_ds_read_tr16_b64_v4i16((LAS s16x4*)(lds + 40960 + t * 4096 + (r0 + 8) * 128 + gam * 32 + pp * 8)));
      stw_sc1(u32x4{a.x, a.y, b2.x, b2.y}, rs, OFF_WD + (c8 * 54 + s_) * 5120 + nb * 1024 + n * 16 + w * 256);
    }
  }
  if (type <= 2) {
    const u32 ra = ((gam >> 1) * 8 + q) * 64 + (gam & 1) * 32 + pp * 8;
    u32x4 sl[10];
#pragma unroll
    for (int j = 0; j < 10; ++j) {
      const u32 o = ra + (4 * j + w) * 1024;
      const u32x2 a = __builtin_bit_cast(u32x2, __builtin_amdgcn_ds_read_tr16_b64_v4i16((LAS s16x4*)(lds + o)));
      const u32x2 b2 = __builtin_bit_cast(u32x2, __builtin_amdgcn_ds_read_tr16_b64_v4i16((LAS s16x4*)(lds + o + 256)));
      sl[j] = u32x4{a.x, a.y, b2.x, b2.y};
    }
    const int c = 32 * idx + (l & 31), hh = l >> 5;
    u32 dbase; int dstep;
    if (type == 0) {
      const int sec = c / 640, cc = c % 640, p = cc / 40, r40 = cc % 40;
      const int n = (sec == 1 ? 0 : sec == 2 ? 40 : 80) + r40;
      const int b = n >> 5, nl = n & 31;
      dbase = OFF_WQ + p * 163840 + (b < 2 ? b * 1024 : 81920 + (b - 2) * 40960) + nl * 16;
      dstep = b < 2 ? 2048 : 1024;
    } else {
      const int fb = c >> 4, nl = (c & 15) + 16 * (type - 1);
      const int half = fb / 54, rem = fb % 54;
      const int ix = rem < 42 ? rem % 7 : (rem - 42) % 6, B0 = 54 * half + rem - ix;
      const bool paired = ix < 6;
      dbase = OFF_WG + (paired ? (B0 + (ix & ~1)) * 40960 + (ix & 1) * 1024 : (B0 + 6) * 40960) + nl * 16;
      dstep = paired ? 2048 : 1024;
    }
    float c0 = 0.f, c1 = 0.f;
#pragma unroll
    for (int j = 0; j < 10; ++j) { c0 = dot2ones(sl[j][0], c0); c1 = dot2ones(sl[j][1], c1); c0 = dot2ones(sl[j][2], c0); c1 = dot2ones(sl[j][3], c1); }
    const float cs = red32_add(c0 + c1);
    LAS float* red = (LAS float*)(lds + 81920);
    if (l < 32) red[w * 32 + l] = cs;
    WAITL0(); BAR();
    SSTAMP(23);
    const float mean = ((red[l & 31] + red[32 + (l & 31)]) + (red[64 + (l & 31)] + red[96 + (l & 31)])) * (1.0f / 640.0f);
#pragma unroll
    for (int j = 0; j < 10; ++j) {
      u32x4 v;
#pragma unroll
      for (int e = 0; e < 4; ++e) v[e] = cvtpk(bflo(sl[j][e]) - mean, bfhi(sl[j][e]) - mean);
      stw_sc1(v, rs, dbase + (4 * j + w) * dstep + hh * 512);
    }
  } else if (type == 3) {
    const int tok = l & 31, h = l >> 5;
    u32x4 sl[10];
#pragma unroll
    for (int i = 0; i < 10; ++i) sl[i] = *(const LAS u32x4*)(lds + tok * 1280 + (((2 * (10 * w + i) + h) ^ (tok & 7)) * 16));
    float s1 = 0.f, s2 = 0.f;
#pragma unroll
    for (int i = 0; i < 10; ++i) {
      stw_sc1(sl[i], rs, OFF_XF + (idx * 40 + 10 * w + i) * 1024 + l * 16);
#pragma unroll
      for (int e = 0; e < 4; ++e) { s1 = dot2ones(sl[i][e], s1); const float a_ = bflo(sl[i][e]), b_ = bfhi(sl[i][e]); s2 += a_ * a_ + b_ * b_; }
    }
    s1 = red32_add(s1); s2 = red32_add(s2);
    if (l < 32) {
      stg2_sc1(u32x2{f2u(s1), f2u(s2)}, rs, (w * 2048 + 32 * idx + tok) * 8, OFF_ST);
      stg2_sc1(u32x2{0u, 0u}, rs, ((w + 4) * 2048 + 32 * idx + tok) * 8, OFF_ST);
    }
  } else if (type == 4) {
    const int cg = idx >> 1, kk = idx & 1, c = 64 * cg + l, c8 = c / 80, nb = (c % 80) >> 4, n = c & 15;
    const u32 dbase = OFF_WO + c8 * 20 * 5120 + nb * 1024 + n * 16;
#pragma unroll
    for (int j = 0; j < 10; ++j) {
      const int o = 4 * j + w, pl = o / 5, ii = o % 5, sg = 40 * kk + o, r0 = 40 * pl + 4 * ii + q;
      const u32x2 a = __builtin_bit_cast(u32x2, __builtin_amdgcn_ds_read_tr16_b64_v4i16((LAS s16x4*)(lds + r0 * 128 + gam * 32 + pp * 8)));
      const u32x2 b2 = __builtin_bit_cast(u32x2, __builtin_amdgcn_ds_read_tr16_b64_v4i16((LAS s16x4*)(lds + (r0 + 20) * 128 + gam * 32 + pp * 8)));
      stw_sc1(u32x4{a.x, a.y, b2.x, b2.y}, rs, dbase + (sg >> 2) * 5120 + (sg & 3) * 256);
    }
  }
  SSTAMP(24);
  WAITV(0); WAITL0();
  BAR();
  SSTAMP(25);
  if (k.tid < 8) __hip_atomic_store(k.flags + FW_PF + k.tid * 256 + u, k.tag, RLX_AGENT);
  if (w == 0) {
    const GAS u32* pf = k.flags + FW_PF + (u & 7) * 256 + l;
    __builtin_amdgcn_s_sleep(8);
    for (u32 spins = 0;;) {
      const u32 v0 = __hip_atomic_load(pf, RLX_AGENT), v1 = __hip_atomic_load(pf + 64, RLX_AGENT), v2 = __hip_atomic_load(pf + 128, RLX_AGENT), v3 = __hip_atomic_load(pf + 192, RLX_AGENT);
      if (__all(v0 == k.tag && v1 == k.tag && v2 == k.tag && v3 == k.tag)) break;
      __builtin_amdgcn_s_sleep(2);
      if (++spins > kSpinCap) { if (l == 0) __hip_atomic_store(k.flags + FG_TMO * 32, k.tag, RLX_AGENT); break; }
      if ((spins & 255u) == 0u && (u32)__builtin_amdgcn_readfirstlane(__hip_atomic_load(k.flags + FG_TMO * 32, RLX_AGENT)) == k.tag) break;
    }
  }
  BAR();
  SSTAMP(26);
}

__global__ __launch_bounds__(256, 1) void main_kernel(const u16* __restrict__ x0, const u16* __restrict__ wqkv, const u16* __restrict__ wo, const u16* __restrict__ wg,
                                                      const u16* __restrict__ wu, const u16* __restrict__ wd, char* __restrict__ ws, u16* __restrict__ out) {
  extern __shared__ __attribute__((aligned(16))) char smem[];
  K k;
  k.lds = (LAS char*)smem;
  k.lds0 = (u32)(size_t)k.lds;
  k.tid = threadIdx.x; k.lane = k.tid & 63; k.w = __builtin_amdgcn_readfirstlane(k.tid >> 6);
  k.rs = __builtin_amdgcn_make_buffer_rsrc(ws, 0, WS_USED, 0x00020000);
  k.rso = __builtin_amdgcn_make_buffer_rsrc(out, 0, 2048 * 640 * 2, 0x00020000);
  k.flags = (GAS u32*)ws;
  const int bid = blockIdx.x, xc = bid & 7, j = bid >> 3, mg = xc >> 1, half = xc & 1;
  k.m128 = 4 * mg + (j >> 3); k.p = 8 * half + (j & 7);
  k.m64 = 8 * mg + (j >> 2); k.c8 = 4 * half + (j & 3);
  const int p7 = j & 7;
  k.B0 = 54 * half + (p7 < 6 ? 7 * p7 : 42 + 6 * (p7 - 6));
  k.nblk = p7 < 6 ? 7 : 6;
  setup(k, bid, x0, wqkv, wo, wg, wu, wd);
  {
    PHASE_IDS();
    const u32 sQ = OFF_WQ + p * 163840;
    sfor<10>([&](auto I) { dma_q<0, 0, decltype(I)::value>(rs, l16, sQ, w, lds0); });
    sfor<10>([&](auto I) { dma_q<1, 1, decltype(I)::value>(rs, l16, sQ, w, lds0); });
    sfor<10>([&](auto I) { dma_q<2, 2, decltype(I)::value>(rs, l16, sQ, w, lds0); });
    const u32 token = 64 * m64 + 16 * w + t16;
    const u32 o = ((token >> 5) * 40 + 5 * c8) * 1024 + ((token & 31) + 32 * (g >> 1)) * 16 + (g & 1) * 8;
#pragma unroll
    for (int nb = 0; nb < 5; ++nb) ald2(k.xr[nb], rs, o, OFF_XF + nb * 1024);
  }
#pragma nounroll
  for (int it = 0; it < 2; ++it) { k.it = it; iteration(k); }
  if (bid == 0 && k.tid == 0) __hip_atomic_fetch_add(k.flags + FG_EPOCH * 32, 1u, RLX_AGENT);
}

extern "C" void kernel_launch(void* const* d_in, const int* in_sizes, int n_in, void* d_out, int out_size, void* d_ws, size_t ws_size,
                              hipStream_t stream) {
  const u16* x0 = (const u16*)d_in[0];
  const u16* wqkv = (const u16*)d_in[1];
  const u16* wo = (const u16*)d_in[2];
  const u16* wg = (const u16*)d_in[4];
  const u16* wu = (const u16*)d_in[5];
  const u16* wd = (const u16*)d_in[6];
  (void)hipFuncSetAttribute((const void*)main_kernel, hipFuncAttributeMaxDynamicSharedMemorySize, LDS_BYTES);
  main_kernel<<<256, 256, LDS_BYTES, stream>>>(x0, wqkv, wo, wg, wu, wd, (char*)d_ws, (u16*)d_out);
}
```

```cpp
#include <hip/hip_runtime.h>
#include <stdint.h>
#include <utility>

#define LAS __attribute__((address_space(3)))
#define GAS __attribute__((address_space(1)))
#define DI __device__ __forceinline__
typedef unsigned u32;
typedef unsigned short u16;
typedef u32 u32x4 __attribute__((ext_vector_type(4)));
typedef u32 u32x2 __attribute__((ext_vector_type(2)));
typedef float f32x2 __attribute__((ext_vector_type(2)));
typedef float f32x4 __attribute__((ext_vector_type(4)));
typedef float f32x16 __attribute__((ext_vector_type(16)));
typedef short s16x8 __attribute__((ext_vector_type(8)));
typedef short s16x4 __attribute__((ext_vector_type(4)));
typedef __bf16 bf16x2 __attribute__((ext_vector_type(2)));
typedef __amdgpu_buffer_rsrc_t rsrc_t;

constexpr u32 FLAG_BYTES = 32768;
constexpr u32 OFF_WQ = 65536;
constexpr u32 OFF_WO = OFF_WQ + 16 * 163840;
constexpr u32 OFF_WG = OFF_WO + 8 * 20 * 5 * 1024;
constexpr u32 OFF_WD = OFF_WG + 16 * 286720;
constexpr u32 OFF_CSQ = OFF_WD + 8 * 54 * 5 * 1024;
constexpr u32 OFF_CSG = OFF_CSQ + 16384;
constexpr u32 SZ_XF = 64 * 40 * 1024;
constexpr u32 OFF_XF = OFF_CSG + 28672;
constexpr u32 SZ_ST = 8 * 2048 * 8;
constexpr u32 OFF_ST = OFF_XF + 4 * SZ_XF;
constexpr u32 SZ_OF = 128 * 20 * 1024;
constexpr u32 OFF_OF = OFF_ST + 4 * SZ_ST;
constexpr u32 SZ_HF = 128 * 54 * 1024;
constexpr u32 OFF_HF = OFF_OF + 2 * SZ_OF;
constexpr u32 WS_USED = OFF_HF + 2 * SZ_HF;
constexpr u32 FG_O = 0, FG_X1 = 16, FG_H = 48, FG_X2 = 64, FG_PER_IT = 96;
constexpr u32 FG_TMO = 2 * FG_PER_IT;

constexpr u32 L_HALO = 122880;
constexpr u32 L_KST = 40960;
constexpr u32 L_VST = L_KST + 144 * 80;
constexpr u32 L_QST = L_VST + 144 * 80;
constexpr u32 L_RED = 100352;
constexpr u32 L_H4 = 143360;
constexpr u32 LDS_BYTES = 163840;

constexpr u32 AUX_SC1 = 16;
#define RLX_AGENT __ATOMIC_RELAXED, __HIP_MEMORY_SCOPE_AGENT
#define SB() __builtin_amdgcn_sched_barrier(0)
#define MEMFENCE() asm volatile("" ::: "memory")
#define WAITV(n) asm volatile("s_waitcnt vmcnt(%0)" ::"i"(n) : "memory")
#define WAITL0() asm volatile("s_waitcnt lgkmcnt(0)" ::: "memory")
#define BAR() do { MEMFENCE(); __builtin_amdgcn_s_barrier(); MEMFENCE(); } while (0)
#define CERT(tag) asm volatile("; CERT " tag ::: "memory")

template <int... I, class Fn> DI void sfor_impl(std::integer_sequence<int, I...>, Fn&& f) { (f(std::integral_constant<int, I>{}), ...); }
template <int N, class Fn> DI void sfor(Fn&& f) { sfor_impl(std::make_integer_sequence<int, N>{}, f); }
DI f32x16 mfma32(u32x4 a, u32x4 b, f32x16 c) { return __builtin_amdgcn_mfma_f32_32x32x16_bf16(__builtin_bit_cast(s16x8, a), __builtin_bit_cast(s16x8, b), c, 0, 0, 0); }
DI f32x4 mfma16(u32x4 a, u32x4 b, f32x4 c) { return __builtin_amdgcn_mfma_f32_16x16x32_bf16(__builtin_bit_cast(s16x8, a), __builtin_bit_cast(s16x8, b), c, 0, 0, 0); }
DI u32 cvtpk(float lo, float hi) { f32x2 v = {lo, hi}; bf16x2 b = __builtin_convertvector(v, bf16x2); return __builtin_bit_cast(u32, b); }
DI float u2f(u32 v) { return __builtin_bit_cast(float, v); }
DI u32 f2u(float v) { return __builtin_bit_cast(u32, v); }
DI float bflo(u32 v) { return __builtin_bit_cast(float, v << 16); }
DI float bfhi(u32 v) { return __builtin_bit_cast(float, v & 0xffff0000u); }
DI float fma_s(float a, float b, float c) { float r; asm("v_fma_f32 %0, %1, %2, %3" : "=v"(r) : "v"(a), "v"(b), "v"(c)); return r; }
DI float mul_s(float a, float b) { float r; asm("v_mul_f32_e32 %0, %1, %2" : "=v"(r) : "v"(a), "v"(b)); return r; }
DI float add_s(float a, float b) { float r; asm("v_add_f32_e32 %0, %1, %2" : "=v"(r) : "v"(a), "v"(b)); return r; }
DI float max_s(float a, float b) { float r; asm("v_max_f32_e32 %0, %1, %2" : "=v"(r) : "v"(a), "v"(b)); return r; }
DI float sel_s(float f, float t, unsigned long long m) { float r; asm("v_cndmask_b32_e64 %0, %1, %2, %3" : "=v"(r) : "v"(f), "v"(t), "s"(m)); return r; }
DI float red16_32_add(float v) {
  auto a = __builtin_amdgcn_permlane16_swap(f2u(v), f2u(v), false, false);
  v = u2f(a[0]) + u2f(a[1]);
  auto b = __builtin_amdgcn_permlane32_swap(f2u(v), f2u(v), false, false);
  return u2f(b[0]) + u2f(b[1]);
}
DI float red16_32_max(float v) {
  auto a = __builtin_amdgcn_permlane16_swap(f2u(v), f2u(v), false, false);
  v = __builtin_fmaxf(u2f(a[0]), u2f(a[1]));
  auto b = __builtin_amdgcn_permlane32_swap(f2u(v), f2u(v), false, false);
  return __builtin_fmaxf(u2f(b[0]), u2f(b[1]));
}
DI float red32_add(float v) {
  auto b = __builtin_amdgcn_permlane32_swap(f2u(v), f2u(v), false, false);
  return u2f(b[0]) + u2f(b[1]);
}
DI void stg_sc1(u32x4 v, rsrc_t rs, u32 voff, u32 soff) { __builtin_amdgcn_raw_buffer_store_b128(v, rs, voff, soff, AUX_SC1); }
DI void stg2_sc1(u32x2 v, rsrc_t rs, u32 voff, u32 soff) { __builtin_amdgcn_raw_buffer_store_b64(v, rs, voff, soff, AUX_SC1); }

template <int IMM> DI void ald_v_sc1(u32x4& d, rsrc_t rs, u32 voff, u32 soff) {
  asm volatile("buffer_load_dwordx4 %0, %1, %2, %3 offen offset:%4 sc1" : "=v"(d) : "v"(voff), "s"(rs), "s"(soff), "i"(IMM) : "memory");
}
template <int IMM> DI void ald_a(u32x4& d, rsrc_t rs, u32 voff, u32 soff) {
  asm volatile("buffer_load_dwordx4 %0, %1, %2, %3 offen offset:%4" : "=a"(d) : "v"(voff), "s"(rs), "s"(soff), "i"(IMM) : "memory");
}
template <int IMM> DI void ald_v(u32x4& d, rsrc_t rs, u32 voff, u32 soff) {
  asm volatile("buffer_load_dwordx4 %0, %1, %2, %3 offen offset:%4" : "=v"(d) : "v"(voff), "s"(rs), "s"(soff), "i"(IMM) : "memory");
}
DI void ald2_sc1(u32x2& d, rsrc_t rs, u32 voff, u32 soff) {
  asm volatile("buffer_load_dwordx2 %0, %1, %2, %3 offen sc1" : "=v"(d) : "v"(voff), "s"(rs), "s"(soff) : "memory");
}
DI void ald2(u32x2& d, rsrc_t rs, u32 voff, u32 soff) {
  asm volatile("buffer_load_dwordx2 %0, %1, %2, %3 offen" : "=v"(d) : "v"(voff), "s"(rs), "s"(soff) : "memory");
}
DI void ald1(u32& d, rsrc_t rs, u32 voff, u32 soff) {
  asm volatile("buffer_load_dword %0, %1, %2, %3 offen" : "=v"(d) : "v"(voff), "s"(rs), "s"(soff) : "memory");
}
template <int IMM> DI void ald_a_sc1(u32x4& d, rsrc_t rs, u32 voff, u32 soff) {
  asm volatile("buffer_load_dwordx4 %0, %1, %2, %3 offen offset:%4 sc1" : "=a"(d) : "v"(voff), "s"(rs), "s"(soff), "i"(IMM) : "memory");
}
template <int N> DI void waitpin_a(u32x4& r) { asm volatile("s_waitcnt vmcnt(%1)" : "+a"(r) : "i"(N) : "memory"); }
template <int N> DI void waitpin_a2(u32x4& r0, u32x4& r1) { asm volatile("s_waitcnt vmcnt(%2)" : "+a"(r0), "+a"(r1) : "i"(N) : "memory"); }
DI void mma32(f32x16& acc, const u32x4& wv, const u32x4& xa) { asm volatile("v_mfma_f32_32x32x16_bf16 %0, %1, %2, %0" : "+v"(acc) : "v"(wv), "a"(xa)); }
DI void mma32_0(f32x16& acc, const u32x4& wv, const u32x4& xa) { asm volatile("v_mfma_f32_32x32x16_bf16 %0, %1, %2, 0" : "=&v"(acc) : "v"(wv), "a"(xa)); }
DI void mma32w(f32x16& acc, const u32x4& wv, const u32x4& xa, const u32x4& wn) { asm volatile("v_mfma_f32_32x32x16_bf16 %0, %1, %2, %0" : "+v"(acc) : "v"(wv), "a"(xa), "v"(wn)); }
DI void mma32w_0(f32x16& acc, const u32x4& wv, const u32x4& xa, const u32x4& wn) { asm volatile("v_mfma_f32_32x32x16_bf16 %0, %1, %2, 0" : "=&v"(acc) : "v"(wv), "a"(xa), "v"(wn)); }
DI void mma16(f32x4& acc, const u32x4& av, const u32x4& bv) { asm volatile("v_mfma_f32_16x16x32_bf16 %0, %1, %2, %0" : "+v"(acc) : "v"(av), "v"(bv)); }
DI void mma16_0(f32x4& acc, const u32x4& av, const u32x4& bv) { asm volatile("v_mfma_f32_16x16x32_bf16 %0, %1, %2, 0" : "=&v"(acc) : "v"(av), "v"(bv)); }
DI void pin_vv(u32x4& a, u32x4& b) { asm volatile("" : "+v"(a), "+v"(b)); }
template <int N> DI void waitpin_v(u32x4& r) { asm volatile("s_waitcnt vmcnt(%1)" : "+v"(r) : "i"(N) : "memory"); }
DI void pin_v(u32x4& r) { asm volatile("" : "+v"(r)::"memory"); }
DI void pin_a(u32x4& r) { asm volatile("" : "+a"(r)::"memory"); }
DI void pin2(u32x2& r) { asm volatile("" : "+v"(r)::"memory"); }
DI void pin1(u32& r) { asm volatile("" : "+v"(r)::"memory"); }
#define DMA_F(TAG, IMM, SC, rs, voff, soff, m0v) \
  asm volatile("s_mov_b32 m0, %2\n\ts_nop 0\n\tbuffer_load_dwordx4 %0, %1, %3 offen offset:%4 " SC "lds ; DMA " TAG ::"v"(voff), "s"(rs), "s"(m0v), "s"(soff), "i"(IMM) : "memory")
#define DMA_N(TAG, IMM, SC, rs, voff, soff) \
  asm volatile("buffer_load_dwordx4 %0, %1, %2 offen offset:%3 " SC "lds ; DMA " TAG ::"v"(voff), "s"(rs), "s"(soff), "i"(IMM) : "memory")

constexpr int PREP_UNITS = 84 + 20 + 60 + 64;
__global__ __launch_bounds__(512) void prep_kernel(const u16* __restrict__ x0, const u16* __restrict__ wqkv, const u16* __restrict__ wo,
                                                   const u16* __restrict__ wg, const u16* __restrict__ wu, const u16* __restrict__ wd, char* __restrict__ ws) {
  __shared__ __attribute__((aligned(16))) char lds[81920 + 1024];
  const int tid = threadIdx.x, l = tid & 63, w = __builtin_amdgcn_readfirstlane(tid >> 6);
  const int u = blockIdx.x;
  if (u == PREP_UNITS - 1) {
    for (int i = tid; i < (int)(FLAG_BYTES / 16); i += 512) ((u32x4*)ws)[i] = u32x4{0, 0, 0, 0};
  }
  const int gam = l >> 4, q = (l & 15) >> 2, pp = l & 3;
  if (u < 84) {
    const int type = u < 30 ? 0 : u < 57 ? 1 : 2, cg = u < 30 ? u : u < 57 ? u - 30 : u - 57;
    const u16* src = type == 0 ? wqkv : type == 1 ? wg : wu;
    const int pitch = type == 0 ? 1920 : 1728;
#pragma unroll
    for (int i = 0; i < 10; ++i) {
      const int r = 8 * i + w;
      const u16* g = src + (size_t)(8 * r + (l >> 3)) * pitch + 64 * cg + (l & 7) * 8;
      __builtin_amdgcn_global_load_lds((const GAS u32*)g, (LAS u32*)(lds + r * 1024), 16, 0, 0);
    }
    const int c = 64 * cg + l;
    u32 dbase; int dstep;
    if (type == 0) {
      const int sec = c / 640, cc = c % 640, p = cc / 40, r40 = cc % 40;
      const int n = (sec == 1 ? 0 : sec == 2 ? 40 : 80) + r40;
      const int b = n >> 5, nl = n & 31;
      dbase = OFF_WQ + p * 163840 + (b < 3 ? b * 1024 : 122880) + nl * 16;
      dstep = b < 3 ? 3072 : 1024;
    } else {
      const int fb = c >> 4, nl = (c & 15) + 16 * (type - 1);
      const int half = fb / 54, rem = fb % 54;
      const int sl_ = 8 * half + (rem < 42 ? rem / 7 : 6 + (rem - 42) / 6), ix = rem < 42 ? rem % 7 : (rem - 42) % 6;
      dbase = OFF_WG + sl_ * 286720 + (ix < 3 ? ix * 1024 : ix < 5 ? 122880 + (ix - 3) * 1024 : 204800 + (ix - 5) * 1024) + nl * 16;
      dstep = ix < 3 ? 3072 : 2048;
    }
    __syncthreads();
    u32x4 sl[10];
    float csum = 0.f;
#pragma unroll
    for (int i = 0; i < 10; ++i) {
      const int o = 8 * i + w;
      const u32x2 a = __builtin_bit_cast(u32x2, __builtin_amdgcn_ds_read_tr16_b64_v4i16((LAS s16x4*)(lds + (8 * o + q) * 128 + gam * 32 + pp * 8)));
      const u32x2 b2 = __builtin_bit_cast(u32x2, __builtin_amdgcn_ds_read_tr16_b64_v4i16((LAS s16x4*)(lds + (8 * o + 4 + q) * 128 + gam * 32 + pp * 8)));
      sl[i] = u32x4{a.x, a.y, b2.x, b2.y};
    }
#pragma unroll
    for (int i = 0; i < 10; ++i)
#pragma unroll
      for (int j = 0; j < 4; ++j) csum += bflo(sl[i][j]) + bfhi(sl[i][j]);
    __syncthreads();
    float* red = (float*)lds;
    red[w * 64 + l] = csum;
    __syncthreads();
    const float mean = (((red[l] + red[64 + l]) + (red[128 + l] + red[192 + l])) + ((red[256 + l] + red[320 + l]) + (red[384 + l] + red[448 + l]))) * (1.0f / 640.0f);
#pragma unroll
    for (int i = 0; i < 10; ++i) {
      const int o = 8 * i + w;
      u32x4 v;
#pragma unroll
      for (int j = 0; j < 4; ++j) v[j] = cvtpk(bflo(sl[i][j]) - mean, bfhi(sl[i][j]) - mean);
      *(u32x4*)(ws + dbase + (o >> 1) * dstep + (o & 1) * 512) = v;
    }
  } else if (u < 164) {
    const bool iswo = u < 104;
    const int v = iswo ? u - 84 : u - 104, cg = iswo ? v >> 1 : v / 6, kk = iswo ? v & 1 : v % 6;
    const u16* src = iswo ? wo : wd;
    const int nrows = iswo ? 320 : 288, row0 = kk * nrows, noct = nrows / 8;
#pragma unroll
    for (int i = 0; i < 5; ++i) {
      const int r = 8 * i + w;
      if (r < noct) {
        const u16* g = src + (size_t)(row0 + 8 * r + (l >> 3)) * 640 + 64 * cg + (l & 7) * 8;
        __builtin_amdgcn_global_load_lds((const GAS u32*)g, (LAS u32*)(lds + r * 1024), 16, 0, 0);
      }
    }
    const int c = 64 * cg + l, c8 = c / 80, nb = (c % 80) >> 4, n = c & 15;
    const u32 dbase = (iswo ? OFF_WO + c8 * 20 * 5120 : OFF_WD + c8 * 54 * 5120) + nb * 1024 + n * 16;
    __syncthreads();
#pragma unroll
    for (int i = 0; i < 5; ++i) {
      const int o = 8 * i + w;
      if (o < noct) {
        int r0, r1; u32 dst;
        if (iswo) {
          const int pl = o / 5, ii = o % 5, sg = 40 * kk + o;
          r0 = 40 * pl + 4 * ii + q; r1 = r0 + 20;
          dst = dbase + (sg >> 2) * 5120 + (sg & 3) * 256;
        } else {
          const int s_ = o >> 2, g = o & 3;
          r0 = 32 * s_ + 16 * (g >> 1) + 4 * (g & 1) + q; r1 = r0 + 8;
          dst = dbase + (9 * kk + s_) * 5120 + g * 256;
        }
        const u32x2 a = __builtin_bit_cast(u32x2, __builtin_amdgcn_ds_read_tr16_b64_v4i16((LAS s16x4*)(lds + r0 * 128 + gam * 32 + pp * 8)));
        const u32x2 b2 = __builtin_bit_cast(u32x2, __builtin_amdgcn_ds_read_tr16_b64_v4i16((LAS s16x4*)(lds + r1 * 128 + gam * 32 + pp * 8)));
        *(u32x4*)(ws + dst) = u32x4{a.x, a.y, b2.x, b2.y};
      }
    }
  } else {
    const int rb = u - 164;
#pragma unroll
    for (int i = 0; i < 5; ++i) {
      const int cidx = 64 * (8 * i + w) + l, row = cidx / 80, cc = cidx % 80;
      const u16* g = x0 + (size_t)(32 * rb + row) * 640 + ((cc ^ (row & 7)) * 8);
      __builtin_amdgcn_global_load_lds((const GAS u32*)g, (LAS u32*)(lds + (8 * i + w) * 1024), 16, 0, 0);
    }
    __syncthreads();
    const int tok = l & 31, h = l >> 5;
    float s1 = 0.f, s2 = 0.f;
#pragma unroll
    for (int i = 0; i < 5; ++i) {
      const int s = 5 * w + i, cc = 2 * s + h;
      const u32x4 v = *(const u32x4*)(lds + tok * 1280 + ((cc ^ (tok & 7)) * 16));
      *(u32x4*)(ws + OFF_XF + (rb * 40 + s) * 1024 + l * 16) = v;
#pragma unroll
      for (int j = 0; j < 4; ++j) { float a = bflo(v[j]), b = bfhi(v[j]); s1 += a + b; s2 += a * a + b * b; }
    }
    s1 = red32_add(s1); s2 = red32_add(s2);
    if (l < 32) {
      f32x2* st = (f32x2*)(ws + OFF_ST);
      st[w * 2048 + 32 * rb + tok] = f32x2{s1, s2};
    }
  }
}

#define STAMP(i) do {} while (0)
#define RSTAMP(i) do {} while (0)
#define GSTAMP(i) STAMP(i)
#define QSTAMP(i) do {} while (0)
constexpr u32 OFF_STAMP = WS_USED + 4096;
constexpr u32 kSpinCap = 1u << 16;

struct K {
  rsrc_t rs, rso;
  LAS char* lds;
  u32 lds0;
  GAS u32* flags;
  int lane, w, m128, p, m64, c8, B0, nblk, tid, it;
  u32x2 xr[5];
  u32x4 wo_r[5][5];
  u32x4 wd_r[14][5];
};

#define PHASE_IDS() int lane = k.lane; asm volatile("" : "+v"(lane)); \
  int it = k.it; asm volatile("" : "+s"(it)); \
  const u32 xin = OFF_XF + it * 2 * SZ_XF, stin = OFF_ST + it * 2 * SZ_ST, x1o = xin + SZ_XF, st1o = stin + SZ_ST; \
  const u32 ofo = OFF_OF + it * SZ_OF, hfo = OFF_HF + it * SZ_HF, fgb = it * FG_PER_IT; \
  (void)xin; (void)stin; (void)x1o; (void)st1o; (void)ofo; (void)hfo; (void)fgb; \
  int w = k.w, m128 = k.m128, p = k.p, m64 = k.m64, c8 = k.c8, B0 = k.B0; \
  asm volatile("" : "+s"(w), "+s"(m128), "+s"(p), "+s"(m64), "+s"(c8), "+s"(B0)); \
  const u32 l16 = lane * 16; const int tok = lane & 31, h = lane >> 5, t16 = lane & 15, g = lane >> 4; \
  const rsrc_t rs = k.rs; LAS char* const lds = k.lds; const u32 lds0 = k.lds0; \
  (void)tok; (void)h; (void)t16; (void)g; (void)l16; (void)m128; (void)p; (void)m64; (void)c8; (void)B0; (void)lds; (void)lds0; (void)rs

DI void poll_flags(GAS u32* flags, u32 fo, bool act, int lane) {
  for (u32 spins = 0;;) {
    u32 v = act ? __hip_atomic_load(flags + fo, RLX_AGENT) : 1u;
    if (__all(v == 1u)) break;
    __builtin_amdgcn_s_sleep(1);
    if (++spins > kSpinCap) {
      if (lane == 0) __hip_atomic_store(flags + FG_TMO * 32, 1u, RLX_AGENT);
      break;
    }
    if ((spins & 255u) == 0u && __builtin_amdgcn_readfirstlane(__hip_atomic_load(flags + FG_TMO * 32, RLX_AGENT)) != 0u) break;
  }
}
DI void stats_finish(float s1, float s2, float& nmean, float& rstd) {
  const float mean = s1 * (1.0f / 640.0f);
  const float var = __builtin_fmaxf(s2 * (1.0f / 640.0f) - mean * mean, 0.f);
  nmean = -mean; rstd = __builtin_amdgcn_rsqf(var + 1e-5f);
}

template <int C, int SLOT, int I> DI void dma_q(rsrc_t rs, u32 l16, u32 sQ, int w, u32 lds0) {
#define Q_EMIT(TAG, FIRST, IMM, SO, MO) do { if constexpr (FIRST) DMA_F(TAG, IMM, "", rs, l16, SO, MO); else DMA_N(TAG, IMM, "", rs, l16, SO); } while (0)
  if constexpr (C < 3) {
    constexpr int grp = I / 4, imm = (I % 4) * 1024;
    const u32 so = sQ + w * 10240 + C * 40960 + grp * 4096, mo = lds0 + SLOT * 40960 + w * 10240 + grp * 4096;
    if constexpr (C == 0) Q_EMIT("Q0", I % 4 == 0, imm, so, mo); else if constexpr (C == 1) Q_EMIT("Q1", I % 4 == 0, imm, so, mo); else Q_EMIT("Q2", I % 4 == 0, imm, so, mo);
  } else {
    constexpr int hf = I / 5, ii = I % 5, grp = ii / 4, imm = (ii % 4) * 1024;
    const u32 so = sQ + w * 5120 + 122880 + hf * 20480 + grp * 4096, mo = lds0 + SLOT * 40960 + w * 5120 + hf * 20480 + grp * 4096;
    if constexpr (hf == 0) Q_EMIT("Q3a", ii % 4 == 0, imm, so, mo); else Q_EMIT("Q3b", ii % 4 == 0, imm, so, mo);
  }
#undef Q_EMIT
}
template <int HH, int I> DI void dma_g(rsrc_t rs, u32 l16, u32 sG, int w, u32 lds0) {
  constexpr int grp = I / 4, imm = (I % 4) * 1024;
  const u32 so = sG + HH * 20480 + grp * 4096, mo = lds0 + w * 5120 + (HH % 7) * 20480 + grp * 4096;
#define G_CASE(n) if constexpr (HH == n) { if constexpr (I % 4 == 0) DMA_F("G" #n, imm, "", rs, l16, so, mo); else DMA_N("G" #n, imm, "", rs, l16, so); }
  G_CASE(0) G_CASE(1) G_CASE(2) G_CASE(3) G_CASE(4) G_CASE(5) G_CASE(6) G_CASE(7) G_CASE(8) G_CASE(9) G_CASE(10) G_CASE(11) G_CASE(12) G_CASE(13)
#undef G_CASE
}

constexpr int XW = 10;
struct QS {
  static constexpr bool x_at(int f) { return f < 120 && f % 3 == 0 && f / 3 + XW < 40; }
  static constexpr int aux_at(int f) { return (f < 32 && f % 3 == 1) ? f / 3 : -1; }
  static constexpr int q3_at(int f) { return (f >= 77 && f <= 104 && f % 3 == 2) ? (f - 77) / 3 : -1; }
  static constexpr int wo_at(int f) {
    if (f >= 34 && f <= 67 && f % 3 == 1) return (f - 34) / 3;
    if (f >= 121 && f <= 157 && (f - 121) % 3 == 0) return 12 + (f - 121) / 3;
    return -1;
  }
  static constexpr int upto(int F) { int n = 0; for (int f = 0; f <= F; ++f) n += x_at(f) + (aux_at(f) >= 0) + (q3_at(f) >= 0) + (wo_at(f) >= 0); return n; }
  static constexpr int xidx(int i) { return i < XW ? i : XW + upto(3 * (i - XW)) - 1; }
  static constexpr int xwait2(int s) { return XW + upto(3 * s) - 1 - xidx(s + 1); }
};
struct GS {
  static constexpr int NWD = 65;
  static constexpr bool x_at(int f) { return f < 120 && f % 3 == 0 && f / 3 + XW < 40; }
  static constexpr int op(int F) {
    if (F <= 10 && F % 3 == 1) return 3000 + F / 3;
    if (F >= 40 && F <= 67 && F % 3 == 1) return (7 + (F - 40) / 15) * 8 + ((F - 40) % 15) / 3;
    if (F >= 79 && F <= 106 && F % 3 == 1) return (9 + (F - 79) / 15) * 8 + ((F - 79) % 15) / 3;
    if (F < 120 || (F & 1)) return -1;
    if (F < 140) return (11 + (F - 120) / 10) * 8 + ((F - 120) % 10) / 2;
    if (F < 160) return 1000 + (F - 140) / 2;
    if (F < 170) return 13 * 8 + (F - 160) / 2;
    if (F < 280) return 1010 + (F - 170) / 2;
    return -1;
  }
  static constexpr bool st_at(int F) { return F == 139 || F == 157 || F == 175 || F == 219 || F == 237; }
  static constexpr int upto(int F) { int n = 0; for (int f = 0; f <= F; ++f) n += x_at(f) + (op(f) >= 0) + st_at(f); return n; }
  static constexpr int slot(int o) { for (int f = 0; f < 300; ++f) if (op(f) == o) return f; return -1; }
  static constexpr int xidx(int i) { return i < XW ? i : XW + upto(3 * (i - XW)) - 1; }
  static constexpr int xwait2(int s) { return XW + upto(3 * s) - 1 - xidx(s + 1); }
};
static_assert(GS::op(278) == 1000 + GS::NWD - 1, "down-weight prefetch schedule");

DI void iteration(K& k) {
  GAS u32* const flags = k.flags;
  const int tid = k.tid;
  {
    PHASE_IDS();
    const u32 sQ = OFF_WQ + p * 163840, sG = OFF_WG + p * 286720 + w * 5120;
    STAMP(0);
    if (it > 0 && w == 0) {
      const int grp = 2 * m128 - 1 + (lane >> 3);
      const bool act = lane < 24 && grp >= 0;
      poll_flags(flags, (FG_X2 + grp) * 32 + (lane & 7), act, lane);
    }
    WAITV(0); CERT("Q0"); CERT("Q1"); CERT("Q2");
    BAR();
    STAMP(1);
    u32x4 xf[40];
    const u32 xbase = xin + (4 * m128 + w) * 40960;
#define XLOAD(i) ald_a_sc1<((i) % 4) * 1024>(xf[i], rs, l16, xbase + ((i) / 4) * 4096)
    u32x2 hs0, hs1, sa0, sa1, sa2, sa3;
    const int rbh = m128 > 0 ? 4 * m128 - 1 : 0;
    const u32 hvo = (g >> 1) * 1024 + (g & 1) * 512 + (16 + t16) * 16;
    const int ht = 128 * m128 - 16 + t16;
    const u32 hso = ((2 * g) * 2048 + (ht < 0 ? 0 : ht)) * 8, sso = (4 * h * 2048 + 128 * m128 + 32 * w + tok) * 8;
    auto aux_load = [&](auto AC) {
      constexpr int a = decltype(AC)::value;
      if constexpr (a < 5) DMA_F("HALO", 0, "sc1 ", rs, hvo, xin + rbh * 40960 + (5 * w + a) * 2048, lds0 + L_HALO + (5 * w + a) * 1024);
      if constexpr (a == 5) ald2_sc1(sa0, rs, sso, stin);
      if constexpr (a == 6) ald2_sc1(sa1, rs, sso, stin + 16384);
      if constexpr (a == 7) ald2_sc1(sa2, rs, sso, stin + 32768);
      if constexpr (a == 8) ald2_sc1(sa3, rs, sso, stin + 49152);
      if constexpr (a == 9) ald2_sc1(hs0, rs, hso, stin);
      if constexpr (a == 10) ald2_sc1(hs1, rs, hso, stin + 16384);
    };
    sfor<XW>([&](auto I) { constexpr int i = decltype(I)::value; XLOAD(i); });
    using Q = QS;
    constexpr int DQ = 4, NBQ = 6;
    u32x4 wb[NBQ];
    f32x16 acc0, acc1, acc2, acc3;
    f32x4 hacc, hacc4;
    u32x4 hw[3], hx[3], h4w[3], h4x[3];
    float rstd = 0.f, hrs = 0.f;
    u32 lb1 = l16 + 65536, lbh = l16 + L_HALO; asm volatile("" : "+v"(lb1), "+v"(lbh));
    u32 hwlo = (w >> 1) * 1024 + (g >> 1) * 3072 + (g & 1) * 512 + (16 * (w & 1) + t16) * 16, hwhi = hwlo + 61440;
    u32 hw4 = 2048 + (g >> 1) * 3072 + (g & 1) * 512 + t16 * 16 + w * 30720, hx4 = l16 + L_HALO + w * 5120;
    asm volatile("" : "+v"(hwlo), "+v"(hwhi), "+v"(hw4), "+v"(hx4));
    const u32 kvrow = L_KST + (16 + 32 * w + tok) * 80 + h * 8;
    const int hn = 16 * w + 4 * g;
    const u32 haddr = L_KST + t16 * 80 + 2 * hn + (hn >= 40 ? (L_VST - L_KST - 80) : 0);
    const u32 haddr4 = L_VST + t16 * 80 + 2 * (64 + 4 * g - 40);
    const bool hzero = (m128 == 0);
    const u32 wo_o = OFF_WO + (c8 * 20 + 5 * w) * 5120;
#define LDSQ(F) (((F) / 40 % 3) * 40960 + ((F) % 40) * 1024)
#define RDW(off) (*(const LAS u32x4*)(lds + ((off) < 65536 ? l16 : lb1) + ((off) & 65535)))
    u32 gp0 = 0;
#define STG_OFF(n0) ((n0) < 40 ? 2 * (n0) : (n0) < 80 ? (L_VST - L_KST) + 2 * ((n0) - 40) : (L_QST - L_KST - 16 * 80) + 2 * ((n0) - 80))
#define GRAN(t, ACC, q, RS, STADDR, ZERO) do { \
      if constexpr ((t) == 0) gp0 = cvtpk(mul_s(ACC[4 * (q)], RS), mul_s(ACC[4 * (q) + 1], RS)); \
      if constexpr ((t) == 1) { u32x2 pk_ = {gp0, cvtpk(mul_s(ACC[4 * (q) + 2], RS), mul_s(ACC[4 * (q) + 3], RS))}; if (ZERO) pk_ = u32x2{0u, 0u}; *(LAS u32x2*)(lds + (STADDR)) = pk_; } \
    } while (0)
#define GRAN_MAIN(t, ACC, q, n0) GRAN(t, ACC, q, rstd, kvrow + STG_OFF(n0), false)
#pragma unroll
    for (int F = 0; F < DQ; ++F) wb[F % NBQ] = RDW(LDSQ(F));
    sfor<160>([&](auto FC) {
      constexpr int F = decltype(FC)::value, Fr = F + DQ;
      if constexpr (F == 30) QSTAMP(21);
      if constexpr (F == 60) QSTAMP(22);
      if constexpr (F == 90) QSTAMP(23);
      if constexpr (F == 140) QSTAMP(26);
      if constexpr (Q::x_at(F)) XLOAD(F / 3 + XW);
      if constexpr (Q::aux_at(F) >= 0) aux_load(std::integral_constant<int, Q::aux_at(F)>{});
      if constexpr (Q::q3_at(F) >= 0) dma_q<3, 0, Q::q3_at(F)>(rs, l16, sQ, w, lds0);
      if constexpr (Q::wo_at(F) >= 0) {
        constexpr int i = Q::wo_at(F), s = i / 5, nb = i % 5;
        if constexpr (i < 24) ald_a<(i % 4) * 1024>(k.wo_r[s][nb], rs, l16, wo_o + (i / 4) * 4096); else ald_v<(i % 4) * 1024>(k.wo_r[s][nb], rs, l16, wo_o + (i / 4) * 4096);
      }
      if constexpr (Fr == 40) { WAITV(Q::upto(F) - Q::upto(13)); CERT("HALO"); BAR(); }
      if constexpr (Fr == 80) { WAITL0(); BAR(); }
      if constexpr (Fr == 120) { WAITV(Q::upto(F) - Q::upto(89)); CERT("Q3a"); WAITL0(); BAR(); STAMP(2); }
      if constexpr (Fr == 140) { WAITV(Q::upto(F) - Q::upto(104)); CERT("Q3b"); BAR(); }
      if constexpr (Fr < 160) wb[Fr % NBQ] = RDW(LDSQ(Fr));
      if constexpr (F < 120) {
        constexpr int s = F / 3, b = F % 3;
        if constexpr (b == 0 && (s & 1) == 0) waitpin_a2<Q::xwait2(s)>(xf[s], xf[s + 1]);
#define QMMA(ACC) do { if constexpr (s == 0) { if constexpr ((F & 1) == 0) mma32w_0(ACC, wb[F % NBQ], xf[s], wb[(F + 1) % NBQ]); else mma32_0(ACC, wb[F % NBQ], xf[s]); } \
                       else { if constexpr ((F & 1) == 0) mma32w(ACC, wb[F % NBQ], xf[s], wb[(F + 1) % NBQ]); else mma32(ACC, wb[F % NBQ], xf[s]); } } while (0)
        if constexpr (b == 0) QMMA(acc0); else if constexpr (b == 1) QMMA(acc1); else QMMA(acc2);
        if constexpr (F == 62) {
          pin2(sa0); pin2(sa1); pin2(sa2); pin2(sa3);
          float nm;
          stats_finish(red32_add((u2f(sa0.x) + u2f(sa1.x)) + (u2f(sa2.x) + u2f(sa3.x))), red32_add((u2f(sa0.y) + u2f(sa1.y)) + (u2f(sa2.y) + u2f(sa3.y))), nm, rstd);
        }
        if constexpr (F == 110) {
          pin2(hs0); pin2(hs1);
          float nm;
          stats_finish(red16_32_add(u2f(hs0.x) + u2f(hs1.x)), red16_32_add(u2f(hs0.y) + u2f(hs1.y)), nm, hrs);
        }
        if constexpr (F == 113) {
#pragma unroll
          for (int nb = 0; nb < 5; ++nb) pin2(k.xr[nb]);
        }
        if constexpr (b == 1) {
          if constexpr (s == 15) mma16_0(hacc, hw[0], hx[0]);
          if constexpr (s > 15 && s <= 34) mma16(hacc, hw[(s - 15) % 3], hx[(s - 15) % 3]);
          if constexpr (s >= 13 && s <= 32) {
            constexpr int sg = s - 13;
            hw[sg % 3] = *(const LAS u32x4*)(lds + (sg < 10 ? hwlo : hwhi) + (sg % 10) * 6144);
            hx[sg % 3] = *(const LAS u32x4*)(lds + lbh + sg * 1024);
          }
        }
      } else {
        constexpr int s = F - 120;
        if constexpr (s == 0) mma32w_0(acc3, wb[F % NBQ], xf[s], wb[(F + 1) % NBQ]); else if constexpr (s & 1) mma32(acc3, wb[F % NBQ], xf[s]); else mma32w(acc3, wb[F % NBQ], xf[s], wb[(F + 1) % NBQ]);
        constexpr int ms = s - 2, gi = ms / 2, st = ms % 2;
        if constexpr (ms >= 0 && gi < 12) {
          constexpr int b = gi >> 2, q = gi & 3, n0 = 32 * b + 8 * q;
          if constexpr (b == 0) GRAN_MAIN(st, acc0, q, n0); else if constexpr (b == 1) GRAN_MAIN(st, acc1, q, n0); else GRAN_MAIN(st, acc2, q, n0);
        }
        if constexpr (s == 22) {
          const f32x4 p0 = *(const LAS f32x4*)(lds + L_H4 + l16), p1 = *(const LAS f32x4*)(lds + L_H4 + 1024 + l16);
          const f32x4 p2 = *(const LAS f32x4*)(lds + L_H4 + 2048 + l16), p3 = *(const LAS f32x4*)(lds + L_H4 + 3072 + l16);
          hacc4 = (p0 + p1) + (p2 + p3);
        }
        if constexpr (ms >= 0 && gi == 12) GRAN(st, hacc, 0, hrs, haddr, hzero);
        if constexpr (ms >= 0 && gi == 14) GRAN(st, hacc4, 0, hrs, haddr4, hzero);
      }
      if constexpr (F < 120 && F % 3 == 2) {
        constexpr int s = F / 3;
        if constexpr (s == 15) mma16_0(hacc4, h4w[0], h4x[0]);
        if constexpr (s > 15 && s <= 19) mma16(hacc4, h4w[(s - 15) % 3], h4x[(s - 15) % 3]);
        if constexpr (s >= 13 && s <= 17) {
          h4w[(s - 13) % 3] = *(const LAS u32x4*)(lds + hw4 + (s - 13) * 6144);
          h4x[(s - 13) % 3] = *(const LAS u32x4*)(lds + hx4 + (s - 13) * 1024);
        }
        if constexpr (s == 21) *(LAS f32x4*)(lds + L_H4 + w * 1024 + l16) = hacc4;
      }
      SB();
    });
    STAMP(3);
    asm volatile("s_nop 11" : "+v"(acc3));
    GRAN_MAIN(0, acc3, 0, 96); GRAN_MAIN(1, acc3, 0, 96); GRAN_MAIN(0, acc3, 1, 104); GRAN_MAIN(1, acc3, 1, 104); GRAN_MAIN(0, acc3, 2, 112); GRAN_MAIN(1, acc3, 2, 112);
    WAITL0(); BAR();
    STAMP(4);
    {
      const int par = t16 & 1;
      const bool ca = (4 * g + par) > t16, cb = (4 * g + 2 + par) > t16;
      const unsigned long long mca = __ballot(ca), mcb = __ballot(cb), mpar = __ballot(par != 0);
      const bool neg = (m128 == 0) && (w == 0);
      const unsigned long long nga = neg ? mca : 0ull, ngb = neg ? mcb : 0ull;
      const u32 gq = g < 2 ? g : 2;
      const u32 mA = g < 3 ? ~0u : 0u, mB = g < 2 ? ~0u : 0u, mC = (g == 1 || g == 2) ? ~0u : 0u;
      const u32 pla = par ? 0x01000c0cu : 0x0c0c0100u, plb = par ? 0x03020c0cu : 0x0c0c0302u;
      const u32 sel0 = ca ? pla : 0x0c0c0c0cu, sel2 = ca ? 0x0c0c0c0cu : pla, sel1 = cb ? plb : 0x0c0c0c0cu, sel3 = cb ? 0x0c0c0c0cu : plb;
      constexpr float cexp = 0.22360679774997896f * 1.4426950408889634f;
      float cx = cexp; asm volatile("" : "+v"(cx));
      const u32 qa = L_QST + (32 * w + t16) * 80 + gq * 16, ka = L_KST + (32 * w + t16) * 80 + gq * 16;
      const u32 va = L_VST + (32 * w + 4 * g + (t16 >> 2)) * 80 + (t16 & 3) * 8;
      u32x4 qf[4], k0[4], k1[4];
      u32x4 v0[4], v1[4];
#pragma unroll
      for (int u = 0; u < 4; ++u) {
        const u32 o = (u >> 1) * 16 * 80 + (u & 1) * 32;
        qf[u] = *(const LAS u32x4*)(lds + qa + o);
        k0[u] = *(const LAS u32x4*)(lds + ka + o);
        k1[u] = *(const LAS u32x4*)(lds + ka + o + 16 * 80);
      }
      dma_g<0, 0>(rs, l16, sG, w, lds0);
#pragma unroll
      for (int u = 0; u < 4; ++u) {
        if (u & 1) { qf[u][0] &= mC; qf[u][1] &= mC; qf[u][2] &= mA; qf[u][3] &= mA; }
        else { qf[u][0] &= mA; qf[u][1] &= mA; qf[u][2] &= mB; qf[u][3] &= mB; }
      }
      f32x4 s0[4], s1[4];
      asm volatile("s_nop 1\n\t"
                   "v_mfma_f32_16x16x32_bf16 %0, %8, %16, 0\n\tv_mfma_f32_16x16x32_bf16 %1, %12, %16, 0\n\t"
                   "v_mfma_f32_16x16x32_bf16 %2, %9, %17, 0\n\tv_mfma_f32_16x16x32_bf16 %3, %13, %17, 0\n\t"
                   "v_mfma_f32_16x16x32_bf16 %4, %10, %18, 0\n\tv_mfma_f32_16x16x32_bf16 %5, %14, %18, 0\n\t"
                   "v_mfma_f32_16x16x32_bf16 %6, %11, %19, 0\n\tv_mfma_f32_16x16x32_bf16 %7, %15, %19, 0"
                   : "=&v"(s0[0]), "=&v"(s1[0]), "=&v"(s0[1]), "=&v"(s1[1]), "=&v"(s0[2]), "=&v"(s1[2]), "=&v"(s0[3]), "=&v"(s1[3])
                   : "v"(k0[0]), "v"(k0[1]), "v"(k0[2]), "v"(k0[3]), "v"(k1[0]), "v"(k1[1]), "v"(k1[2]), "v"(k1[3]), "v"(qf[0]), "v"(qf[1]), "v"(qf[2]), "v"(qf[3]));
#pragma unroll
      for (int u = 0; u < 4; ++u) {
        const u32 o = (u >> 1) * 16 * 80 + (u & 1) * 40;
        const u32x2 t0 = __builtin_bit_cast(u32x2, __builtin_amdgcn_ds_read_tr16_b64_v4i16((LAS s16x4*)(lds + va + o)));
        const u32x2 t1 = __builtin_bit_cast(u32x2, __builtin_amdgcn_ds_read_tr16_b64_v4i16((LAS s16x4*)(lds + va + o + 16 * 80)));
        const u32x2 t2 = __builtin_bit_cast(u32x2, __builtin_amdgcn_ds_read_tr16_b64_v4i16((LAS s16x4*)(lds + va + o + 32)));
        const u32x2 t3 = __builtin_bit_cast(u32x2, __builtin_amdgcn_ds_read_tr16_b64_v4i16((LAS s16x4*)(lds + va + o + 32 + 16 * 80)));
        v0[u] = u32x4{t0.x, t0.y, t1.x, t1.y}; v1[u] = u32x4{t2.x, t2.y, t3.x, t3.y};
      }
      dma_g<0, 1>(rs, l16, sG, w, lds0);
      asm volatile("s_nop 7" : "+v"(s0[0]), "+v"(s1[0]), "+v"(s0[1]), "+v"(s1[1]), "+v"(s0[2]), "+v"(s1[2]), "+v"(s0[3]), "+v"(s1[3]));
      float a[4], b[4], mx[4], sm[4];
#pragma unroll
      for (int u = 0; u < 4; ++u) {
        const float x00 = s0[u][0], x01 = s0[u][1], x02 = s0[u][2], x03 = s0[u][3], x10 = s1[u][0], x11 = s1[u][1], x12 = s1[u][2], x13 = s1[u][3];
        a[u] = sel_s(sel_s(x10, x11, mpar), sel_s(x00, x01, mpar), mca);
        b[u] = sel_s(sel_s(x12, x13, mpar), sel_s(x02, x03, mpar), mcb);
        if (u < 2) { a[u] = sel_s(a[u], -__builtin_inff(), nga); b[u] = sel_s(b[u], -__builtin_inff(), ngb); }
        mx[u] = max_s(a[u], b[u]);
      }
      dma_g<0, 2>(rs, l16, sG, w, lds0);
#pragma unroll
      for (int u = 0; u < 4; ++u) { auto r = __builtin_amdgcn_permlane16_swap(f2u(mx[u]), f2u(mx[u]), false, false); mx[u] = max_s(u2f(r[0]), u2f(r[1])); }
      dma_g<0, 3>(rs, l16, sG, w, lds0);
#pragma unroll
      for (int u = 0; u < 4; ++u) { auto r = __builtin_amdgcn_permlane32_swap(f2u(mx[u]), f2u(mx[u]), false, false); mx[u] = max_s(u2f(r[0]), u2f(r[1])); }
      dma_g<0, 4>(rs, l16, sG, w, lds0);
#pragma unroll
      for (int u = 0; u < 4; ++u) {
        const float t = -mul_s(mx[u], cx);
        a[u] = __builtin_amdgcn_exp2f(fma_s(a[u], cx, t)); b[u] = __builtin_amdgcn_exp2f(fma_s(b[u], cx, t));
        sm[u] = a[u] + b[u];
      }
      dma_g<1, 0>(rs, l16, sG, w, lds0);
#pragma unroll
      for (int u = 0; u < 4; ++u) { auto r = __builtin_amdgcn_permlane16_swap(f2u(sm[u]), f2u(sm[u]), false, false); sm[u] = u2f(r[0]) + u2f(r[1]); }
      dma_g<1, 1>(rs, l16, sG, w, lds0);
#pragma unroll
      for (int u = 0; u < 4; ++u) { auto r = __builtin_amdgcn_permlane32_swap(f2u(sm[u]), f2u(sm[u]), false, false); sm[u] = u2f(r[0]) + u2f(r[1]); }
      dma_g<1, 2>(rs, l16, sG, w, lds0);
      u32x4 pf[4];
#pragma unroll
      for (int u = 0; u < 4; ++u) {
        const float inv = __builtin_amdgcn_rcpf(sm[u]);
        const u32 pk = cvtpk(a[u] * inv, b[u] * inv);
        pf[u] = u32x4{__builtin_amdgcn_perm(pk, pk, sel0), __builtin_amdgcn_perm(pk, pk, sel1), __builtin_amdgcn_perm(pk, pk, sel2), __builtin_amdgcn_perm(pk, pk, sel3)};
      }
      dma_g<1, 3>(rs, l16, sG, w, lds0);
      f32x4 o0[4], o1[4];
      asm volatile("s_nop 1\n\t"
                   "v_mfma_f32_16x16x32_bf16 %0, %8, %16, 0\n\tv_mfma_f32_16x16x32_bf16 %1, %12, %16, 0\n\t"
                   "v_mfma_f32_16x16x32_bf16 %2, %9, %17, 0\n\tv_mfma_f32_16x16x32_bf16 %3, %13, %17, 0\n\t"
                   "v_mfma_f32_16x16x32_bf16 %4, %10, %18, 0\n\tv_mfma_f32_16x16x32_bf16 %5, %14, %18, 0\n\t"
                   "v_mfma_f32_16x16x32_bf16 %6, %11, %19, 0\n\tv_mfma_f32_16x16x32_bf16 %7, %15, %19, 0\n\ts_nop 7"
                   : "=&v"(o0[0]), "=&v"(o1[0]), "=&v"(o0[1]), "=&v"(o1[1]), "=&v"(o0[2]), "=&v"(o1[2]), "=&v"(o0[3]), "=&v"(o1[3])
                   : "v"(v0[0]), "v"(v0[1]), "v"(v0[2]), "v"(v0[3]), "v"(v1[0]), "v"(v1[1]), "v"(v1[2]), "v"(v1[3]), "v"(pf[0]), "v"(pf[1]), "v"(pf[2]), "v"(pf[3]));
      dma_g<1, 4>(rs, l16, sG, w, lds0);
      const u32 sg = 5 * p + g, sg4 = 5 * p + 4;
      const u32 oa = (sg >> 2) * 1024 + (t16 + 16 * (sg & 3)) * 16, oa4 = (sg4 >> 2) * 1024 + (t16 + 16 * (sg4 & 3)) * 16;
#pragma unroll
      for (int qb = 0; qb < 2; ++qb) {
        const u32 so = ofo + (8 * m128 + 2 * w + qb) * 20480;
        const f32x4 A0 = o0[2 * qb], B0_ = o0[2 * qb + 1], A1 = o1[2 * qb], B1 = o1[2 * qb + 1];
        stg_sc1(u32x4{cvtpk(A0[0], A0[1]), cvtpk(A0[2], A0[3]), cvtpk(B0_[0], B0_[1]), cvtpk(B0_[2], B0_[3])}, rs, oa, so);
        if (g == 0) stg_sc1(u32x4{cvtpk(A1[0], A1[1]), cvtpk(A1[2], A1[3]), cvtpk(B1[0], B1[1]), cvtpk(B1[2], B1[3])}, rs, oa4, so);
      }
    }
    SB();
    STAMP(5);
    WAITV(0); CERT("G0"); CERT("G1");
    WAITL0();
    BAR();
    if (tid == 0) __hip_atomic_store(flags + (fgb + FG_O + m128) * 32 + p, 1u, RLX_AGENT);
    STAMP(6); RSTAMP(0);
    sfor<5>([&](auto I) { dma_g<2, decltype(I)::value>(rs, l16, sG, w, lds0); });
    sfor<5>([&](auto I) { dma_g<3, decltype(I)::value>(rs, l16, sG, w, lds0); });
  }
  {
    PHASE_IDS();
    if (w == 0) poll_flags(flags, (fgb + FG_O + (m64 >> 1)) * 32 + (lane & 15), lane < 16, lane);
    BAR();
    STAMP(7); RSTAMP(1);
#pragma unroll
    for (int s = 0; s < 5; ++s)
#pragma unroll
      for (int nb = 0; nb < 5; ++nb) { if (s * 5 + nb < 24) pin_a(k.wo_r[s][nb]); else pin_v(k.wo_r[s][nb]); }
    u32x4 af[20];
    u32 tbo[4];
#pragma unroll
    for (int tbi = 0; tbi < 4; ++tbi) tbo[tbi] = ofo + ((4 * m64 + ((w + 1 + tbi) & 3)) * 20 + 5 * w) * 1024;
    constexpr int OW = 10;
#define OLOAD(q) ald_v_sc1<((q) % 5 % 4) * 1024>(af[q], rs, l16, tbo[(q) / 5] + ((q) % 5 / 4) * 4096)
    sfor<OW>([&](auto I) { constexpr int q = decltype(I)::value; OLOAD(q); });
    f32x4 acc[2][5];
    f32x4 part[3][5];
    sfor<20>([&](auto QC) {
      constexpr int q = decltype(QC)::value, tbi = q / 5, s = q % 5;
      if constexpr (q + OW < 20) OLOAD(q + OW);
      waitpin_v<(q < 10 ? OW : (19 - q))>(af[q]);
#pragma unroll
      for (int nb = 0; nb < 5; ++nb) acc[tbi & 1][nb] = mfma16(k.wo_r[s][nb], af[q], s == 0 ? f32x4{0.f, 0.f, 0.f, 0.f} : acc[tbi & 1][nb]);
      if constexpr (tbi > 0 && s < 3) {
        const u32 tbp = (w + tbi) & 3;
        *(LAS f32x4*)(lds + L_RED + ((tbp * 3 + (3 - tbi)) * 5 + 2 * s) * 1024 + l16) = acc[(tbi - 1) & 1][2 * s];
        if constexpr (2 * s + 1 < 5) *(LAS f32x4*)(lds + L_RED + ((tbp * 3 + (3 - tbi)) * 5 + 2 * s + 1) * 1024 + l16) = acc[(tbi - 1) & 1][2 * s + 1];
      }
      if constexpr (q == 18) {
        WAITL0(); BAR();
#pragma unroll
        for (int sl = 0; sl < 3; ++sl)
#pragma unroll
          for (int nb = 0; nb < 5; ++nb) part[sl][nb] = *(const LAS f32x4*)(lds + L_RED + ((w * 3 + sl) * 5 + nb) * 1024 + l16);
      }
      SB();
    });
    STAMP(8);
    float s1 = 0.f, s2 = 0.f;
#pragma unroll
    for (int nb = 0; nb < 5; ++nb) {
      const f32x4 v = (acc[1][nb] + part[0][nb]) + (part[1][nb] + part[2][nb]);
      const u32 a = cvtpk(bflo(k.xr[nb].x) + v[0], bfhi(k.xr[nb].x) + v[1]), b = cvtpk(bflo(k.xr[nb].y) + v[2], bfhi(k.xr[nb].y) + v[3]);
      k.xr[nb] = u32x2{a, b};
    }
    const u32 token = 64 * m64 + 16 * w + t16;
    const u32 xo = ((token >> 5) * 40 + 5 * c8) * 1024 + ((token & 31) + 32 * (g >> 1)) * 16;
#pragma unroll
    for (int pr = 0; pr < 3; ++pr) {
      const int A = 2 * pr, Bn = pr < 2 ? 2 * pr + 1 : 4;
      auto r0 = __builtin_amdgcn_permlane16_swap(k.xr[A].x, k.xr[Bn].x, false, false);
      auto r1 = __builtin_amdgcn_permlane16_swap(k.xr[A].y, k.xr[Bn].y, false, false);
      const u32x4 v = {r0[0], r1[0], r0[1], r1[1]};
      if (pr < 2) stg_sc1(v, rs, xo + (A + (g & 1)) * 1024, x1o);
      else if ((g & 1) == 0) stg_sc1(v, rs, xo + 4 * 1024, x1o);
    }
#pragma unroll
    for (int nb = 0; nb < 5; ++nb) {
      const float a = bflo(k.xr[nb].x), b = bfhi(k.xr[nb].x), c = bflo(k.xr[nb].y), d = bfhi(k.xr[nb].y);
      s1 += (a + b) + (c + d); s2 += (a * a + b * b) + (c * c + d * d);
    }
    s1 = red16_32_add(s1); s2 = red16_32_add(s2);
    if (g == 0) stg2_sc1(u32x2{f2u(s1), f2u(s2)}, rs, (c8 * 2048 + token) * 8, st1o);
    SB();
    STAMP(9);
    WAITV(0); CERT("G2"); CERT("G3");
    WAITL0();
    BAR();
    if (tid == 0) __hip_atomic_store(flags + (fgb + FG_X1 + m64) * 32 + c8, 1u, RLX_AGENT);
    STAMP(10); RSTAMP(2);
  }
  {
    PHASE_IDS();
    const int nblk = k.nblk;
    const int ds0 = w < 2 ? 14 * w : 28 + 13 * (w - 2);
    const u32 sG = OFF_WG + p * 286720 + w * 5120;
    sfor<5>([&](auto I) { dma_g<4, decltype(I)::value>(rs, l16, sG, w, lds0); });
    sfor<5>([&](auto I) { dma_g<5, decltype(I)::value>(rs, l16, sG, w, lds0); });
    sfor<5>([&](auto I) { dma_g<6, decltype(I)::value>(rs, l16, sG, w, lds0); });
    if (w == 0) poll_flags(flags, (fgb + FG_X1 + 2 * m128 + ((lane >> 3) & 1)) * 32 + (lane & 7), lane < 16, lane);
    BAR();
    STAMP(11); RSTAMP(3);
    u32x2 sa0, sa1, sa2, sa3;
    u32x4 xf[40];
    const u32 xbase = x1o + (4 * m128 + w) * 40960;
    const u32 sso = (4 * h * 2048 + 128 * m128 + 32 * w + tok) * 8;
    sfor<XW>([&](auto I) { constexpr int i = decltype(I)::value; XLOAD(i); });
    constexpr int NWD = GS::NWD;
    float r2 = 0.f;
    constexpr int DG = 4, NBG = 6;
    u32x4 wb[NBG];
    f32x16 aA0, aA1, aA2, aB0, aB1;
    u32 lb1 = l16 + 65536, lb2 = l16 + 131072; asm volatile("" : "+v"(lb1), "+v"(lb2));
    const u32 hbase = (8 * m128 + 2 * w + (tok >> 4)) * 54 * 1024 + ((tok & 15) + 16 * h) * 16;
    const u32 hlast = nblk == 7 ? hbase : 0x7fff0000u;
    const u32 wd_o = OFF_WD + (c8 * 54 + ds0) * 5120;
#define LDSG(F) ((((F) / 20) % 7) * 20480 + ((F) % 20) * 1024)
#define RDG(off) (*(const LAS u32x4*)(lds + ((off) < 65536 ? l16 : (off) < 131072 ? lb1 : lb2) + ((off) & 65535)))
    float hv0 = 0.f;
    u32 hp[4];
#define GU_STAGE(ACC, idx, t) do { \
      if constexpr ((t) < 8) { const float hv_ = mul_s(mul_s(ACC[t], r2), ACC[8 + (t)]); if constexpr ((t) & 1) hp[(t) >> 1] = cvtpk(hv0, hv_); else hv0 = hv_; } \
      if constexpr ((t) == 8) { const u32 fb_ = B0 + (idx); stg_sc1(u32x4{hp[0], hp[1], hp[2], hp[3]}, rs, ((idx) == 6 ? hlast : hbase) + (fb_ & 1) * 512, hfo + (fb_ >> 1) * 1024); } \
    } while (0)
    auto wd_load = [&](auto OC) {
      constexpr int i = decltype(OC)::value, s = i / 5, nb = i % 5;
      if constexpr (i < 24 || i >= 50) ald_a<(i % 4) * 1024>(k.wd_r[s][nb], rs, l16, wd_o + (i / 4) * 4096);
      else ald_v<(i % 4) * 1024>(k.wd_r[s][nb], rs, l16, wd_o + (i / 4) * 4096);
    };
#pragma unroll
    for (int F = 0; F < DG; ++F) wb[F % NBG] = RDG(LDSG(F));
    sfor<280>([&](auto FC) {
      constexpr int F = decltype(FC)::value, Fr = F + DG;
      if constexpr (GS::x_at(F)) XLOAD(F / 3 + XW);
      if constexpr (GS::op(F) >= 0 && GS::op(F) < 1000) dma_g<GS::op(F) / 8, GS::op(F) % 8>(rs, l16, sG, w, lds0);
      if constexpr (GS::op(F) >= 1000 && GS::op(F) < 2000) wd_load(std::integral_constant<int, GS::op(F) - 1000>{});
      if constexpr (GS::op(F) == 3000) ald2_sc1(sa0, rs, sso, st1o);
      if constexpr (GS::op(F) == 3001) ald2_sc1(sa1, rs, sso, st1o + 16384);
      if constexpr (GS::op(F) == 3002) ald2_sc1(sa2, rs, sso, st1o + 32768);
      if constexpr (GS::op(F) == 3003) ald2_sc1(sa3, rs, sso, st1o + 49152);
      if constexpr (Fr == 40) { WAITL0(); BAR(); }
      if constexpr (Fr == 80) { CERT("G4"); CERT("G5"); WAITL0(); BAR(); }
      if constexpr (Fr == 120) { WAITV(GS::upto(F) - GS::upto(GS::slot(7 * 8 + 4))); CERT("G6"); CERT("G7"); WAITL0(); BAR(); STAMP(12); }
      if constexpr (Fr == 160) { GSTAMP(23); WAITV(GS::upto(F) - GS::upto(GS::slot(9 * 8 + 4))); CERT("G8"); CERT("G9"); WAITL0(); BAR(); GSTAMP(24); }
      if constexpr (Fr == 200) { GSTAMP(25); WAITV(GS::upto(F) - GS::upto(GS::slot(11 * 8 + 4))); CERT("G10"); CERT("G11"); BAR(); GSTAMP(26); }
      if constexpr (Fr == 240) { WAITV(GS::upto(F) - GS::upto(GS::slot(13 * 8 + 4))); CERT("G12"); CERT("G13"); BAR(); STAMP(13); }
      if constexpr (Fr < 280) wb[Fr % NBG] = RDG(LDSG(Fr));
      constexpr int s = F < 120 ? F / 3 : F < 200 ? (F - 120) / 2 : (F - 200) / 2, b = F < 120 ? F % 3 : F & 1;
      if constexpr (F < 120 && b == 0 && (s & 1) == 0) waitpin_a2<GS::xwait2(s)>(xf[s], xf[s + 1]);
#define GMMA(ACC) do { if constexpr (s == 0) { if constexpr ((F & 1) == 0) mma32w_0(ACC, wb[F % NBG], xf[s], wb[(F + 1) % NBG]); else mma32_0(ACC, wb[F % NBG], xf[s]); } \
                       else { if constexpr ((F & 1) == 0) mma32w(ACC, wb[F % NBG], xf[s], wb[(F + 1) % NBG]); else mma32(ACC, wb[F % NBG], xf[s]); } } while (0)
      if constexpr (F < 120) { if constexpr (b == 0) GMMA(aA0); else if constexpr (b == 1) GMMA(aA1); else GMMA(aA2); }
      else if constexpr (F < 200) { if constexpr (b == 0) GMMA(aB0); else GMMA(aB1); }
      else { if constexpr (b == 0) GMMA(aA0); else GMMA(aA1); }
      if constexpr (F == 62) {
        pin2(sa0); pin2(sa1); pin2(sa2); pin2(sa3);
        float rstd, nm;
        stats_finish(red32_add((u2f(sa0.x) + u2f(sa1.x)) + (u2f(sa2.x) + u2f(sa3.x))), red32_add((u2f(sa0.y) + u2f(sa1.y)) + (u2f(sa2.y) + u2f(sa3.y))), nm, rstd);
        r2 = rstd * rstd;
      }
      if constexpr ((F & 1) && F >= 123 && F <= 139) GU_STAGE(aA0, 0, (F - 123) / 2);
      if constexpr ((F & 1) && F >= 141 && F <= 157) GU_STAGE(aA1, 1, (F - 141) / 2);
      if constexpr ((F & 1) && F >= 159 && F <= 175) GU_STAGE(aA2, 2, (F - 159) / 2);
      if constexpr ((F & 1) && F >= 203 && F <= 219) GU_STAGE(aB0, 3, (F - 203) / 2);
      if constexpr ((F & 1) && F >= 221 && F <= 237) GU_STAGE(aB1, 4, (F - 221) / 2);
      SB();
    });
    asm volatile("s_nop 11" : "+v"(aA0), "+v"(aA1));
    sfor<9>([&](auto T) { GU_STAGE(aA0, 5, decltype(T)::value); });
    sfor<9>([&](auto T) { GU_STAGE(aA1, 6, decltype(T)::value); });
    SB();
    STAMP(14);
    WAITV(0);
    WAITL0();
    BAR();
    if (tid == 0) __hip_atomic_store(flags + (fgb + FG_H + m128) * 32 + p, 1u, RLX_AGENT);
    {
      const u32 wd_o = OFF_WD + (c8 * 54 + ds0) * 5120;
      sfor<70 - GS::NWD>([&](auto I) { constexpr int i = GS::NWD + decltype(I)::value; ald_a<(i % 4) * 1024>(k.wd_r[i / 5][i % 5], rs, l16, wd_o + (i / 4) * 4096); });
    }
    STAMP(15); RSTAMP(4);
  }
  {
    PHASE_IDS();
    const int ds0 = w < 2 ? 14 * w : 28 + 13 * (w - 2);
    const u32 sQ = OFF_WQ + p * 163840;
    STAMP(16);
    if (w == 0) poll_flags(flags, (fgb + FG_H + (m64 >> 1)) * 32 + (lane & 15), lane < 16, lane);
    else WAITV(0);
    BAR();
    STAMP(17); RSTAMP(5);
    sfor<70>([&](auto I) { constexpr int i = decltype(I)::value; if constexpr (i < 24 || i >= 50) pin_a(k.wd_r[i / 5][i % 5]); else pin_v(k.wd_r[i / 5][i % 5]); });
    constexpr int DH = 12;
    u32x4 af[DH];
    const bool s13 = w < 2;
    u32 tbo[4];
#pragma unroll
    for (int tbi = 0; tbi < 4; ++tbi) tbo[tbi] = hfo + ((4 * m64 + ((w + 1 + tbi) & 3)) * 54 + ds0) * 1024;
#define HLOAD(q) ald_a_sc1<((q) % 14 % 4) * 1024>(af[(q) % DH], rs, l16, tbo[(q) / 14] + ((q) % 14 / 4) * 4096)
    sfor<DH>([&](auto I) { constexpr int q = decltype(I)::value; HLOAD(q); });
    f32x4 acc[2][5];
    f32x4 part[3][5];
    sfor<56>([&](auto QC) {
      constexpr int q = decltype(QC)::value, tbi = q / 14, s = q % 14;
      constexpr int yf = (q + DH - 1 < 55 ? q + DH - 1 : 55) - q;
      constexpr int nd = q - 44;
      constexpr int yd = (nd > 0) ? (nd + (nd < 8 ? nd : 8)) : 0;
      waitpin_a<yf + yd>(af[q % DH]);
      if constexpr (s == 0) {
#pragma unroll
        for (int nb = 0; nb < 5; ++nb) acc[tbi & 1][nb] = mfma16(k.wd_r[s][nb], af[q % DH], f32x4{0.f, 0.f, 0.f, 0.f});
      } else if (s < 13 || s13) {
#pragma unroll
        for (int nb = 0; nb < 5; ++nb) acc[tbi & 1][nb] = mfma16(k.wd_r[s][nb], af[q % DH], acc[tbi & 1][nb]);
      }
      if constexpr (tbi > 0 && s >= 1 && s < 6) {
        const u32 tbp = (w + tbi) & 3;
        *(LAS f32x4*)(lds + L_RED + ((tbp * 3 + (3 - tbi)) * 5 + (s - 1)) * 1024 + l16) = acc[(tbi - 1) & 1][s - 1];
      }
      if constexpr (q == 50) {
        WAITL0(); BAR();
#pragma unroll
        for (int sl = 0; sl < 3; ++sl)
#pragma unroll
          for (int nb = 0; nb < 5; ++nb) part[sl][nb] = *(const LAS f32x4*)(lds + L_RED + ((w * 3 + sl) * 5 + nb) * 1024 + l16);
      }
      SB();
      if constexpr (q + DH < 56) HLOAD(q + DH);
      if constexpr (q >= 44) {
        constexpr int j0 = nd + (nd < 8 ? nd : 8);
        if constexpr (j0 < 10) dma_q<0, 0, j0>(rs, l16, sQ, w, lds0); else dma_q<1, 1, j0 - 10>(rs, l16, sQ, w, lds0);
        if constexpr (nd < 8) {
          constexpr int j1 = j0 + 1;
          if constexpr (j1 < 10) dma_q<0, 0, j1>(rs, l16, sQ, w, lds0); else dma_q<1, 1, j1 - 10>(rs, l16, sQ, w, lds0);
        }
      }
      SB();
    });
    STAMP(18);
#pragma unroll
    for (int nb = 0; nb < 5; ++nb) {
      const f32x4 v = (acc[1][nb] + part[0][nb]) + (part[1][nb] + part[2][nb]);
      const u32 a = cvtpk(bflo(k.xr[nb].x) + v[0], bfhi(k.xr[nb].x) + v[1]), b = cvtpk(bflo(k.xr[nb].y) + v[2], bfhi(k.xr[nb].y) + v[3]);
      k.xr[nb] = u32x2{a, b};
    }
    const u32 token = 64 * m64 + 16 * w + t16;
    STAMP(19);
    if (it == 0) {
      float s1 = 0.f, s2 = 0.f;
      const u32 xo = ((token >> 5) * 40 + 5 * c8) * 1024 + ((token & 31) + 32 * (g >> 1)) * 16;
#pragma unroll
      for (int pr = 0; pr < 3; ++pr) {
        const int A = 2 * pr, Bn = pr < 2 ? 2 * pr + 1 : 4;
        auto r0 = __builtin_amdgcn_permlane16_swap(k.xr[A].x, k.xr[Bn].x, false, false);
        auto r1 = __builtin_amdgcn_permlane16_swap(k.xr[A].y, k.xr[Bn].y, false, false);
        const u32x4 v = {r0[0], r1[0], r0[1], r1[1]};
        if (pr < 2) stg_sc1(v, rs, xo + (A + (g & 1)) * 1024, OFF_XF + 2 * SZ_XF);
        else if ((g & 1) == 0) stg_sc1(v, rs, xo + 4 * 1024, OFF_XF + 2 * SZ_XF);
      }
#pragma unroll
      for (int nb = 0; nb < 5; ++nb) {
        const float a = bflo(k.xr[nb].x), b = bfhi(k.xr[nb].x), c = bflo(k.xr[nb].y), d = bfhi(k.xr[nb].y);
        s1 += (a + b) + (c + d); s2 += (a * a + b * b) + (c * c + d * d);
      }
      s1 = red16_32_add(s1); s2 = red16_32_add(s2);
      if (g == 0) stg2_sc1(u32x2{f2u(s1), f2u(s2)}, rs, (c8 * 2048 + token) * 8, OFF_ST + 2 * SZ_ST);
      SB();
      WAITV(0);
      WAITL0();
      BAR();
      if (tid == 0) __hip_atomic_store(flags + (FG_X2 + m64) * 32 + c8, 1u, RLX_AGENT);
      sfor<10>([&](auto I) { dma_q<2, 2, decltype(I)::value>(rs, l16, sQ, w, lds0); });
    } else {
      const u32 oo = token * 1280 + (80 * c8 + 8 * (g >> 1)) * 2;
#pragma unroll
      for (int pr = 0; pr < 3; ++pr) {
        const int A = 2 * pr, Bn = pr < 2 ? 2 * pr + 1 : 4;
        auto r0 = __builtin_amdgcn_permlane16_swap(k.xr[A].x, k.xr[Bn].x, false, false);
        auto r1 = __builtin_amdgcn_permlane16_swap(k.xr[A].y, k.xr[Bn].y, false, false);
        const u32x4 v = {r0[0], r1[0], r0[1], r1[1]};
        if (pr < 2) __builtin_amdgcn_raw_buffer_store_b128(v, k.rso, oo + (A + (g & 1)) * 32, 0, 0);
        else if ((g & 1) == 0) __builtin_amdgcn_raw_buffer_store_b128(v, k.rso, oo + 4 * 32, 0, 0);
      }
    }
    STAMP(20);
  }
}

__global__ __launch_bounds__(256, 1) void main_kernel(char* __restrict__ ws, u16* __restrict__ out) {
  extern __shared__ __attribute__((aligned(16))) char smem[];
  K k;
  k.lds = (LAS char*)smem;
  k.lds0 = (u32)(size_t)k.lds;
  k.tid = threadIdx.x; k.lane = k.tid & 63; k.w = __builtin_amdgcn_readfirstlane(k.tid >> 6);
  k.rs = __builtin_amdgcn_make_buffer_rsrc(ws, 0, WS_USED, 0x00020000);
  k.rso = __builtin_amdgcn_make_buffer_rsrc(out, 0, 2048 * 640 * 2, 0x00020000);
  k.flags = (GAS u32*)ws;
  const int bid = blockIdx.x, xc = bid & 7, j = bid >> 3, mg = xc >> 1, half = xc & 1;
  k.m128 = 4 * mg + (j >> 3); k.p = 8 * half + (j & 7);
  k.m64 = 8 * mg + (j >> 2); k.c8 = 4 * half + (j & 3);
  const int p7 = j & 7;
  k.B0 = 54 * half + (p7 < 6 ? 7 * p7 : 42 + 6 * (p7 - 6));
  k.nblk = p7 < 6 ? 7 : 6;
  {
    PHASE_IDS();
    const u32 sQ = OFF_WQ + p * 163840;
    sfor<10>([&](auto I) { dma_q<0, 0, decltype(I)::value>(rs, l16, sQ, w, lds0); });
    sfor<10>([&](auto I) { dma_q<1, 1, decltype(I)::value>(rs, l16, sQ, w, lds0); });
    sfor<10>([&](auto I) { dma_q<2, 2, decltype(I)::value>(rs, l16, sQ, w, lds0); });
    const u32 token = 64 * m64 + 16 * w + t16;
    const u32 o = ((token >> 5) * 40 + 5 * c8) * 1024 + ((token & 31) + 32 * (g >> 1)) * 16 + (g & 1) * 8;
#pragma unroll
    for (int nb = 0; nb < 5; ++nb) ald2(k.xr[nb], rs, o, OFF_XF + nb * 1024);
  }
#pragma nounroll
  for (int it = 0; it < 2; ++it) { k.it = it; iteration(k); }
}

extern "C" void kernel_launch(void* const* d_in, const int* in_sizes, int n_in, void* d_out, int out_size, void* d_ws, size_t ws_size,
                              hipStream_t stream) {
  const u16* x0 = (const u16*)d_in[0];
  const u16* wqkv = (const u16*)d_in[1];
  const u16* wo = (const u16*)d_in[2];
  const u16* wg = (const u16*)d_in[4];
  const u16* wu = (const u16*)d_in[5];
  const u16* wd = (const u16*)d_in[6];
  (void)hipFuncSetAttribute((const void*)main_kernel, hipFuncAttributeMaxDynamicSharedMemorySize, LDS_BYTES);
  prep_kernel<<<PREP_UNITS, 512, 0, stream>>>(x0, wqkv, wo, wg, wu, wd, (char*)d_ws);
  main_kernel<<<256, 256, LDS_BYTES, stream>>>((char*)d_ws, (u16*)d_out);
}
```

```cpp
#include <hip/hip_runtime.h>
#include <stdint.h>
#include <utility>

#define LAS __attribute__((address_space(3)))
#define GAS __attribute__((address_space(1)))
#define DI __device__ __forceinline__
typedef unsigned u32;
typedef unsigned short u16;
typedef u32 u32x4 __attribute__((ext_vector_type(4)));
typedef u32 u32x2 __attribute__((ext_vector_type(2)));
typedef float f32x2 __attribute__((ext_vector_type(2)));
typedef float f32x4 __attribute__((ext_vector_type(4)));
typedef float f32x16 __attribute__((ext_vector_type(16)));
typedef short s16x8 __attribute__((ext_vector_type(8)));
typedef short s16x4 __attribute__((ext_vector_type(4)));
typedef __bf16 bf16x2 __attribute__((ext_vector_type(2)));
typedef __amdgpu_buffer_rsrc_t rsrc_t;

constexpr u32 FLAG_BYTES = 193 * 2048;
constexpr u32 OFF_WQ = 409600;
constexpr u32 OFF_WO = OFF_WQ + 16 * 163840;
constexpr u32 OFF_WG = OFF_WO + 8 * 20 * 5 * 1024;
constexpr u32 OFF_WD = OFF_WG + 16 * 286720;
constexpr u32 OFF_CSQ = OFF_WD + 8 * 54 * 5 * 1024;
constexpr u32 OFF_CSG = OFF_CSQ + 16384;
constexpr u32 SZ_XF = 64 * 40 * 1024;
constexpr u32 OFF_XF = OFF_CSG + 28672;
constexpr u32 SZ_ST = 8 * 2048 * 8;
constexpr u32 OFF_ST = OFF_XF + 4 * SZ_XF;
constexpr u32 SZ_OF = 128 * 20 * 1024;
constexpr u32 OFF_OF = OFF_ST + 4 * SZ_ST;
constexpr u32 SZ_HF = 128 * 54 * 1024;
constexpr u32 OFF_HF = OFF_OF + 2 * SZ_OF;
constexpr u32 WS_USED = OFF_HF + 2 * SZ_HF;
constexpr u32 FG_O = 0, FG_X1 = 16, FG_H = 48, FG_X2 = 64, FG_PER_IT = 96;
constexpr u32 FG_TMO = 2 * FG_PER_IT;

constexpr u32 L_HALO = 122880;
constexpr u32 L_KST = 40960;
constexpr u32 L_VST = L_KST + 144 * 80;
constexpr u32 L_QST = L_VST + 144 * 80;
constexpr u32 L_RED = 100352;
constexpr u32 L_H4 = 143360;
constexpr u32 LDS_BYTES = 163840;

constexpr u32 AUX_SC1 = 16;
#define RLX_AGENT __ATOMIC_RELAXED, __HIP_MEMORY_SCOPE_AGENT
#define SB() __builtin_amdgcn_sched_barrier(0)
#define MEMFENCE() asm volatile("" ::: "memory")
#define WAITV(n) asm volatile("s_waitcnt vmcnt(%0)" ::"i"(n) : "memory")
#define WAITL0() asm volatile("s_waitcnt lgkmcnt(0)" ::: "memory")
#define BAR() do { MEMFENCE(); __builtin_amdgcn_s_barrier(); MEMFENCE(); } while (0)
#define CERT(tag) asm volatile("; CERT " tag ::: "memory")

template <int... I, class Fn> DI void sfor_impl(std::integer_sequence<int, I...>, Fn&& f) { (f(std::integral_constant<int, I>{}), ...); }
template <int N, class Fn> DI void sfor(Fn&& f) { sfor_impl(std::make_integer_sequence<int, N>{}, f); }
DI f32x16 mfma32(u32x4 a, u32x4 b, f32x16 c) { return __builtin_amdgcn_mfma_f32_32x32x16_bf16(__builtin_bit_cast(s16x8, a), __builtin_bit_cast(s16x8, b), c, 0, 0, 0); }
DI f32x4 mfma16(u32x4 a, u32x4 b, f32x4 c) { return __builtin_amdgcn_mfma_f32_16x16x32_bf16(__builtin_bit_cast(s16x8, a), __builtin_bit_cast(s16x8, b), c, 0, 0, 0); }
DI u32 cvtpk(float lo, float hi) { f32x2 v = {lo, hi}; bf16x2 b = __builtin_convertvector(v, bf16x2); return __builtin_bit_cast(u32, b); }
DI float u2f(u32 v) { return __builtin_bit_cast(float, v); }
DI u32 f2u(float v) { return __builtin_bit_cast(u32, v); }
DI float bflo(u32 v) { return __builtin_bit_cast(float, v << 16); }
DI float bfhi(u32 v) { return __builtin_bit_cast(float, v & 0xffff0000u); }
DI float fma_s(float a, float b, float c) { float r; asm("v_fma_f32 %0, %1, %2, %3" : "=v"(r) : "v"(a), "v"(b), "v"(c)); return r; }
DI float mul_s(float a, float b) { float r; asm("v_mul_f32_e32 %0, %1, %2" : "=v"(r) : "v"(a), "v"(b)); return r; }
DI float add_s(float a, float b) { float r; asm("v_add_f32_e32 %0, %1, %2" : "=v"(r) : "v"(a), "v"(b)); return r; }
DI float max_s(float a, float b) { float r; asm("v_max_f32_e32 %0, %1, %2" : "=v"(r) : "v"(a), "v"(b)); return r; }
DI float sel_s(float f, float t, unsigned long long m) { float r; asm("v_cndmask_b32_e64 %0, %1, %2, %3" : "=v"(r) : "v"(f), "v"(t), "s"(m)); return r; }
DI float red16_32_add(float v) {
  auto a = __builtin_amdgcn_permlane16_swap(f2u(v), f2u(v), false, false);
  v = u2f(a[0]) + u2f(a[1]);
  auto b = __builtin_amdgcn_permlane32_swap(f2u(v), f2u(v), false, false);
  return u2f(b[0]) + u2f(b[1]);
}
DI float red16_32_max(float v) {
  auto a = __builtin_amdgcn_permlane16_swap(f2u(v), f2u(v), false, false);
  v = __builtin_fmaxf(u2f(a[0]), u2f(a[1]));
  auto b = __builtin_amdgcn_permlane32_swap(f2u(v), f2u(v), false, false);
  return __builtin_fmaxf(u2f(b[0]), u2f(b[1]));
}
DI float red32_add(float v) {
  auto b = __builtin_amdgcn_permlane32_swap(f2u(v), f2u(v), false, false);
  return u2f(b[0]) + u2f(b[1]);
}
DI void stg_sc1(u32x4 v, rsrc_t rs, u32 voff, u32 soff) { __builtin_amdgcn_raw_buffer_store_b128(v, rs, voff, soff, AUX_SC1); }
DI void stg2_sc1(u32x2 v, rsrc_t rs, u32 voff, u32 soff) { __builtin_amdgcn_raw_buffer_store_b64(v, rs, voff, soff, AUX_SC1); }

template <int IMM> DI void ald_v_sc1(u32x4& d, rsrc_t rs, u32 voff, u32 soff) {
  asm volatile("buffer_load_dwordx4 %0, %1, %2, %3 offen offset:%4 sc1" : "=v"(d) : "v"(voff), "s"(rs), "s"(soff), "i"(IMM) : "memory");
}
template <int IMM> DI void ald_a(u32x4& d, rsrc_t rs, u32 voff, u32 soff) {
  asm volatile("buffer_load_dwordx4 %0, %1, %2, %3 offen offset:%4" : "=a"(d) : "v"(voff), "s"(rs), "s"(soff), "i"(IMM) : "memory");
}
template <int IMM> DI void ald_v(u32x4& d, rsrc_t rs, u32 voff, u32 soff) {
  asm volatile("buffer_load_dwordx4 %0, %1, %2, %3 offen offset:%4" : "=v"(d) : "v"(voff), "s"(rs), "s"(soff), "i"(IMM) : "memory");
}
DI void ald2_sc1(u32x2& d, rsrc_t rs, u32 voff, u32 soff) {
  asm volatile("buffer_load_dwordx2 %0, %1, %2, %3 offen sc1" : "=v"(d) : "v"(voff), "s"(rs), "s"(soff) : "memory");
}
DI void ald2(u32x2& d, rsrc_t rs, u32 voff, u32 soff) {
  asm volatile("buffer_load_dwordx2 %0, %1, %2, %3 offen" : "=v"(d) : "v"(voff), "s"(rs), "s"(soff) : "memory");
}
DI void ald1(u32& d, rsrc_t rs, u32 voff, u32 soff) {
  asm volatile("buffer_load_dword %0, %1, %2, %3 offen" : "=v"(d) : "v"(voff), "s"(rs), "s"(soff) : "memory");
}
template <int IMM> DI void ald_a_sc1(u32x4& d, rsrc_t rs, u32 voff, u32 soff) {
  asm volatile("buffer_load_dwordx4 %0, %1, %2, %3 offen offset:%4 sc1" : "=a"(d) : "v"(voff), "s"(rs), "s"(soff), "i"(IMM) : "memory");
}
template <int N> DI void waitpin_a(u32x4& r) { asm volatile("s_waitcnt vmcnt(%1)" : "+a"(r) : "i"(N) : "memory"); }
template <int N> DI void waitpin_a2(u32x4& r0, u32x4& r1) { asm volatile("s_waitcnt vmcnt(%2)" : "+a"(r0), "+a"(r1) : "i"(N) : "memory"); }
DI void mma32(f32x16& acc, const u32x4& wv, const u32x4& xa) { asm volatile("v_mfma_f32_32x32x16_bf16 %0, %1, %2, %0" : "+v"(acc) : "v"(wv), "a"(xa)); }
DI void mma32_0(f32x16& acc, const u32x4& wv, const u32x4& xa) { asm volatile("v_mfma_f32_32x32x16_bf16 %0, %1, %2, 0" : "=&v"(acc) : "v"(wv), "a"(xa)); }
DI void mma32w(f32x16& acc, const u32x4& wv, const u32x4& xa, const u32x4& wn) { asm volatile("v_mfma_f32_32x32x16_bf16 %0, %1, %2, %0" : "+v"(acc) : "v"(wv), "a"(xa), "v"(wn)); }
DI void mma32w_0(f32x16& acc, const u32x4& wv, const u32x4& xa, const u32x4& wn) { asm volatile("v_mfma_f32_32x32x16_bf16 %0, %1, %2, 0" : "=&v"(acc) : "v"(wv), "a"(xa), "v"(wn)); }
DI void mma16(f32x4& acc, const u32x4& av, const u32x4& bv) { asm volatile("v_mfma_f32_16x16x32_bf16 %0, %1, %2, %0" : "+v"(acc) : "v"(av), "v"(bv)); }
DI void mma16_0(f32x4& acc, const u32x4& av, const u32x4& bv) { asm volatile("v_mfma_f32_16x16x32_bf16 %0, %1, %2, 0" : "=&v"(acc) : "v"(av), "v"(bv)); }
DI void pin_vv(u32x4& a, u32x4& b) { asm volatile("" : "+v"(a), "+v"(b)); }
template <int N> DI void waitpin_v(u32x4& r) { asm volatile("s_waitcnt vmcnt(%1)" : "+v"(r) : "i"(N) : "memory"); }
DI void pin_v(u32x4& r) { asm volatile("" : "+v"(r)::"memory"); }
DI void pin_a(u32x4& r) { asm volatile("" : "+a"(r)::"memory"); }
DI void pin2(u32x2& r) { asm volatile("" : "+v"(r)::"memory"); }
DI void pin1(u32& r) { asm volatile("" : "+v"(r)::"memory"); }
#define DMA_F(TAG, IMM, SC, rs, voff, soff, m0v) \
  asm volatile("s_mov_b32 m0, %2\n\ts_nop 0\n\tbuffer_load_dwordx4 %0, %1, %3 offen offset:%4 " SC "lds ; DMA " TAG ::"v"(voff), "s"(rs), "s"(m0v), "s"(soff), "i"(IMM) : "memory")
#define DMA_N(TAG, IMM, SC, rs, voff, soff) \
  asm volatile("buffer_load_dwordx4 %0, %1, %2 offen offset:%3 " SC "lds ; DMA " TAG ::"v"(voff), "s"(rs), "s"(soff), "i"(IMM) : "memory")

constexpr int PREP_UNITS = 84 + 20 + 60 + 64;
__global__ __launch_bounds__(512) void prep_kernel(const u16* __restrict__ x0, const u16* __restrict__ wqkv, const u16* __restrict__ wo,
                                                   const u16* __restrict__ wg, const u16* __restrict__ wu, const u16* __restrict__ wd, char* __restrict__ ws) {
  __shared__ __attribute__((aligned(16))) char lds[81920 + 1024];
  const int tid = threadIdx.x, l = tid & 63, w = __builtin_amdgcn_readfirstlane(tid >> 6);
  const int u = blockIdx.x;
  { const int i = u * 512 + tid; if (i < (int)(FLAG_BYTES / 128)) ((u32*)ws)[i * 32] = 0u; }
  const int gam = l >> 4, q = (l & 15) >> 2, pp = l & 3;
  if (u < 84) {
    const int type = u < 30 ? 0 : u < 57 ? 1 : 2, cg = u < 30 ? u : u < 57 ? u - 30 : u - 57;
    const u16* src = type == 0 ? wqkv : type == 1 ? wg : wu;
    const int pitch = type == 0 ? 1920 : 1728;
#pragma unroll
    for (int i = 0; i < 10; ++i) {
      const int r = 8 * i + w;
      const u16* g = src + (size_t)(8 * r + (l >> 3)) * pitch + 64 * cg + (l & 7) * 8;
      __builtin_amdgcn_global_load_lds((const GAS u32*)g, (LAS u32*)(lds + r * 1024), 16, 0, 0);
    }
    const int c = 64 * cg + l;
    u32 dbase; int dstep;
    if (type == 0) {
      const int sec = c / 640, cc = c % 640, p = cc / 40, r40 = cc % 40;
      const int n = (sec == 1 ? 0 : sec == 2 ? 40 : 80) + r40;
      const int b = n >> 5, nl = n & 31;
      dbase = OFF_WQ + p * 163840 + (b < 3 ? b * 1024 : 122880) + nl * 16;
      dstep = b < 3 ? 3072 : 1024;
    } else {
      const int fb = c >> 4, nl = (c & 15) + 16 * (type - 1);
      const int half = fb / 54, rem = fb % 54;
      const int sl_ = 8 * half + (rem < 42 ? rem / 7 : 6 + (rem - 42) / 6), ix = rem < 42 ? rem % 7 : (rem - 42) % 6;
      dbase = OFF_WG + sl_ * 286720 + (ix < 3 ? ix * 1024 : ix < 5 ? 122880 + (ix - 3) * 1024 : 204800 + (ix - 5) * 1024) + nl * 16;
      dstep = ix < 3 ? 3072 : 2048;
    }
    __syncthreads();
    u32x4 sl[10];
    float csum = 0.f;
#pragma unroll
    for (int i = 0; i < 10; ++i) {
      const int o = 8 * i + w;
      const u32x2 a = __builtin_bit_cast(u32x2, __builtin_amdgcn_ds_read_tr16_b64_v4i16((LAS s16x4*)(lds + (8 * o + q) * 128 + gam * 32 + pp * 8)));
      const u32x2 b2 = __builtin_bit_cast(u32x2, __builtin_amdgcn_ds_read_tr16_b64_v4i16((LAS s16x4*)(lds + (8 * o + 4 + q) * 128 + gam * 32 + pp * 8)));
      sl[i] = u32x4{a.x, a.y, b2.x, b2.y};
    }
#pragma unroll
    for (int i = 0; i < 10; ++i)
#pragma unroll
      for (int j = 0; j < 4; ++j) csum += bflo(sl[i][j]) + bfhi(sl[i][j]);
    __syncthreads();
    float* red = (float*)lds;
    red[w * 64 + l] = csum;
    __syncthreads();
    const float mean = (((red[l] + red[64 + l]) + (red[128 + l] + red[192 + l])) + ((red[256 + l] + red[320 + l]) + (red[384 + l] + red[448 + l]))) * (1.0f / 640.0f);
#pragma unroll
    for (int i = 0; i < 10; ++i) {
      const int o = 8 * i + w;
      u32x4 v;
#pragma unroll
      for (int j = 0; j < 4; ++j) v[j] = cvtpk(bflo(sl[i][j]) - mean, bfhi(sl[i][j]) - mean);
      *(u32x4*)(ws + dbase + (o >> 1) * dstep + (o & 1) * 512) = v;
    }
  } else if (u < 164) {
    const bool iswo = u < 104;
    const int v = iswo ? u - 84 : u - 104, cg = iswo ? v >> 1 : v / 6, kk = iswo ? v & 1 : v % 6;
    const u16* src = iswo ? wo : wd;
    const int nrows = iswo ? 320 : 288, row0 = kk * nrows, noct = nrows / 8;
#pragma unroll
    for (int i = 0; i < 5; ++i) {
      const int r = 8 * i + w;
      if (r < noct) {
        const u16* g = src + (size_t)(row0 + 8 * r + (l >> 3)) * 640 + 64 * cg + (l & 7) * 8;
        __builtin_amdgcn_global_load_lds((const GAS u32*)g, (LAS u32*)(lds + r * 1024), 16, 0, 0);
      }
    }
    const int c = 64 * cg + l, c8 = c / 80, nb = (c % 80) >> 4, n = c & 15;
    const u32 dbase = (iswo ? OFF_WO + c8 * 20 * 5120 : OFF_WD + c8 * 54 * 5120) + nb * 1024 + n * 16;
    __syncthreads();
#pragma unroll
    for (int i = 0; i < 5; ++i) {
      const int o = 8 * i + w;
      if (o < noct) {
        int r0, r1; u32 dst;
        if (iswo) {
          const int pl = o / 5, ii = o % 5, sg = 40 * kk + o;
          r0 = 40 * pl + 4 * ii + q; r1 = r0 + 20;
          dst = dbase + (sg >> 2) * 5120 + (sg & 3) * 256;
        } else {
          const int s_ = o >> 2, g = o & 3;
          r0 = 32 * s_ + 16 * (g >> 1) + 4 * (g & 1) + q; r1 = r0 + 8;
          dst = dbase + (9 * kk + s_) * 5120 + g * 256;
        }
        const u32x2 a = __builtin_bit_cast(u32x2, __builtin_amdgcn_ds_read_tr16_b64_v4i16((LAS s16x4*)(lds + r0 * 128 + gam * 32 + pp * 8)));
        const u32x2 b2 = __builtin_bit_cast(u32x2, __builtin_amdgcn_ds_read_tr16_b64_v4i16((LAS s16x4*)(lds + r1 * 128 + gam * 32 + pp * 8)));
        *(u32x4*)(ws + dst) = u32x4{a.x, a.y, b2.x, b2.y};
      }
    }
  } else {
    const int rb = u - 164;
#pragma unroll
    for (int i = 0; i < 5; ++i) {
      const int cidx = 64 * (8 * i + w) + l, row = cidx / 80, cc = cidx % 80;
      const u16* g = x0 + (size_t)(32 * rb + row) * 640 + ((cc ^ (row & 7)) * 8);
      __builtin_amdgcn_global_load_lds((const GAS u32*)g, (LAS u32*)(lds + (8 * i + w) * 1024), 16, 0, 0);
    }
    __syncthreads();
    const int tok = l & 31, h = l >> 5;
    float s1 = 0.f, s2 = 0.f;
#pragma unroll
    for (int i = 0; i < 5; ++i) {
      const int s = 5 * w + i, cc = 2 * s + h;
      const u32x4 v = *(const u32x4*)(lds + tok * 1280 + ((cc ^ (tok & 7)) * 16));
      *(u32x4*)(ws + OFF_XF + (rb * 40 + s) * 1024 + l * 16) = v;
#pragma unroll
      for (int j = 0; j < 4; ++j) { float a = bflo(v[j]), b = bfhi(v[j]); s1 += a + b; s2 += a * a + b * b; }
    }
    s1 = red32_add(s1); s2 = red32_add(s2);
    if (l < 32) {
      f32x2* st = (f32x2*)(ws + OFF_ST);
      st[w * 2048 + 32 * rb + tok] = f32x2{s1, s2};
    }
  }
}

#define STAMP(i) do {} while (0)
#define RSTAMP(i) do {} while (0)
#define GSTAMP(i) STAMP(i)
#define QSTAMP(i) do {} while (0)
constexpr u32 OFF_STAMP = WS_USED + 4096;
constexpr u32 kSpinCap = 1u << 15;

struct K {
  rsrc_t rs, rso;
  LAS char* lds;
  u32 lds0;
  GAS u32* flags;
  int lane, w, m128, p, m64, c8, B0, nblk, tid, it;
  u32x2 xr[5];
  u32x4 wo_r[5][5];
  u32x4 wd_r[14][5];
};

#define PHASE_IDS() int lane = k.lane; asm volatile("" : "+v"(lane)); \
  int it = k.it; asm volatile("" : "+s"(it)); \
  const u32 xin = OFF_XF + it * 2 * SZ_XF, stin = OFF_ST + it * 2 * SZ_ST, x1o = xin + SZ_XF, st1o = stin + SZ_ST; \
  const u32 ofo = OFF_OF + it * SZ_OF, hfo = OFF_HF + it * SZ_HF, fgb = it * FG_PER_IT; \
  (void)xin; (void)stin; (void)x1o; (void)st1o; (void)ofo; (void)hfo; (void)fgb; \
  int w = k.w, m128 = k.m128, p = k.p, m64 = k.m64, c8 = k.c8, B0 = k.B0; \
  asm volatile("" : "+s"(w), "+s"(m128), "+s"(p), "+s"(m64), "+s"(c8), "+s"(B0)); \
  const u32 l16 = lane * 16; const int tok = lane & 31, h = lane >> 5, t16 = lane & 15, g = lane >> 4; \
  const rsrc_t rs = k.rs; LAS char* const lds = k.lds; const u32 lds0 = k.lds0; \
  (void)tok; (void)h; (void)t16; (void)g; (void)l16; (void)m128; (void)p; (void)m64; (void)c8; (void)B0; (void)lds; (void)lds0; (void)rs

#define POLL_GAP 4
struct Polls { u32 a, b, c; };
DI void poll_issue(u32& d, const GAS u32* p) { asm volatile("global_load_dword %0, %1, off sc1 ; POLL" : "=v"(d) : "v"(p) : "memory"); }
DI bool poll_check(u32& d, bool act, u32 want) { asm volatile("s_waitcnt vmcnt(2) ; POLLWAIT" : "+v"(d)::"memory"); return __all(!act || d == want); }
DI void poll_flags(GAS u32* flags, u32 fo, bool act, int lane, u32 want, Polls& pl) {
  const GAS u32* p = flags + (act ? fo : FG_TMO * 512);
  asm volatile("; POLL_BEGIN" ::: "memory");
  poll_issue(pl.a, p); __builtin_amdgcn_s_sleep(POLL_GAP); poll_issue(pl.b, p); __builtin_amdgcn_s_sleep(POLL_GAP); poll_issue(pl.c, p);
  for (u32 spins = 0;;) {
    if (poll_check(pl.a, act, want)) break;
    poll_issue(pl.a, p);
    if (poll_check(pl.b, act, want)) break;
    poll_issue(pl.b, p);
    if (poll_check(pl.c, act, want)) break;
    poll_issue(pl.c, p);
    if (++spins > kSpinCap) {
      if (lane == 0) __hip_atomic_store(flags + FG_TMO * 512, want, RLX_AGENT);
      break;
    }
    if ((spins & 63u) == 0u && (u32)__builtin_amdgcn_readfirstlane(__hip_atomic_load(flags + FG_TMO * 512, RLX_AGENT)) == want) break;
  }
  asm volatile("; POLL_END" ::: "memory");
}
DI void poll_pin(Polls& pl) { asm volatile("; POLL_PIN" : "+v"(pl.a), "+v"(pl.b), "+v"(pl.c)::"memory"); }
DI void stats_finish(float s1, float s2, float& nmean, float& rstd) {
  const float mean = s1 * (1.0f / 640.0f);
  const float var = __builtin_fmaxf(s2 * (1.0f / 640.0f) - mean * mean, 0.f);
  nmean = -mean; rstd = __builtin_amdgcn_rsqf(var + 1e-5f);
}

template <int C, int SLOT, int I> DI void dma_q(rsrc_t rs, u32 l16, u32 sQ, int w, u32 lds0) {
#define Q_EMIT(TAG, FIRST, IMM, SO, MO) do { if constexpr (FIRST) DMA_F(TAG, IMM, "", rs, l16, SO, MO); else DMA_N(TAG, IMM, "", rs, l16, SO); } while (0)
  if constexpr (C < 3) {
    constexpr int grp = I / 4, imm = (I % 4) * 1024;
    const u32 so = sQ + w * 10240 + C * 40960 + grp * 4096, mo = lds0 + SLOT * 40960 + w * 10240 + grp * 4096;
    if constexpr (C == 0) Q_EMIT("Q0", I % 4 == 0, imm, so, mo); else if constexpr (C == 1) Q_EMIT("Q1", I % 4 == 0, imm, so, mo); else Q_EMIT("Q2", I % 4 == 0, imm, so, mo);
  } else {
    constexpr int hf = I / 5, ii = I % 5, grp = ii / 4, imm = (ii % 4) * 1024;
    const u32 so = sQ + w * 5120 + 122880 + hf * 20480 + grp * 4096, mo = lds0 + SLOT * 40960 + w * 5120 + hf * 20480 + grp * 4096;
    if constexpr (hf == 0) Q_EMIT("Q3a", ii % 4 == 0, imm, so, mo); else Q_EMIT("Q3b", ii % 4 == 0, imm, so, mo);
  }
#undef Q_EMIT
}
template <int HH, int I> DI void dma_g(rsrc_t rs, u32 l16, u32 sG, int w, u32 lds0) {
  constexpr int grp = I / 4, imm = (I % 4) * 1024;
  const u32 so = sG + HH * 20480 + grp * 4096, mo = lds0 + w * 5120 + (HH % 7) * 20480 + grp * 4096;
#define G_CASE(n) if constexpr (HH == n) { if constexpr (I % 4 == 0) DMA_F("G" #n, imm, "", rs, l16, so, mo); else DMA_N("G" #n, imm, "", rs, l16, so); }
  G_CASE(0) G_CASE(1) G_CASE(2) G_CASE(3) G_CASE(4) G_CASE(5) G_CASE(6) G_CASE(7) G_CASE(8) G_CASE(9) G_CASE(10) G_CASE(11) G_CASE(12) G_CASE(13)
#undef G_CASE
}

template <int HH> DI void dma_g3(rsrc_t rs, u32 l16, u32 sG0, int w, u32 lds0) {
  const u32 f0 = 7 * (w - 1);
  const u32 so = sG0 + HH * 20480 + f0 * 1024, mo = lds0 + (HH % 7) * 20480 + f0 * 1024;
#define G3_CASE(n) if constexpr (HH == n) { DMA_F("G" #n, 0, "", rs, l16, so, mo); DMA_N("G" #n, 1024, "", rs, l16, so); DMA_N("G" #n, 2048, "", rs, l16, so); DMA_N("G" #n, 3072, "", rs, l16, so); \
    DMA_F("G" #n, 0, "", rs, l16, so + 4096, mo + 4096); DMA_N("G" #n, 1024, "", rs, l16, so + 4096); if (w < 3) DMA_N("G" #n, 2048, "", rs, l16, so + 4096); }
  G3_CASE(2) G3_CASE(3) G3_CASE(4) G3_CASE(5) G3_CASE(6)
#undef G3_CASE
}
DI void dma_q2w3(rsrc_t rs, u32 l16, u32 sQ, int w, u32 lds0) {
  const u32 f0 = w == 1 ? 0 : 1 + 13 * (w - 1), so = sQ + 81920 + f0 * 1024, mo = lds0 + 81920 + f0 * 1024;
#define Q2G(o) DMA_F("Q2", 0, "", rs, l16, so + (o), mo + (o)); DMA_N("Q2", 1024, "", rs, l16, so + (o)); DMA_N("Q2", 2048, "", rs, l16, so + (o)); DMA_N("Q2", 3072, "", rs, l16, so + (o))
  Q2G(0); Q2G(4096); Q2G(8192);
#undef Q2G
  DMA_F("Q2", 0, "", rs, l16, so + 12288, mo + 12288);
  if (w == 1) DMA_N("Q2", 1024, "", rs, l16, so + 12288);
}
constexpr int XW = 10;
struct QS {
  static constexpr bool x_at(int f) { return f < 120 && f % 3 == 0 && f / 3 + XW < 40; }
  static constexpr int aux_at(int f) { return (f < 32 && f % 3 == 1) ? f / 3 : -1; }
  static constexpr int q3_at(int f) { return (f >= 77 && f <= 104 && f % 3 == 2) ? (f - 77) / 3 : -1; }
  static constexpr int wo_at(int f) {
    if (f >= 34 && f <= 67 && f % 3 == 1) return (f - 34) / 3;
    if (f >= 121 && f <= 157 && (f - 121) % 3 == 0) return 12 + (f - 121) / 3;
    return -1;
  }
  static constexpr int upto(int F) { int n = 0; for (int f = 0; f <= F; ++f) n += x_at(f) + (aux_at(f) >= 0) + (q3_at(f) >= 0) + (wo_at(f) >= 0); return n; }
  static constexpr int xidx(int i) { return i < XW ? i : XW + upto(3 * (i - XW)) - 1; }
  static constexpr int xwait2(int s) { return XW + upto(3 * s) - 1 - xidx(s + 1); }
};
struct GS {
  static constexpr int NWD = 65;
  static constexpr bool x_at(int f) { return f < 120 && f % 3 == 0 && f / 3 + XW < 40; }
  static constexpr int op(int F) {
    if (F <= 10 && F % 3 == 1) return 3000 + F / 3;
    if (F >= 40 && F <= 67 && F % 3 == 1) return (7 + (F - 40) / 15) * 8 + ((F - 40) % 15) / 3;
    if (F >= 79 && F <= 106 && F % 3 == 1) return (9 + (F - 79) / 15) * 8 + ((F - 79) % 15) / 3;
    if (F < 120 || (F & 1)) return -1;
    if (F < 140) return (11 + (F - 120) / 10) * 8 + ((F - 120) % 10) / 2;
    if (F < 160) return 1000 + (F - 140) / 2;
    if (F < 170) return 13 * 8 + (F - 160) / 2;
    if (F < 280) return 1010 + (F - 170) / 2;
    return -1;
  }
  static constexpr bool st_at(int F) { return F == 139 || F == 157 || F == 175 || F == 219 || F == 237; }
  static constexpr int upto(int F) { int n = 0; for (int f = 0; f <= F; ++f) n += x_at(f) + (op(f) >= 0) + st_at(f); return n; }
  static constexpr int slot(int o) { for (int f = 0; f < 300; ++f) if (op(f) == o) return f; return -1; }
  static constexpr int xidx(int i) { return i < XW ? i : XW + upto(3 * (i - XW)) - 1; }
  static constexpr int xwait2(int s) { return XW + upto(3 * s) - 1 - xidx(s + 1); }
};
static_assert(GS::op(278) == 1000 + GS::NWD - 1, "down-weight prefetch schedule");

DI void iteration(K& k) {
  GAS u32* const flags = k.flags;
  const int tid = k.tid;
  {
    PHASE_IDS();
    const u32 sQ = OFF_WQ + p * 163840, sG = OFF_WG + p * 286720 + w * 5120;
    STAMP(0);
    Polls pl = {0u, 0u, 0u};
    if (it > 0 && w == 0) {
      const int grp = 2 * m128 - 1 + (lane >> 3);
      const bool act = lane < 24 && grp >= 0;
      poll_flags(flags, ((FG_X2 + grp) * 16 + (lane & 7)) * 32, act, lane, 1u, pl);
    }
    if (!(it > 0 && w == 0)) WAITV(0);
    CERT("Q0"); CERT("Q1"); CERT("Q2");
    BAR();
    STAMP(1);
    u32x4 xf[40];
    const u32 xbase = xin + (4 * m128 + w) * 40960;
#define XLOAD(i) ald_a_sc1<((i) % 4) * 1024>(xf[i], rs, l16, xbase + ((i) / 4) * 4096)
    u32x2 hs0, hs1, sa0, sa1, sa2, sa3;
    const int rbh = m128 > 0 ? 4 * m128 - 1 : 0;
    const u32 hvo = (g >> 1) * 1024 + (g & 1) * 512 + (16 + t16) * 16;
    const int ht = 128 * m128 - 16 + t16;
    const u32 hso = ((2 * g) * 2048 + (ht < 0 ? 0 : ht)) * 8, sso = (4 * h * 2048 + 128 * m128 + 32 * w + tok) * 8;
    auto aux_load = [&](auto AC) {
      constexpr int a = decltype(AC)::value;
      if constexpr (a < 5) DMA_F("HALO", 0, "sc1 ", rs, hvo, xin + rbh * 40960 + (5 * w + a) * 2048, lds0 + L_HALO + (5 * w + a) * 1024);
      if constexpr (a == 5) ald2_sc1(sa0, rs, sso, stin);
      if constexpr (a == 6) ald2_sc1(sa1, rs, sso, stin + 16384);
      if constexpr (a == 7) ald2_sc1(sa2, rs, sso, stin + 32768);
      if constexpr (a == 8) ald2_sc1(sa3, rs, sso, stin + 49152);
      if constexpr (a == 9) ald2_sc1(hs0, rs, hso, stin);
      if constexpr (a == 10) ald2_sc1(hs1, rs, hso, stin + 16384);
    };
    sfor<XW>([&](auto I) { constexpr int i = decltype(I)::value; XLOAD(i); });
    using Q = QS;
    constexpr int DQ = 4, NBQ = 6;
    u32x4 wb[NBQ];
    f32x16 acc0, acc1, acc2, acc3;
    f32x4 hacc, hacc4;
    u32x4 hw[3], hx[3], h4w[3], h4x[3];
    float rstd = 0.f, hrs = 0.f;
    u32 lb1 = l16 + 65536, lbh = l16 + L_HALO; asm volatile("" : "+v"(lb1), "+v"(lbh));
    u32 hwlo = (w >> 1) * 1024 + (g >> 1) * 3072 + (g & 1) * 512 + (16 * (w & 1) + t16) * 16, hwhi = hwlo + 61440;
    u32 hw4 = 2048 + (g >> 1) * 3072 + (g & 1) * 512 + t16 * 16 + w * 30720, hx4 = l16 + L_HALO + w * 5120;
    asm volatile("" : "+v"(hwlo), "+v"(hwhi), "+v"(hw4), "+v"(hx4));
    const u32 kvrow = L_KST + (16 + 32 * w + tok) * 80 + h * 8;
    const int hn = 16 * w + 4 * g;
    const u32 haddr = L_KST + t16 * 80 + 2 * hn + (hn >= 40 ? (L_VST - L_KST - 80) : 0);
    const u32 haddr4 = L_VST + t16 * 80 + 2 * (64 + 4 * g - 40);
    const bool hzero = (m128 == 0);
    const u32 wo_o = OFF_WO + (c8 * 20 + 5 * w) * 5120;
#define LDSQ(F) (((F) / 40 % 3) * 40960 + ((F) % 40) * 1024)
#define RDW(off) (*(const LAS u32x4*)(lds + ((off) < 65536 ? l16 : lb1) + ((off) & 65535)))
    u32 gp0 = 0;
#define STG_OFF(n0) ((n0) < 40 ? 2 * (n0) : (n0) < 80 ? (L_VST - L_KST) + 2 * ((n0) - 40) : (L_QST - L_KST - 16 * 80) + 2 * ((n0) - 80))
#define GRAN(t, ACC, q, RS, STADDR, ZERO) do { \
      if constexpr ((t) == 0) gp0 = cvtpk(mul_s(ACC[4 * (q)], RS), mul_s(ACC[4 * (q) + 1], RS)); \
      if constexpr ((t) == 1) { u32x2 pk_ = {gp0, cvtpk(mul_s(ACC[4 * (q) + 2], RS), mul_s(ACC[4 * (q) + 3], RS))}; if (ZERO) pk_ = u32x2{0u, 0u}; *(LAS u32x2*)(lds + (STADDR)) = pk_; } \
    } while (0)
#define GRAN_MAIN(t, ACC, q, n0) GRAN(t, ACC, q, rstd, kvrow + STG_OFF(n0), false)
#pragma unroll
    for (int F = 0; F < DQ; ++F) wb[F % NBQ] = RDW(LDSQ(F));
    sfor<160>([&](auto FC) {
      constexpr int F = decltype(FC)::value, Fr = F + DQ;
      if constexpr (F == 30) QSTAMP(21);
      if constexpr (F == 60) QSTAMP(22);
      if constexpr (F == 90) QSTAMP(23);
      if constexpr (F == 140) QSTAMP(26);
      if constexpr (Q::x_at(F)) XLOAD(F / 3 + XW);
      if constexpr (Q::aux_at(F) >= 0) aux_load(std::integral_constant<int, Q::aux_at(F)>{});
      if constexpr (Q::q3_at(F) >= 0) dma_q<3, 0, Q::q3_at(F)>(rs, l16, sQ, w, lds0);
      if constexpr (Q::wo_at(F) >= 0) {
        constexpr int i = Q::wo_at(F), s = i / 5, nb = i % 5;
        if constexpr (i < 24) ald_a<(i % 4) * 1024>(k.wo_r[s][nb], rs, l16, wo_o + (i / 4) * 4096); else ald_v<(i % 4) * 1024>(k.wo_r[s][nb], rs, l16, wo_o + (i / 4) * 4096);
      }
      if constexpr (Fr == 40) { WAITV(Q::upto(F) - Q::upto(13)); CERT("HALO"); BAR(); }
      if constexpr (Fr == 80) { WAITL0(); BAR(); }
      if constexpr (Fr == 120) { WAITV(Q::upto(F) - Q::upto(89)); CERT("Q3a"); WAITL0(); BAR(); STAMP(2); }
      if constexpr (Fr == 140) { WAITV(Q::upto(F) - Q::upto(104)); CERT("Q3b"); BAR(); }
      if constexpr (Fr < 160) wb[Fr % NBQ] = RDW(LDSQ(Fr));
      if constexpr (F < 120) {
        constexpr int s = F / 3, b = F % 3;
        if constexpr (b == 0 && (s & 1) == 0) waitpin_a2<Q::xwait2(s)>(xf[s], xf[s + 1]);
#define QMMA(ACC) do { if constexpr (s == 0) { if constexpr ((F & 1) == 0) mma32w_0(ACC, wb[F % NBQ], xf[s], wb[(F + 1) % NBQ]); else mma32_0(ACC, wb[F % NBQ], xf[s]); } \
                       else { if constexpr ((F & 1) == 0) mma32w(ACC, wb[F % NBQ], xf[s], wb[(F + 1) % NBQ]); else mma32(ACC, wb[F % NBQ], xf[s]); } } while (0)
        if constexpr (b == 0) QMMA(acc0); else if constexpr (b == 1) QMMA(acc1); else QMMA(acc2);
        if constexpr (F == 3) poll_pin(pl);
        if constexpr (F == 62) {
          pin2(sa0); pin2(sa1); pin2(sa2); pin2(sa3);
          float nm;
          stats_finish(red32_add((u2f(sa0.x) + u2f(sa1.x)) + (u2f(sa2.x) + u2f(sa3.x))), red32_add((u2f(sa0.y) + u2f(sa1.y)) + (u2f(sa2.y) + u2f(sa3.y))), nm, rstd);
        }
        if constexpr (F == 110) {
          pin2(hs0); pin2(hs1);
          float nm;
          stats_finish(red16_32_add(u2f(hs0.x) + u2f(hs1.x)), red16_32_add(u2f(hs0.y) + u2f(hs1.y)), nm, hrs);
        }
        if constexpr (F == 113) {
#pragma unroll
          for (int nb = 0; nb < 5; ++nb) pin2(k.xr[nb]);
        }
        if constexpr (b == 1) {
          if constexpr (s == 15) mma16_0(hacc, hw[0], hx[0]);
          if constexpr (s > 15 && s <= 34) mma16(hacc, hw[(s - 15) % 3], hx[(s - 15) % 3]);
          if constexpr (s >= 13 && s <= 32) {
            constexpr int sg = s - 13;
            hw[sg % 3] = *(const LAS u32x4*)(lds + (sg < 10 ? hwlo : hwhi) + (sg % 10) * 6144);
            hx[sg % 3] = *(const LAS u32x4*)(lds + lbh + sg * 1024);
          }
        }
      } else {
        constexpr int s = F - 120;
        if constexpr (s == 0) mma32w_0(acc3, wb[F % NBQ], xf[s], wb[(F + 1) % NBQ]); else if constexpr (s & 1) mma32(acc3, wb[F % NBQ], xf[s]); else mma32w(acc3, wb[F % NBQ], xf[s], wb[(F + 1) % NBQ]);
        constexpr int ms = s - 2, gi = ms / 2, st = ms % 2;
        if constexpr (ms >= 0 && gi < 12) {
          constexpr int b = gi >> 2, q = gi & 3, n0 = 32 * b + 8 * q;
          if constexpr (b == 0) GRAN_MAIN(st, acc0, q, n0); else if constexpr (b == 1) GRAN_MAIN(st, acc1, q, n0); else GRAN_MAIN(st, acc2, q, n0);
        }
        if constexpr (s == 22) {
          const f32x4 p0 = *(const LAS f32x4*)(lds + L_H4 + l16), p1 = *(const LAS f32x4*)(lds + L_H4 + 1024 + l16);
          const f32x4 p2 = *(const LAS f32x4*)(lds + L_H4 + 2048 + l16), p3 = *(const LAS f32x4*)(lds + L_H4 + 3072 + l16);
          hacc4 = (p0 + p1) + (p2 + p3);
        }
        if constexpr (ms >= 0 && gi == 12) GRAN(st, hacc, 0, hrs, haddr, hzero);
        if constexpr (ms >= 0 && gi == 14) GRAN(st, hacc4, 0, hrs, haddr4, hzero);
      }
      if constexpr (F < 120 && F % 3 == 2) {
        constexpr int s = F / 3;
        if constexpr (s == 15) mma16_0(hacc4, h4w[0], h4x[0]);
        if constexpr (s > 15 && s <= 19) mma16(hacc4, h4w[(s - 15) % 3], h4x[(s - 15) % 3]);
        if constexpr (s >= 13 && s <= 17) {
          h4w[(s - 13) % 3] = *(const LAS u32x4*)(lds + hw4 + (s - 13) * 6144);
          h4x[(s - 13) % 3] = *(const LAS u32x4*)(lds + hx4 + (s - 13) * 1024);
        }
        if constexpr (s == 21) *(LAS f32x4*)(lds + L_H4 + w * 1024 + l16) = hacc4;
      }
      SB();
    });
    STAMP(3);
    asm volatile("s_nop 11" : "+v"(acc3));
    GRAN_MAIN(0, acc3, 0, 96); GRAN_MAIN(1, acc3, 0, 96); GRAN_MAIN(0, acc3, 1, 104); GRAN_MAIN(1, acc3, 1, 104); GRAN_MAIN(0, acc3, 2, 112); GRAN_MAIN(1, acc3, 2, 112);
    WAITL0(); BAR();
    STAMP(4);
    {
      const int par = t16 & 1;
      const bool ca = (4 * g + par) > t16, cb = (4 * g + 2 + par) > t16;
      const unsigned long long mca = __ballot(ca), mcb = __ballot(cb), mpar = __ballot(par != 0);
      const bool neg = (m128 == 0) && (w == 0);
      const unsigned long long nga = neg ? mca : 0ull, ngb = neg ? mcb : 0ull;
      const u32 gq = g < 2 ? g : 2;
      const u32 mA = g < 3 ? ~0u : 0u, mB = g < 2 ? ~0u : 0u, mC = (g == 1 || g == 2) ? ~0u : 0u;
      const u32 pla = par ? 0x01000c0cu : 0x0c0c0100u, plb = par ? 0x03020c0cu : 0x0c0c0302u;
      const u32 sel0 = ca ? pla : 0x0c0c0c0cu, sel2 = ca ? 0x0c0c0c0cu : pla, sel1 = cb ? plb : 0x0c0c0c0cu, sel3 = cb ? 0x0c0c0c0cu : plb;
      constexpr float cexp = 0.22360679774997896f * 1.4426950408889634f;
      float cx = cexp; asm volatile("" : "+v"(cx));
      const u32 qa = L_QST + (32 * w + t16) * 80 + gq * 16, ka = L_KST + (32 * w + t16) * 80 + gq * 16;
      const u32 va = L_VST + (32 * w + 4 * g + (t16 >> 2)) * 80 + (t16 & 3) * 8;
      u32x4 qf[4], k0[4], k1[4];
      u32x4 v0[4], v1[4];
#pragma unroll
      for (int u = 0; u < 4; ++u) {
        const u32 o = (u >> 1) * 16 * 80 + (u & 1) * 32;
        qf[u] = *(const LAS u32x4*)(lds + qa + o);
        k0[u] = *(const LAS u32x4*)(lds + ka + o);
        k1[u] = *(const LAS u32x4*)(lds + ka + o + 16 * 80);
      }
      dma_g<0, 0>(rs, l16, sG, w, lds0);
#pragma unroll
      for (int u = 0; u < 4; ++u) {
        if (u & 1) { qf[u][0] &= mC; qf[u][1] &= mC; qf[u][2] &= mA; qf[u][3] &= mA; }
        else { qf[u][0] &= mA; qf[u][1] &= mA; qf[u][2] &= mB; qf[u][3] &= mB; }
      }
      f32x4 s0[4], s1[4];
      asm volatile("s_nop 1\n\t"
                   "v_mfma_f32_16x16x32_bf16 %0, %8, %16, 0\n\tv_mfma_f32_16x16x32_bf16 %1, %12, %16, 0\n\t"
                   "v_mfma_f32_16x16x32_bf16 %2, %9, %17, 0\n\tv_mfma_f32_16x16x32_bf16 %3, %13, %17, 0\n\t"
                   "v_mfma_f32_16x16x32_bf16 %4, %10, %18, 0\n\tv_mfma_f32_16x16x32_bf16 %5, %14, %18, 0\n\t"
                   "v_mfma_f32_16x16x32_bf16 %6, %11, %19, 0\n\tv_mfma_f32_16x16x32_bf16 %7, %15, %19, 0"
                   : "=&v"(s0[0]), "=&v"(s1[0]), "=&v"(s0[1]), "=&v"(s1[1]), "=&v"(s0[2]), "=&v"(s1[2]), "=&v"(s0[3]), "=&v"(s1[3])
                   : "v"(k0[0]), "v"(k0[1]), "v"(k0[2]), "v"(k0[3]), "v"(k1[0]), "v"(k1[1]), "v"(k1[2]), "v"(k1[3]), "v"(qf[0]), "v"(qf[1]), "v"(qf[2]), "v"(qf[3]));
#pragma unroll
      for (int u = 0; u < 4; ++u) {
        const u32 o = (u >> 1) * 16 * 80 + (u & 1) * 40;
        const u32x2 t0 = __builtin_bit_cast(u32x2, __builtin_amdgcn_ds_read_tr16_b64_v4i16((LAS s16x4*)(lds + va + o)));
        const u32x2 t1 = __builtin_bit_cast(u32x2, __builtin_amdgcn_ds_read_tr16_b64_v4i16((LAS s16x4*)(lds + va + o + 16 * 80)));
        const u32x2 t2 = __builtin_bit_cast(u32x2, __builtin_amdgcn_ds_read_tr16_b64_v4i16((LAS s16x4*)(lds + va + o + 32)));
        const u32x2 t3 = __builtin_bit_cast(u32x2, __builtin_amdgcn_ds_read_tr16_b64_v4i16((LAS s16x4*)(lds + va + o + 32 + 16 * 80)));
        v0[u] = u32x4{t0.x, t0.y, t1.x, t1.y}; v1[u] = u32x4{t2.x, t2.y, t3.x, t3.y};
      }
      dma_g<0, 1>(rs, l16, sG, w, lds0);
      asm volatile("s_nop 7" : "+v"(s0[0]), "+v"(s1[0]), "+v"(s0[1]), "+v"(s1[1]), "+v"(s0[2]), "+v"(s1[2]), "+v"(s0[3]), "+v"(s1[3]));
      float a[4], b[4], mx[4], sm[4];
#pragma unroll
      for (int u = 0; u < 4; ++u) {
        const float x00 = s0[u][0], x01 = s0[u][1], x02 = s0[u][2], x03 = s0[u][3], x10 = s1[u][0], x11 = s1[u][1], x12 = s1[u][2], x13 = s1[u][3];
        a[u] = sel_s(sel_s(x10, x11, mpar), sel_s(x00, x01, mpar), mca);
        b[u] = sel_s(sel_s(x12, x13, mpar), sel_s(x02, x03, mpar), mcb);
        if (u < 2) { a[u] = sel_s(a[u], -__builtin_inff(), nga); b[u] = sel_s(b[u], -__builtin_inff(), ngb); }
        mx[u] = max_s(a[u], b[u]);
      }
      dma_g<0, 2>(rs, l16, sG, w, lds0);
#pragma unroll
      for (int u = 0; u < 4; ++u) { auto r = __builtin_amdgcn_permlane16_swap(f2u(mx[u]), f2u(mx[u]), false, false); mx[u] = max_s(u2f(r[0]), u2f(r[1])); }
      dma_g<0, 3>(rs, l16, sG, w, lds0);
#pragma unroll
      for (int u = 0; u < 4; ++u) { auto r = __builtin_amdgcn_permlane32_swap(f2u(mx[u]), f2u(mx[u]), false, false); mx[u] = max_s(u2f(r[0]), u2f(r[1])); }
      dma_g<0, 4>(rs, l16, sG, w, lds0);
#pragma unroll
      for (int u = 0; u < 4; ++u) {
        const float t = -mul_s(mx[u], cx);
        a[u] = __builtin_amdgcn_exp2f(fma_s(a[u], cx, t)); b[u] = __builtin_amdgcn_exp2f(fma_s(b[u], cx, t));
        sm[u] = a[u] + b[u];
      }
      dma_g<1, 0>(rs, l16, sG, w, lds0);
#pragma unroll
      for (int u = 0; u < 4; ++u) { auto r = __builtin_amdgcn_permlane16_swap(f2u(sm[u]), f2u(sm[u]), false, false); sm[u] = u2f(r[0]) + u2f(r[1]); }
      dma_g<1, 1>(rs, l16, sG, w, lds0);
#pragma unroll
      for (int u = 0; u < 4; ++u) { auto r = __builtin_amdgcn_permlane32_swap(f2u(sm[u]), f2u(sm[u]), false, false); sm[u] = u2f(r[0]) + u2f(r[1]); }
      dma_g<1, 2>(rs, l16, sG, w, lds0);
      u32x4 pf[4];
#pragma unroll
      for (int u = 0; u < 4; ++u) {
        const float inv = __builtin_amdgcn_rcpf(sm[u]);
        const u32 pk = cvtpk(a[u] * inv, b[u] * inv);
        pf[u] = u32x4{__builtin_amdgcn_perm(pk, pk, sel0), __builtin_amdgcn_perm(pk, pk, sel1), __builtin_amdgcn_perm(pk, pk, sel2), __builtin_amdgcn_perm(pk, pk, sel3)};
      }
      dma_g<1, 3>(rs, l16, sG, w, lds0);
      f32x4 o0[4], o1[4];
      asm volatile("s_nop 1\n\t"
                   "v_mfma_f32_16x16x32_bf16 %0, %8, %16, 0\n\tv_mfma_f32_16x16x32_bf16 %1, %12, %16, 0\n\t"
                   "v_mfma_f32_16x16x32_bf16 %2, %9, %17, 0\n\tv_mfma_f32_16x16x32_bf16 %3, %13, %17, 0\n\t"
                   "v_mfma_f32_16x16x32_bf16 %4, %10, %18, 0\n\tv_mfma_f32_16x16x32_bf16 %5, %14, %18, 0\n\t"
                   "v_mfma_f32_16x16x32_bf16 %6, %11, %19, 0\n\tv_mfma_f32_16x16x32_bf16 %7, %15, %19, 0\n\ts_nop 7"
                   : "=&v"(o0[0]), "=&v"(o1[0]), "=&v"(o0[1]), "=&v"(o1[1]), "=&v"(o0[2]), "=&v"(o1[2]), "=&v"(o0[3]), "=&v"(o1[3])
                   : "v"(v0[0]), "v"(v0[1]), "v"(v0[2]), "v"(v0[3]), "v"(v1[0]), "v"(v1[1]), "v"(v1[2]), "v"(v1[3]), "v"(pf[0]), "v"(pf[1]), "v"(pf[2]), "v"(pf[3]));
      dma_g<1, 4>(rs, l16, sG, w, lds0);
      const u32 sg = 5 * p + g, sg4 = 5 * p + 4;
      const u32 oa = (sg >> 2) * 1024 + (t16 + 16 * (sg & 3)) * 16, oa4 = (sg4 >> 2) * 1024 + (t16 + 16 * (sg4 & 3)) * 16;
#pragma unroll
      for (int qb = 0; qb < 2; ++qb) {
        const u32 so = ofo + (8 * m128 + 2 * w + qb) * 20480;
        const f32x4 A0 = o0[2 * qb], B0_ = o0[2 * qb + 1], A1 = o1[2 * qb], B1 = o1[2 * qb + 1];
        stg_sc1(u32x4{cvtpk(A0[0], A0[1]), cvtpk(A0[2], A0[3]), cvtpk(B0_[0], B0_[1]), cvtpk(B0_[2], B0_[3])}, rs, oa, so);
        if (g == 0) stg_sc1(u32x4{cvtpk(A1[0], A1[1]), cvtpk(A1[2], A1[3]), cvtpk(B1[0], B1[1]), cvtpk(B1[2], B1[3])}, rs, oa4, so);
      }
    }
    SB();
    STAMP(5);
    WAITV(0); CERT("G0"); CERT("G1");
    WAITL0();
    BAR();
    if (tid == 0) __hip_atomic_store(flags + ((fgb + FG_O + m128) * 16 + p) * 32, 1u, RLX_AGENT);
    STAMP(6); RSTAMP(0);
    if (w != 0) { dma_g3<2>(rs, l16, sG - w * 5120, w, lds0); dma_g3<3>(rs, l16, sG - w * 5120, w, lds0); }
  }
  {
    PHASE_IDS();
    Polls pl = {0u, 0u, 0u};
    if (w == 0) poll_flags(flags, ((fgb + FG_O + (m64 >> 1)) * 16 + (lane & 15)) * 32, lane < 16, lane, 1u, pl);
    BAR();
    STAMP(7); RSTAMP(1);
#pragma unroll
    for (int s = 0; s < 5; ++s)
#pragma unroll
      for (int nb = 0; nb < 5; ++nb) { if (s * 5 + nb < 24) pin_a(k.wo_r[s][nb]); else pin_v(k.wo_r[s][nb]); }
    u32x4 af[20];
    u32 tbo[4];
#pragma unroll
    for (int tbi = 0; tbi < 4; ++tbi) tbo[tbi] = ofo + ((4 * m64 + ((w + 1 + tbi) & 3)) * 20 + 5 * w) * 1024;
    constexpr int OW = 10;
#define OLOAD(q) ald_v_sc1<((q) % 5 % 4) * 1024>(af[q], rs, l16, tbo[(q) / 5] + ((q) % 5 / 4) * 4096)
    sfor<OW>([&](auto I) { constexpr int q = decltype(I)::value; OLOAD(q); });
    f32x4 acc[2][5];
    f32x4 part[3][5];
    sfor<20>([&](auto QC) {
      constexpr int q = decltype(QC)::value, tbi = q / 5, s = q % 5;
      if constexpr (q + OW < 20) OLOAD(q + OW);
      waitpin_v<(q < 10 ? OW : (19 - q))>(af[q]);
      if constexpr (q == 1) poll_pin(pl);
#pragma unroll
      for (int nb = 0; nb < 5; ++nb) acc[tbi & 1][nb] = mfma16(k.wo_r[s][nb], af[q], s == 0 ? f32x4{0.f, 0.f, 0.f, 0.f} : acc[tbi & 1][nb]);
      if constexpr (tbi > 0 && s < 3) {
        const u32 tbp = (w + tbi) & 3;
        *(LAS f32x4*)(lds + L_RED + ((tbp * 3 + (3 - tbi)) * 5 + 2 * s) * 1024 + l16) = acc[(tbi - 1) & 1][2 * s];
        if constexpr (2 * s + 1 < 5) *(LAS f32x4*)(lds + L_RED + ((tbp * 3 + (3 - tbi)) * 5 + 2 * s + 1) * 1024 + l16) = acc[(tbi - 1) & 1][2 * s + 1];
      }
      if constexpr (q == 18) {
        WAITL0(); BAR();
#pragma unroll
        for (int sl = 0; sl < 3; ++sl)
#pragma unroll
          for (int nb = 0; nb < 5; ++nb) part[sl][nb] = *(const LAS f32x4*)(lds + L_RED + ((w * 3 + sl) * 5 + nb) * 1024 + l16);
      }
      SB();
    });
    STAMP(8);
    float s1 = 0.f, s2 = 0.f;
#pragma unroll
    for (int nb = 0; nb < 5; ++nb) {
      const f32x4 v = (acc[1][nb] + part[0][nb]) + (part[1][nb] + part[2][nb]);
      const u32 a = cvtpk(bflo(k.xr[nb].x) + v[0], bfhi(k.xr[nb].x) + v[1]), b = cvtpk(bflo(k.xr[nb].y) + v[2], bfhi(k.xr[nb].y) + v[3]);
      k.xr[nb] = u32x2{a, b};
    }
    const u32 token = 64 * m64 + 16 * w + t16;
    const u32 xo = ((token >> 5) * 40 + 5 * c8) * 1024 + ((token & 31) + 32 * (g >> 1)) * 16;
#pragma unroll
    for (int pr = 0; pr < 3; ++pr) {
      const int A = 2 * pr, Bn = pr < 2 ? 2 * pr + 1 : 4;
      auto r0 = __builtin_amdgcn_permlane16_swap(k.xr[A].x, k.xr[Bn].x, false, false);
      auto r1 = __builtin_amdgcn_permlane16_swap(k.xr[A].y, k.xr[Bn].y, false, false);
      const u32x4 v = {r0[0], r1[0], r0[1], r1[1]};
      if (pr < 2) stg_sc1(v, rs, xo + (A + (g & 1)) * 1024, x1o);
      else if ((g & 1) == 0) stg_sc1(v, rs, xo + 4 * 1024, x1o);
    }
#pragma unroll
    for (int nb = 0; nb < 5; ++nb) {
      const float a = bflo(k.xr[nb].x), b = bfhi(k.xr[nb].x), c = bflo(k.xr[nb].y), d = bfhi(k.xr[nb].y);
      s1 += (a + b) + (c + d); s2 += (a * a + b * b) + (c * c + d * d);
    }
    s1 = red16_32_add(s1); s2 = red16_32_add(s2);
    if (g == 0) stg2_sc1(u32x2{f2u(s1), f2u(s2)}, rs, (c8 * 2048 + token) * 8, st1o);
    SB();
    STAMP(9);
    WAITV(0); CERT("G2"); CERT("G3");
    WAITL0();
    BAR();
    if (tid == 0) __hip_atomic_store(flags + ((fgb + FG_X1 + m64) * 16 + c8) * 32, 1u, RLX_AGENT);
    STAMP(10); RSTAMP(2);
  }
  {
    PHASE_IDS();
    const int nblk = k.nblk;
    const int ds0 = w < 2 ? 14 * w : 28 + 13 * (w - 2);
    const u32 sG = OFF_WG + p * 286720 + w * 5120;
    Polls pl = {0u, 0u, 0u};
    if (w != 0) { dma_g3<4>(rs, l16, sG - w * 5120, w, lds0); dma_g3<5>(rs, l16, sG - w * 5120, w, lds0); dma_g3<6>(rs, l16, sG - w * 5120, w, lds0); }
    if (w == 0) poll_flags(flags, ((fgb + FG_X1 + 2 * m128 + ((lane >> 3) & 1)) * 16 + (lane & 7)) * 32, lane < 16, lane, 1u, pl);
    BAR();
    STAMP(11); RSTAMP(3);
    u32x2 sa0, sa1, sa2, sa3;
    u32x4 xf[40];
    const u32 xbase = x1o + (4 * m128 + w) * 40960;
    const u32 sso = (4 * h * 2048 + 128 * m128 + 32 * w + tok) * 8;
    sfor<XW>([&](auto I) { constexpr int i = decltype(I)::value; XLOAD(i); });
    constexpr int NWD = GS::NWD;
    float r2 = 0.f;
    constexpr int DG = 4, NBG = 6;
    u32x4 wb[NBG];
    f32x16 aA0, aA1, aA2, aB0, aB1;
    u32 lb1 = l16 + 65536, lb2 = l16 + 131072; asm volatile("" : "+v"(lb1), "+v"(lb2));
    const u32 hbase = (8 * m128 + 2 * w + (tok >> 4)) * 54 * 1024 + ((tok & 15) + 16 * h) * 16;
    const u32 hlast = nblk == 7 ? hbase : 0x7fff0000u;
    const u32 wd_o = OFF_WD + (c8 * 54 + ds0) * 5120;
#define LDSG(F) ((((F) / 20) % 7) * 20480 + ((F) % 20) * 1024)
#define RDG(off) (*(const LAS u32x4*)(lds + ((off) < 65536 ? l16 : (off) < 131072 ? lb1 : lb2) + ((off) & 65535)))
    float hv0 = 0.f;
    u32 hp[4];
#define GU_STAGE(ACC, idx, t) do { \
      if constexpr ((t) < 8) { const float hv_ = mul_s(mul_s(ACC[t], r2), ACC[8 + (t)]); if constexpr ((t) & 1) hp[(t) >> 1] = cvtpk(hv0, hv_); else hv0 = hv_; } \
      if constexpr ((t) == 8) { const u32 fb_ = B0 + (idx); stg_sc1(u32x4{hp[0], hp[1], hp[2], hp[3]}, rs, ((idx) == 6 ? hlast : hbase) + (fb_ & 1) * 512, hfo + (fb_ >> 1) * 1024); } \
    } while (0)
    auto wd_load = [&](auto OC) {
      constexpr int i = decltype(OC)::value, s = i / 5, nb = i % 5;
      if constexpr (i < 24 || i >= 50) ald_a<(i % 4) * 1024>(k.wd_r[s][nb], rs, l16, wd_o + (i / 4) * 4096);
      else ald_v<(i % 4) * 1024>(k.wd_r[s][nb], rs, l16, wd_o + (i / 4) * 4096);
    };
#pragma unroll
    for (int F = 0; F < DG; ++F) wb[F % NBG] = RDG(LDSG(F));
    sfor<280>([&](auto FC) {
      constexpr int F = decltype(FC)::value, Fr = F + DG;
      if constexpr (GS::x_at(F)) XLOAD(F / 3 + XW);
      if constexpr (GS::op(F) >= 0 && GS::op(F) < 1000) dma_g<GS::op(F) / 8, GS::op(F) % 8>(rs, l16, sG, w, lds0);
      if constexpr (GS::op(F) >= 1000 && GS::op(F) < 2000) wd_load(std::integral_constant<int, GS::op(F) - 1000>{});
      if constexpr (GS::op(F) == 3000) ald2_sc1(sa0, rs, sso, st1o);
      if constexpr (GS::op(F) == 3001) ald2_sc1(sa1, rs, sso, st1o + 16384);
      if constexpr (GS::op(F) == 3002) ald2_sc1(sa2, rs, sso, st1o + 32768);
      if constexpr (GS::op(F) == 3003) ald2_sc1(sa3, rs, sso, st1o + 49152);
      if constexpr (Fr == 40) { WAITL0(); BAR(); }
      if constexpr (Fr == 80) { CERT("G4"); CERT("G5"); WAITL0(); BAR(); }
      if constexpr (Fr == 120) { WAITV(GS::upto(F) - GS::upto(GS::slot(7 * 8 + 4))); CERT("G6"); CERT("G7"); WAITL0(); BAR(); STAMP(12); }
      if constexpr (Fr == 160) { GSTAMP(23); WAITV(GS::upto(F) - GS::upto(GS::slot(9 * 8 + 4))); CERT("G8"); CERT("G9"); WAITL0(); BAR(); GSTAMP(24); }
      if constexpr (Fr == 200) { GSTAMP(25); WAITV(GS::upto(F) - GS::upto(GS::slot(11 * 8 + 4))); CERT("G10"); CERT("G11"); BAR(); GSTAMP(26); }
      if constexpr (Fr == 240) { WAITV(GS::upto(F) - GS::upto(GS::slot(13 * 8 + 4))); CERT("G12"); CERT("G13"); BAR(); STAMP(13); }
      if constexpr (Fr < 280) wb[Fr % NBG] = RDG(LDSG(Fr));
      constexpr int s = F < 120 ? F / 3 : F < 200 ? (F - 120) / 2 : (F - 200) / 2, b = F < 120 ? F % 3 : F & 1;
      if constexpr (F < 120 && b == 0 && (s & 1) == 0) waitpin_a2<GS::xwait2(s)>(xf[s], xf[s + 1]);
#define GMMA(ACC) do { if constexpr (s == 0) { if constexpr ((F & 1) == 0) mma32w_0(ACC, wb[F % NBG], xf[s], wb[(F + 1) % NBG]); else mma32_0(ACC, wb[F % NBG], xf[s]); } \
                       else { if constexpr ((F & 1) == 0) mma32w(ACC, wb[F % NBG], xf[s], wb[(F + 1) % NBG]); else mma32(ACC, wb[F % NBG], xf[s]); } } while (0)
      if constexpr (F < 120) { if constexpr (b == 0) GMMA(aA0); else if constexpr (b == 1) GMMA(aA1); else GMMA(aA2); }
      else if constexpr (F < 200) { if constexpr (b == 0) GMMA(aB0); else GMMA(aB1); }
      else { if constexpr (b == 0) GMMA(aA0); else GMMA(aA1); }
      if constexpr (F == 3) poll_pin(pl);
      if constexpr (F == 62) {
        pin2(sa0); pin2(sa1); pin2(sa2); pin2(sa3);
        float rstd, nm;
        stats_finish(red32_add((u2f(sa0.x) + u2f(sa1.x)) + (u2f(sa2.x) + u2f(sa3.x))), red32_add((u2f(sa0.y) + u2f(sa1.y)) + (u2f(sa2.y) + u2f(sa3.y))), nm, rstd);
        r2 = rstd * rstd;
      }
      if constexpr ((F & 1) && F >= 123 && F <= 139) GU_STAGE(aA0, 0, (F - 123) / 2);
      if constexpr ((F & 1) && F >= 141 && F <= 157) GU_STAGE(aA1, 1, (F - 141) / 2);
      if constexpr ((F & 1) && F >= 159 && F <= 175) GU_STAGE(aA2, 2, (F - 159) / 2);
      if constexpr ((F & 1) && F >= 203 && F <= 219) GU_STAGE(aB0, 3, (F - 203) / 2);
      if constexpr ((F & 1) && F >= 221 && F <= 237) GU_STAGE(aB1, 4, (F - 221) / 2);
      SB();
    });
    asm volatile("s_nop 11" : "+v"(aA0), "+v"(aA1));
    sfor<9>([&](auto T) { GU_STAGE(aA0, 5, decltype(T)::value); });
    sfor<9>([&](auto T) { GU_STAGE(aA1, 6, decltype(T)::value); });
    SB();
    STAMP(14);
    WAITV(0);
    WAITL0();
    BAR();
    if (tid == 0) __hip_atomic_store(flags + ((fgb + FG_H + m128) * 16 + p) * 32, 1u, RLX_AGENT);

    STAMP(15); RSTAMP(4);
  }
  {
    PHASE_IDS();
    const int ds0 = w < 2 ? 14 * w : 28 + 13 * (w - 2);
    const u32 sQ = OFF_WQ + p * 163840;
    STAMP(16);
    Polls pl = {0u, 0u, 0u};
    if (w == 0) poll_flags(flags, ((fgb + FG_H + (m64 >> 1)) * 16 + (lane & 15)) * 32, lane < 16, lane, 1u, pl);
    BAR();
    STAMP(17); RSTAMP(5);
    sfor<GS::NWD>([&](auto I) { constexpr int i = decltype(I)::value; if constexpr (i < 24 || i >= 50) pin_a(k.wd_r[i / 5][i % 5]); else pin_v(k.wd_r[i / 5][i % 5]); });
    const u32 wd_o = OFF_WD + (c8 * 54 + ds0) * 5120;
    constexpr int DH = 12;
    u32x4 af[DH];
    const bool s13 = w < 2;
    u32 tbo[4];
#pragma unroll
    for (int tbi = 0; tbi < 4; ++tbi) tbo[tbi] = hfo + ((4 * m64 + ((w + 1 + tbi) & 3)) * 54 + ds0) * 1024;
#define HLOAD(q) ald_a_sc1<((q) % 14 % 4) * 1024>(af[(q) % DH], rs, l16, tbo[(q) / 14] + ((q) % 14 / 4) * 4096)
    sfor<DH>([&](auto I) { constexpr int q = decltype(I)::value; HLOAD(q); });
    sfor<5>([&](auto I) { constexpr int i = 65 + decltype(I)::value; ald_a<(i % 4) * 1024>(k.wd_r[i / 5][i % 5], rs, l16, wd_o + (i / 4) * 4096); });
    f32x4 acc[2][5];
    f32x4 part[3][5];
    sfor<56>([&](auto QC) {
      constexpr int q = decltype(QC)::value, tbi = q / 14, s = q % 14;
      constexpr int yf = (q + DH - 1 < 55 ? q + DH - 1 : 55) - q;
      constexpr int nd = q - 44;
      constexpr int yd = (nd > 0) ? (nd + (nd < 8 ? nd : 8)) : 0;
      constexpr int yw = q < DH ? 5 : 0;
      waitpin_a<yf + yd + yw>(af[q % DH]);
      if constexpr (q == 12) { sfor<5>([&](auto I) { constexpr int i = 65 + decltype(I)::value; pin_a(k.wd_r[i / 5][i % 5]); }); }
      if constexpr (q == 1) poll_pin(pl);
      if constexpr (s == 0) {
#pragma unroll
        for (int nb = 0; nb < 5; ++nb) acc[tbi & 1][nb] = mfma16(k.wd_r[s][nb], af[q % DH], f32x4{0.f, 0.f, 0.f, 0.f});
      } else if (s < 13 || s13) {
#pragma unroll
        for (int nb = 0; nb < 5; ++nb) acc[tbi & 1][nb] = mfma16(k.wd_r[s][nb], af[q % DH], acc[tbi & 1][nb]);
      }
      if constexpr (tbi > 0 && s >= 1 && s < 6) {
        const u32 tbp = (w + tbi) & 3;
        *(LAS f32x4*)(lds + L_RED + ((tbp * 3 + (3 - tbi)) * 5 + (s - 1)) * 1024 + l16) = acc[(tbi - 1) & 1][s - 1];
      }
      if constexpr (q == 50) {
        WAITL0(); BAR();
#pragma unroll
        for (int sl = 0; sl < 3; ++sl)
#pragma unroll
          for (int nb = 0; nb < 5; ++nb) part[sl][nb] = *(const LAS f32x4*)(lds + L_RED + ((w * 3 + sl) * 5 + nb) * 1024 + l16);
      }
      SB();
      if constexpr (q + DH < 56) HLOAD(q + DH);
      if constexpr (q >= 44) {
        constexpr int j0 = nd + (nd < 8 ? nd : 8);
        if constexpr (j0 < 10) dma_q<0, 0, j0>(rs, l16, sQ, w, lds0); else dma_q<1, 1, j0 - 10>(rs, l16, sQ, w, lds0);
        if constexpr (nd < 8) {
          constexpr int j1 = j0 + 1;
          if constexpr (j1 < 10) dma_q<0, 0, j1>(rs, l16, sQ, w, lds0); else dma_q<1, 1, j1 - 10>(rs, l16, sQ, w, lds0);
        }
      }
      SB();
    });
    STAMP(18);
#pragma unroll
    for (int nb = 0; nb < 5; ++nb) {
      const f32x4 v = (acc[1][nb] + part[0][nb]) + (part[1][nb] + part[2][nb]);
      const u32 a = cvtpk(bflo(k.xr[nb].x) + v[0], bfhi(k.xr[nb].x) + v[1]), b = cvtpk(bflo(k.xr[nb].y) + v[2], bfhi(k.xr[nb].y) + v[3]);
      k.xr[nb] = u32x2{a, b};
    }
    const u32 token = 64 * m64 + 16 * w + t16;
    STAMP(19);
    if (it == 0) {
      float s1 = 0.f, s2 = 0.f;
      const u32 xo = ((token >> 5) * 40 + 5 * c8) * 1024 + ((token & 31) + 32 * (g >> 1)) * 16;
#pragma unroll
      for (int pr = 0; pr < 3; ++pr) {
        const int A = 2 * pr, Bn = pr < 2 ? 2 * pr + 1 : 4;
        auto r0 = __builtin_amdgcn_permlane16_swap(k.xr[A].x, k.xr[Bn].x, false, false);
        auto r1 = __builtin_amdgcn_permlane16_swap(k.xr[A].y, k.xr[Bn].y, false, false);
        const u32x4 v = {r0[0], r1[0], r0[1], r1[1]};
        if (pr < 2) stg_sc1(v, rs, xo + (A + (g & 1)) * 1024, OFF_XF + 2 * SZ_XF);
        else if ((g & 1) == 0) stg_sc1(v, rs, xo + 4 * 1024, OFF_XF + 2 * SZ_XF);
      }
#pragma unroll
      for (int nb = 0; nb < 5; ++nb) {
        const float a = bflo(k.xr[nb].x), b = bfhi(k.xr[nb].x), c = bflo(k.xr[nb].y), d = bfhi(k.xr[nb].y);
        s1 += (a + b) + (c + d); s2 += (a * a + b * b) + (c * c + d * d);
      }
      s1 = red16_32_add(s1); s2 = red16_32_add(s2);
      if (g == 0) stg2_sc1(u32x2{f2u(s1), f2u(s2)}, rs, (c8 * 2048 + token) * 8, OFF_ST + 2 * SZ_ST);
      SB();
      WAITV(0);
      WAITL0();
      BAR();
      if (tid == 0) __hip_atomic_store(flags + ((FG_X2 + m64) * 16 + c8) * 32, 1u, RLX_AGENT);
      if (w != 0) dma_q2w3(rs, l16, sQ, w, lds0);
    } else {
      const u32 oo = token * 1280 + (80 * c8 + 8 * (g >> 1)) * 2;
#pragma unroll
      for (int pr = 0; pr < 3; ++pr) {
        const int A = 2 * pr, Bn = pr < 2 ? 2 * pr + 1 : 4;
        auto r0 = __builtin_amdgcn_permlane16_swap(k.xr[A].x, k.xr[Bn].x, false, false);
        auto r1 = __builtin_amdgcn_permlane16_swap(k.xr[A].y, k.xr[Bn].y, false, false);
        const u32x4 v = {r0[0], r1[0], r0[1], r1[1]};
        if (pr < 2) __builtin_amdgcn_raw_buffer_store_b128(v, k.rso, oo + (A + (g & 1)) * 32, 0, 0);
        else if ((g & 1) == 0) __builtin_amdgcn_raw_buffer_store_b128(v, k.rso, oo + 4 * 32, 0, 0);
      }
    }
    STAMP(20);
  }
}

__global__ __launch_bounds__(256, 1) void main_kernel(char* __restrict__ ws, u16* __restrict__ out) {
  extern __shared__ __attribute__((aligned(16))) char smem[];
  K k;
  k.lds = (LAS char*)smem;
  k.lds0 = (u32)(size_t)k.lds;
  k.tid = threadIdx.x; k.lane = k.tid & 63; k.w = __builtin_amdgcn_readfirstlane(k.tid >> 6);
  k.rs = __builtin_amdgcn_make_buffer_rsrc(ws, 0, WS_USED, 0x00020000);
  k.rso = __builtin_amdgcn_make_buffer_rsrc(out, 0, 2048 * 640 * 2, 0x00020000);
  k.flags = (GAS u32*)ws;
  const int bid = blockIdx.x, xc = bid & 7, j = bid >> 3, mg = xc >> 1, half = xc & 1;
  k.m128 = 4 * mg + (j >> 3); k.p = 8 * half + (j & 7);
  k.m64 = 8 * mg + (j >> 2); k.c8 = 4 * half + (j & 3);
  const int p7 = j & 7;
  k.B0 = 54 * half + (p7 < 6 ? 7 * p7 : 42 + 6 * (p7 - 6));
  k.nblk = p7 < 6 ? 7 : 6;
  {
    PHASE_IDS();
    const u32 sQ = OFF_WQ + p * 163840;
    sfor<10>([&](auto I) { dma_q<0, 0, decltype(I)::value>(rs, l16, sQ, w, lds0); });
    sfor<10>([&](auto I) { dma_q<1, 1, decltype(I)::value>(rs, l16, sQ, w, lds0); });
    sfor<10>([&](auto I) { dma_q<2, 2, decltype(I)::value>(rs, l16, sQ, w, lds0); });
    const u32 token = 64 * m64 + 16 * w + t16;
    const u32 o = ((token >> 5) * 40 + 5 * c8) * 1024 + ((token & 31) + 32 * (g >> 1)) * 16 + (g & 1) * 8;
#pragma unroll
    for (int nb = 0; nb < 5; ++nb) ald2(k.xr[nb], rs, o, OFF_XF + nb * 1024);
  }
#pragma nounroll
  for (int it = 0; it < 2; ++it) { k.it = it; iteration(k); }
}

extern "C" void kernel_launch(void* const* d_in, const int* in_sizes, int n_in, void* d_out, int out_size, void* d_ws, size_t ws_size,
                              hipStream_t stream) {
  const u16* x0 = (const u16*)d_in[0];
  const u16* wqkv = (const u16*)d_in[1];
  const u16* wo = (const u16*)d_in[2];
  const u16* wg = (const u16*)d_in[4];
  const u16* wu = (const u16*)d_in[5];
  const u16* wd = (const u16*)d_in[6];
  (void)hipFuncSetAttribute((const void*)main_kernel, hipFuncAttributeMaxDynamicSharedMemorySize, LDS_BYTES);
  prep_kernel<<<PREP_UNITS, 512, 0, stream>>>(x0, wqkv, wo, wg, wu, wd, (char*)d_ws);
  main_kernel<<<256, 256, LDS_BYTES, stream>>>((char*)d_ws, (u16*)d_out);
}
```

```cpp
#include <hip/hip_runtime.h>
#include <stdint.h>
#include <utility>

#define LAS __attribute__((address_space(3)))
#define GAS __attribute__((address_space(1)))
#define DI __device__ __forceinline__
typedef unsigned u32;
typedef unsigned short u16;
typedef u32 u32x4 __attribute__((ext_vector_type(4)));
typedef u32 u32x2 __attribute__((ext_vector_type(2)));
typedef float f32x2 __attribute__((ext_vector_type(2)));
typedef float f32x4 __attribute__((ext_vector_type(4)));
typedef float f32x16 __attribute__((ext_vector_type(16)));
typedef short s16x8 __attribute__((ext_vector_type(8)));
typedef short s16x4 __attribute__((ext_vector_type(4)));
typedef __bf16 bf16x2 __attribute__((ext_vector_type(2)));
typedef __amdgpu_buffer_rsrc_t rsrc_t;

constexpr u32 FLAG_BYTES = 193 * 2048;
constexpr u32 OFF_WQ = 409600;
constexpr u32 OFF_WO = OFF_WQ + 16 * 163840;
constexpr u32 OFF_WG = OFF_WO + 8 * 20 * 5 * 1024;
constexpr u32 OFF_WD = OFF_WG + 16 * 286720;
constexpr u32 OFF_CSQ = OFF_WD + 8 * 54 * 5 * 1024;
constexpr u32 OFF_CSG = OFF_CSQ + 16384;
constexpr u32 SZ_XF = 64 * 40 * 1024;
constexpr u32 OFF_XF = OFF_CSG + 28672;
constexpr u32 SZ_ST = 8 * 2048 * 8;
constexpr u32 OFF_ST = OFF_XF + 4 * SZ_XF;
constexpr u32 SZ_OF = 128 * 20 * 1024;
constexpr u32 OFF_OF = OFF_ST + 4 * SZ_ST;
constexpr u32 SZ_HF = 128 * 54 * 1024;
constexpr u32 OFF_HF = OFF_OF + 2 * SZ_OF;
constexpr u32 WS_USED = OFF_HF + 2 * SZ_HF;
constexpr u32 FG_O = 0, FG_X1 = 16, FG_H = 48, FG_X2 = 64, FG_PER_IT = 96;
constexpr u32 FG_TMO = 2 * FG_PER_IT;

constexpr u32 L_HALO = 122880;
constexpr u32 L_KST = 40960;
constexpr u32 L_VST = L_KST + 144 * 80;
constexpr u32 L_QST = L_VST + 144 * 80;
constexpr u32 L_RED = 100352;
constexpr u32 L_H4 = 143360;
constexpr u32 LDS_BYTES = 163840;

constexpr u32 AUX_SC1 = 16;
#define RLX_AGENT __ATOMIC_RELAXED, __HIP_MEMORY_SCOPE_AGENT
#define SB() __builtin_amdgcn_sched_barrier(0)
#define MEMFENCE() asm volatile("" ::: "memory")
#define WAITV(n) asm volatile("s_waitcnt vmcnt(%0)" ::"i"(n) : "memory")
#define WAITL0() asm volatile("s_waitcnt lgkmcnt(0)" ::: "memory")
#define BAR() do { MEMFENCE(); __builtin_amdgcn_s_barrier(); MEMFENCE(); } while (0)
#define CERT(tag) asm volatile("; CERT " tag ::: "memory")

template <int... I, class Fn> DI void sfor_impl(std::integer_sequence<int, I...>, Fn&& f) { (f(std::integral_constant<int, I>{}), ...); }
template <int N, class Fn> DI void sfor(Fn&& f) { sfor_impl(std::make_integer_sequence<int, N>{}, f); }
DI f32x16 mfma32(u32x4 a, u32x4 b, f32x16 c) { return __builtin_amdgcn_mfma_f32_32x32x16_bf16(__builtin_bit_cast(s16x8, a), __builtin_bit_cast(s16x8, b), c, 0, 0, 0); }
DI f32x4 mfma16(u32x4 a, u32x4 b, f32x4 c) { return __builtin_amdgcn_mfma_f32_16x16x32_bf16(__builtin_bit_cast(s16x8, a), __builtin_bit_cast(s16x8, b), c, 0, 0, 0); }
DI u32 cvtpk(float lo, float hi) { f32x2 v = {lo, hi}; bf16x2 b = __builtin_convertvector(v, bf16x2); return __builtin_bit_cast(u32, b); }
DI float u2f(u32 v) { return __builtin_bit_cast(float, v); }
DI u32 f2u(float v) { return __builtin_bit_cast(u32, v); }
DI float bflo(u32 v) { return __builtin_bit_cast(float, v << 16); }
DI float bfhi(u32 v) { return __builtin_bit_cast(float, v & 0xffff0000u); }
DI float fma_s(float a, float b, float c) { float r; asm("v_fma_f32 %0, %1, %2, %3" : "=v"(r) : "v"(a), "v"(b), "v"(c)); return r; }
DI float mul_s(float a, float b) { float r; asm("v_mul_f32_e32 %0, %1, %2" : "=v"(r) : "v"(a), "v"(b)); return r; }
DI float add_s(float a, float b) { float r; asm("v_add_f32_e32 %0, %1, %2" : "=v"(r) : "v"(a), "v"(b)); return r; }
DI float max_s(float a, float b) { float r; asm("v_max_f32_e32 %0, %1, %2" : "=v"(r) : "v"(a), "v"(b)); return r; }
DI float sel_s(float f, float t, unsigned long long m) { float r; asm("v_cndmask_b32_e64 %0, %1, %2, %3" : "=v"(r) : "v"(f), "v"(t), "s"(m)); return r; }
DI float red16_32_add(float v) {
  auto a = __builtin_amdgcn_permlane16_swap(f2u(v), f2u(v), false, false);
  v = u2f(a[0]) + u2f(a[1]);
  auto b = __builtin_amdgcn_permlane32_swap(f2u(v), f2u(v), false, false);
  return u2f(b[0]) + u2f(b[1]);
}
DI float red16_32_max(float v) {
  auto a = __builtin_amdgcn_permlane16_swap(f2u(v), f2u(v), false, false);
  v = __builtin_fmaxf(u2f(a[0]), u2f(a[1]));
  auto b = __builtin_amdgcn_permlane32_swap(f2u(v), f2u(v), false, false);
  return __builtin_fmaxf(u2f(b[0]), u2f(b[1]));
}
DI float red32_add(float v) {
  auto b = __builtin_amdgcn_permlane32_swap(f2u(v), f2u(v), false, false);
  return u2f(b[0]) + u2f(b[1]);
}
DI void stg_sc1(u32x4 v, rsrc_t rs, u32 voff, u32 soff) { __builtin_amdgcn_raw_buffer_store_b128(v, rs, voff, soff, AUX_SC1); }
DI void stg2_sc1(u32x2 v, rsrc_t rs, u32 voff, u32 soff) { __builtin_amdgcn_raw_buffer_store_b64(v, rs, voff, soff, AUX_SC1); }

template <int IMM> DI void ald_v_sc1(u32x4& d, rsrc_t rs, u32 voff, u32 soff) {
  asm volatile("buffer_load_dwordx4 %0, %1, %2, %3 offen offset:%4 sc1" : "=v"(d) : "v"(voff), "s"(rs), "s"(soff), "i"(IMM) : "memory");
}
template <int IMM> DI void ald_a(u32x4& d, rsrc_t rs, u32 voff, u32 soff) {
  asm volatile("buffer_load_dwordx4 %0, %1, %2, %3 offen offset:%4" : "=a"(d) : "v"(voff), "s"(rs), "s"(soff), "i"(IMM) : "memory");
}
template <int IMM> DI void ald_v(u32x4& d, rsrc_t rs, u32 voff, u32 soff) {
  asm volatile("buffer_load_dwordx4 %0, %1, %2, %3 offen offset:%4" : "=v"(d) : "v"(voff), "s"(rs), "s"(soff), "i"(IMM) : "memory");
}
DI void ald2_sc1(u32x2& d, rsrc_t rs, u32 voff, u32 soff) {
  asm volatile("buffer_load_dwordx2 %0, %1, %2, %3 offen sc1" : "=v"(d) : "v"(voff), "s"(rs), "s"(soff) : "memory");
}
DI void ald2(u32x2& d, rsrc_t rs, u32 voff, u32 soff) {
  asm volatile("buffer_load_dwordx2 %0, %1, %2, %3 offen" : "=v"(d) : "v"(voff), "s"(rs), "s"(soff) : "memory");
}
DI void ald1(u32& d, rsrc_t rs, u32 voff, u32 soff) {
  asm volatile("buffer_load_dword %0, %1, %2, %3 offen" : "=v"(d) : "v"(voff), "s"(rs), "s"(soff) : "memory");
}
template <int IMM> DI void ald_a_sc1(u32x4& d, rsrc_t rs, u32 voff, u32 soff) {
  asm volatile("buffer_load_dwordx4 %0, %1, %2, %3 offen offset:%4 sc1" : "=a"(d) : "v"(voff), "s"(rs), "s"(soff), "i"(IMM) : "memory");
}
template <int N> DI void waitpin_a(u32x4& r) { asm volatile("s_waitcnt vmcnt(%1)" : "+a"(r) : "i"(N) : "memory"); }
template <int N> DI void waitpin_a2(u32x4& r0, u32x4& r1) { asm volatile("s_waitcnt vmcnt(%2)" : "+a"(r0), "+a"(r1) : "i"(N) : "memory"); }
DI void mma32(f32x16& acc, const u32x4& wv, const u32x4& xa) { asm volatile("v_mfma_f32_32x32x16_bf16 %0, %1, %2, %0" : "+v"(acc) : "v"(wv), "a"(xa)); }
DI void mma32_0(f32x16& acc, const u32x4& wv, const u32x4& xa) { asm volatile("v_mfma_f32_32x32x16_bf16 %0, %1, %2, 0" : "=&v"(acc) : "v"(wv), "a"(xa)); }
DI void mma32w(f32x16& acc, const u32x4& wv, const u32x4& xa, const u32x4& wn) { asm volatile("v_mfma_f32_32x32x16_bf16 %0, %1, %2, %0" : "+v"(acc) : "v"(wv), "a"(xa), "v"(wn)); }
DI void mma32w_0(f32x16& acc, const u32x4& wv, const u32x4& xa, const u32x4& wn) { asm volatile("v_mfma_f32_32x32x16_bf16 %0, %1, %2, 0" : "=&v"(acc) : "v"(wv), "a"(xa), "v"(wn)); }
DI void mma16(f32x4& acc, const u32x4& av, const u32x4& bv) { asm volatile("v_mfma_f32_16x16x32_bf16 %0, %1, %2, %0" : "+v"(acc) : "v"(av), "v"(bv)); }
DI void mma16_0(f32x4& acc, const u32x4& av, const u32x4& bv) { asm volatile("v_mfma_f32_16x16x32_bf16 %0, %1, %2, 0" : "=&v"(acc) : "v"(av), "v"(bv)); }
DI void pin_vv(u32x4& a, u32x4& b) { asm volatile("" : "+v"(a), "+v"(b)); }
template <int N> DI void waitpin_v(u32x4& r) { asm volatile("s_waitcnt vmcnt(%1)" : "+v"(r) : "i"(N) : "memory"); }
DI void pin_v(u32x4& r) { asm volatile("" : "+v"(r)::"memory"); }
DI void pin_a(u32x4& r) { asm volatile("" : "+a"(r)::"memory"); }
DI void pin2(u32x2& r) { asm volatile("" : "+v"(r)::"memory"); }
DI void pin1(u32& r) { asm volatile("" : "+v"(r)::"memory"); }
#define DMA_F(TAG, IMM, SC, rs, voff, soff, m0v) \
  asm volatile("s_mov_b32 m0, %2\n\ts_nop 0\n\tbuffer_load_dwordx4 %0, %1, %3 offen offset:%4 " SC "lds ; DMA " TAG ::"v"(voff), "s"(rs), "s"(m0v), "s"(soff), "i"(IMM) : "memory")
#define DMA_N(TAG, IMM, SC, rs, voff, soff) \
  asm volatile("buffer_load_dwordx4 %0, %1, %2 offen offset:%3 " SC "lds ; DMA " TAG ::"v"(voff), "s"(rs), "s"(soff), "i"(IMM) : "memory")

constexpr int PREP_UNITS = 84 + 20 + 60 + 64;
__global__ __launch_bounds__(512) void prep_kernel(const u16* __restrict__ x0, const u16* __restrict__ wqkv, const u16* __restrict__ wo,
                                                   const u16* __restrict__ wg, const u16* __restrict__ wu, const u16* __restrict__ wd, char* __restrict__ ws) {
  __shared__ __attribute__((aligned(16))) char lds[81920 + 1024];
  const int tid = threadIdx.x, l = tid & 63, w = __builtin_amdgcn_readfirstlane(tid >> 6);
  const int u = blockIdx.x;
  { const int i = u * 512 + tid; if (i < (int)(FLAG_BYTES / 128)) ((u32*)ws)[i * 32] = 0u; }
  const int gam = l >> 4, q = (l & 15) >> 2, pp = l & 3;
  if (u < 84) {
    const int type = u < 30 ? 0 : u < 57 ? 1 : 2, cg = u < 30 ? u : u < 57 ? u - 30 : u - 57;
    const u16* src = type == 0 ? wqkv : type == 1 ? wg : wu;
    const int pitch = type == 0 ? 1920 : 1728;
#pragma unroll
    for (int i = 0; i < 10; ++i) {
      const int r = 8 * i + w;
      const u16* g = src + (size_t)(8 * r + (l >> 3)) * pitch + 64 * cg + (l & 7) * 8;
      __builtin_amdgcn_global_load_lds((const GAS u32*)g, (LAS u32*)(lds + r * 1024), 16, 0, 0);
    }
    const int c = 64 * cg + l;
    u32 dbase; int dstep;
    if (type == 0) {
      const int sec = c / 640, cc = c % 640, p = cc / 40, r40 = cc % 40;
      const int n = (sec == 1 ? 0 : sec == 2 ? 40 : 80) + r40;
      const int b = n >> 5, nl = n & 31;
      dbase = OFF_WQ + p * 163840 + (b < 3 ? b * 1024 : 122880) + nl * 16;
      dstep = b < 3 ? 3072 : 1024;
    } else {
      const int fb = c >> 4, nl = (c & 15) + 16 * (type - 1);
      const int half = fb / 54, rem = fb % 54;
      const int sl_ = 8 * half + (rem < 42 ? rem / 7 : 6 + (rem - 42) / 6), ix = rem < 42 ? rem % 7 : (rem - 42) % 6;
      dbase = OFF_WG + sl_ * 286720 + (ix < 3 ? ix * 1024 : ix < 5 ? 122880 + (ix - 3) * 1024 : 204800 + (ix - 5) * 1024) + nl * 16;
      dstep = ix < 3 ? 3072 : 2048;
    }
    __syncthreads();
    u32x4 sl[10];
    float csum = 0.f;
#pragma unroll
    for (int i = 0; i < 10; ++i) {
      const int o = 8 * i + w;
      const u32x2 a = __builtin_bit_cast(u32x2, __builtin_amdgcn_ds_read_tr16_b64_v4i16((LAS s16x4*)(lds + (8 * o + q) * 128 + gam * 32 + pp * 8)));
      const u32x2 b2 = __builtin_bit_cast(u32x2, __builtin_amdgcn_ds_read_tr16_b64_v4i16((LAS s16x4*)(lds + (8 * o + 4 + q) * 128 + gam * 32 + pp * 8)));
      sl[i] = u32x4{a.x, a.y, b2.x, b2.y};
    }
#pragma unroll
    for (int i = 0; i < 10; ++i)
#pragma unroll
      for (int j = 0; j < 4; ++j) csum += bflo(sl[i][j]) + bfhi(sl[i][j]);
    __syncthreads();
    float* red = (float*)lds;
    red[w * 64 + l] = csum;
    __syncthreads();
    const float mean = (((red[l] + red[64 + l]) + (red[128 + l] + red[192 + l])) + ((red[256 + l] + red[320 + l]) + (red[384 + l] + red[448 + l]))) * (1.0f / 640.0f);
#pragma unroll
    for (int i = 0; i < 10; ++i) {
      const int o = 8 * i + w;
      u32x4 v;
#pragma unroll
      for (int j = 0; j < 4; ++j) v[j] = cvtpk(bflo(sl[i][j]) - mean, bfhi(sl[i][j]) - mean);
      *(u32x4*)(ws + dbase + (o >> 1) * dstep + (o & 1) * 512) = v;
    }
  } else if (u < 164) {
    const bool iswo = u < 104;
    const int v = iswo ? u - 84 : u - 104, cg = iswo ? v >> 1 : v / 6, kk = iswo ? v & 1 : v % 6;
    const u16* src = iswo ? wo : wd;
    const int nrows = iswo ? 320 : 288, row0 = kk * nrows, noct = nrows / 8;
#pragma unroll
    for (int i = 0; i < 5; ++i) {
      const int r = 8 * i + w;
      if (r < noct) {
        const u16* g = src + (size_t)(row0 + 8 * r + (l >> 3)) * 640 + 64 * cg + (l & 7) * 8;
        __builtin_amdgcn_global_load_lds((const GAS u32*)g, (LAS u32*)(lds + r * 1024), 16, 0, 0);
      }
    }
    const int c = 64 * cg + l, c8 = c / 80, nb = (c % 80) >> 4, n = c & 15;
    const u32 dbase = (iswo ? OFF_WO + c8 * 20 * 5120 : OFF_WD + c8 * 54 * 5120) + nb * 1024 + n * 16;
    __syncthreads();
#pragma unroll
    for (int i = 0; i < 5; ++i) {
      const int o = 8 * i + w;
      if (o < noct) {
        int r0, r1; u32 dst;
        if (iswo) {
          const int pl = o / 5, ii = o % 5, sg = 40 * kk + o;
          r0 = 40 * pl + 4 * ii + q; r1 = r0 + 20;
          dst = dbase + (sg >> 2) * 5120 + (sg & 3) * 256;
        } else {
          const int s_ = o >> 2, g = o & 3;
          r0 = 32 * s_ + 16 * (g >> 1) + 4 * (g & 1) + q; r1 = r0 + 8;
          dst = dbase + (9 * kk + s_) * 5120 + g * 256;
        }
        const u32x2 a = __builtin_bit_cast(u32x2, __builtin_amdgcn_ds_read_tr16_b64_v4i16((LAS s16x4*)(lds + r0 * 128 + gam * 32 + pp * 8)));
        const u32x2 b2 = __builtin_bit_cast(u32x2, __builtin_amdgcn_ds_read_tr16_b64_v4i16((LAS s16x4*)(lds + r1 * 128 + gam * 32 + pp * 8)));
        *(u32x4*)(ws + dst) = u32x4{a.x, a.y, b2.x, b2.y};
      }
    }
  } else {
    const int rb = u - 164;
#pragma unroll
    for (int i = 0; i < 5; ++i) {
      const int cidx = 64 * (8 * i + w) + l, row = cidx / 80, cc = cidx % 80;
      const u16* g = x0 + (size_t)(32 * rb + row) * 640 + ((cc ^ (row & 7)) * 8);
      __builtin_amdgcn_global_load_lds((const GAS u32*)g, (LAS u32*)(lds + (8 * i + w) * 1024), 16, 0, 0);
    }
    __syncthreads();
    const int tok = l & 31, h = l >> 5;
    float s1 = 0.f, s2 = 0.f;
#pragma unroll
    for (int i = 0; i < 5; ++i) {
      const int s = 5 * w + i, cc = 2 * s + h;
      const u32x4 v = *(const u32x4*)(lds + tok * 1280 + ((cc ^ (tok & 7)) * 16));
      *(u32x4*)(ws + OFF_XF + (rb * 40 + s) * 1024 + l * 16) = v;
#pragma unroll
      for (int j = 0; j < 4; ++j) { float a = bflo(v[j]), b = bfhi(v[j]); s1 += a + b; s2 += a * a + b * b; }
    }
    s1 = red32_add(s1); s2 = red32_add(s2);
    if (l < 32) {
      f32x2* st = (f32x2*)(ws + OFF_ST);
      st[w * 2048 + 32 * rb + tok] = f32x2{s1, s2};
    }
  }
}

#define STAMP(i) do {} while (0)
#define RSTAMP(i) do {} while (0)
#define GSTAMP(i) STAMP(i)
#define QSTAMP(i) do {} while (0)
constexpr u32 OFF_STAMP = WS_USED + 4096;
constexpr u32 kSpinCap = 1u << 16;

struct K {
  rsrc_t rs, rso;
  LAS char* lds;
  u32 lds0;
  GAS u32* flags;
  int lane, w, m128, p, m64, c8, B0, nblk, tid, it;
  u32x2 xr[5];
  u32x4 wo_r[5][5];
  u32x4 wd_r[14][5];
};

#define PHASE_IDS() int lane = k.lane; asm volatile("" : "+v"(lane)); \
  int it = k.it; asm volatile("" : "+s"(it)); \
  const u32 xin = OFF_XF + it * 2 * SZ_XF, stin = OFF_ST + it * 2 * SZ_ST, x1o = xin + SZ_XF, st1o = stin + SZ_ST; \
  const u32 ofo = OFF_OF + it * SZ_OF, hfo = OFF_HF + it * SZ_HF, fgb = it * FG_PER_IT; \
  (void)xin; (void)stin; (void)x1o; (void)st1o; (void)ofo; (void)hfo; (void)fgb; \
  int w = k.w, m128 = k.m128, p = k.p, m64 = k.m64, c8 = k.c8, B0 = k.B0; \
  asm volatile("" : "+s"(w), "+s"(m128), "+s"(p), "+s"(m64), "+s"(c8), "+s"(B0)); \
  const u32 l16 = lane * 16; const int tok = lane & 31, h = lane >> 5, t16 = lane & 15, g = lane >> 4; \
  const rsrc_t rs = k.rs; LAS char* const lds = k.lds; const u32 lds0 = k.lds0; \
  (void)tok; (void)h; (void)t16; (void)g; (void)l16; (void)m128; (void)p; (void)m64; (void)c8; (void)B0; (void)lds; (void)lds0; (void)rs

DI void poll_flags(GAS u32* flags, u32 fo, bool act, int lane) {
  for (u32 spins = 0;;) {
    u32 v = act ? __hip_atomic_load(flags + fo, RLX_AGENT) : 1u;
    if (__all(v == 1u)) break;
    __builtin_amdgcn_s_sleep(1);
    if (++spins > kSpinCap) {
      if (lane == 0) __hip_atomic_store(flags + FG_TMO * 512, 1u, RLX_AGENT);
      break;
    }
    if ((spins & 255u) == 0u && __builtin_amdgcn_readfirstlane(__hip_atomic_load(flags + FG_TMO * 512, RLX_AGENT)) != 0u) break;
  }
}
#define POLL_GAP 2
struct Polls { u32 a, b, c; };
DI void poll_issue(u32& d, const GAS u32* p) { asm volatile("global_load_dword %0, %1, off sc1 ; POLL" : "=v"(d) : "v"(p) : "memory"); }
DI bool poll_check(u32& d, bool act, u32 want) { asm volatile("s_waitcnt vmcnt(2) ; POLLWAIT" : "+v"(d)::"memory"); return __all(!act || d == want); }
DI void poll_flags_p(GAS u32* flags, u32 fo, bool act, int lane, u32 want, Polls& pl) {
  const GAS u32* p = flags + (act ? fo : FG_TMO * 512);
  asm volatile("; POLL_BEGIN" ::: "memory");
  poll_issue(pl.a, p); __builtin_amdgcn_s_sleep(POLL_GAP); poll_issue(pl.b, p); __builtin_amdgcn_s_sleep(POLL_GAP); poll_issue(pl.c, p);
  for (u32 spins = 0;;) {
    if (poll_check(pl.a, act, want)) break;
    poll_issue(pl.a, p);
    if (poll_check(pl.b, act, want)) break;
    poll_issue(pl.b, p);
    if (poll_check(pl.c, act, want)) break;
    poll_issue(pl.c, p);
    if (++spins > kSpinCap) {
      if (lane == 0) __hip_atomic_store(flags + FG_TMO * 512, want, RLX_AGENT);
      break;
    }
    if ((spins & 63u) == 0u && (u32)__builtin_amdgcn_readfirstlane(__hip_atomic_load(flags + FG_TMO * 512, RLX_AGENT)) == want) break;
  }
  asm volatile("; POLL_END" ::: "memory");
}
DI void poll_pin(Polls& pl) { asm volatile("; POLL_PIN" : "+v"(pl.a), "+v"(pl.b), "+v"(pl.c)::"memory"); }
DI void stats_finish(float s1, float s2, float& nmean, float& rstd) {
  const float mean = s1 * (1.0f / 640.0f);
  const float var = __builtin_fmaxf(s2 * (1.0f / 640.0f) - mean * mean, 0.f);
  nmean = -mean; rstd = __builtin_amdgcn_rsqf(var + 1e-5f);
}

template <int C, int SLOT, int I> DI void dma_q(rsrc_t rs, u32 l16, u32 sQ, int w, u32 lds0) {
#define Q_EMIT(TAG, FIRST, IMM, SO, MO) do { if constexpr (FIRST) DMA_F(TAG, IMM, "", rs, l16, SO, MO); else DMA_N(TAG, IMM, "", rs, l16, SO); } while (0)
  if constexpr (C < 3) {
    constexpr int grp = I / 4, imm = (I % 4) * 1024;
    const u32 so = sQ + w * 10240 + C * 40960 + grp * 4096, mo = lds0 + SLOT * 40960 + w * 10240 + grp * 4096;
    if constexpr (C == 0) Q_EMIT("Q0", I % 4 == 0, imm, so, mo); else if constexpr (C == 1) Q_EMIT("Q1", I % 4 == 0, imm, so, mo); else Q_EMIT("Q2", I % 4 == 0, imm, so, mo);
  } else {
    constexpr int hf = I / 5, ii = I % 5, grp = ii / 4, imm = (ii % 4) * 1024;
    const u32 so = sQ + w * 5120 + 122880 + hf * 20480 + grp * 4096, mo = lds0 + SLOT * 40960 + w * 5120 + hf * 20480 + grp * 4096;
    if constexpr (hf == 0) Q_EMIT("Q3a", ii % 4 == 0, imm, so, mo); else Q_EMIT("Q3b", ii % 4 == 0, imm, so, mo);
  }
#undef Q_EMIT
}
template <int HH, int I> DI void dma_g(rsrc_t rs, u32 l16, u32 sG, int w, u32 lds0) {
  constexpr int grp = I / 4, imm = (I % 4) * 1024;
  const u32 so = sG + HH * 20480 + grp * 4096, mo = lds0 + w * 5120 + (HH % 7) * 20480 + grp * 4096;
#define G_CASE(n) if constexpr (HH == n) { if constexpr (I % 4 == 0) DMA_F("G" #n, imm, "", rs, l16, so, mo); else DMA_N("G" #n, imm, "", rs, l16, so); }
  G_CASE(0) G_CASE(1) G_CASE(2) G_CASE(3) G_CASE(4) G_CASE(5) G_CASE(6) G_CASE(7) G_CASE(8) G_CASE(9) G_CASE(10) G_CASE(11) G_CASE(12) G_CASE(13)
#undef G_CASE
}

template <int HH> DI void dma_g3(rsrc_t rs, u32 l16, u32 sG0, int w, u32 lds0) {
  const u32 f0 = 7 * (w - 1);
  const u32 so = sG0 + HH * 20480 + f0 * 1024, mo = lds0 + (HH % 7) * 20480 + f0 * 1024;
#define G3_CASE(n) if constexpr (HH == n) { DMA_F("G" #n, 0, "", rs, l16, so, mo); DMA_N("G" #n, 1024, "", rs, l16, so); DMA_N("G" #n, 2048, "", rs, l16, so); DMA_N("G" #n, 3072, "", rs, l16, so); \
    DMA_F("G" #n, 0, "", rs, l16, so + 4096, mo + 4096); DMA_N("G" #n, 1024, "", rs, l16, so + 4096); if (w < 3) DMA_N("G" #n, 2048, "", rs, l16, so + 4096); }
  G3_CASE(2) G3_CASE(3) G3_CASE(4) G3_CASE(5) G3_CASE(6)
#undef G3_CASE
}
constexpr int XW = 10;
struct QS {
  static constexpr bool x_at(int f) { return f < 120 && f % 3 == 0 && f / 3 + XW < 40; }
  static constexpr int aux_at(int f) { return (f < 32 && f % 3 == 1) ? f / 3 : -1; }
  static constexpr int q3_at(int f) { return (f >= 77 && f <= 104 && f % 3 == 2) ? (f - 77) / 3 : -1; }
  static constexpr int wo_at(int f) {
    if (f >= 34 && f <= 67 && f % 3 == 1) return (f - 34) / 3;
    if (f >= 121 && f <= 157 && (f - 121) % 3 == 0) return 12 + (f - 121) / 3;
    return -1;
  }
  static constexpr int upto(int F) { int n = 0; for (int f = 0; f <= F; ++f) n += x_at(f) + (aux_at(f) >= 0) + (q3_at(f) >= 0) + (wo_at(f) >= 0); return n; }
  static constexpr int xidx(int i) { return i < XW ? i : XW + upto(3 * (i - XW)) - 1; }
  static constexpr int xwait2(int s) { return XW + upto(3 * s) - 1 - xidx(s + 1); }
};
struct GS {
  static constexpr int NWD = 65;
  static constexpr bool x_at(int f) { return f < 120 && f % 3 == 0 && f / 3 + XW < 40; }
  static constexpr int op(int F) {
    if (F <= 10 && F % 3 == 1) return 3000 + F / 3;
    if (F >= 40 && F <= 67 && F % 3 == 1) return (7 + (F - 40) / 15) * 8 + ((F - 40) % 15) / 3;
    if (F >= 79 && F <= 106 && F % 3 == 1) return (9 + (F - 79) / 15) * 8 + ((F - 79) % 15) / 3;
    if (F < 120 || (F & 1)) return -1;
    if (F < 140) return (11 + (F - 120) / 10) * 8 + ((F - 120) % 10) / 2;
    if (F < 160) return 1000 + (F - 140) / 2;
    if (F < 170) return 13 * 8 + (F - 160) / 2;
    if (F < 280) return 1010 + (F - 170) / 2;
    return -1;
  }
  static constexpr bool st_at(int F) { return F == 139 || F == 157 || F == 175 || F == 219 || F == 237; }
  static constexpr int upto(int F) { int n = 0; for (int f = 0; f <= F; ++f) n += x_at(f) + (op(f) >= 0) + st_at(f); return n; }
  static constexpr int slot(int o) { for (int f = 0; f < 300; ++f) if (op(f) == o) return f; return -1; }
  static constexpr int xidx(int i) { return i < XW ? i : XW + upto(3 * (i - XW)) - 1; }
  static constexpr int xwait2(int s) { return XW + upto(3 * s) - 1 - xidx(s + 1); }
};
static_assert(GS::op(278) == 1000 + GS::NWD - 1, "down-weight prefetch schedule");

DI void iteration(K& k) {
  GAS u32* const flags = k.flags;
  const int tid = k.tid;
  {
    PHASE_IDS();
    const u32 sQ = OFF_WQ + p * 163840, sG = OFF_WG + p * 286720 + w * 5120;
    STAMP(0);
    if (it > 0 && w == 0) {
      const int grp = 2 * m128 - 1 + (lane >> 3);
      const bool act = lane < 24 && grp >= 0;
      poll_flags(flags, ((FG_X2 + grp) * 16 + (lane & 7)) * 32, act, lane);
    }
    WAITV(0); CERT("Q0"); CERT("Q1"); CERT("Q2");
    BAR();
    STAMP(1);
    u32x4 xf[40];
    const u32 xbase = xin + (4 * m128 + w) * 40960;
#define XLOAD(i) ald_a_sc1<((i) % 4) * 1024>(xf[i], rs, l16, xbase + ((i) / 4) * 4096)
    u32x2 hs0, hs1, sa0, sa1, sa2, sa3;
    const int rbh = m128 > 0 ? 4 * m128 - 1 : 0;
    const u32 hvo = (g >> 1) * 1024 + (g & 1) * 512 + (16 + t16) * 16;
    const int ht = 128 * m128 - 16 + t16;
    const u32 hso = ((2 * g) * 2048 + (ht < 0 ? 0 : ht)) * 8, sso = (4 * h * 2048 + 128 * m128 + 32 * w + tok) * 8;
    auto aux_load = [&](auto AC) {
      constexpr int a = decltype(AC)::value;
      if constexpr (a < 5) DMA_F("HALO", 0, "sc1 ", rs, hvo, xin + rbh * 40960 + (5 * w + a) * 2048, lds0 + L_HALO + (5 * w + a) * 1024);
      if constexpr (a == 5) ald2_sc1(sa0, rs, sso, stin);
      if constexpr (a == 6) ald2_sc1(sa1, rs, sso, stin + 16384);
      if constexpr (a == 7) ald2_sc1(sa2, rs, sso, stin + 32768);
      if constexpr (a == 8) ald2_sc1(sa3, rs, sso, stin + 49152);
      if constexpr (a == 9) ald2_sc1(hs0, rs, hso, stin);
      if constexpr (a == 10) ald2_sc1(hs1, rs, hso, stin + 16384);
    };
    sfor<XW>([&](auto I) { constexpr int i = decltype(I)::value; XLOAD(i); });
    using Q = QS;
    constexpr int DQ = 4, NBQ = 6;
    u32x4 wb[NBQ];
    f32x16 acc0, acc1, acc2, acc3;
    f32x4 hacc, hacc4;
    u32x4 hw[3], hx[3], h4w[3], h4x[3];
    float rstd = 0.f, hrs = 0.f;
    u32 lb1 = l16 + 65536, lbh = l16 + L_HALO; asm volatile("" : "+v"(lb1), "+v"(lbh));
    u32 hwlo = (w >> 1) * 1024 + (g >> 1) * 3072 + (g & 1) * 512 + (16 * (w & 1) + t16) * 16, hwhi = hwlo + 61440;
    u32 hw4 = 2048 + (g >> 1) * 3072 + (g & 1) * 512 + t16 * 16 + w * 30720, hx4 = l16 + L_HALO + w * 5120;
    asm volatile("" : "+v"(hwlo), "+v"(hwhi), "+v"(hw4), "+v"(hx4));
    const u32 kvrow = L_KST + (16 + 32 * w + tok) * 80 + h * 8;
    const int hn = 16 * w + 4 * g;
    const u32 haddr = L_KST + t16 * 80 + 2 * hn + (hn >= 40 ? (L_VST - L_KST - 80) : 0);
    const u32 haddr4 = L_VST + t16 * 80 + 2 * (64 + 4 * g - 40);
    const bool hzero = (m128 == 0);
    const u32 wo_o = OFF_WO + (c8 * 20 + 5 * w) * 5120;
#define LDSQ(F) (((F) / 40 % 3) * 40960 + ((F) % 40) * 1024)
#define RDW(off) (*(const LAS u32x4*)(lds + ((off) < 65536 ? l16 : lb1) + ((off) & 65535)))
    u32 gp0 = 0;
#define STG_OFF(n0) ((n0) < 40 ? 2 * (n0) : (n0) < 80 ? (L_VST - L_KST) + 2 * ((n0) - 40) : (L_QST - L_KST - 16 * 80) + 2 * ((n0) - 80))
#define GRAN(t, ACC, q, RS, STADDR, ZERO) do { \
      if constexpr ((t) == 0) gp0 = cvtpk(mul_s(ACC[4 * (q)], RS), mul_s(ACC[4 * (q) + 1], RS)); \
      if constexpr ((t) == 1) { u32x2 pk_ = {gp0, cvtpk(mul_s(ACC[4 * (q) + 2], RS), mul_s(ACC[4 * (q) + 3], RS))}; if (ZERO) pk_ = u32x2{0u, 0u}; *(LAS u32x2*)(lds + (STADDR)) = pk_; } \
    } while (0)
#define GRAN_MAIN(t, ACC, q, n0) GRAN(t, ACC, q, rstd, kvrow + STG_OFF(n0), false)
#pragma unroll
    for (int F = 0; F < DQ; ++F) wb[F % NBQ] = RDW(LDSQ(F));
    sfor<160>([&](auto FC) {
      constexpr int F = decltype(FC)::value, Fr = F + DQ;
      if constexpr (F == 30) QSTAMP(21);
      if constexpr (F == 60) QSTAMP(22);
      if constexpr (F == 90) QSTAMP(23);
      if constexpr (F == 140) QSTAMP(26);
      if constexpr (Q::x_at(F)) XLOAD(F / 3 + XW);
      if constexpr (Q::aux_at(F) >= 0) aux_load(std::integral_constant<int, Q::aux_at(F)>{});
      if constexpr (Q::q3_at(F) >= 0) dma_q<3, 0, Q::q3_at(F)>(rs, l16, sQ, w, lds0);
      if constexpr (Q::wo_at(F) >= 0) {
        constexpr int i = Q::wo_at(F), s = i / 5, nb = i % 5;
        if constexpr (i < 24) ald_a<(i % 4) * 1024>(k.wo_r[s][nb], rs, l16, wo_o + (i / 4) * 4096); else ald_v<(i % 4) * 1024>(k.wo_r[s][nb], rs, l16, wo_o + (i / 4) * 4096);
      }
      if constexpr (Fr == 40) { WAITV(Q::upto(F) - Q::upto(13)); CERT("HALO"); BAR(); }
      if constexpr (Fr == 80) { WAITL0(); BAR(); }
      if constexpr (Fr == 120) { WAITV(Q::upto(F) - Q::upto(89)); CERT("Q3a"); WAITL0(); BAR(); STAMP(2); }
      if constexpr (Fr == 140) { WAITV(Q::upto(F) - Q::upto(104)); CERT("Q3b"); BAR(); }
      if constexpr (Fr < 160) wb[Fr % NBQ] = RDW(LDSQ(Fr));
      if constexpr (F < 120) {
        constexpr int s = F / 3, b = F % 3;
        if constexpr (b == 0 && (s & 1) == 0) waitpin_a2<Q::xwait2(s)>(xf[s], xf[s + 1]);
#define QMMA(ACC) do { if constexpr (s == 0) { if constexpr ((F & 1) == 0) mma32w_0(ACC, wb[F % NBQ], xf[s], wb[(F + 1) % NBQ]); else mma32_0(ACC, wb[F % NBQ], xf[s]); } \
                       else { if constexpr ((F & 1) == 0) mma32w(ACC, wb[F % NBQ], xf[s], wb[(F + 1) % NBQ]); else mma32(ACC, wb[F % NBQ], xf[s]); } } while (0)
        if constexpr (b == 0) QMMA(acc0); else if constexpr (b == 1) QMMA(acc1); else QMMA(acc2);
        if constexpr (F == 62) {
          pin2(sa0); pin2(sa1); pin2(sa2); pin2(sa3);
          float nm;
          stats_finish(red32_add((u2f(sa0.x) + u2f(sa1.x)) + (u2f(sa2.x) + u2f(sa3.x))), red32_add((u2f(sa0.y) + u2f(sa1.y)) + (u2f(sa2.y) + u2f(sa3.y))), nm, rstd);
        }
        if constexpr (F == 110) {
          pin2(hs0); pin2(hs1);
          float nm;
          stats_finish(red16_32_add(u2f(hs0.x) + u2f(hs1.x)), red16_32_add(u2f(hs0.y) + u2f(hs1.y)), nm, hrs);
        }
        if constexpr (F == 113) {
#pragma unroll
          for (int nb = 0; nb < 5; ++nb) pin2(k.xr[nb]);
        }
        if constexpr (b == 1) {
          if constexpr (s == 15) mma16_0(hacc, hw[0], hx[0]);
          if constexpr (s > 15 && s <= 34) mma16(hacc, hw[(s - 15) % 3], hx[(s - 15) % 3]);
          if constexpr (s >= 13 && s <= 32) {
            constexpr int sg = s - 13;
            hw[sg % 3] = *(const LAS u32x4*)(lds + (sg < 10 ? hwlo : hwhi) + (sg % 10) * 6144);
            hx[sg % 3] = *(const LAS u32x4*)(lds + lbh + sg * 1024);
          }
        }
      } else {
        constexpr int s = F - 120;
        if constexpr (s == 0) mma32w_0(acc3, wb[F % NBQ], xf[s], wb[(F + 1) % NBQ]); else if constexpr (s & 1) mma32(acc3, wb[F % NBQ], xf[s]); else mma32w(acc3, wb[F % NBQ], xf[s], wb[(F + 1) % NBQ]);
        constexpr int ms = s - 2, gi = ms / 2, st = ms % 2;
        if constexpr (ms >= 0 && gi < 12) {
          constexpr int b = gi >> 2, q = gi & 3, n0 = 32 * b + 8 * q;
          if constexpr (b == 0) GRAN_MAIN(st, acc0, q, n0); else if constexpr (b == 1) GRAN_MAIN(st, acc1, q, n0); else GRAN_MAIN(st, acc2, q, n0);
        }
        if constexpr (s == 22) {
          const f32x4 p0 = *(const LAS f32x4*)(lds + L_H4 + l16), p1 = *(const LAS f32x4*)(lds + L_H4 + 1024 + l16);
          const f32x4 p2 = *(const LAS f32x4*)(lds + L_H4 + 2048 + l16), p3 = *(const LAS f32x4*)(lds + L_H4 + 3072 + l16);
          hacc4 = (p0 + p1) + (p2 + p3);
        }
        if constexpr (ms >= 0 && gi == 12) GRAN(st, hacc, 0, hrs, haddr, hzero);
        if constexpr (ms >= 0 && gi == 14) GRAN(st, hacc4, 0, hrs, haddr4, hzero);
      }
      if constexpr (F < 120 && F % 3 == 2) {
        constexpr int s = F / 3;
        if constexpr (s == 15) mma16_0(hacc4, h4w[0], h4x[0]);
        if constexpr (s > 15 && s <= 19) mma16(hacc4, h4w[(s - 15) % 3], h4x[(s - 15) % 3]);
        if constexpr (s >= 13 && s <= 17) {
          h4w[(s - 13) % 3] = *(const LAS u32x4*)(lds + hw4 + (s - 13) * 6144);
          h4x[(s - 13) % 3] = *(const LAS u32x4*)(lds + hx4 + (s - 13) * 1024);
        }
        if constexpr (s == 21) *(LAS f32x4*)(lds + L_H4 + w * 1024 + l16) = hacc4;
      }
      SB();
    });
    STAMP(3);
    asm volatile("s_nop 11" : "+v"(acc3));
    GRAN_MAIN(0, acc3, 0, 96); GRAN_MAIN(1, acc3, 0, 96); GRAN_MAIN(0, acc3, 1, 104); GRAN_MAIN(1, acc3, 1, 104); GRAN_MAIN(0, acc3, 2, 112); GRAN_MAIN(1, acc3, 2, 112);
    WAITL0(); BAR();
    STAMP(4);
    {
      const int par = t16 & 1;
      const bool ca = (4 * g + par) > t16, cb = (4 * g + 2 + par) > t16;
      const unsigned long long mca = __ballot(ca), mcb = __ballot(cb), mpar = __ballot(par != 0);
      const bool neg = (m128 == 0) && (w == 0);
      const unsigned long long nga = neg ? mca : 0ull, ngb = neg ? mcb : 0ull;
      const u32 gq = g < 2 ? g : 2;
      const u32 mA = g < 3 ? ~0u : 0u, mB = g < 2 ? ~0u : 0u, mC = (g == 1 || g == 2) ? ~0u : 0u;
      const u32 pla = par ? 0x01000c0cu : 0x0c0c0100u, plb = par ? 0x03020c0cu : 0x0c0c0302u;
      const u32 sel0 = ca ? pla : 0x0c0c0c0cu, sel2 = ca ? 0x0c0c0c0cu : pla, sel1 = cb ? plb : 0x0c0c0c0cu, sel3 = cb ? 0x0c0c0c0cu : plb;
      constexpr float cexp = 0.22360679774997896f * 1.4426950408889634f;
      float cx = cexp; asm volatile("" : "+v"(cx));
      const u32 qa = L_QST + (32 * w + t16) * 80 + gq * 16, ka = L_KST + (32 * w + t16) * 80 + gq * 16;
      const u32 va = L_VST + (32 * w + 4 * g + (t16 >> 2)) * 80 + (t16 & 3) * 8;
      u32x4 qf[4], k0[4], k1[4];
      u32x4 v0[4], v1[4];
#pragma unroll
      for (int u = 0; u < 4; ++u) {
        const u32 o = (u >> 1) * 16 * 80 + (u & 1) * 32;
        qf[u] = *(const LAS u32x4*)(lds + qa + o);
        k0[u] = *(const LAS u32x4*)(lds + ka + o);
        k1[u] = *(const LAS u32x4*)(lds + ka + o + 16 * 80);
      }
      dma_g<0, 0>(rs, l16, sG, w, lds0);
#pragma unroll
      for (int u = 0; u < 4; ++u) {
        if (u & 1) { qf[u][0] &= mC; qf[u][1] &= mC; qf[u][2] &= mA; qf[u][3] &= mA; }
        else { qf[u][0] &= mA; qf[u][1] &= mA; qf[u][2] &= mB; qf[u][3] &= mB; }
      }
      f32x4 s0[4], s1[4];
      asm volatile("s_nop 1\n\t"
                   "v_mfma_f32_16x16x32_bf16 %0, %8, %16, 0\n\tv_mfma_f32_16x16x32_bf16 %1, %12, %16, 0\n\t"
                   "v_mfma_f32_16x16x32_bf16 %2, %9, %17, 0\n\tv_mfma_f32_16x16x32_bf16 %3, %13, %17, 0\n\t"
                   "v_mfma_f32_16x16x32_bf16 %4, %10, %18, 0\n\tv_mfma_f32_16x16x32_bf16 %5, %14, %18, 0\n\t"
                   "v_mfma_f32_16x16x32_bf16 %6, %11, %19, 0\n\tv_mfma_f32_16x16x32_bf16 %7, %15, %19, 0"
                   : "=&v"(s0[0]), "=&v"(s1[0]), "=&v"(s0[1]), "=&v"(s1[1]), "=&v"(s0[2]), "=&v"(s1[2]), "=&v"(s0[3]), "=&v"(s1[3])
                   : "v"(k0[0]), "v"(k0[1]), "v"(k0[2]), "v"(k0[3]), "v"(k1[0]), "v"(k1[1]), "v"(k1[2]), "v"(k1[3]), "v"(qf[0]), "v"(qf[1]), "v"(qf[2]), "v"(qf[3]));
#pragma unroll
      for (int u = 0; u < 4; ++u) {
        const u32 o = (u >> 1) * 16 * 80 + (u & 1) * 40;
        const u32x2 t0 = __builtin_bit_cast(u32x2, __builtin_amdgcn_ds_read_tr16_b64_v4i16((LAS s16x4*)(lds + va + o)));
        const u32x2 t1 = __builtin_bit_cast(u32x2, __builtin_amdgcn_ds_read_tr16_b64_v4i16((LAS s16x4*)(lds + va + o + 16 * 80)));
        const u32x2 t2 = __builtin_bit_cast(u32x2, __builtin_amdgcn_ds_read_tr16_b64_v4i16((LAS s16x4*)(lds + va + o + 32)));
        const u32x2 t3 = __builtin_bit_cast(u32x2, __builtin_amdgcn_ds_read_tr16_b64_v4i16((LAS s16x4*)(lds + va + o + 32 + 16 * 80)));
        v0[u] = u32x4{t0.x, t0.y, t1.x, t1.y}; v1[u] = u32x4{t2.x, t2.y, t3.x, t3.y};
      }
      dma_g<0, 1>(rs, l16, sG, w, lds0);
      asm volatile("s_nop 7" : "+v"(s0[0]), "+v"(s1[0]), "+v"(s0[1]), "+v"(s1[1]), "+v"(s0[2]), "+v"(s1[2]), "+v"(s0[3]), "+v"(s1[3]));
      float a[4], b[4], mx[4], sm[4];
#pragma unroll
      for (int u = 0; u < 4; ++u) {
        const float x00 = s0[u][0], x01 = s0[u][1], x02 = s0[u][2], x03 = s0[u][3], x10 = s1[u][0], x11 = s1[u][1], x12 = s1[u][2], x13 = s1[u][3];
        a[u] = sel_s(sel_s(x10, x11, mpar), sel_s(x00, x01, mpar), mca);
        b[u] = sel_s(sel_s(x12, x13, mpar), sel_s(x02, x03, mpar), mcb);
        if (u < 2) { a[u] = sel_s(a[u], -__builtin_inff(), nga); b[u] = sel_s(b[u], -__builtin_inff(), ngb); }
        mx[u] = max_s(a[u], b[u]);
      }
      dma_g<0, 2>(rs, l16, sG, w, lds0);
#pragma unroll
      for (int u = 0; u < 4; ++u) { auto r = __builtin_amdgcn_permlane16_swap(f2u(mx[u]), f2u(mx[u]), false, false); mx[u] = max_s(u2f(r[0]), u2f(r[1])); }
      dma_g<0, 3>(rs, l16, sG, w, lds0);
#pragma unroll
      for (int u = 0; u < 4; ++u) { auto r = __builtin_amdgcn_permlane32_swap(f2u(mx[u]), f2u(mx[u]), false, false); mx[u] = max_s(u2f(r[0]), u2f(r[1])); }
      dma_g<0, 4>(rs, l16, sG, w, lds0);
#pragma unroll
      for (int u = 0; u < 4; ++u) {
        const float t = -mul_s(mx[u], cx);
        a[u] = __builtin_amdgcn_exp2f(fma_s(a[u], cx, t)); b[u] = __builtin_amdgcn_exp2f(fma_s(b[u], cx, t));
        sm[u] = a[u] + b[u];
      }
      dma_g<1, 0>(rs, l16, sG, w, lds0);
#pragma unroll
      for (int u = 0; u < 4; ++u) { auto r = __builtin_amdgcn_permlane16_swap(f2u(sm[u]), f2u(sm[u]), false, false); sm[u] = u2f(r[0]) + u2f(r[1]); }
      dma_g<1, 1>(rs, l16, sG, w, lds0);
#pragma unroll
      for (int u = 0; u < 4; ++u) { auto r = __builtin_amdgcn_permlane32_swap(f2u(sm[u]), f2u(sm[u]), false, false); sm[u] = u2f(r[0]) + u2f(r[1]); }
      dma_g<1, 2>(rs, l16, sG, w, lds0);
      u32x4 pf[4];
#pragma unroll
      for (int u = 0; u < 4; ++u) {
        const float inv = __builtin_amdgcn_rcpf(sm[u]);
        const u32 pk = cvtpk(a[u] * inv, b[u] * inv);
        pf[u] = u32x4{__builtin_amdgcn_perm(pk, pk, sel0), __builtin_amdgcn_perm(pk, pk, sel1), __builtin_amdgcn_perm(pk, pk, sel2), __builtin_amdgcn_perm(pk, pk, sel3)};
      }
      dma_g<1, 3>(rs, l16, sG, w, lds0);
      f32x4 o0[4], o1[4];
      asm volatile("s_nop 1\n\t"
                   "v_mfma_f32_16x16x32_bf16 %0, %8, %16, 0\n\tv_mfma_f32_16x16x32_bf16 %1, %12, %16, 0\n\t"
                   "v_mfma_f32_16x16x32_bf16 %2, %9, %17, 0\n\tv_mfma_f32_16x16x32_bf16 %3, %13, %17, 0\n\t"
                   "v_mfma_f32_16x16x32_bf16 %4, %10, %18, 0\n\tv_mfma_f32_16x16x32_bf16 %5, %14, %18, 0\n\t"
                   "v_mfma_f32_16x16x32_bf16 %6, %11, %19, 0\n\tv_mfma_f32_16x16x32_bf16 %7, %15, %19, 0\n\ts_nop 7"
                   : "=&v"(o0[0]), "=&v"(o1[0]), "=&v"(o0[1]), "=&v"(o1[1]), "=&v"(o0[2]), "=&v"(o1[2]), "=&v"(o0[3]), "=&v"(o1[3])
                   : "v"(v0[0]), "v"(v0[1]), "v"(v0[2]), "v"(v0[3]), "v"(v1[0]), "v"(v1[1]), "v"(v1[2]), "v"(v1[3]), "v"(pf[0]), "v"(pf[1]), "v"(pf[2]), "v"(pf[3]));
      dma_g<1, 4>(rs, l16, sG, w, lds0);
      const u32 sg = 5 * p + g, sg4 = 5 * p + 4;
      const u32 oa = (sg >> 2) * 1024 + (t16 + 16 * (sg & 3)) * 16, oa4 = (sg4 >> 2) * 1024 + (t16 + 16 * (sg4 & 3)) * 16;
#pragma unroll
      for (int qb = 0; qb < 2; ++qb) {
        const u32 so = ofo + (8 * m128 + 2 * w + qb) * 20480;
        const f32x4 A0 = o0[2 * qb], B0_ = o0[2 * qb + 1], A1 = o1[2 * qb], B1 = o1[2 * qb + 1];
        stg_sc1(u32x4{cvtpk(A0[0], A0[1]), cvtpk(A0[2], A0[3]), cvtpk(B0_[0], B0_[1]), cvtpk(B0_[2], B0_[3])}, rs, oa, so);
        if (g == 0) stg_sc1(u32x4{cvtpk(A1[0], A1[1]), cvtpk(A1[2], A1[3]), cvtpk(B1[0], B1[1]), cvtpk(B1[2], B1[3])}, rs, oa4, so);
      }
    }
    SB();
    STAMP(5);
    WAITV(0); CERT("G0"); CERT("G1");
    WAITL0();
    BAR();
    if (tid == 0) __hip_atomic_store(flags + ((fgb + FG_O + m128) * 16 + p) * 32, 1u, RLX_AGENT);
    STAMP(6); RSTAMP(0);
    sfor<5>([&](auto I) { dma_g<2, decltype(I)::value>(rs, l16, sG, w, lds0); });
    sfor<5>([&](auto I) { dma_g<3, decltype(I)::value>(rs, l16, sG, w, lds0); });
  }
  {
    PHASE_IDS();
    if (w == 0) poll_flags(flags, ((fgb + FG_O + (m64 >> 1)) * 16 + (lane & 15)) * 32, lane < 16, lane);
    BAR();
    STAMP(7); RSTAMP(1);
#pragma unroll
    for (int s = 0; s < 5; ++s)
#pragma unroll
      for (int nb = 0; nb < 5; ++nb) { if (s * 5 + nb < 24) pin_a(k.wo_r[s][nb]); else pin_v(k.wo_r[s][nb]); }
    u32x4 af[20];
    u32 tbo[4];
#pragma unroll
    for (int tbi = 0; tbi < 4; ++tbi) tbo[tbi] = ofo + ((4 * m64 + ((w + 1 + tbi) & 3)) * 20 + 5 * w) * 1024;
    constexpr int OW = 10;
#define OLOAD(q) ald_v_sc1<((q) % 5 % 4) * 1024>(af[q], rs, l16, tbo[(q) / 5] + ((q) % 5 / 4) * 4096)
    sfor<OW>([&](auto I) { constexpr int q = decltype(I)::value; OLOAD(q); });
    f32x4 acc[2][5];
    f32x4 part[3][5];
    sfor<20>([&](auto QC) {
      constexpr int q = decltype(QC)::value, tbi = q / 5, s = q % 5;
      if constexpr (q + OW < 20) OLOAD(q + OW);
      waitpin_v<(q < 10 ? OW : (19 - q))>(af[q]);
#pragma unroll
      for (int nb = 0; nb < 5; ++nb) acc[tbi & 1][nb] = mfma16(k.wo_r[s][nb], af[q], s == 0 ? f32x4{0.f, 0.f, 0.f, 0.f} : acc[tbi & 1][nb]);
      if constexpr (tbi > 0 && s < 3) {
        const u32 tbp = (w + tbi) & 3;
        *(LAS f32x4*)(lds + L_RED + ((tbp * 3 + (3 - tbi)) * 5 + 2 * s) * 1024 + l16) = acc[(tbi - 1) & 1][2 * s];
        if constexpr (2 * s + 1 < 5) *(LAS f32x4*)(lds + L_RED + ((tbp * 3 + (3 - tbi)) * 5 + 2 * s + 1) * 1024 + l16) = acc[(tbi - 1) & 1][2 * s + 1];
      }
      if constexpr (q == 18) {
        WAITL0(); BAR();
#pragma unroll
        for (int sl = 0; sl < 3; ++sl)
#pragma unroll
          for (int nb = 0; nb < 5; ++nb) part[sl][nb] = *(const LAS f32x4*)(lds + L_RED + ((w * 3 + sl) * 5 + nb) * 1024 + l16);
      }
      SB();
    });
    STAMP(8);
    float s1 = 0.f, s2 = 0.f;
#pragma unroll
    for (int nb = 0; nb < 5; ++nb) {
      const f32x4 v = (acc[1][nb] + part[0][nb]) + (part[1][nb] + part[2][nb]);
      const u32 a = cvtpk(bflo(k.xr[nb].x) + v[0], bfhi(k.xr[nb].x) + v[1]), b = cvtpk(bflo(k.xr[nb].y) + v[2], bfhi(k.xr[nb].y) + v[3]);
      k.xr[nb] = u32x2{a, b};
    }
    const u32 token = 64 * m64 + 16 * w + t16;
    const u32 xo = ((token >> 5) * 40 + 5 * c8) * 1024 + ((token & 31) + 32 * (g >> 1)) * 16;
#pragma unroll
    for (int pr = 0; pr < 3; ++pr) {
      const int A = 2 * pr, Bn = pr < 2 ? 2 * pr + 1 : 4;
      auto r0 = __builtin_amdgcn_permlane16_swap(k.xr[A].x, k.xr[Bn].x, false, false);
      auto r1 = __builtin_amdgcn_permlane16_swap(k.xr[A].y, k.xr[Bn].y, false, false);
      const u32x4 v = {r0[0], r1[0], r0[1], r1[1]};
      if (pr < 2) stg_sc1(v, rs, xo + (A + (g & 1)) * 1024, x1o);
      else if ((g & 1) == 0) stg_sc1(v, rs, xo + 4 * 1024, x1o);
    }
#pragma unroll
    for (int nb = 0; nb < 5; ++nb) {
      const float a = bflo(k.xr[nb].x), b = bfhi(k.xr[nb].x), c = bflo(k.xr[nb].y), d = bfhi(k.xr[nb].y);
      s1 += (a + b) + (c + d); s2 += (a * a + b * b) + (c * c + d * d);
    }
    s1 = red16_32_add(s1); s2 = red16_32_add(s2);
    if (g == 0) stg2_sc1(u32x2{f2u(s1), f2u(s2)}, rs, (c8 * 2048 + token) * 8, st1o);
    SB();
    STAMP(9);
    WAITV(0); CERT("G2"); CERT("G3");
    WAITL0();
    BAR();
    if (tid == 0) __hip_atomic_store(flags + ((fgb + FG_X1 + m64) * 16 + c8) * 32, 1u, RLX_AGENT);
    STAMP(10); RSTAMP(2);
  }
  {
    PHASE_IDS();
    const int nblk = k.nblk;
    const int ds0 = w < 2 ? 14 * w : 28 + 13 * (w - 2);
    const u32 sG = OFF_WG + p * 286720 + w * 5120;
    Polls pl = {0u, 0u, 0u};
    if (w != 0) { dma_g3<4>(rs, l16, sG - w * 5120, w, lds0); dma_g3<5>(rs, l16, sG - w * 5120, w, lds0); dma_g3<6>(rs, l16, sG - w * 5120, w, lds0); }
    if (w == 0) poll_flags_p(flags, ((fgb + FG_X1 + 2 * m128 + ((lane >> 3) & 1)) * 16 + (lane & 7)) * 32, lane < 16, lane, 1u, pl);
    BAR();
    STAMP(11); RSTAMP(3);
    u32x2 sa0, sa1, sa2, sa3;
    u32x4 xf[40];
    const u32 xbase = x1o + (4 * m128 + w) * 40960;
    const u32 sso = (4 * h * 2048 + 128 * m128 + 32 * w + tok) * 8;
    sfor<XW>([&](auto I) { constexpr int i = decltype(I)::value; XLOAD(i); });
    constexpr int NWD = GS::NWD;
    float r2 = 0.f;
    constexpr int DG = 4, NBG = 6;
    u32x4 wb[NBG];
    f32x16 aA0, aA1, aA2, aB0, aB1;
    u32 lb1 = l16 + 65536, lb2 = l16 + 131072; asm volatile("" : "+v"(lb1), "+v"(lb2));
    const u32 hbase = (8 * m128 + 2 * w + (tok >> 4)) * 54 * 1024 + ((tok & 15) + 16 * h) * 16;
    const u32 hlast = nblk == 7 ? hbase : 0x7fff0000u;
    const u32 wd_o = OFF_WD + (c8 * 54 + ds0) * 5120;
#define LDSG(F) ((((F) / 20) % 7) * 20480 + ((F) % 20) * 1024)
#define RDG(off) (*(const LAS u32x4*)(lds + ((off) < 65536 ? l16 : (off) < 131072 ? lb1 : lb2) + ((off) & 65535)))
    float hv0 = 0.f;
    u32 hp[4];
#define GU_STAGE(ACC, idx, t) do { \
      if constexpr ((t) < 8) { const float hv_ = mul_s(mul_s(ACC[t], r2), ACC[8 + (t)]); if constexpr ((t) & 1) hp[(t) >> 1] = cvtpk(hv0, hv_); else hv0 = hv_; } \
      if constexpr ((t) == 8) { const u32 fb_ = B0 + (idx); stg_sc1(u32x4{hp[0], hp[1], hp[2], hp[3]}, rs, ((idx) == 6 ? hlast : hbase) + (fb_ & 1) * 512, hfo + (fb_ >> 1) * 1024); } \
    } while (0)
    auto wd_load = [&](auto OC) {
      constexpr int i = decltype(OC)::value, s = i / 5, nb = i % 5;
      if constexpr (i < 24 || i >= 50) ald_a<(i % 4) * 1024>(k.wd_r[s][nb], rs, l16, wd_o + (i / 4) * 4096);
      else ald_v<(i % 4) * 1024>(k.wd_r[s][nb], rs, l16, wd_o + (i / 4) * 4096);
    };
#pragma unroll
    for (int F = 0; F < DG; ++F) wb[F % NBG] = RDG(LDSG(F));
    sfor<280>([&](auto FC) {
      constexpr int F = decltype(FC)::value, Fr = F + DG;
      if constexpr (GS::x_at(F)) XLOAD(F / 3 + XW);
      if constexpr (GS::op(F) >= 0 && GS::op(F) < 1000) dma_g<GS::op(F) / 8, GS::op(F) % 8>(rs, l16, sG, w, lds0);
      if constexpr (GS::op(F) >= 1000 && GS::op(F) < 2000) wd_load(std::integral_constant<int, GS::op(F) - 1000>{});
      if constexpr (GS::op(F) == 3000) ald2_sc1(sa0, rs, sso, st1o);
      if constexpr (GS::op(F) == 3001) ald2_sc1(sa1, rs, sso, st1o + 16384);
      if constexpr (GS::op(F) == 3002) ald2_sc1(sa2, rs, sso, st1o + 32768);
      if constexpr (GS::op(F) == 3003) ald2_sc1(sa3, rs, sso, st1o + 49152);
      if constexpr (Fr == 40) { WAITL0(); BAR(); }
      if constexpr (Fr == 80) { CERT("G4"); CERT("G5"); WAITL0(); BAR(); }
      if constexpr (Fr == 120) { WAITV(GS::upto(F) - GS::upto(GS::slot(7 * 8 + 4))); CERT("G6"); CERT("G7"); WAITL0(); BAR(); STAMP(12); }
      if constexpr (Fr == 160) { GSTAMP(23); WAITV(GS::upto(F) - GS::upto(GS::slot(9 * 8 + 4))); CERT("G8"); CERT("G9"); WAITL0(); BAR(); GSTAMP(24); }
      if constexpr (Fr == 200) { GSTAMP(25); WAITV(GS::upto(F) - GS::upto(GS::slot(11 * 8 + 4))); CERT("G10"); CERT("G11"); BAR(); GSTAMP(26); }
      if constexpr (Fr == 240) { WAITV(GS::upto(F) - GS::upto(GS::slot(13 * 8 + 4))); CERT("G12"); CERT("G13"); BAR(); STAMP(13); }
      if constexpr (Fr < 280) wb[Fr % NBG] = RDG(LDSG(Fr));
      constexpr int s = F < 120 ? F / 3 : F < 200 ? (F - 120) / 2 : (F - 200) / 2, b = F < 120 ? F % 3 : F & 1;
      if constexpr (F < 120 && b == 0 && (s & 1) == 0) waitpin_a2<GS::xwait2(s)>(xf[s], xf[s + 1]);
#define GMMA(ACC) do { if constexpr (s == 0) { if constexpr ((F & 1) == 0) mma32w_0(ACC, wb[F % NBG], xf[s], wb[(F + 1) % NBG]); else mma32_0(ACC, wb[F % NBG], xf[s]); } \
                       else { if constexpr ((F & 1) == 0) mma32w(ACC, wb[F % NBG], xf[s], wb[(F + 1) % NBG]); else mma32(ACC, wb[F % NBG], xf[s]); } } while (0)
      if constexpr (F < 120) { if constexpr (b == 0) GMMA(aA0); else if constexpr (b == 1) GMMA(aA1); else GMMA(aA2); }
      else if constexpr (F < 200) { if constexpr (b == 0) GMMA(aB0); else GMMA(aB1); }
      else { if constexpr (b == 0) GMMA(aA0); else GMMA(aA1); }
      if constexpr (F == 3) poll_pin(pl);
      if constexpr (F == 62) {
        pin2(sa0); pin2(sa1); pin2(sa2); pin2(sa3);
        float rstd, nm;
        stats_finish(red32_add((u2f(sa0.x) + u2f(sa1.x)) + (u2f(sa2.x) + u2f(sa3.x))), red32_add((u2f(sa0.y) + u2f(sa1.y)) + (u2f(sa2.y) + u2f(sa3.y))), nm, rstd);
        r2 = rstd * rstd;
      }
      if constexpr ((F & 1) && F >= 123 && F <= 139) GU_STAGE(aA0, 0, (F - 123) / 2);
      if constexpr ((F & 1) && F >= 141 && F <= 157) GU_STAGE(aA1, 1, (F - 141) / 2);
      if constexpr ((F & 1) && F >= 159 && F <= 175) GU_STAGE(aA2, 2, (F - 159) / 2);
      if constexpr ((F & 1) && F >= 203 && F <= 219) GU_STAGE(aB0, 3, (F - 203) / 2);
      if constexpr ((F & 1) && F >= 221 && F <= 237) GU_STAGE(aB1, 4, (F - 221) / 2);
      SB();
    });
    asm volatile("s_nop 11" : "+v"(aA0), "+v"(aA1));
    sfor<9>([&](auto T) { GU_STAGE(aA0, 5, decltype(T)::value); });
    sfor<9>([&](auto T) { GU_STAGE(aA1, 6, decltype(T)::value); });
    SB();
    STAMP(14);
    WAITV(0);
    WAITL0();
    BAR();
    if (tid == 0) __hip_atomic_store(flags + ((fgb + FG_H + m128) * 16 + p) * 32, 1u, RLX_AGENT);
    {
      const u32 wd_o = OFF_WD + (c8 * 54 + ds0) * 5120;
      sfor<70 - GS::NWD>([&](auto I) { constexpr int i = GS::NWD + decltype(I)::value; ald_a<(i % 4) * 1024>(k.wd_r[i / 5][i % 5], rs, l16, wd_o + (i / 4) * 4096); });
    }
    STAMP(15); RSTAMP(4);
  }
  {
    PHASE_IDS();
    const int ds0 = w < 2 ? 14 * w : 28 + 13 * (w - 2);
    const u32 sQ = OFF_WQ + p * 163840;
    STAMP(16);
    if (w == 0) poll_flags(flags, ((fgb + FG_H + (m64 >> 1)) * 16 + (lane & 15)) * 32, lane < 16, lane);
    else WAITV(0);
    BAR();
    STAMP(17); RSTAMP(5);
    sfor<70>([&](auto I) { constexpr int i = decltype(I)::value; if constexpr (i < 24 || i >= 50) pin_a(k.wd_r[i / 5][i % 5]); else pin_v(k.wd_r[i / 5][i % 5]); });
    constexpr int DH = 12;
    u32x4 af[DH];
    const bool s13 = w < 2;
    u32 tbo[4];
#pragma unroll
    for (int tbi = 0; tbi < 4; ++tbi) tbo[tbi] = hfo + ((4 * m64 + ((w + 1 + tbi) & 3)) * 54 + ds0) * 1024;
#define HLOAD(q) ald_a_sc1<((q) % 14 % 4) * 1024>(af[(q) % DH], rs, l16, tbo[(q) / 14] + ((q) % 14 / 4) * 4096)
    sfor<DH>([&](auto I) { constexpr int q = decltype(I)::value; HLOAD(q); });
    f32x4 acc[2][5];
    f32x4 part[3][5];
    sfor<56>([&](auto QC) {
      constexpr int q = decltype(QC)::value, tbi = q / 14, s = q % 14;
      constexpr int yf = (q + DH - 1 < 55 ? q + DH - 1 : 55) - q;
      constexpr int nd = q - 44;
      constexpr int yd = (nd > 0) ? (nd + (nd < 8 ? nd : 8)) : 0;
      waitpin_a<yf + yd>(af[q % DH]);
      if constexpr (s == 0) {
#pragma unroll
        for (int nb = 0; nb < 5; ++nb) acc[tbi & 1][nb] = mfma16(k.wd_r[s][nb], af[q % DH], f32x4{0.f, 0.f, 0.f, 0.f});
      } else if (s < 13 || s13) {
#pragma unroll
        for (int nb = 0; nb < 5; ++nb) acc[tbi & 1][nb] = mfma16(k.wd_r[s][nb], af[q % DH], acc[tbi & 1][nb]);
      }
      if constexpr (tbi > 0 && s >= 1 && s < 6) {
        const u32 tbp = (w + tbi) & 3;
        *(LAS f32x4*)(lds + L_RED + ((tbp * 3 + (3 - tbi)) * 5 + (s - 1)) * 1024 + l16) = acc[(tbi - 1) & 1][s - 1];
      }
      if constexpr (q == 50) {
        WAITL0(); BAR();
#pragma unroll
        for (int sl = 0; sl < 3; ++sl)
#pragma unroll
          for (int nb = 0; nb < 5; ++nb) part[sl][nb] = *(const LAS f32x4*)(lds + L_RED + ((w * 3 + sl) * 5 + nb) * 1024 + l16);
      }
      SB();
      if constexpr (q + DH < 56) HLOAD(q + DH);
      if constexpr (q >= 44) {
        constexpr int j0 = nd + (nd < 8 ? nd : 8);
        if constexpr (j0 < 10) dma_q<0, 0, j0>(rs, l16, sQ, w, lds0); else dma_q<1, 1, j0 - 10>(rs, l16, sQ, w, lds0);
        if constexpr (nd < 8) {
          constexpr int j1 = j0 + 1;
          if constexpr (j1 < 10) dma_q<0, 0, j1>(rs, l16, sQ, w, lds0); else dma_q<1, 1, j1 - 10>(rs, l16, sQ, w, lds0);
        }
      }
      SB();
    });
    STAMP(18);
#pragma unroll
    for (int nb = 0; nb < 5; ++nb) {
      const f32x4 v = (acc[1][nb] + part[0][nb]) + (part[1][nb] + part[2][nb]);
      const u32 a = cvtpk(bflo(k.xr[nb].x) + v[0], bfhi(k.xr[nb].x) + v[1]), b = cvtpk(bflo(k.xr[nb].y) + v[2], bfhi(k.xr[nb].y) + v[3]);
      k.xr[nb] = u32x2{a, b};
    }
    const u32 token = 64 * m64 + 16 * w + t16;
    STAMP(19);
    if (it == 0) {
      float s1 = 0.f, s2 = 0.f;
      const u32 xo = ((token >> 5) * 40 + 5 * c8) * 1024 + ((token & 31) + 32 * (g >> 1)) * 16;
#pragma unroll
      for (int pr = 0; pr < 3; ++pr) {
        const int A = 2 * pr, Bn = pr < 2 ? 2 * pr + 1 : 4;
        auto r0 = __builtin_amdgcn_permlane16_swap(k.xr[A].x, k.xr[Bn].x, false, false);
        auto r1 = __builtin_amdgcn_permlane16_swap(k.xr[A].y, k.xr[Bn].y, false, false);
        const u32x4 v = {r0[0], r1[0], r0[1], r1[1]};
        if (pr < 2) stg_sc1(v, rs, xo + (A + (g & 1)) * 1024, OFF_XF + 2 * SZ_XF);
        else if ((g & 1) == 0) stg_sc1(v, rs, xo + 4 * 1024, OFF_XF + 2 * SZ_XF);
      }
#pragma unroll
      for (int nb = 0; nb < 5; ++nb) {
        const float a = bflo(k.xr[nb].x), b = bfhi(k.xr[nb].x), c = bflo(k.xr[nb].y), d = bfhi(k.xr[nb].y);
        s1 += (a + b) + (c + d); s2 += (a * a + b * b) + (c * c + d * d);
      }
      s1 = red16_32_add(s1); s2 = red16_32_add(s2);
      if (g == 0) stg2_sc1(u32x2{f2u(s1), f2u(s2)}, rs, (c8 * 2048 + token) * 8, OFF_ST + 2 * SZ_ST);
      SB();
      WAITV(0);
      WAITL0();
      BAR();
      if (tid == 0) __hip_atomic_store(flags + ((FG_X2 + m64) * 16 + c8) * 32, 1u, RLX_AGENT);
      sfor<10>([&](auto I) { dma_q<2, 2, decltype(I)::value>(rs, l16, sQ, w, lds0); });
    } else {
      const u32 oo = token * 1280 + (80 * c8 + 8 * (g >> 1)) * 2;
#pragma unroll
      for (int pr = 0; pr < 3; ++pr) {
        const int A = 2 * pr, Bn = pr < 2 ? 2 * pr + 1 : 4;
        auto r0 = __builtin_amdgcn_permlane16_swap(k.xr[A].x, k.xr[Bn].x, false, false);
        auto r1 = __builtin_amdgcn_permlane16_swap(k.xr[A].y, k.xr[Bn].y, false, false);
        const u32x4 v = {r0[0], r1[0], r0[1], r1[1]};
        if (pr < 2) __builtin_amdgcn_raw_buffer_store_b128(v, k.rso, oo + (A + (g & 1)) * 32, 0, 0);
        else if ((g & 1) == 0) __builtin_amdgcn_raw_buffer_store_b128(v, k.rso, oo + 4 * 32, 0, 0);
      }
    }
    STAMP(20);
  }
}

__global__ __launch_bounds__(256, 1) void main_kernel(char* __restrict__ ws, u16* __restrict__ out) {
  extern __shared__ __attribute__((aligned(16))) char smem[];
  K k;
  k.lds = (LAS char*)smem;
  k.lds0 = (u32)(size_t)k.lds;
  k.tid = threadIdx.x; k.lane = k.tid & 63; k.w = __builtin_amdgcn_readfirstlane(k.tid >> 6);
  k.rs = __builtin_amdgcn_make_buffer_rsrc(ws, 0, WS_USED, 0x00020000);
  k.rso = __builtin_amdgcn_make_buffer_rsrc(out, 0, 2048 * 640 * 2, 0x00020000);
  k.flags = (GAS u32*)ws;
  const int bid = blockIdx.x, xc = bid & 7, j = bid >> 3, mg = xc >> 1, half = xc & 1;
  k.m128 = 4 * mg + (j >> 3); k.p = 8 * half + (j & 7);
  k.m64 = 8 * mg + (j >> 2); k.c8 = 4 * half + (j & 3);
  const int p7 = j & 7;
  k.B0 = 54 * half + (p7 < 6 ? 7 * p7 : 42 + 6 * (p7 - 6));
  k.nblk = p7 < 6 ? 7 : 6;
  {
    PHASE_IDS();
    const u32 sQ = OFF_WQ + p * 163840;
    sfor<10>([&](auto I) { dma_q<0, 0, decltype(I)::value>(rs, l16, sQ, w, lds0); });
    sfor<10>([&](auto I) { dma_q<1, 1, decltype(I)::value>(rs, l16, sQ, w, lds0); });
    sfor<10>([&](auto I) { dma_q<2, 2, decltype(I)::value>(rs, l16, sQ, w, lds0); });
    const u32 token = 64 * m64 + 16 * w + t16;
    const u32 o = ((token >> 5) * 40 + 5 * c8) * 1024 + ((token & 31) + 32 * (g >> 1)) * 16 + (g & 1) * 8;
#pragma unroll
    for (int nb = 0; nb < 5; ++nb) ald2(k.xr[nb], rs, o, OFF_XF + nb * 1024);
  }
#pragma nounroll
  for (int it = 0; it < 2; ++it) { k.it = it; iteration(k); }
}

extern "C" void kernel_launch(void* const* d_in, const int* in_sizes, int n_in, void* d_out, int out_size, void* d_ws, size_t ws_size,
                              hipStream_t stream) {
  const u16* x0 = (const u16*)d_in[0];
  const u16* wqkv = (const u16*)d_in[1];
  const u16* wo = (const u16*)d_in[2];
  const u16* wg = (const u16*)d_in[4];
  const u16* wu = (const u16*)d_in[5];
  const u16* wd = (const u16*)d_in[6];
  (void)hipFuncSetAttribute((const void*)main_kernel, hipFuncAttributeMaxDynamicSharedMemorySize, LDS_BYTES);
  prep_kernel<<<PREP_UNITS, 512, 0, stream>>>(x0, wqkv, wo, wg, wu, wd, (char*)d_ws);
  main_kernel<<<256, 256, LDS_BYTES, stream>>>((char*)d_ws, (u16*)d_out);
}
```

```cpp
#include <hip/hip_runtime.h>
#include <stdint.h>
#include <utility>

#define LAS __attribute__((address_space(3)))
#define GAS __attribute__((address_space(1)))
#define DI __device__ __forceinline__
typedef unsigned u32;
typedef unsigned short u16;
typedef u32 u32x4 __attribute__((ext_vector_type(4)));
typedef u32 u32x2 __attribute__((ext_vector_type(2)));
typedef float f32x2 __attribute__((ext_vector_type(2)));
typedef float f32x4 __attribute__((ext_vector_type(4)));
typedef float f32x16 __attribute__((ext_vector_type(16)));
typedef short s16x8 __attribute__((ext_vector_type(8)));
typedef short s16x4 __attribute__((ext_vector_type(4)));
typedef __bf16 bf16x2 __attribute__((ext_vector_type(2)));
typedef __amdgpu_buffer_rsrc_t rsrc_t;

constexpr u32 FLAG_BYTES = 193 * 2048;
constexpr u32 OFF_WQ = 409600;
constexpr u32 OFF_WO = OFF_WQ + 16 * 163840;
constexpr u32 OFF_WG = OFF_WO + 8 * 20 * 5 * 1024;
constexpr u32 OFF_WD = OFF_WG + 16 * 286720;
constexpr u32 OFF_CSQ = OFF_WD + 8 * 54 * 5 * 1024;
constexpr u32 OFF_CSG = OFF_CSQ + 16384;
constexpr u32 SZ_XF = 64 * 40 * 1024;
constexpr u32 OFF_XF = OFF_CSG + 28672;
constexpr u32 SZ_ST = 8 * 2048 * 8;
constexpr u32 OFF_ST = OFF_XF + 4 * SZ_XF;
constexpr u32 SZ_OF = 128 * 20 * 1024;
constexpr u32 OFF_OF = OFF_ST + 4 * SZ_ST;
constexpr u32 SZ_HF = 128 * 54 * 1024;
constexpr u32 OFF_HF = OFF_OF + 2 * SZ_OF;
constexpr u32 WS_USED = OFF_HF + 2 * SZ_HF;
constexpr u32 FG_O = 0, FG_X1 = 16, FG_H = 48, FG_X2 = 64, FG_PER_IT = 96;
constexpr u32 FG_TMO = 2 * FG_PER_IT;

constexpr u32 L_HALO = 122880;
constexpr u32 L_KST = 40960;
constexpr u32 L_VST = L_KST + 144 * 80;
constexpr u32 L_QST = L_VST + 144 * 80;
constexpr u32 L_RED = 100352;
constexpr u32 L_H4 = 143360;
constexpr u32 LDS_BYTES = 163840;

constexpr u32 AUX_SC1 = 16;
#define RLX_AGENT __ATOMIC_RELAXED, __HIP_MEMORY_SCOPE_AGENT
#define SB() __builtin_amdgcn_sched_barrier(0)
#define MEMFENCE() asm volatile("" ::: "memory")
#define WAITV(n) asm volatile("s_waitcnt vmcnt(%0)" ::"i"(n) : "memory")
#define WAITL0() asm volatile("s_waitcnt lgkmcnt(0)" ::: "memory")
#define BAR() do { MEMFENCE(); __builtin_amdgcn_s_barrier(); MEMFENCE(); } while (0)
#define CERT(tag) asm volatile("; CERT " tag ::: "memory")

template <int... I, class Fn> DI void sfor_impl(std::integer_sequence<int, I...>, Fn&& f) { (f(std::integral_constant<int, I>{}), ...); }
template <int N, class Fn> DI void sfor(Fn&& f) { sfor_impl(std::make_integer_sequence<int, N>{}, f); }
DI f32x16 mfma32(u32x4 a, u32x4 b, f32x16 c) { return __builtin_amdgcn_mfma_f32_32x32x16_bf16(__builtin_bit_cast(s16x8, a), __builtin_bit_cast(s16x8, b), c, 0, 0, 0); }
DI f32x4 mfma16(u32x4 a, u32x4 b, f32x4 c) { return __builtin_amdgcn_mfma_f32_16x16x32_bf16(__builtin_bit_cast(s16x8, a), __builtin_bit_cast(s16x8, b), c, 0, 0, 0); }
DI u32 cvtpk(float lo, float hi) { f32x2 v = {lo, hi}; bf16x2 b = __builtin_convertvector(v, bf16x2); return __builtin_bit_cast(u32, b); }
DI float u2f(u32 v) { return __builtin_bit_cast(float, v); }
DI u32 f2u(float v) { return __builtin_bit_cast(u32, v); }
DI float bflo(u32 v) { return __builtin_bit_cast(float, v << 16); }
DI float bfhi(u32 v) { return __builtin_bit_cast(float, v & 0xffff0000u); }
DI float fma_s(float a, float b, float c) { float r; asm("v_fma_f32 %0, %1, %2, %3" : "=v"(r) : "v"(a), "v"(b), "v"(c)); return r; }
DI float mul_s(float a, float b) { float r; asm("v_mul_f32_e32 %0, %1, %2" : "=v"(r) : "v"(a), "v"(b)); return r; }
DI float add_s(float a, float b) { float r; asm("v_add_f32_e32 %0, %1, %2" : "=v"(r) : "v"(a), "v"(b)); return r; }
DI float max_s(float a, float b) { float r; asm("v_max_f32_e32 %0, %1, %2" : "=v"(r) : "v"(a), "v"(b)); return r; }
DI float sel_s(float f, float t, unsigned long long m) { float r; asm("v_cndmask_b32_e64 %0, %1, %2, %3" : "=v"(r) : "v"(f), "v"(t), "s"(m)); return r; }
DI float red16_32_add(float v) {
  auto a = __builtin_amdgcn_permlane16_swap(f2u(v), f2u(v), false, false);
  v = u2f(a[0]) + u2f(a[1]);
  auto b = __builtin_amdgcn_permlane32_swap(f2u(v), f2u(v), false, false);
  return u2f(b[0]) + u2f(b[1]);
}
DI float red16_32_max(float v) {
  auto a = __builtin_amdgcn_permlane16_swap(f2u(v), f2u(v), false, false);
  v = __builtin_fmaxf(u2f(a[0]), u2f(a[1]));
  auto b = __builtin_amdgcn_permlane32_swap(f2u(v), f2u(v), false, false);
  return __builtin_fmaxf(u2f(b[0]), u2f(b[1]));
}
DI float red32_add(float v) {
  auto b = __builtin_amdgcn_permlane32_swap(f2u(v), f2u(v), false, false);
  return u2f(b[0]) + u2f(b[1]);
}
DI void stg_sc1(u32x4 v, rsrc_t rs, u32 voff, u32 soff) { __builtin_amdgcn_raw_buffer_store_b128(v, rs, voff, soff, AUX_SC1); }
DI void stg2_sc1(u32x2 v, rsrc_t rs, u32 voff, u32 soff) { __builtin_amdgcn_raw_buffer_store_b64(v, rs, voff, soff, AUX_SC1); }

template <int IMM> DI void ald_v_sc1(u32x4& d, rsrc_t rs, u32 voff, u32 soff) {
  asm volatile("buffer_load_dwordx4 %0, %1, %2, %3 offen offset:%4 sc1" : "=v"(d) : "v"(voff), "s"(rs), "s"(soff), "i"(IMM) : "memory");
}
template <int IMM> DI void ald_a(u32x4& d, rsrc_t rs, u32 voff, u32 soff) {
  asm volatile("buffer_load_dwordx4 %0, %1, %2, %3 offen offset:%4" : "=a"(d) : "v"(voff), "s"(rs), "s"(soff), "i"(IMM) : "memory");
}
template <int IMM> DI void ald_v(u32x4& d, rsrc_t rs, u32 voff, u32 soff) {
  asm volatile("buffer_load_dwordx4 %0, %1, %2, %3 offen offset:%4" : "=v"(d) : "v"(voff), "s"(rs), "s"(soff), "i"(IMM) : "memory");
}
DI void ald2_sc1(u32x2& d, rsrc_t rs, u32 voff, u32 soff) {
  asm volatile("buffer_load_dwordx2 %0, %1, %2, %3 offen sc1" : "=v"(d) : "v"(voff), "s"(rs), "s"(soff) : "memory");
}
DI void ald2(u32x2& d, rsrc_t rs, u32 voff, u32 soff) {
  asm volatile("buffer_load_dwordx2 %0, %1, %2, %3 offen" : "=v"(d) : "v"(voff), "s"(rs), "s"(soff) : "memory");
}
DI void ald1(u32& d, rsrc_t rs, u32 voff, u32 soff) {
  asm volatile("buffer_load_dword %0, %1, %2, %3 offen" : "=v"(d) : "v"(voff), "s"(rs), "s"(soff) : "memory");
}
template <int IMM> DI void ald_a_sc1(u32x4& d, rsrc_t rs, u32 voff, u32 soff) {
  asm volatile("buffer_load_dwordx4 %0, %1, %2, %3 offen offset:%4 sc1" : "=a"(d) : "v"(voff), "s"(rs), "s"(soff), "i"(IMM) : "memory");
}
template <int N> DI void waitpin_a(u32x4& r) { asm volatile("s_waitcnt vmcnt(%1)" : "+a"(r) : "i"(N) : "memory"); }
template <int N> DI void waitpin_a2(u32x4& r0, u32x4& r1) { asm volatile("s_waitcnt vmcnt(%2)" : "+a"(r0), "+a"(r1) : "i"(N) : "memory"); }
DI void mma32(f32x16& acc, const u32x4& wv, const u32x4& xa) { asm volatile("v_mfma_f32_32x32x16_bf16 %0, %1, %2, %0" : "+v"(acc) : "v"(wv), "a"(xa)); }
DI void mma32_0(f32x16& acc, const u32x4& wv, const u32x4& xa) { asm volatile("v_mfma_f32_32x32x16_bf16 %0, %1, %2, 0" : "=&v"(acc) : "v"(wv), "a"(xa)); }
DI void mma32w(f32x16& acc, const u32x4& wv, const u32x4& xa, const u32x4& wn) { asm volatile("v_mfma_f32_32x32x16_bf16 %0, %1, %2, %0" : "+v"(acc) : "v"(wv), "a"(xa), "v"(wn)); }
DI void mma32w_0(f32x16& acc, const u32x4& wv, const u32x4& xa, const u32x4& wn) { asm volatile("v_mfma_f32_32x32x16_bf16 %0, %1, %2, 0" : "=&v"(acc) : "v"(wv), "a"(xa), "v"(wn)); }
DI void mma16(f32x4& acc, const u32x4& av, const u32x4& bv) { asm volatile("v_mfma_f32_16x16x32_bf16 %0, %1, %2, %0" : "+v"(acc) : "v"(av), "v"(bv)); }
DI void mma16_0(f32x4& acc, const u32x4& av, const u32x4& bv) { asm volatile("v_mfma_f32_16x16x32_bf16 %0, %1, %2, 0" : "=&v"(acc) : "v"(av), "v"(bv)); }
DI void pin_vv(u32x4& a, u32x4& b) { asm volatile("" : "+v"(a), "+v"(b)); }
template <int N> DI void waitpin_v(u32x4& r) { asm volatile("s_waitcnt vmcnt(%1)" : "+v"(r) : "i"(N) : "memory"); }
DI void pin_v(u32x4& r) { asm volatile("" : "+v"(r)::"memory"); }
DI void pin_a(u32x4& r) { asm volatile("" : "+a"(r)::"memory"); }
DI void pin2(u32x2& r) { asm volatile("" : "+v"(r)::"memory"); }
DI void pin1(u32& r) { asm volatile("" : "+v"(r)::"memory"); }
#define DMA_F(TAG, IMM, SC, rs, voff, soff, m0v) \
  asm volatile("s_mov_b32 m0, %2\n\ts_nop 0\n\tbuffer_load_dwordx4 %0, %1, %3 offen offset:%4 " SC "lds ; DMA " TAG ::"v"(voff), "s"(rs), "s"(m0v), "s"(soff), "i"(IMM) : "memory")
#define DMA_N(TAG, IMM, SC, rs, voff, soff) \
  asm volatile("buffer_load_dwordx4 %0, %1, %2 offen offset:%3 " SC "lds ; DMA " TAG ::"v"(voff), "s"(rs), "s"(soff), "i"(IMM) : "memory")

DI void stw(u32x4 v, rsrc_t rs, u32 off) { __builtin_amdgcn_raw_buffer_store_b128(v, rs, off, 0, 0); }
DI void stw2(u32x2 v, rsrc_t rs, u32 voff, u32 soff) { __builtin_amdgcn_raw_buffer_store_b64(v, rs, voff, soff, 0); }
DI float dot2ones(u32 v, float c) { return __builtin_amdgcn_fdot2_f32_bf16(__builtin_bit_cast(bf16x2, v), __builtin_bit_cast(bf16x2, 0x3f803f80u), c, false); }
__global__ __launch_bounds__(256) void prep_kernel(const u16* __restrict__ x0, const u16* __restrict__ wqkv, const u16* __restrict__ wo,
                                                   const u16* __restrict__ wg, const u16* __restrict__ wu, const u16* __restrict__ wd, char* __restrict__ ws) {
  __shared__ __attribute__((aligned(16))) char smem_[81920 + 1024];
  const int tid = threadIdx.x, l = tid & 63, w = __builtin_amdgcn_readfirstlane(tid >> 6), u = blockIdx.x;
  const rsrc_t rs = __builtin_amdgcn_make_buffer_rsrc(ws, 0, WS_USED, 0x00020000);
  LAS char* const lds = (LAS char*)smem_;
  const u32 lds0 = (u32)(size_t)lds;
  { for (int i = u * 256 + tid; i < (int)(FLAG_BYTES / 128); i += 65536) ((u32*)ws)[i * 32] = 0u; }
  const int type = u < 60 ? 0 : u < 124 ? 3 : u < 178 ? 1 : u < 232 ? 2 : u < 252 ? 4 : 5;
  const int idx = u < 60 ? u : u < 124 ? u - 60 : u < 178 ? u - 124 : u < 232 ? u - 178 : u - 232;
  const u16* src = type == 0 ? wqkv : type == 1 ? wg : type == 2 ? wu : type == 3 ? x0 : wo;
  const u32 pitch = type == 0 ? 3840 : type <= 2 ? 3456 : 1280;
  const u32 nrec = type == 0 ? 640 * 3840 : type <= 2 ? 640 * 3456 : type == 3 ? 2048 * 1280 : type == 4 ? 640 * 1280 : 0;
  const rsrc_t rsrcA = __builtin_amdgcn_make_buffer_rsrc((void*)src, 0, nrec, 0x00020000);
  const rsrc_t rsrcD = __builtin_amdgcn_make_buffer_rsrc((void*)wd, 0, 1728 * 1280, 0x00020000);
  {
    const u32 voW = (16 * w + (l >> 2)) * pitch + idx * 64 + (l & 3) * 16;
    const u32 voO = ((idx & 1) * 320 + 8 * w + (l >> 3)) * 1280 + (idx >> 1) * 128 + (l & 7) * 16;
    const u32 vo = type == 4 ? voO : voW, step = type == 4 ? 32 * 1280 : 64 * pitch;
#pragma unroll
    for (int j = 0; j < 10; ++j) {
      const u32 cidx = 64 * (4 * j + w) + l, row = (cidx * 52429u) >> 22, cc = cidx - row * 80;
      const u32 vx = (32 * idx + row) * 1280 + ((cc ^ (row & 7)) * 16);
      const u32 v = type == 3 ? vx : vo + j * step;
      DMA_F("SA", 0, "", rsrcA, v, 0u, lds0 + (4 * j + w) * 1024);
    }
#pragma unroll
    for (int t = 0; t < 3; ++t) {
      const u32 d = u + 256 * t;
      const u32 v = d < 540 ? ((d / 10) * 32 + 8 * w + (l >> 3)) * 1280 + (d % 10) * 128 + (l & 7) * 16 : 0x7fffffffu;
      DMA_F("SB", 0, "", rsrcD, v, 0u, lds0 + 40960 + t * 4096 + w * 1024);
    }
  }
  WAITV(0);
  BAR();
  const int gam = l >> 4, q = (l & 15) >> 2, pp = l & 3;
#pragma unroll
  for (int t = 0; t < 3; ++t) {
    const u32 d = u + 256 * t;
    if (d < 540) {
      const u32 cg = d % 10, s_ = d / 10, c = 64 * cg + l, c8 = c / 80, nb = (c % 80) >> 4, n = c & 15;
      const u32 r0 = 16 * (w >> 1) + 4 * (w & 1) + q;
      const u32x2 a = __builtin_bit_cast(u32x2, __builtin_amdgcn_ds_read_tr16_b64_v4i16((LAS s16x4*)(lds + 40960 + t * 4096 + r0 * 128 + gam * 32 + pp * 8)));
      const u32x2 b2 = __builtin_bit_cast(u32x2, __builtin_amdgcn_ds_read_tr16_b64_v4i16((LAS s16x4*)(lds + 40960 + t * 4096 + (r0 + 8) * 128 + gam * 32 + pp * 8)));
      stw(u32x4{a.x, a.y, b2.x, b2.y}, rs, OFF_WD + (c8 * 54 + s_) * 5120 + nb * 1024 + n * 16 + w * 256);
    }
  }
  if (type <= 2) {
    const u32 ra = ((gam >> 1) * 8 + q) * 64 + (gam & 1) * 32 + pp * 8;
    u32x4 sl[10];
#pragma unroll
    for (int j = 0; j < 10; ++j) {
      const u32 o = ra + (4 * j + w) * 1024;
      const u32x2 a = __builtin_bit_cast(u32x2, __builtin_amdgcn_ds_read_tr16_b64_v4i16((LAS s16x4*)(lds + o)));
      const u32x2 b2 = __builtin_bit_cast(u32x2, __builtin_amdgcn_ds_read_tr16_b64_v4i16((LAS s16x4*)(lds + o + 256)));
      sl[j] = u32x4{a.x, a.y, b2.x, b2.y};
    }
    const int c = 32 * idx + (l & 31), hh = l >> 5;
    u32 dbase; int dstep;
    if (type == 0) {
      const int sec = c / 640, cc = c % 640, p = cc / 40, r40 = cc % 40;
      const int n = (sec == 1 ? 0 : sec == 2 ? 40 : 80) + r40;
      const int b = n >> 5, nl = n & 31;
      dbase = OFF_WQ + p * 163840 + (b < 3 ? b * 1024 : 122880) + nl * 16;
      dstep = b < 3 ? 3072 : 1024;
    } else {
      const int fb = c >> 4, nl = (c & 15) + 16 * (type - 1);
      const int half = fb / 54, rem = fb % 54;
      const int sl_ = 8 * half + (rem < 42 ? rem / 7 : 6 + (rem - 42) / 6), ix = rem < 42 ? rem % 7 : (rem - 42) % 6;
      dbase = OFF_WG + sl_ * 286720 + (ix < 3 ? ix * 1024 : ix < 5 ? 122880 + (ix - 3) * 1024 : 204800 + (ix - 5) * 1024) + nl * 16;
      dstep = ix < 3 ? 3072 : 2048;
    }
    float c0 = 0.f, c1 = 0.f;
#pragma unroll
    for (int j = 0; j < 10; ++j) { c0 = dot2ones(sl[j][0], c0); c1 = dot2ones(sl[j][1], c1); c0 = dot2ones(sl[j][2], c0); c1 = dot2ones(sl[j][3], c1); }
    const float cs = red32_add(c0 + c1);
    LAS float* red = (LAS float*)(lds + 81920);
    if (l < 32) red[w * 32 + l] = cs;
    WAITL0(); BAR();
    const float mean = ((red[l & 31] + red[32 + (l & 31)]) + (red[64 + (l & 31)] + red[96 + (l & 31)])) * (1.0f / 640.0f);
#pragma unroll
    for (int j = 0; j < 10; ++j) {
      u32x4 v;
#pragma unroll
      for (int e = 0; e < 4; ++e) v[e] = cvtpk(bflo(sl[j][e]) - mean, bfhi(sl[j][e]) - mean);
      stw(v, rs, dbase + (4 * j + w) * dstep + hh * 512);
    }
  } else if (type == 3) {
    const int tok = l & 31, h = l >> 5;
    u32x4 sl[10];
#pragma unroll
    for (int i = 0; i < 10; ++i) sl[i] = *(const LAS u32x4*)(lds + tok * 1280 + (((2 * (10 * w + i) + h) ^ (tok & 7)) * 16));
    float s1 = 0.f, s2 = 0.f;
#pragma unroll
    for (int i = 0; i < 10; ++i) {
      stw(sl[i], rs, OFF_XF + (idx * 40 + 10 * w + i) * 1024 + l * 16);
#pragma unroll
      for (int e = 0; e < 4; ++e) { s1 = dot2ones(sl[i][e], s1); const float a_ = bflo(sl[i][e]), b_ = bfhi(sl[i][e]); s2 += a_ * a_ + b_ * b_; }
    }
    s1 = red32_add(s1); s2 = red32_add(s2);
    if (l < 32) {
      stw2(u32x2{f2u(s1), f2u(s2)}, rs, (w * 2048 + 32 * idx + tok) * 8, OFF_ST);
      stw2(u32x2{0u, 0u}, rs, ((w + 4) * 2048 + 32 * idx + tok) * 8, OFF_ST);
    }
  } else if (type == 4) {
    const int cg = idx >> 1, kk = idx & 1, c = 64 * cg + l, c8 = c / 80, nb = (c % 80) >> 4, n = c & 15;
    const u32 dbase = OFF_WO + c8 * 20 * 5120 + nb * 1024 + n * 16;
#pragma unroll
    for (int j = 0; j < 10; ++j) {
      const int o = 4 * j + w, pl = o / 5, ii = o % 5, sg = 40 * kk + o, r0 = 40 * pl + 4 * ii + q;
      const u32x2 a = __builtin_bit_cast(u32x2, __builtin_amdgcn_ds_read_tr16_b64_v4i16((LAS s16x4*)(lds + r0 * 128 + gam * 32 + pp * 8)));
      const u32x2 b2 = __builtin_bit_cast(u32x2, __builtin_amdgcn_ds_read_tr16_b64_v4i16((LAS s16x4*)(lds + (r0 + 20) * 128 + gam * 32 + pp * 8)));
      stw(u32x4{a.x, a.y, b2.x, b2.y}, rs, dbase + (sg >> 2) * 5120 + (sg & 3) * 256);
    }
  }
}

#define STAMP(i) do {} while (0)
#define RSTAMP(i) do {} while (0)
#define GSTAMP(i) STAMP(i)
#define QSTAMP(i) do {} while (0)
constexpr u32 OFF_STAMP = WS_USED + 4096;
constexpr u32 kSpinCap = 1u << 16;

struct K {
  rsrc_t rs, rso;
  LAS char* lds;
  u32 lds0;
  GAS u32* flags;
  int lane, w, m128, p, m64, c8, B0, nblk, tid, it;
  u32x2 xr[5];
  u32x4 wo_r[5][5];
  u32x4 wd_r[14][5];
};

#define PHASE_IDS() int lane = k.lane; asm volatile("" : "+v"(lane)); \
  int it = k.it; asm volatile("" : "+s"(it)); \
  const u32 xin = OFF_XF + it * 2 * SZ_XF, stin = OFF_ST + it * 2 * SZ_ST, x1o = xin + SZ_XF, st1o = stin + SZ_ST; \
  const u32 ofo = OFF_OF + it * SZ_OF, hfo = OFF_HF + it * SZ_HF, fgb = it * FG_PER_IT; \
  (void)xin; (void)stin; (void)x1o; (void)st1o; (void)ofo; (void)hfo; (void)fgb; \
  int w = k.w, m128 = k.m128, p = k.p, m64 = k.m64, c8 = k.c8, B0 = k.B0; \
  asm volatile("" : "+s"(w), "+s"(m128), "+s"(p), "+s"(m64), "+s"(c8), "+s"(B0)); \
  const u32 l16 = lane * 16; const int tok = lane & 31, h = lane >> 5, t16 = lane & 15, g = lane >> 4; \
  const rsrc_t rs = k.rs; LAS char* const lds = k.lds; const u32 lds0 = k.lds0; \
  (void)tok; (void)h; (void)t16; (void)g; (void)l16; (void)m128; (void)p; (void)m64; (void)c8; (void)B0; (void)lds; (void)lds0; (void)rs

DI void poll_flags(GAS u32* flags, u32 fo, bool act, int lane) {
  for (u32 spins = 0;;) {
    u32 v = act ? __hip_atomic_load(flags + fo, RLX_AGENT) : 1u;
    if (__all(v == 1u)) break;
    __builtin_amdgcn_s_sleep(1);
    if (++spins > kSpinCap) {
      if (lane == 0) __hip_atomic_store(flags + FG_TMO * 512, 1u, RLX_AGENT);
      break;
    }
    if ((spins & 255u) == 0u && __builtin_amdgcn_readfirstlane(__hip_atomic_load(flags + FG_TMO * 512, RLX_AGENT)) != 0u) break;
  }
}
#define POLL_GAP 2
struct Polls { u32 a, b, c; };
DI void poll_issue(u32& d, const GAS u32* p) { asm volatile("global_load_dword %0, %1, off sc1 ; POLL" : "=v"(d) : "v"(p) : "memory"); }
DI bool poll_check(u32& d, bool act, u32 want) { asm volatile("s_waitcnt vmcnt(2) ; POLLWAIT" : "+v"(d)::"memory"); return __all(!act || d == want); }
DI void poll_flags_p(GAS u32* flags, u32 fo, bool act, int lane, u32 want, Polls& pl) {
  const GAS u32* p = flags + (act ? fo : FG_TMO * 512);
  asm volatile("; POLL_BEGIN" ::: "memory");
  poll_issue(pl.a, p); __builtin_amdgcn_s_sleep(POLL_GAP); poll_issue(pl.b, p); __builtin_amdgcn_s_sleep(POLL_GAP); poll_issue(pl.c, p);
  for (u32 spins = 0;;) {
    if (poll_check(pl.a, act, want)) break;
    poll_issue(pl.a, p);
    if (poll_check(pl.b, act, want)) break;
    poll_issue(pl.b, p);
    if (poll_check(pl.c, act, want)) break;
    poll_issue(pl.c, p);
    if (++spins > kSpinCap) {
      if (lane == 0) __hip_atomic_store(flags + FG_TMO * 512, want, RLX_AGENT);
      break;
    }
    if ((spins & 63u) == 0u && (u32)__builtin_amdgcn_readfirstlane(__hip_atomic_load(flags + FG_TMO * 512, RLX_AGENT)) == want) break;
  }
  asm volatile("; POLL_END" ::: "memory");
}
DI void poll_pin(Polls& pl) { asm volatile("; POLL_PIN" : "+v"(pl.a), "+v"(pl.b), "+v"(pl.c)::"memory"); }
DI void stats_finish(float s1, float s2, float& nmean, float& rstd) {
  const float mean = s1 * (1.0f / 640.0f);
  const float var = __builtin_fmaxf(s2 * (1.0f / 640.0f) - mean * mean, 0.f);
  nmean = -mean; rstd = __builtin_amdgcn_rsqf(var + 1e-5f);
}

template <int C, int SLOT, int I> DI void dma_q(rsrc_t rs, u32 l16, u32 sQ, int w, u32 lds0) {
#define Q_EMIT(TAG, FIRST, IMM, SO, MO) do { if constexpr (FIRST) DMA_F(TAG, IMM, "", rs, l16, SO, MO); else DMA_N(TAG, IMM, "", rs, l16, SO); } while (0)
  if constexpr (C < 3) {
    constexpr int grp = I / 4, imm = (I % 4) * 1024;
    const u32 so = sQ + w * 10240 + C * 40960 + grp * 4096, mo = lds0 + SLOT * 40960 + w * 10240 + grp * 4096;
    if constexpr (C == 0) Q_EMIT("Q0", I % 4 == 0, imm, so, mo); else if constexpr (C == 1) Q_EMIT("Q1", I % 4 == 0, imm, so, mo); else Q_EMIT("Q2", I % 4 == 0, imm, so, mo);
  } else {
    constexpr int hf = I / 5, ii = I % 5, grp = ii / 4, imm = (ii % 4) * 1024;
    const u32 so = sQ + w * 5120 + 122880 + hf * 20480 + grp * 4096, mo = lds0 + SLOT * 40960 + w * 5120 + hf * 20480 + grp * 4096;
    if constexpr (hf == 0) Q_EMIT("Q3a", ii % 4 == 0, imm, so, mo); else Q_EMIT("Q3b", ii % 4 == 0, imm, so, mo);
  }
#undef Q_EMIT
}
template <int HH, int I> DI void dma_g(rsrc_t rs, u32 l16, u32 sG, int w, u32 lds0) {
  constexpr int grp = I / 4, imm = (I % 4) * 1024;
  const u32 so = sG + HH * 20480 + grp * 4096, mo = lds0 + w * 5120 + (HH % 7) * 20480 + grp * 4096;
#define G_CASE(n) if constexpr (HH == n) { if constexpr (I % 4 == 0) DMA_F("G" #n, imm, "", rs, l16, so, mo); else DMA_N("G" #n, imm, "", rs, l16, so); }
  G_CASE(0) G_CASE(1) G_CASE(2) G_CASE(3) G_CASE(4) G_CASE(5) G_CASE(6) G_CASE(7) G_CASE(8) G_CASE(9) G_CASE(10) G_CASE(11) G_CASE(12) G_CASE(13)
#undef G_CASE
}

template <int HH> DI void dma_g3(rsrc_t rs, u32 l16, u32 sG0, int w, u32 lds0) {
  const u32 f0 = 7 * (w - 1);
  const u32 so = sG0 + HH * 20480 + f0 * 1024, mo = lds0 + (HH % 7) * 20480 + f0 * 1024;
#define G3_CASE(n) if constexpr (HH == n) { DMA_F("G" #n, 0, "", rs, l16, so, mo); DMA_N("G" #n, 1024, "", rs, l16, so); DMA_N("G" #n, 2048, "", rs, l16, so); DMA_N("G" #n, 3072, "", rs, l16, so); \
    DMA_F("G" #n, 0, "", rs, l16, so + 4096, mo + 4096); DMA_N("G" #n, 1024, "", rs, l16, so + 4096); if (w < 3) DMA_N("G" #n, 2048, "", rs, l16, so + 4096); }
  G3_CASE(2) G3_CASE(3) G3_CASE(4) G3_CASE(5) G3_CASE(6)
#undef G3_CASE
}
constexpr int XW = 10;
struct QS {
  static constexpr bool x_at(int f) { return f < 120 && f % 3 == 0 && f / 3 + XW < 40; }
  static constexpr int aux_at(int f) { return (f < 32 && f % 3 == 1) ? f / 3 : -1; }
  static constexpr int q3_at(int f) { return (f >= 77 && f <= 104 && f % 3 == 2) ? (f - 77) / 3 : -1; }
  static constexpr int wo_at(int f) {
    if (f >= 34 && f <= 67 && f % 3 == 1) return (f - 34) / 3;
    if (f >= 121 && f <= 157 && (f - 121) % 3 == 0) return 12 + (f - 121) / 3;
    return -1;
  }
  static constexpr int upto(int F) { int n = 0; for (int f = 0; f <= F; ++f) n += x_at(f) + (aux_at(f) >= 0) + (q3_at(f) >= 0) + (wo_at(f) >= 0); return n; }
  static constexpr int xidx(int i) { return i < XW ? i : XW + upto(3 * (i - XW)) - 1; }
  static constexpr int xwait2(int s) { return XW + upto(3 * s) - 1 - xidx(s + 1); }
};
struct GS {
  static constexpr int NWD = 65;
  static constexpr bool x_at(int f) { return f < 120 && f % 3 == 0 && f / 3 + XW < 40; }
  static constexpr int op(int F) {
    if (F <= 10 && F % 3 == 1) return 3000 + F / 3;
    if (F >= 40 && F <= 67 && F % 3 == 1) return (7 + (F - 40) / 15) * 8 + ((F - 40) % 15) / 3;
    if (F >= 79 && F <= 106 && F % 3 == 1) return (9 + (F - 79) / 15) * 8 + ((F - 79) % 15) / 3;
    if (F < 120 || (F & 1)) return -1;
    if (F < 140) return (11 + (F - 120) / 10) * 8 + ((F - 120) % 10) / 2;
    if (F < 160) return 1000 + (F - 140) / 2;
    if (F < 170) return 13 * 8 + (F - 160) / 2;
    if (F < 280) return 1010 + (F - 170) / 2;
    return -1;
  }
  static constexpr bool st_at(int F) { return F == 139 || F == 157 || F == 175 || F == 219 || F == 237; }
  static constexpr int upto(int F) { int n = 0; for (int f = 0; f <= F; ++f) n += x_at(f) + (op(f) >= 0) + st_at(f); return n; }
  static constexpr int slot(int o) { for (int f = 0; f < 300; ++f) if (op(f) == o) return f; return -1; }
  static constexpr int xidx(int i) { return i < XW ? i : XW + upto(3 * (i - XW)) - 1; }
  static constexpr int xwait2(int s) { return XW + upto(3 * s) - 1 - xidx(s + 1); }
};
static_assert(GS::op(278) == 1000 + GS::NWD - 1, "down-weight prefetch schedule");

DI void iteration(K& k) {
  GAS u32* const flags = k.flags;
  const int tid = k.tid;
  {
    PHASE_IDS();
    const u32 sQ = OFF_WQ + p * 163840, sG = OFF_WG + p * 286720 + w * 5120;
    STAMP(0);
    if (it > 0 && w == 0) {
      const int grp = 2 * m128 - 1 + (lane >> 3);
      const bool act = lane < 24 && grp >= 0;
      poll_flags(flags, ((FG_X2 + grp) * 16 + (lane & 7)) * 32, act, lane);
    }
    WAITV(0); CERT("Q0"); CERT("Q1"); CERT("Q2");
    BAR();
    STAMP(1);
    u32x4 xf[40];
    const u32 xbase = xin + (4 * m128 + w) * 40960;
#define XLOAD(i) ald_a_sc1<((i) % 4) * 1024>(xf[i], rs, l16, xbase + ((i) / 4) * 4096)
    u32x2 hs0, hs1, sa0, sa1, sa2, sa3;
    const int rbh = m128 > 0 ? 4 * m128 - 1 : 0;
    const u32 hvo = (g >> 1) * 1024 + (g & 1) * 512 + (16 + t16) * 16;
    const int ht = 128 * m128 - 16 + t16;
    const u32 hso = ((2 * g) * 2048 + (ht < 0 ? 0 : ht)) * 8, sso = (4 * h * 2048 + 128 * m128 + 32 * w + tok) * 8;
    auto aux_load = [&](auto AC) {
      constexpr int a = decltype(AC)::value;
      if constexpr (a < 5) DMA_F("HALO", 0, "sc1 ", rs, hvo, xin + rbh * 40960 + (5 * w + a) * 2048, lds0 + L_HALO + (5 * w + a) * 1024);
      if constexpr (a == 5) ald2_sc1(sa0, rs, sso, stin);
      if constexpr (a == 6) ald2_sc1(sa1, rs, sso, stin + 16384);
      if constexpr (a == 7) ald2_sc1(sa2, rs, sso, stin + 32768);
      if constexpr (a == 8) ald2_sc1(sa3, rs, sso, stin + 49152);
      if constexpr (a == 9) ald2_sc1(hs0, rs, hso, stin);
      if constexpr (a == 10) ald2_sc1(hs1, rs, hso, stin + 16384);
    };
    sfor<XW>([&](auto I) { constexpr int i = decltype(I)::value; XLOAD(i); });
    using Q = QS;
    constexpr int DQ = 4, NBQ = 6;
    u32x4 wb[NBQ];
    f32x16 acc0, acc1, acc2, acc3;
    f32x4 hacc, hacc4;
    u32x4 hw[3], hx[3], h4w[3], h4x[3];
    float rstd = 0.f, hrs = 0.f;
    u32 lb1 = l16 + 65536, lbh = l16 + L_HALO; asm volatile("" : "+v"(lb1), "+v"(lbh));
    u32 hwlo = (w >> 1) * 1024 + (g >> 1) * 3072 + (g & 1) * 512 + (16 * (w & 1) + t16) * 16, hwhi = hwlo + 61440;
    u32 hw4 = 2048 + (g >> 1) * 3072 + (g & 1) * 512 + t16 * 16 + w * 30720, hx4 = l16 + L_HALO + w * 5120;
    asm volatile("" : "+v"(hwlo), "+v"(hwhi), "+v"(hw4), "+v"(hx4));
    const u32 kvrow = L_KST + (16 + 32 * w + tok) * 80 + h * 8;
    const int hn = 16 * w + 4 * g;
    const u32 haddr = L_KST + t16 * 80 + 2 * hn + (hn >= 40 ? (L_VST - L_KST - 80) : 0);
    const u32 haddr4 = L_VST + t16 * 80 + 2 * (64 + 4 * g - 40);
    const bool hzero = (m128 == 0);
    const u32 wo_o = OFF_WO + (c8 * 20 + 5 * w) * 5120;
#define LDSQ(F) (((F) / 40 % 3) * 40960 + ((F) % 40) * 1024)
#define RDW(off) (*(const LAS u32x4*)(lds + ((off) < 65536 ? l16 : lb1) + ((off) & 65535)))
    u32 gp0 = 0;
#define STG_OFF(n0) ((n0) < 40 ? 2 * (n0) : (n0) < 80 ? (L_VST - L_KST) + 2 * ((n0) - 40) : (L_QST - L_KST - 16 * 80) + 2 * ((n0) - 80))
#define GRAN(t, ACC, q, RS, STADDR, ZERO) do { \
      if constexpr ((t) == 0) gp0 = cvtpk(mul_s(ACC[4 * (q)], RS), mul_s(ACC[4 * (q) + 1], RS)); \
      if constexpr ((t) == 1) { u32x2 pk_ = {gp0, cvtpk(mul_s(ACC[4 * (q) + 2], RS), mul_s(ACC[4 * (q) + 3], RS))}; if (ZERO) pk_ = u32x2{0u, 0u}; *(LAS u32x2*)(lds + (STADDR)) = pk_; } \
    } while (0)
#define GRAN_MAIN(t, ACC, q, n0) GRAN(t, ACC, q, rstd, kvrow + STG_OFF(n0), false)
#pragma unroll
    for (int F = 0; F < DQ; ++F) wb[F % NBQ] = RDW(LDSQ(F));
    sfor<160>([&](auto FC) {
      constexpr int F = decltype(FC)::value, Fr = F + DQ;
      if constexpr (F == 30) QSTAMP(21);
      if constexpr (F == 60) QSTAMP(22);
      if constexpr (F == 90) QSTAMP(23);
      if constexpr (F == 140) QSTAMP(26);
      if constexpr (Q::x_at(F)) XLOAD(F / 3 + XW);
      if constexpr (Q::aux_at(F) >= 0) aux_load(std::integral_constant<int, Q::aux_at(F)>{});
      if constexpr (Q::q3_at(F) >= 0) dma_q<3, 0, Q::q3_at(F)>(rs, l16, sQ, w, lds0);
      if constexpr (Q::wo_at(F) >= 0) {
        constexpr int i = Q::wo_at(F), s = i / 5, nb = i % 5;
        if constexpr (i < 24) ald_a<(i % 4) * 1024>(k.wo_r[s][nb], rs, l16, wo_o + (i / 4) * 4096); else ald_v<(i % 4) * 1024>(k.wo_r[s][nb], rs, l16, wo_o + (i / 4) * 4096);
      }
      if constexpr (Fr == 40) { WAITV(Q::upto(F) - Q::upto(13)); CERT("HALO"); BAR(); }
      if constexpr (Fr == 80) { WAITL0(); BAR(); }
      if constexpr (Fr == 120) { WAITV(Q::upto(F) - Q::upto(89)); CERT("Q3a"); WAITL0(); BAR(); STAMP(2); }
      if constexpr (Fr == 140) { WAITV(Q::upto(F) - Q::upto(104)); CERT("Q3b"); BAR(); }
      if constexpr (Fr < 160) wb[Fr % NBQ] = RDW(LDSQ(Fr));
      if constexpr (F < 120) {
        constexpr int s = F / 3, b = F % 3;
        if constexpr (b == 0 && (s & 1) == 0) waitpin_a2<Q::xwait2(s)>(xf[s], xf[s + 1]);
#define QMMA(ACC) do { if constexpr (s == 0) { if constexpr ((F & 1) == 0) mma32w_0(ACC, wb[F % NBQ], xf[s], wb[(F + 1) % NBQ]); else mma32_0(ACC, wb[F % NBQ], xf[s]); } \
                       else { if constexpr ((F & 1) == 0) mma32w(ACC, wb[F % NBQ], xf[s], wb[(F + 1) % NBQ]); else mma32(ACC, wb[F % NBQ], xf[s]); } } while (0)
        if constexpr (b == 0) QMMA(acc0); else if constexpr (b == 1) QMMA(acc1); else QMMA(acc2);
        if constexpr (F == 62) {
          pin2(sa0); pin2(sa1); pin2(sa2); pin2(sa3);
          float nm;
          stats_finish(red32_add((u2f(sa0.x) + u2f(sa1.x)) + (u2f(sa2.x) + u2f(sa3.x))), red32_add((u2f(sa0.y) + u2f(sa1.y)) + (u2f(sa2.y) + u2f(sa3.y))), nm, rstd);
        }
        if constexpr (F == 110) {
          pin2(hs0); pin2(hs1);
          float nm;
          stats_finish(red16_32_add(u2f(hs0.x) + u2f(hs1.x)), red16_32_add(u2f(hs0.y) + u2f(hs1.y)), nm, hrs);
        }
        if constexpr (F == 113) {
#pragma unroll
          for (int nb = 0; nb < 5; ++nb) pin2(k.xr[nb]);
        }
        if constexpr (b == 1) {
          if constexpr (s == 15) mma16_0(hacc, hw[0], hx[0]);
          if constexpr (s > 15 && s <= 34) mma16(hacc, hw[(s - 15) % 3], hx[(s - 15) % 3]);
          if constexpr (s >= 13 && s <= 32) {
            constexpr int sg = s - 13;
            hw[sg % 3] = *(const LAS u32x4*)(lds + (sg < 10 ? hwlo : hwhi) + (sg % 10) * 6144);
            hx[sg % 3] = *(const LAS u32x4*)(lds + lbh + sg * 1024);
          }
        }
      } else {
        constexpr int s = F - 120;
        if constexpr (s == 0) mma32w_0(acc3, wb[F % NBQ], xf[s], wb[(F + 1) % NBQ]); else if constexpr (s & 1) mma32(acc3, wb[F % NBQ], xf[s]); else mma32w(acc3, wb[F % NBQ], xf[s], wb[(F + 1) % NBQ]);
        constexpr int ms = s - 2, gi = ms / 2, st = ms % 2;
        if constexpr (ms >= 0 && gi < 12) {
          constexpr int b = gi >> 2, q = gi & 3, n0 = 32 * b + 8 * q;
          if constexpr (b == 0) GRAN_MAIN(st, acc0, q, n0); else if constexpr (b == 1) GRAN_MAIN(st, acc1, q, n0); else GRAN_MAIN(st, acc2, q, n0);
        }
        if constexpr (s == 22) {
          const f32x4 p0 = *(const LAS f32x4*)(lds + L_H4 + l16), p1 = *(const LAS f32x4*)(lds + L_H4 + 1024 + l16);
          const f32x4 p2 = *(const LAS f32x4*)(lds + L_H4 + 2048 + l16), p3 = *(const LAS f32x4*)(lds + L_H4 + 3072 + l16);
          hacc4 = (p0 + p1) + (p2 + p3);
        }
        if constexpr (ms >= 0 && gi == 12) GRAN(st, hacc, 0, hrs, haddr, hzero);
        if constexpr (ms >= 0 && gi == 14) GRAN(st, hacc4, 0, hrs, haddr4, hzero);
      }
      if constexpr (F < 120 && F % 3 == 2) {
        constexpr int s = F / 3;
        if constexpr (s == 15) mma16_0(hacc4, h4w[0], h4x[0]);
        if constexpr (s > 15 && s <= 19) mma16(hacc4, h4w[(s - 15) % 3], h4x[(s - 15) % 3]);
        if constexpr (s >= 13 && s <= 17) {
          h4w[(s - 13) % 3] = *(const LAS u32x4*)(lds + hw4 + (s - 13) * 6144);
          h4x[(s - 13) % 3] = *(const LAS u32x4*)(lds + hx4 + (s - 13) * 1024);
        }
        if constexpr (s == 21) *(LAS f32x4*)(lds + L_H4 + w * 1024 + l16) = hacc4;
      }
      SB();
    });
    STAMP(3);
    asm volatile("s_nop 11" : "+v"(acc3));
    GRAN_MAIN(0, acc3, 0, 96); GRAN_MAIN(1, acc3, 0, 96); GRAN_MAIN(0, acc3, 1, 104); GRAN_MAIN(1, acc3, 1, 104); GRAN_MAIN(0, acc3, 2, 112); GRAN_MAIN(1, acc3, 2, 112);
    WAITL0(); BAR();
    STAMP(4);
    {
      const int par = t16 & 1;
      const bool ca = (4 * g + par) > t16, cb = (4 * g + 2 + par) > t16;
      const unsigned long long mca = __ballot(ca), mcb = __ballot(cb), mpar = __ballot(par != 0);
      const bool neg = (m128 == 0) && (w == 0);
      const unsigned long long nga = neg ? mca : 0ull, ngb = neg ? mcb : 0ull;
      const u32 gq = g < 2 ? g : 2;
      const u32 mA = g < 3 ? ~0u : 0u, mB = g < 2 ? ~0u : 0u, mC = (g == 1 || g == 2) ? ~0u : 0u;
      const u32 pla = par ? 0x01000c0cu : 0x0c0c0100u, plb = par ? 0x03020c0cu : 0x0c0c0302u;
      const u32 sel0 = ca ? pla : 0x0c0c0c0cu, sel2 = ca ? 0x0c0c0c0cu : pla, sel1 = cb ? plb : 0x0c0c0c0cu, sel3 = cb ? 0x0c0c0c0cu : plb;
      constexpr float cexp = 0.22360679774997896f * 1.4426950408889634f;
      float cx = cexp; asm volatile("" : "+v"(cx));
      const u32 qa = L_QST + (32 * w + t16) * 80 + gq * 16, ka = L_KST + (32 * w + t16) * 80 + gq * 16;
      const u32 va = L_VST + (32 * w + 4 * g + (t16 >> 2)) * 80 + (t16 & 3) * 8;
      u32x4 qf[4], k0[4], k1[4];
      u32x4 v0[4], v1[4];
#pragma unroll
      for (int u = 0; u < 4; ++u) {
        const u32 o = (u >> 1) * 16 * 80 + (u & 1) * 32;
        qf[u] = *(const LAS u32x4*)(lds + qa + o);
        k0[u] = *(const LAS u32x4*)(lds + ka + o);
        k1[u] = *(const LAS u32x4*)(lds + ka + o + 16 * 80);
      }
      dma_g<0, 0>(rs, l16, sG, w, lds0);
#pragma unroll
      for (int u = 0; u < 4; ++u) {
        if (u & 1) { qf[u][0] &= mC; qf[u][1] &= mC; qf[u][2] &= mA; qf[u][3] &= mA; }
        else { qf[u][0] &= mA; qf[u][1] &= mA; qf[u][2] &= mB; qf[u][3] &= mB; }
      }
      f32x4 s0[4], s1[4];
      asm volatile("s_nop 1\n\t"
                   "v_mfma_f32_16x16x32_bf16 %0, %8, %16, 0\n\tv_mfma_f32_16x16x32_bf16 %1, %12, %16, 0\n\t"
                   "v_mfma_f32_16x16x32_bf16 %2, %9, %17, 0\n\tv_mfma_f32_16x16x32_bf16 %3, %13, %17, 0\n\t"
                   "v_mfma_f32_16x16x32_bf16 %4, %10, %18, 0\n\tv_mfma_f32_16x16x32_bf16 %5, %14, %18, 0\n\t"
                   "v_mfma_f32_16x16x32_bf16 %6, %11, %19, 0\n\tv_mfma_f32_16x16x32_bf16 %7, %15, %19, 0"
                   : "=&v"(s0[0]), "=&v"(s1[0]), "=&v"(s0[1]), "=&v"(s1[1]), "=&v"(s0[2]), "=&v"(s1[2]), "=&v"(s0[3]), "=&v"(s1[3])
                   : "v"(k0[0]), "v"(k0[1]), "v"(k0[2]), "v"(k0[3]), "v"(k1[0]), "v"(k1[1]), "v"(k1[2]), "v"(k1[3]), "v"(qf[0]), "v"(qf[1]), "v"(qf[2]), "v"(qf[3]));
#pragma unroll
      for (int u = 0; u < 4; ++u) {
        const u32 o = (u >> 1) * 16 * 80 + (u & 1) * 40;
        const u32x2 t0 = __builtin_bit_cast(u32x2, __builtin_amdgcn_ds_read_tr16_b64_v4i16((LAS s16x4*)(lds + va + o)));
        const u32x2 t1 = __builtin_bit_cast(u32x2, __builtin_amdgcn_ds_read_tr16_b64_v4i16((LAS s16x4*)(lds + va + o + 16 * 80)));
        const u32x2 t2 = __builtin_bit_cast(u32x2, __builtin_amdgcn_ds_read_tr16_b64_v4i16((LAS s16x4*)(lds + va + o + 32)));
        const u32x2 t3 = __builtin_bit_cast(u32x2, __builtin_amdgcn_ds_read_tr16_b64_v4i16((LAS s16x4*)(lds + va + o + 32 + 16 * 80)));
        v0[u] = u32x4{t0.x, t0.y, t1.x, t1.y}; v1[u] = u32x4{t2.x, t2.y, t3.x, t3.y};
      }
      dma_g<0, 1>(rs, l16, sG, w, lds0);
      asm volatile("s_nop 7" : "+v"(s0[0]), "+v"(s1[0]), "+v"(s0[1]), "+v"(s1[1]), "+v"(s0[2]), "+v"(s1[2]), "+v"(s0[3]), "+v"(s1[3]));
      float a[4], b[4], mx[4], sm[4];
#pragma unroll
      for (int u = 0; u < 4; ++u) {
        const float x00 = s0[u][0], x01 = s0[u][1], x02 = s0[u][2], x03 = s0[u][3], x10 = s1[u][0], x11 = s1[u][1], x12 = s1[u][2], x13 = s1[u][3];
        a[u] = sel_s(sel_s(x10, x11, mpar), sel_s(x00, x01, mpar), mca);
        b[u] = sel_s(sel_s(x12, x13, mpar), sel_s(x02, x03, mpar), mcb);
        if (u < 2) { a[u] = sel_s(a[u], -__builtin_inff(), nga); b[u] = sel_s(b[u], -__builtin_inff(), ngb); }
        mx[u] = max_s(a[u], b[u]);
      }
      dma_g<0, 2>(rs, l16, sG, w, lds0);
#pragma unroll
      for (int u = 0; u < 4; ++u) { auto r = __builtin_amdgcn_permlane16_swap(f2u(mx[u]), f2u(mx[u]), false, false); mx[u] = max_s(u2f(r[0]), u2f(r[1])); }
      dma_g<0, 3>(rs, l16, sG, w, lds0);
#pragma unroll
      for (int u = 0; u < 4; ++u) { auto r = __builtin_amdgcn_permlane32_swap(f2u(mx[u]), f2u(mx[u]), false, false); mx[u] = max_s(u2f(r[0]), u2f(r[1])); }
      dma_g<0, 4>(rs, l16, sG, w, lds0);
#pragma unroll
      for (int u = 0; u < 4; ++u) {
        const float t = -mul_s(mx[u], cx);
        a[u] = __builtin_amdgcn_exp2f(fma_s(a[u], cx, t)); b[u] = __builtin_amdgcn_exp2f(fma_s(b[u], cx, t));
        sm[u] = a[u] + b[u];
      }
      dma_g<1, 0>(rs, l16, sG, w, lds0);
#pragma unroll
      for (int u = 0; u < 4; ++u) { auto r = __builtin_amdgcn_permlane16_swap(f2u(sm[u]), f2u(sm[u]), false, false); sm[u] = u2f(r[0]) + u2f(r[1]); }
      dma_g<1, 1>(rs, l16, sG, w, lds0);
#pragma unroll
      for (int u = 0; u < 4; ++u) { auto r = __builtin_amdgcn_permlane32_swap(f2u(sm[u]), f2u(sm[u]), false, false); sm[u] = u2f(r[0]) + u2f(r[1]); }
      dma_g<1, 2>(rs, l16, sG, w, lds0);
      u32x4 pf[4];
#pragma unroll
      for (int u = 0; u < 4; ++u) {
        const float inv = __builtin_amdgcn_rcpf(sm[u]);
        const u32 pk = cvtpk(a[u] * inv, b[u] * inv);
        pf[u] = u32x4{__builtin_amdgcn_perm(pk, pk, sel0), __builtin_amdgcn_perm(pk, pk, sel1), __builtin_amdgcn_perm(pk, pk, sel2), __builtin_amdgcn_perm(pk, pk, sel3)};
      }
      dma_g<1, 3>(rs, l16, sG, w, lds0);
      f32x4 o0[4], o1[4];
      asm volatile("s_nop 1\n\t"
                   "v_mfma_f32_16x16x32_bf16 %0, %8, %16, 0\n\tv_mfma_f32_16x16x32_bf16 %1, %12, %16, 0\n\t"
                   "v_mfma_f32_16x16x32_bf16 %2, %9, %17, 0\n\tv_mfma_f32_16x16x32_bf16 %3, %13, %17, 0\n\t"
                   "v_mfma_f32_16x16x32_bf16 %4, %10, %18, 0\n\tv_mfma_f32_16x16x32_bf16 %5, %14, %18, 0\n\t"
                   "v_mfma_f32_16x16x32_bf16 %6, %11, %19, 0\n\tv_mfma_f32_16x16x32_bf16 %7, %15, %19, 0\n\ts_nop 7"
                   : "=&v"(o0[0]), "=&v"(o1[0]), "=&v"(o0[1]), "=&v"(o1[1]), "=&v"(o0[2]), "=&v"(o1[2]), "=&v"(o0[3]), "=&v"(o1[3])
                   : "v"(v0[0]), "v"(v0[1]), "v"(v0[2]), "v"(v0[3]), "v"(v1[0]), "v"(v1[1]), "v"(v1[2]), "v"(v1[3]), "v"(pf[0]), "v"(pf[1]), "v"(pf[2]), "v"(pf[3]));
      dma_g<1, 4>(rs, l16, sG, w, lds0);
      const u32 sg = 5 * p + g, sg4 = 5 * p + 4;
      const u32 oa = (sg >> 2) * 1024 + (t16 + 16 * (sg & 3)) * 16, oa4 = (sg4 >> 2) * 1024 + (t16 + 16 * (sg4 & 3)) * 16;
#pragma unroll
      for (int qb = 0; qb < 2; ++qb) {
        const u32 so = ofo + (8 * m128 + 2 * w + qb) * 20480;
        const f32x4 A0 = o0[2 * qb], B0_ = o0[2 * qb + 1], A1 = o1[2 * qb], B1 = o1[2 * qb + 1];
        stg_sc1(u32x4{cvtpk(A0[0], A0[1]), cvtpk(A0[2], A0[3]), cvtpk(B0_[0], B0_[1]), cvtpk(B0_[2], B0_[3])}, rs, oa, so);
        if (g == 0) stg_sc1(u32x4{cvtpk(A1[0], A1[1]), cvtpk(A1[2], A1[3]), cvtpk(B1[0], B1[1]), cvtpk(B1[2], B1[3])}, rs, oa4, so);
      }
    }
    SB();
    STAMP(5);
    WAITV(0); CERT("G0"); CERT("G1");
    WAITL0();
    BAR();
    if (tid == 0) __hip_atomic_store(flags + ((fgb + FG_O + m128) * 16 + p) * 32, 1u, RLX_AGENT);
    STAMP(6); RSTAMP(0);
    sfor<5>([&](auto I) { dma_g<2, decltype(I)::value>(rs, l16, sG, w, lds0); });
    sfor<5>([&](auto I) { dma_g<3, decltype(I)::value>(rs, l16, sG, w, lds0); });
  }
  {
    PHASE_IDS();
    if (w == 0) poll_flags(flags, ((fgb + FG_O + (m64 >> 1)) * 16 + (lane & 15)) * 32, lane < 16, lane);
    BAR();
    STAMP(7); RSTAMP(1);
#pragma unroll
    for (int s = 0; s < 5; ++s)
#pragma unroll
      for (int nb = 0; nb < 5; ++nb) { if (s * 5 + nb < 24) pin_a(k.wo_r[s][nb]); else pin_v(k.wo_r[s][nb]); }
    u32x4 af[20];
    u32 tbo[4];
#pragma unroll
    for (int tbi = 0; tbi < 4; ++tbi) tbo[tbi] = ofo + ((4 * m64 + ((w + 1 + tbi) & 3)) * 20 + 5 * w) * 1024;
    constexpr int OW = 10;
#define OLOAD(q) ald_v_sc1<((q) % 5 % 4) * 1024>(af[q], rs, l16, tbo[(q) / 5] + ((q) % 5 / 4) * 4096)
    sfor<OW>([&](auto I) { constexpr int q = decltype(I)::value; OLOAD(q); });
    f32x4 acc[2][5];
    f32x4 part[3][5];
    sfor<20>([&](auto QC) {
      constexpr int q = decltype(QC)::value, tbi = q / 5, s = q % 5;
      if constexpr (q + OW < 20) OLOAD(q + OW);
      waitpin_v<(q < 10 ? OW : (19 - q))>(af[q]);
#pragma unroll
      for (int nb = 0; nb < 5; ++nb) acc[tbi & 1][nb] = mfma16(k.wo_r[s][nb], af[q], s == 0 ? f32x4{0.f, 0.f, 0.f, 0.f} : acc[tbi & 1][nb]);
      if constexpr (tbi > 0 && s < 3) {
        const u32 tbp = (w + tbi) & 3;
        *(LAS f32x4*)(lds + L_RED + ((tbp * 3 + (3 - tbi)) * 5 + 2 * s) * 1024 + l16) = acc[(tbi - 1) & 1][2 * s];
        if constexpr (2 * s + 1 < 5) *(LAS f32x4*)(lds + L_RED + ((tbp * 3 + (3 - tbi)) * 5 + 2 * s + 1) * 1024 + l16) = acc[(tbi - 1) & 1][2 * s + 1];
      }
      if constexpr (q == 18) {
        WAITL0(); BAR();
#pragma unroll
        for (int sl = 0; sl < 3; ++sl)
#pragma unroll
          for (int nb = 0; nb < 5; ++nb) part[sl][nb] = *(const LAS f32x4*)(lds + L_RED + ((w * 3 + sl) * 5 + nb) * 1024 + l16);
      }
      SB();
    });
    STAMP(8);
    float s1 = 0.f, s2 = 0.f;
#pragma unroll
    for (int nb = 0; nb < 5; ++nb) {
      const f32x4 v = (acc[1][nb] + part[0][nb]) + (part[1][nb] + part[2][nb]);
      const u32 a = cvtpk(bflo(k.xr[nb].x) + v[0], bfhi(k.xr[nb].x) + v[1]), b = cvtpk(bflo(k.xr[nb].y) + v[2], bfhi(k.xr[nb].y) + v[3]);
      k.xr[nb] = u32x2{a, b};
    }
    const u32 token = 64 * m64 + 16 * w + t16;
    const u32 xo = ((token >> 5) * 40 + 5 * c8) * 1024 + ((token & 31) + 32 * (g >> 1)) * 16;
#pragma unroll
    for (int pr = 0; pr < 3; ++pr) {
      const int A = 2 * pr, Bn = pr < 2 ? 2 * pr + 1 : 4;
      auto r0 = __builtin_amdgcn_permlane16_swap(k.xr[A].x, k.xr[Bn].x, false, false);
      auto r1 = __builtin_amdgcn_permlane16_swap(k.xr[A].y, k.xr[Bn].y, false, false);
      const u32x4 v = {r0[0], r1[0], r0[1], r1[1]};
      if (pr < 2) stg_sc1(v, rs, xo + (A + (g & 1)) * 1024, x1o);
      else if ((g & 1) == 0) stg_sc1(v, rs, xo + 4 * 1024, x1o);
    }
#pragma unroll
    for (int nb = 0; nb < 5; ++nb) {
      const float a = bflo(k.xr[nb].x), b = bfhi(k.xr[nb].x), c = bflo(k.xr[nb].y), d = bfhi(k.xr[nb].y);
      s1 += (a + b) + (c + d); s2 += (a * a + b * b) + (c * c + d * d);
    }
    s1 = red16_32_add(s1); s2 = red16_32_add(s2);
    if (g == 0) stg2_sc1(u32x2{f2u(s1), f2u(s2)}, rs, (c8 * 2048 + token) * 8, st1o);
    SB();
    STAMP(9);
    WAITV(0); CERT("G2"); CERT("G3");
    WAITL0();
    BAR();
    if (tid == 0) __hip_atomic_store(flags + ((fgb + FG_X1 + m64) * 16 + c8) * 32, 1u, RLX_AGENT);
    STAMP(10); RSTAMP(2);
  }
  {
    PHASE_IDS();
    const int nblk = k.nblk;
    const int ds0 = w < 2 ? 14 * w : 28 + 13 * (w - 2);
    const u32 sG = OFF_WG + p * 286720 + w * 5120;
    Polls pl = {0u, 0u, 0u};
    if (w != 0) { dma_g3<4>(rs, l16, sG - w * 5120, w, lds0); dma_g3<5>(rs, l16, sG - w * 5120, w, lds0); dma_g3<6>(rs, l16, sG - w * 5120, w, lds0); }
    if (w == 0) poll_flags_p(flags, ((fgb + FG_X1 + 2 * m128 + ((lane >> 3) & 1)) * 16 + (lane & 7)) * 32, lane < 16, lane, 1u, pl);
    BAR();
    STAMP(11); RSTAMP(3);
    u32x2 sa0, sa1, sa2, sa3;
    u32x4 xf[40];
    const u32 xbase = x1o + (4 * m128 + w) * 40960;
    const u32 sso = (4 * h * 2048 + 128 * m128 + 32 * w + tok) * 8;
    sfor<XW>([&](auto I) { constexpr int i = decltype(I)::value; XLOAD(i); });
    constexpr int NWD = GS::NWD;
    float r2 = 0.f;
    constexpr int DG = 4, NBG = 6;
    u32x4 wb[NBG];
    f32x16 aA0, aA1, aA2, aB0, aB1;
    u32 lb1 = l16 + 65536, lb2 = l16 + 131072; asm volatile("" : "+v"(lb1), "+v"(lb2));
    const u32 hbase = (8 * m128 + 2 * w + (tok >> 4)) * 54 * 1024 + ((tok & 15) + 16 * h) * 16;
    const u32 hlast = nblk == 7 ? hbase : 0x7fff0000u;
    const u32 wd_o = OFF_WD + (c8 * 54 + ds0) * 5120;
#define LDSG(F) ((((F) / 20) % 7) * 20480 + ((F) % 20) * 1024)
#define RDG(off) (*(const LAS u32x4*)(lds + ((off) < 65536 ? l16 : (off) < 131072 ? lb1 : lb2) + ((off) & 65535)))
    float hv0 = 0.f;
    u32 hp[4];
#define GU_STAGE(ACC, idx, t) do { \
      if constexpr ((t) < 8) { const float hv_ = mul_s(mul_s(ACC[t], r2), ACC[8 + (t)]); if constexpr ((t) & 1) hp[(t) >> 1] = cvtpk(hv0, hv_); else hv0 = hv_; } \
      if constexpr ((t) == 8) { const u32 fb_ = B0 + (idx); stg_sc1(u32x4{hp[0], hp[1], hp[2], hp[3]}, rs, ((idx) == 6 ? hlast : hbase) + (fb_ & 1) * 512, hfo + (fb_ >> 1) * 1024); } \
    } while (0)
    auto wd_load = [&](auto OC) {
      constexpr int i = decltype(OC)::value, s = i / 5, nb = i % 5;
      if constexpr (i < 24 || i >= 50) ald_a<(i % 4) * 1024>(k.wd_r[s][nb], rs, l16, wd_o + (i / 4) * 4096);
      else ald_v<(i % 4) * 1024>(k.wd_r[s][nb], rs, l16, wd_o + (i / 4) * 4096);
    };
#pragma unroll
    for (int F = 0; F < DG; ++F) wb[F % NBG] = RDG(LDSG(F));
    sfor<280>([&](auto FC) {
      constexpr int F = decltype(FC)::value, Fr = F + DG;
      if constexpr (GS::x_at(F)) XLOAD(F / 3 + XW);
      if constexpr (GS::op(F) >= 0 && GS::op(F) < 1000) dma_g<GS::op(F) / 8, GS::op(F) % 8>(rs, l16, sG, w, lds0);
      if constexpr (GS::op(F) >= 1000 && GS::op(F) < 2000) wd_load(std::integral_constant<int, GS::op(F) - 1000>{});
      if constexpr (GS::op(F) == 3000) ald2_sc1(sa0, rs, sso, st1o);
      if constexpr (GS::op(F) == 3001) ald2_sc1(sa1, rs, sso, st1o + 16384);
      if constexpr (GS::op(F) == 3002) ald2_sc1(sa2, rs, sso, st1o + 32768);
      if constexpr (GS::op(F) == 3003) ald2_sc1(sa3, rs, sso, st1o + 49152);
      if constexpr (Fr == 40) { WAITL0(); BAR(); }
      if constexpr (Fr == 80) { CERT("G4"); CERT("G5"); WAITL0(); BAR(); }
      if constexpr (Fr == 120) { WAITV(GS::upto(F) - GS::upto(GS::slot(7 * 8 + 4))); CERT("G6"); CERT("G7"); WAITL0(); BAR(); STAMP(12); }
      if constexpr (Fr == 160) { GSTAMP(23); WAITV(GS::upto(F) - GS::upto(GS::slot(9 * 8 + 4))); CERT("G8"); CERT("G9"); WAITL0(); BAR(); GSTAMP(24); }
      if constexpr (Fr == 200) { GSTAMP(25); WAITV(GS::upto(F) - GS::upto(GS::slot(11 * 8 + 4))); CERT("G10"); CERT("G11"); BAR(); GSTAMP(26); }
      if constexpr (Fr == 240) { WAITV(GS::upto(F) - GS::upto(GS::slot(13 * 8 + 4))); CERT("G12"); CERT("G13"); BAR(); STAMP(13); }
      if constexpr (Fr < 280) wb[Fr % NBG] = RDG(LDSG(Fr));
      constexpr int s = F < 120 ? F / 3 : F < 200 ? (F - 120) / 2 : (F - 200) / 2, b = F < 120 ? F % 3 : F & 1;
      if constexpr (F < 120 && b == 0 && (s & 1) == 0) waitpin_a2<GS::xwait2(s)>(xf[s], xf[s + 1]);
#define GMMA(ACC) do { if constexpr (s == 0) { if constexpr ((F & 1) == 0) mma32w_0(ACC, wb[F % NBG], xf[s], wb[(F + 1) % NBG]); else mma32_0(ACC, wb[F % NBG], xf[s]); } \
                       else { if constexpr ((F & 1) == 0) mma32w(ACC, wb[F % NBG], xf[s], wb[(F + 1) % NBG]); else mma32(ACC, wb[F % NBG], xf[s]); } } while (0)
      if constexpr (F < 120) { if constexpr (b == 0) GMMA(aA0); else if constexpr (b == 1) GMMA(aA1); else GMMA(aA2); }
      else if constexpr (F < 200) { if constexpr (b == 0) GMMA(aB0); else GMMA(aB1); }
      else { if constexpr (b == 0) GMMA(aA0); else GMMA(aA1); }
      if constexpr (F == 3) poll_pin(pl);
      if constexpr (F == 62) {
        pin2(sa0); pin2(sa1); pin2(sa2); pin2(sa3);
        float rstd, nm;
        stats_finish(red32_add((u2f(sa0.x) + u2f(sa1.x)) + (u2f(sa2.x) + u2f(sa3.x))), red32_add((u2f(sa0.y) + u2f(sa1.y)) + (u2f(sa2.y) + u2f(sa3.y))), nm, rstd);
        r2 = rstd * rstd;
      }
      if constexpr ((F & 1) && F >= 123 && F <= 139) GU_STAGE(aA0, 0, (F - 123) / 2);
      if constexpr ((F & 1) && F >= 141 && F <= 157) GU_STAGE(aA1, 1, (F - 141) / 2);
      if constexpr ((F & 1) && F >= 159 && F <= 175) GU_STAGE(aA2, 2, (F - 159) / 2);
      if constexpr ((F & 1) && F >= 203 && F <= 219) GU_STAGE(aB0, 3, (F - 203) / 2);
      if constexpr ((F & 1) && F >= 221 && F <= 237) GU_STAGE(aB1, 4, (F - 221) / 2);
      SB();
    });
    asm volatile("s_nop 11" : "+v"(aA0), "+v"(aA1));
    sfor<9>([&](auto T) { GU_STAGE(aA0, 5, decltype(T)::value); });
    sfor<9>([&](auto T) { GU_STAGE(aA1, 6, decltype(T)::value); });
    SB();
    STAMP(14);
    WAITV(0);
    WAITL0();
    BAR();
    if (tid == 0) __hip_atomic_store(flags + ((fgb + FG_H + m128) * 16 + p) * 32, 1u, RLX_AGENT);
    {
      const u32 wd_o = OFF_WD + (c8 * 54 + ds0) * 5120;
      sfor<70 - GS::NWD>([&](auto I) { constexpr int i = GS::NWD + decltype(I)::value; ald_a<(i % 4) * 1024>(k.wd_r[i / 5][i % 5], rs, l16, wd_o + (i / 4) * 4096); });
    }
    STAMP(15); RSTAMP(4);
  }
  {
    PHASE_IDS();
    const int ds0 = w < 2 ? 14 * w : 28 + 13 * (w - 2);
    const u32 sQ = OFF_WQ + p * 163840;
    STAMP(16);
    if (w == 0) poll_flags(flags, ((fgb + FG_H + (m64 >> 1)) * 16 + (lane & 15)) * 32, lane < 16, lane);
    else WAITV(0);
    BAR();
    STAMP(17); RSTAMP(5);
    sfor<70>([&](auto I) { constexpr int i = decltype(I)::value; if constexpr (i < 24 || i >= 50) pin_a(k.wd_r[i / 5][i % 5]); else pin_v(k.wd_r[i / 5][i % 5]); });
    constexpr int DH = 12;
    u32x4 af[DH];
    const bool s13 = w < 2;
    u32 tbo[4];
#pragma unroll
    for (int tbi = 0; tbi < 4; ++tbi) tbo[tbi] = hfo + ((4 * m64 + ((w + 1 + tbi) & 3)) * 54 + ds0) * 1024;
#define HLOAD(q) ald_a_sc1<((q) % 14 % 4) * 1024>(af[(q) % DH], rs, l16, tbo[(q) / 14] + ((q) % 14 / 4) * 4096)
    sfor<DH>([&](auto I) { constexpr int q = decltype(I)::value; HLOAD(q); });
    f32x4 acc[2][5];
    f32x4 part[3][5];
    sfor<56>([&](auto QC) {
      constexpr int q = decltype(QC)::value, tbi = q / 14, s = q % 14;
      constexpr int yf = (q + DH - 1 < 55 ? q + DH - 1 : 55) - q;
      constexpr int nd = q - 44;
      constexpr int yd = (nd > 0) ? (nd + (nd < 8 ? nd : 8)) : 0;
      waitpin_a<yf + yd>(af[q % DH]);
      if constexpr (s == 0) {
#pragma unroll
        for (int nb = 0; nb < 5; ++nb) acc[tbi & 1][nb] = mfma16(k.wd_r[s][nb], af[q % DH], f32x4{0.f, 0.f, 0.f, 0.f});
      } else if (s < 13 || s13) {
#pragma unroll
        for (int nb = 0; nb < 5; ++nb) acc[tbi & 1][nb] = mfma16(k.wd_r[s][nb], af[q % DH], acc[tbi & 1][nb]);
      }
      if constexpr (tbi > 0 && s >= 1 && s < 6) {
        const u32 tbp = (w + tbi) & 3;
        *(LAS f32x4*)(lds + L_RED + ((tbp * 3 + (3 - tbi)) * 5 + (s - 1)) * 1024 + l16) = acc[(tbi - 1) & 1][s - 1];
      }
      if constexpr (q == 50) {
        WAITL0(); BAR();
#pragma unroll
        for (int sl = 0; sl < 3; ++sl)
#pragma unroll
          for (int nb = 0; nb < 5; ++nb) part[sl][nb] = *(const LAS f32x4*)(lds + L_RED + ((w * 3 + sl) * 5 + nb) * 1024 + l16);
      }
      SB();
      if constexpr (q + DH < 56) HLOAD(q + DH);
      if constexpr (q >= 44) {
        constexpr int j0 = nd + (nd < 8 ? nd : 8);
        if constexpr (j0 < 10) dma_q<0, 0, j0>(rs, l16, sQ, w, lds0); else dma_q<1, 1, j0 - 10>(rs, l16, sQ, w, lds0);
        if constexpr (nd < 8) {
          constexpr int j1 = j0 + 1;
          if constexpr (j1 < 10) dma_q<0, 0, j1>(rs, l16, sQ, w, lds0); else dma_q<1, 1, j1 - 10>(rs, l16, sQ, w, lds0);
        }
      }
      SB();
    });
    STAMP(18);
#pragma unroll
    for (int nb = 0; nb < 5; ++nb) {
      const f32x4 v = (acc[1][nb] + part[0][nb]) + (part[1][nb] + part[2][nb]);
      const u32 a = cvtpk(bflo(k.xr[nb].x) + v[0], bfhi(k.xr[nb].x) + v[1]), b = cvtpk(bflo(k.xr[nb].y) + v[2], bfhi(k.xr[nb].y) + v[3]);
      k.xr[nb] = u32x2{a, b};
    }
    const u32 token = 64 * m64 + 16 * w + t16;
    STAMP(19);
    if (it == 0) {
      float s1 = 0.f, s2 = 0.f;
      const u32 xo = ((token >> 5) * 40 + 5 * c8) * 1024 + ((token & 31) + 32 * (g >> 1)) * 16;
#pragma unroll
      for (int pr = 0; pr < 3; ++pr) {
        const int A = 2 * pr, Bn = pr < 2 ? 2 * pr + 1 : 4;
        auto r0 = __builtin_amdgcn_permlane16_swap(k.xr[A].x, k.xr[Bn].x, false, false);
        auto r1 = __builtin_amdgcn_permlane16_swap(k.xr[A].y, k.xr[Bn].y, false, false);
        const u32x4 v = {r0[0], r1[0], r0[1], r1[1]};
        if (pr < 2) stg_sc1(v, rs, xo + (A + (g & 1)) * 1024, OFF_XF + 2 * SZ_XF);
        else if ((g & 1) == 0) stg_sc1(v, rs, xo + 4 * 1024, OFF_XF + 2 * SZ_XF);
      }
#pragma unroll
      for (int nb = 0; nb < 5; ++nb) {
        const float a = bflo(k.xr[nb].x), b = bfhi(k.xr[nb].x), c = bflo(k.xr[nb].y), d = bfhi(k.xr[nb].y);
        s1 += (a + b) + (c + d); s2 += (a * a + b * b) + (c * c + d * d);
      }
      s1 = red16_32_add(s1); s2 = red16_32_add(s2);
      if (g == 0) stg2_sc1(u32x2{f2u(s1), f2u(s2)}, rs, (c8 * 2048 + token) * 8, OFF_ST + 2 * SZ_ST);
      SB();
      WAITV(0);
      WAITL0();
      BAR();
      if (tid == 0) __hip_atomic_store(flags + ((FG_X2 + m64) * 16 + c8) * 32, 1u, RLX_AGENT);
      sfor<10>([&](auto I) { dma_q<2, 2, decltype(I)::value>(rs, l16, sQ, w, lds0); });
    } else {
      const u32 oo = token * 1280 + (80 * c8 + 8 * (g >> 1)) * 2;
#pragma unroll
      for (int pr = 0; pr < 3; ++pr) {
        const int A = 2 * pr, Bn = pr < 2 ? 2 * pr + 1 : 4;
        auto r0 = __builtin_amdgcn_permlane16_swap(k.xr[A].x, k.xr[Bn].x, false, false);
        auto r1 = __builtin_amdgcn_permlane16_swap(k.xr[A].y, k.xr[Bn].y, false, false);
        const u32x4 v = {r0[0], r1[0], r0[1], r1[1]};
        if (pr < 2) __builtin_amdgcn_raw_buffer_store_b128(v, k.rso, oo + (A + (g & 1)) * 32, 0, 0);
        else if ((g & 1) == 0) __builtin_amdgcn_raw_buffer_store_b128(v, k.rso, oo + 4 * 32, 0, 0);
      }
    }
    STAMP(20);
  }
}

__global__ __launch_bounds__(256, 1) void main_kernel(char* __restrict__ ws, u16* __restrict__ out) {
  extern __shared__ __attribute__((aligned(16))) char smem[];
  K k;
  k.lds = (LAS char*)smem;
  k.lds0 = (u32)(size_t)k.lds;
  k.tid = threadIdx.x; k.lane = k.tid & 63; k.w = __builtin_amdgcn_readfirstlane(k.tid >> 6);
  k.rs = __builtin_amdgcn_make_buffer_rsrc(ws, 0, WS_USED, 0x00020000);
  k.rso = __builtin_amdgcn_make_buffer_rsrc(out, 0, 2048 * 640 * 2, 0x00020000);
  k.flags = (GAS u32*)ws;
  const int bid = blockIdx.x, xc = bid & 7, j = bid >> 3, mg = xc >> 1, half = xc & 1;
  k.m128 = 4 * mg + (j >> 3); k.p = 8 * half + (j & 7);
  k.m64 = 8 * mg + (j >> 2); k.c8 = 4 * half + (j & 3);
  const int p7 = j & 7;
  k.B0 = 54 * half + (p7 < 6 ? 7 * p7 : 42 + 6 * (p7 - 6));
  k.nblk = p7 < 6 ? 7 : 6;
  {
    PHASE_IDS();
    const u32 sQ = OFF_WQ + p * 163840;
    sfor<10>([&](auto I) { dma_q<0, 0, decltype(I)::value>(rs, l16, sQ, w, lds0); });
    sfor<10>([&](auto I) { dma_q<1, 1, decltype(I)::value>(rs, l16, sQ, w, lds0); });
    sfor<10>([&](auto I) { dma_q<2, 2, decltype(I)::value>(rs, l16, sQ, w, lds0); });
    const u32 token = 64 * m64 + 16 * w + t16;
    const u32 o = ((token >> 5) * 40 + 5 * c8) * 1024 + ((token & 31) + 32 * (g >> 1)) * 16 + (g & 1) * 8;
#pragma unroll
    for (int nb = 0; nb < 5; ++nb) ald2(k.xr[nb], rs, o, OFF_XF + nb * 1024);
  }
#pragma nounroll
  for (int it = 0; it < 2; ++it) { k.it = it; iteration(k); }
}

extern "C" void kernel_launch(void* const* d_in, const int* in_sizes, int n_in, void* d_out, int out_size, void* d_ws, size_t ws_size,
                              hipStream_t stream) {
  const u16* x0 = (const u16*)d_in[0];
  const u16* wqkv = (const u16*)d_in[1];
  const u16* wo = (const u16*)d_in[2];
  const u16* wg = (const u16*)d_in[4];
  const u16* wu = (const u16*)d_in[5];
  const u16* wd = (const u16*)d_in[6];
  (void)hipFuncSetAttribute((const void*)main_kernel, hipFuncAttributeMaxDynamicSharedMemorySize, LDS_BYTES);
  prep_kernel<<<256, 256, 0, stream>>>(x0, wqkv, wo, wg, wu, wd, (char*)d_ws);
  main_kernel<<<256, 256, LDS_BYTES, stream>>>((char*)d_ws, (u16*)d_out);
}
```

```cpp
#include <hip/hip_runtime.h>
#include <stdint.h>
#include <utility>

#define LAS __attribute__((address_space(3)))
#define GAS __attribute__((address_space(1)))
#define DI __device__ __forceinline__
typedef unsigned u32;
typedef unsigned short u16;
typedef u32 u32x4 __attribute__((ext_vector_type(4)));
typedef u32 u32x2 __attribute__((ext_vector_type(2)));
typedef float f32x2 __attribute__((ext_vector_type(2)));
typedef float f32x4 __attribute__((ext_vector_type(4)));
typedef float f32x16 __attribute__((ext_vector_type(16)));
typedef short s16x8 __attribute__((ext_vector_type(8)));
typedef short s16x4 __attribute__((ext_vector_type(4)));
typedef __bf16 bf16x2 __attribute__((ext_vector_type(2)));
typedef __amdgpu_buffer_rsrc_t rsrc_t;

constexpr u32 FLAG_BYTES = 200 * 2048 + 34 * 2048;
constexpr u32 OFF_WQ = 483328;
constexpr u32 OFF_WO = OFF_WQ + 16 * 163840;
constexpr u32 OFF_WG = OFF_WO + 8 * 20 * 5 * 1024;
constexpr u32 OFF_WD = OFF_WG + 16 * 286720;
constexpr u32 OFF_CSQ = OFF_WD + 8 * 54 * 5 * 1024;
constexpr u32 OFF_CSG = OFF_CSQ + 16384;
constexpr u32 SZ_XF = 64 * 40 * 1024;
constexpr u32 OFF_XF = OFF_CSG + 28672;
constexpr u32 SZ_ST = 8 * 2048 * 8;
constexpr u32 OFF_ST = OFF_XF + 4 * SZ_XF;
constexpr u32 SZ_OF = 128 * 20 * 1024;
constexpr u32 OFF_OF = OFF_ST + 4 * SZ_ST;
constexpr u32 SZ_HF = 128 * 54 * 1024;
constexpr u32 OFF_HF = OFF_OF + 2 * SZ_OF;
constexpr u32 WS_USED = OFF_HF + 2 * SZ_HF;
constexpr u32 FG_O = 0, FG_X1 = 16, FG_H = 48, FG_X2 = 64, FG_PER_IT = 96;
constexpr u32 FG_TMO = 2 * FG_PER_IT;
constexpr u32 FG_EPOCH = FG_TMO + 1;
constexpr u32 FG_PF1 = FG_EPOCH + 1;
constexpr u32 FG_PF2 = FG_PF1 + 16;

constexpr u32 L_HALO = 122880;
constexpr u32 L_KST = 40960;
constexpr u32 L_VST = L_KST + 144 * 80;
constexpr u32 L_QST = L_VST + 144 * 80;
constexpr u32 L_RED = 100352;
constexpr u32 L_H4 = 143360;
constexpr u32 LDS_BYTES = 163840;

constexpr u32 AUX_SC1 = 16;
#define RLX_AGENT __ATOMIC_RELAXED, __HIP_MEMORY_SCOPE_AGENT
#define SB() __builtin_amdgcn_sched_barrier(0)
#define MEMFENCE() asm volatile("" ::: "memory")
#define WAITV(n) asm volatile("s_waitcnt vmcnt(%0)" ::"i"(n) : "memory")
#define WAITL0() asm volatile("s_waitcnt lgkmcnt(0)" ::: "memory")
#define BAR() do { MEMFENCE(); __builtin_amdgcn_s_barrier(); MEMFENCE(); } while (0)
#define CERT(tag) asm volatile("; CERT " tag ::: "memory")

template <int... I, class Fn> DI void sfor_impl(std::integer_sequence<int, I...>, Fn&& f) { (f(std::integral_constant<int, I>{}), ...); }
template <int N, class Fn> DI void sfor(Fn&& f) { sfor_impl(std::make_integer_sequence<int, N>{}, f); }
DI f32x16 mfma32(u32x4 a, u32x4 b, f32x16 c) { return __builtin_amdgcn_mfma_f32_32x32x16_bf16(__builtin_bit_cast(s16x8, a), __builtin_bit_cast(s16x8, b), c, 0, 0, 0); }
DI f32x4 mfma16(u32x4 a, u32x4 b, f32x4 c) { return __builtin_amdgcn_mfma_f32_16x16x32_bf16(__builtin_bit_cast(s16x8, a), __builtin_bit_cast(s16x8, b), c, 0, 0, 0); }
DI u32 cvtpk(float lo, float hi) { f32x2 v = {lo, hi}; bf16x2 b = __builtin_convertvector(v, bf16x2); return __builtin_bit_cast(u32, b); }
DI float u2f(u32 v) { return __builtin_bit_cast(float, v); }
DI u32 f2u(float v) { return __builtin_bit_cast(u32, v); }
DI float bflo(u32 v) { return __builtin_bit_cast(float, v << 16); }
DI float bfhi(u32 v) { return __builtin_bit_cast(float, v & 0xffff0000u); }
DI float fma_s(float a, float b, float c) { float r; asm("v_fma_f32 %0, %1, %2, %3" : "=v"(r) : "v"(a), "v"(b), "v"(c)); return r; }
DI float mul_s(float a, float b) { float r; asm("v_mul_f32_e32 %0, %1, %2" : "=v"(r) : "v"(a), "v"(b)); return r; }
DI float add_s(float a, float b) { float r; asm("v_add_f32_e32 %0, %1, %2" : "=v"(r) : "v"(a), "v"(b)); return r; }
DI float max_s(float a, float b) { float r; asm("v_max_f32_e32 %0, %1, %2" : "=v"(r) : "v"(a), "v"(b)); return r; }
DI float sel_s(float f, float t, unsigned long long m) { float r; asm("v_cndmask_b32_e64 %0, %1, %2, %3" : "=v"(r) : "v"(f), "v"(t), "s"(m)); return r; }
DI float red16_32_add(float v) {
  auto a = __builtin_amdgcn_permlane16_swap(f2u(v), f2u(v), false, false);
  v = u2f(a[0]) + u2f(a[1]);
  auto b = __builtin_amdgcn_permlane32_swap(f2u(v), f2u(v), false, false);
  return u2f(b[0]) + u2f(b[1]);
}
DI float red16_32_max(float v) {
  auto a = __builtin_amdgcn_permlane16_swap(f2u(v), f2u(v), false, false);
  v = __builtin_fmaxf(u2f(a[0]), u2f(a[1]));
  auto b = __builtin_amdgcn_permlane32_swap(f2u(v), f2u(v), false, false);
  return __builtin_fmaxf(u2f(b[0]), u2f(b[1]));
}
DI float red32_add(float v) {
  auto b = __builtin_amdgcn_permlane32_swap(f2u(v), f2u(v), false, false);
  return u2f(b[0]) + u2f(b[1]);
}
DI void stg_sc1(u32x4 v, rsrc_t rs, u32 voff, u32 soff) { __builtin_amdgcn_raw_buffer_store_b128(v, rs, voff, soff, AUX_SC1); }
DI void stg2_sc1(u32x2 v, rsrc_t rs, u32 voff, u32 soff) { __builtin_amdgcn_raw_buffer_store_b64(v, rs, voff, soff, AUX_SC1); }

template <int IMM> DI void ald_v_sc1(u32x4& d, rsrc_t rs, u32 voff, u32 soff) {
  asm volatile("buffer_load_dwordx4 %0, %1, %2, %3 offen offset:%4 sc1" : "=v"(d) : "v"(voff), "s"(rs), "s"(soff), "i"(IMM) : "memory");
}
template <int IMM> DI void ald_a(u32x4& d, rsrc_t rs, u32 voff, u32 soff) {
  asm volatile("buffer_load_dwordx4 %0, %1, %2, %3 offen offset:%4 sc1" : "=a"(d) : "v"(voff), "s"(rs), "s"(soff), "i"(IMM) : "memory");
}
template <int IMM> DI void ald_v(u32x4& d, rsrc_t rs, u32 voff, u32 soff) {
  asm volatile("buffer_load_dwordx4 %0, %1, %2, %3 offen offset:%4 sc1" : "=v"(d) : "v"(voff), "s"(rs), "s"(soff), "i"(IMM) : "memory");
}
DI void ald2_sc1(u32x2& d, rsrc_t rs, u32 voff, u32 soff) {
  asm volatile("buffer_load_dwordx2 %0, %1, %2, %3 offen sc1" : "=v"(d) : "v"(voff), "s"(rs), "s"(soff) : "memory");
}
DI void ald2(u32x2& d, rsrc_t rs, u32 voff, u32 soff) {
  asm volatile("buffer_load_dwordx2 %0, %1, %2, %3 offen sc1" : "=v"(d) : "v"(voff), "s"(rs), "s"(soff) : "memory");
}
DI void ald1(u32& d, rsrc_t rs, u32 voff, u32 soff) {
  asm volatile("buffer_load_dword %0, %1, %2, %3 offen" : "=v"(d) : "v"(voff), "s"(rs), "s"(soff) : "memory");
}
template <int IMM> DI void ald_a_sc1(u32x4& d, rsrc_t rs, u32 voff, u32 soff) {
  asm volatile("buffer_load_dwordx4 %0, %1, %2, %3 offen offset:%4 sc1" : "=a"(d) : "v"(voff), "s"(rs), "s"(soff), "i"(IMM) : "memory");
}
template <int N> DI void waitpin_a(u32x4& r) { asm volatile("s_waitcnt vmcnt(%1)" : "+a"(r) : "i"(N) : "memory"); }
template <int N> DI void waitpin_a2(u32x4& r0, u32x4& r1) { asm volatile("s_waitcnt vmcnt(%2)" : "+a"(r0), "+a"(r1) : "i"(N) : "memory"); }
DI void mma32(f32x16& acc, const u32x4& wv, const u32x4& xa) { asm volatile("v_mfma_f32_32x32x16_bf16 %0, %1, %2, %0" : "+v"(acc) : "v"(wv), "a"(xa)); }
DI void mma32_0(f32x16& acc, const u32x4& wv, const u32x4& xa) { asm volatile("v_mfma_f32_32x32x16_bf16 %0, %1, %2, 0" : "=&v"(acc) : "v"(wv), "a"(xa)); }
DI void mma32w(f32x16& acc, const u32x4& wv, const u32x4& xa, const u32x4& wn) { asm volatile("v_mfma_f32_32x32x16_bf16 %0, %1, %2, %0" : "+v"(acc) : "v"(wv), "a"(xa), "v"(wn)); }
DI void mma32w_0(f32x16& acc, const u32x4& wv, const u32x4& xa, const u32x4& wn) { asm volatile("v_mfma_f32_32x32x16_bf16 %0, %1, %2, 0" : "=&v"(acc) : "v"(wv), "a"(xa), "v"(wn)); }
DI void mma16(f32x4& acc, const u32x4& av, const u32x4& bv) { asm volatile("v_mfma_f32_16x16x32_bf16 %0, %1, %2, %0" : "+v"(acc) : "v"(av), "v"(bv)); }
DI void mma16_0(f32x4& acc, const u32x4& av, const u32x4& bv) { asm volatile("v_mfma_f32_16x16x32_bf16 %0, %1, %2, 0" : "=&v"(acc) : "v"(av), "v"(bv)); }
DI void pin_vv(u32x4& a, u32x4& b) { asm volatile("" : "+v"(a), "+v"(b)); }
template <int N> DI void waitpin_v(u32x4& r) { asm volatile("s_waitcnt vmcnt(%1)" : "+v"(r) : "i"(N) : "memory"); }
DI void pin_v(u32x4& r) { asm volatile("" : "+v"(r)::"memory"); }
DI void pin_a(u32x4& r) { asm volatile("" : "+a"(r)::"memory"); }
DI void pin2(u32x2& r) { asm volatile("" : "+v"(r)::"memory"); }
DI void pin1(u32& r) { asm volatile("" : "+v"(r)::"memory"); }
#define DMA_F(TAG, IMM, SC, rs, voff, soff, m0v) \
  asm volatile("s_mov_b32 m0, %2\n\ts_nop 0\n\tbuffer_load_dwordx4 %0, %1, %3 offen offset:%4 " SC "lds ; DMA " TAG ::"v"(voff), "s"(rs), "s"(m0v), "s"(soff), "i"(IMM) : "memory")
#define DMA_N(TAG, IMM, SC, rs, voff, soff) \
  asm volatile("buffer_load_dwordx4 %0, %1, %2 offen offset:%3 " SC "lds ; DMA " TAG ::"v"(voff), "s"(rs), "s"(soff), "i"(IMM) : "memory")

#define STAMP(i) do {} while (0)
#define RSTAMP(i) do {} while (0)
#define GSTAMP(i) STAMP(i)
#define QSTAMP(i) do {} while (0)
#define SSTAMP(i) do {} while (0)
constexpr u32 OFF_STAMP = WS_USED + 4096;
constexpr u32 kSpinCap = 1u << 16;

struct K {
  rsrc_t rs, rso, rsnull;
  LAS char* lds;
  u32 lds0;
  GAS u32* flags;
  int bid, lane, w, m128, p, m64, c8, B0, nblk, tid, it;
  u32 tag;
  u32x2 xr[5];
  u32x4 wo_r[5][5];
  u32x4 wd_r[14][5];
};

#define PHASE_IDS() int lane = k.lane; asm volatile("" : "+v"(lane)); \
  int it = k.it; asm volatile("" : "+s"(it)); \
  const u32 xin = OFF_XF + it * 2 * SZ_XF, stin = OFF_ST + it * 2 * SZ_ST, x1o = xin + SZ_XF, st1o = stin + SZ_ST; \
  const u32 ofo = OFF_OF + it * SZ_OF, hfo = OFF_HF + it * SZ_HF, fgb = it * FG_PER_IT; \
  (void)xin; (void)stin; (void)x1o; (void)st1o; (void)ofo; (void)hfo; (void)fgb; \
  int w = k.w, m128 = k.m128, p = k.p, m64 = k.m64, c8 = k.c8, B0 = k.B0; \
  asm volatile("" : "+s"(w), "+s"(m128), "+s"(p), "+s"(m64), "+s"(c8), "+s"(B0)); \
  const u32 l16 = lane * 16; const int tok = lane & 31, h = lane >> 5, t16 = lane & 15, g = lane >> 4; \
  const rsrc_t rs = k.rs; LAS char* const lds = k.lds; const u32 lds0 = k.lds0; \
  (void)tok; (void)h; (void)t16; (void)g; (void)l16; (void)m128; (void)p; (void)m64; (void)c8; (void)B0; (void)lds; (void)lds0; (void)rs

DI void poll_flags(GAS u32* flags, u32 fo, bool act, int lane, u32 tag) {
  for (u32 spins = 0;;) {
    u32 v = act ? __hip_atomic_load(flags + fo, RLX_AGENT) : tag;
    if (__all(v == tag)) break;
    __builtin_amdgcn_s_sleep(1);
    if (++spins > kSpinCap) {
      if (lane == 0) __hip_atomic_store(flags + FG_TMO * 512, tag, RLX_AGENT);
      break;
    }
    if ((spins & 255u) == 0u && (u32)__builtin_amdgcn_readfirstlane(__hip_atomic_load(flags + FG_TMO * 512, RLX_AGENT)) == tag) break;
  }
}
#define POLL_GAP 2
struct Polls { u32 a, b, c; };
DI void poll_issue(u32& d, const GAS u32* p) { asm volatile("global_load_dword %0, %1, off sc1 ; POLL" : "=v"(d) : "v"(p) : "memory"); }
DI bool poll_check(u32& d, bool act, u32 want) { asm volatile("s_waitcnt vmcnt(2) ; POLLWAIT" : "+v"(d)::"memory"); return __all(!act || d == want); }
DI void poll_flags_p(GAS u32* flags, u32 fo, bool act, int lane, u32 want, Polls& pl) {
  const GAS u32* p = flags + (act ? fo : FG_TMO * 512);
  asm volatile("; POLL_BEGIN" ::: "memory");
  poll_issue(pl.a, p); __builtin_amdgcn_s_sleep(POLL_GAP); poll_issue(pl.b, p); __builtin_amdgcn_s_sleep(POLL_GAP); poll_issue(pl.c, p);
  for (u32 spins = 0;;) {
    if (poll_check(pl.a, act, want)) break;
    poll_issue(pl.a, p);
    if (poll_check(pl.b, act, want)) break;
    poll_issue(pl.b, p);
    if (poll_check(pl.c, act, want)) break;
    poll_issue(pl.c, p);
    if (++spins > kSpinCap) {
      if (lane == 0) __hip_atomic_store(flags + FG_TMO * 512, want, RLX_AGENT);
      break;
    }
    if ((spins & 63u) == 0u && (u32)__builtin_amdgcn_readfirstlane(__hip_atomic_load(flags + FG_TMO * 512, RLX_AGENT)) == want) break;
  }
  asm volatile("; POLL_END" ::: "memory");
}
DI void poll_pin(Polls& pl) { asm volatile("; POLL_PIN" : "+v"(pl.a), "+v"(pl.b), "+v"(pl.c)::"memory"); }
DI void stats_finish(float s1, float s2, float& nmean, float& rstd) {
  const float mean = s1 * (1.0f / 640.0f);
  const float var = __builtin_fmaxf(s2 * (1.0f / 640.0f) - mean * mean, 0.f);
  nmean = -mean; rstd = __builtin_amdgcn_rsqf(var + 1e-5f);
}

template <int C, int SLOT, int I> DI void dma_q(rsrc_t rs, u32 l16, u32 sQ, int w, u32 lds0) {
#define Q_EMIT(TAG, FIRST, IMM, SO, MO) do { if constexpr (FIRST) DMA_F(TAG, IMM, "sc1 ", rs, l16, SO, MO); else DMA_N(TAG, IMM, "sc1 ", rs, l16, SO); } while (0)
  if constexpr (C < 3) {
    constexpr int grp = I / 4, imm = (I % 4) * 1024;
    const u32 so = sQ + w * 10240 + C * 40960 + grp * 4096, mo = lds0 + SLOT * 40960 + w * 10240 + grp * 4096;
    if constexpr (C == 0) Q_EMIT("Q0", I % 4 == 0, imm, so, mo); else if constexpr (C == 1) Q_EMIT("Q1", I % 4 == 0, imm, so, mo); else Q_EMIT("Q2", I % 4 == 0, imm, so, mo);
  } else {
    constexpr int hf = I / 5, ii = I % 5, grp = ii / 4, imm = (ii % 4) * 1024;
    const u32 so = sQ + w * 5120 + 122880 + hf * 20480 + grp * 4096, mo = lds0 + SLOT * 40960 + w * 5120 + hf * 20480 + grp * 4096;
    if constexpr (hf == 0) Q_EMIT("Q3a", ii % 4 == 0, imm, so, mo); else Q_EMIT("Q3b", ii % 4 == 0, imm, so, mo);
  }
#undef Q_EMIT
}
template <int HH, int I> DI void dma_g(rsrc_t rs, u32 l16, u32 sG, int w, u32 lds0) {
  constexpr int grp = I / 4, imm = (I % 4) * 1024;
  const u32 so = sG + HH * 20480 + grp * 4096, mo = lds0 + w * 5120 + (HH % 7) * 20480 + grp * 4096;
#define G_CASE(n) if constexpr (HH == n) { if constexpr (I % 4 == 0) DMA_F("G" #n, imm, "sc1 ", rs, l16, so, mo); else DMA_N("G" #n, imm, "sc1 ", rs, l16, so); }
  G_CASE(0) G_CASE(1) G_CASE(2) G_CASE(3) G_CASE(4) G_CASE(5) G_CASE(6) G_CASE(7) G_CASE(8) G_CASE(9) G_CASE(10) G_CASE(11) G_CASE(12) G_CASE(13)
#undef G_CASE
}

template <int HH> DI void dma_g3(rsrc_t rs, u32 l16, u32 sG0, int w, u32 lds0) {
  const u32 f0 = 7 * (w - 1);
  const u32 so = sG0 + HH * 20480 + f0 * 1024, mo = lds0 + (HH % 7) * 20480 + f0 * 1024;
#define G3_CASE(n) if constexpr (HH == n) { DMA_F("G" #n, 0, "sc1 ", rs, l16, so, mo); DMA_N("G" #n, 1024, "sc1 ", rs, l16, so); DMA_N("G" #n, 2048, "sc1 ", rs, l16, so); DMA_N("G" #n, 3072, "sc1 ", rs, l16, so); \
    DMA_F("G" #n, 0, "sc1 ", rs, l16, so + 4096, mo + 4096); DMA_N("G" #n, 1024, "sc1 ", rs, l16, so + 4096); if (w < 3) DMA_N("G" #n, 2048, "sc1 ", rs, l16, so + 4096); }
  G3_CASE(2) G3_CASE(3) G3_CASE(4) G3_CASE(5) G3_CASE(6)
#undef G3_CASE
}
constexpr int XW = 10;
struct QS {
  static constexpr bool x_at(int f) { return f < 120 && f % 3 == 0 && f / 3 + XW < 40; }
  static constexpr int aux_at(int f) { return (f < 32 && f % 3 == 1) ? f / 3 : -1; }
  static constexpr int q3_at(int f) { return (f >= 77 && f <= 104 && f % 3 == 2) ? (f - 77) / 3 : -1; }
  static constexpr int wo_at(int f) {
    if (f >= 34 && f <= 67 && f % 3 == 1) return (f - 34) / 3;
    if (f >= 121 && f <= 157 && (f - 121) % 3 == 0) return 12 + (f - 121) / 3;
    return -1;
  }
  static constexpr int upto(int F) { int n = 0; for (int f = 0; f <= F; ++f) n += x_at(f) + (aux_at(f) >= 0) + (q3_at(f) >= 0) + (wo_at(f) >= 0); return n; }
  static constexpr int xidx(int i) { return i < XW ? i : XW + upto(3 * (i - XW)) - 1; }
  static constexpr int xwait2(int s) { return XW + upto(3 * s) - 1 - xidx(s + 1); }
};
struct GS {
  static constexpr int NWD = 65;
  static constexpr bool x_at(int f) { return f < 120 && f % 3 == 0 && f / 3 + XW < 40; }
  static constexpr int op(int F) {
    if (F <= 10 && F % 3 == 1) return 3000 + F / 3;
    if (F >= 40 && F <= 67 && F % 3 == 1) return (7 + (F - 40) / 15) * 8 + ((F - 40) % 15) / 3;
    if (F >= 79 && F <= 106 && F % 3 == 1) return (9 + (F - 79) / 15) * 8 + ((F - 79) % 15) / 3;
    if (F < 120 || (F & 1)) return -1;
    if (F < 140) return (11 + (F - 120) / 10) * 8 + ((F - 120) % 10) / 2;
    if (F < 160) return 1000 + (F - 140) / 2;
    if (F < 170) return 13 * 8 + (F - 160) / 2;
    if (F < 280) return 1010 + (F - 170) / 2;
    return -1;
  }
  static constexpr bool st_at(int F) { return F == 139 || F == 157 || F == 175 || F == 219 || F == 237; }
  static constexpr int upto(int F) { int n = 0; for (int f = 0; f <= F; ++f) n += x_at(f) + (op(f) >= 0) + st_at(f); return n; }
  static constexpr int slot(int o) { for (int f = 0; f < 300; ++f) if (op(f) == o) return f; return -1; }
  static constexpr int xidx(int i) { return i < XW ? i : XW + upto(3 * (i - XW)) - 1; }
  static constexpr int xwait2(int s) { return XW + upto(3 * s) - 1 - xidx(s + 1); }
};
static_assert(GS::op(278) == 1000 + GS::NWD - 1, "down-weight prefetch schedule");

DI void iteration(K& k) {
  GAS u32* const flags = k.flags;
  const int tid = k.tid;
  {
    PHASE_IDS();
    const u32 sQ = OFF_WQ + p * 163840, sG = OFF_WG + p * 286720 + w * 5120;
    STAMP(0);
    if (it > 0 && w == 0) {
      const int grp = 2 * m128 - 1 + (lane >> 3);
      const bool act = lane < 24 && grp >= 0;
      poll_flags(flags, ((FG_X2 + grp) * 16 + (lane & 7)) * 32, act, lane, k.tag);
    }
    WAITV(0); CERT("Q0"); CERT("Q1"); CERT("Q2");
    BAR();
    STAMP(1);
    u32x4 xf[40];
    const u32 xbase = xin + (4 * m128 + w) * 40960;
#define XLOAD(i) ald_a_sc1<((i) % 4) * 1024>(xf[i], rs, l16, xbase + ((i) / 4) * 4096)
    u32x2 hs0, hs1, sa0, sa1, sa2, sa3;
    const int rbh = m128 > 0 ? 4 * m128 - 1 : 0;
    const u32 hvo = (g >> 1) * 1024 + (g & 1) * 512 + (16 + t16) * 16;
    const int ht = 128 * m128 - 16 + t16;
    const u32 hso = ((2 * g) * 2048 + (ht < 0 ? 0 : ht)) * 8, sso = (4 * h * 2048 + 128 * m128 + 32 * w + tok) * 8;
    auto aux_load = [&](auto AC) {
      constexpr int a = decltype(AC)::value;
      if constexpr (a < 5) DMA_F("HALO", 0, "sc1 ", rs, hvo, xin + rbh * 40960 + (5 * w + a) * 2048, lds0 + L_HALO + (5 * w + a) * 1024);
      if constexpr (a == 5) ald2_sc1(sa0, rs, sso, stin);
      if constexpr (a == 6) ald2_sc1(sa1, rs, sso, stin + 16384);
      if constexpr (a == 7) ald2_sc1(sa2, rs, sso, stin + 32768);
      if constexpr (a == 8) ald2_sc1(sa3, rs, sso, stin + 49152);
      if constexpr (a == 9) ald2_sc1(hs0, rs, hso, stin);
      if constexpr (a == 10) ald2_sc1(hs1, rs, hso, stin + 16384);
    };
    sfor<XW>([&](auto I) { constexpr int i = decltype(I)::value; XLOAD(i); });
    using Q = QS;
    constexpr int DQ = 4, NBQ = 6;
    u32x4 wb[NBQ];
    f32x16 acc0, acc1, acc2, acc3;
    f32x4 hacc, hacc4;
    u32x4 hw[3], hx[3], h4w[3], h4x[3];
    float rstd = 0.f, hrs = 0.f;
    u32 lb1 = l16 + 65536, lbh = l16 + L_HALO; asm volatile("" : "+v"(lb1), "+v"(lbh));
    u32 hwlo = (w >> 1) * 1024 + (g >> 1) * 3072 + (g & 1) * 512 + (16 * (w & 1) + t16) * 16, hwhi = hwlo + 61440;
    u32 hw4 = 2048 + (g >> 1) * 3072 + (g & 1) * 512 + t16 * 16 + w * 30720, hx4 = l16 + L_HALO + w * 5120;
    asm volatile("" : "+v"(hwlo), "+v"(hwhi), "+v"(hw4), "+v"(hx4));
    const u32 kvrow = L_KST + (16 + 32 * w + tok) * 80 + h * 8;
    const int hn = 16 * w + 4 * g;
    const u32 haddr = L_KST + t16 * 80 + 2 * hn + (hn >= 40 ? (L_VST - L_KST - 80) : 0);
    const u32 haddr4 = L_VST + t16 * 80 + 2 * (64 + 4 * g - 40);
    const bool hzero = (m128 == 0);
    const u32 wo_o = OFF_WO + (c8 * 20 + 5 * w) * 5120;
#define LDSQ(F) (((F) / 40 % 3) * 40960 + ((F) % 40) * 1024)
#define RDW(off) (*(const LAS u32x4*)(lds + ((off) < 65536 ? l16 : lb1) + ((off) & 65535)))
    u32 gp0 = 0;
#define STG_OFF(n0) ((n0) < 40 ? 2 * (n0) : (L_VST - L_KST) + 2 * ((n0) - 40))
    const u32 qrow = L_QST + (32 * w + tok) * 144 + h * 8, qrow2 = L_QST + (32 * w + tok) * 144 + (h ? 72 : 32);
#define Q_ADDR(n0) ((n0) == 96 ? qrow2 : qrow + ((n0) < 96 ? 2 * ((n0) - 80) : 2 * ((n0) - 80) + 32))
#define GRAN(t, ACC, q, RS, STADDR, ZERO) do { \
      if constexpr ((t) == 0) gp0 = cvtpk(mul_s(ACC[4 * (q)], RS), mul_s(ACC[4 * (q) + 1], RS)); \
      if constexpr ((t) == 1) { u32x2 pk_ = {gp0, cvtpk(mul_s(ACC[4 * (q) + 2], RS), mul_s(ACC[4 * (q) + 3], RS))}; if (ZERO) pk_ = u32x2{0u, 0u}; *(LAS u32x2*)(lds + (STADDR)) = pk_; } \
    } while (0)
#define GRAN_MAIN(t, ACC, q, n0) GRAN(t, ACC, q, rstd, ((n0) < 80 ? kvrow + STG_OFF(n0) : Q_ADDR(n0)), false)
#pragma unroll
    for (int F = 0; F < DQ; ++F) wb[F % NBQ] = RDW(LDSQ(F));
    sfor<160>([&](auto FC) {
      constexpr int F = decltype(FC)::value, Fr = F + DQ;
      if constexpr (F == 30) QSTAMP(21);
      if constexpr (F == 60) QSTAMP(22);
      if constexpr (F == 90) QSTAMP(23);
      if constexpr (F == 140) QSTAMP(26);
      if constexpr (Q::x_at(F)) XLOAD(F / 3 + XW);
      if constexpr (Q::aux_at(F) >= 0) aux_load(std::integral_constant<int, Q::aux_at(F)>{});
      if constexpr (Q::q3_at(F) >= 0) dma_q<3, 0, Q::q3_at(F)>(rs, l16, sQ, w, lds0);
      if constexpr (Q::wo_at(F) >= 0) {
        constexpr int i = Q::wo_at(F), s = i / 5, nb = i % 5;
        if constexpr (i < 24) ald_a<(i % 4) * 1024>(k.wo_r[s][nb], rs, l16, wo_o + (i / 4) * 4096); else ald_v<(i % 4) * 1024>(k.wo_r[s][nb], rs, l16, wo_o + (i / 4) * 4096);
      }
      if constexpr (Fr == 40) {
        WAITV(Q::upto(F) - Q::upto(13)); CERT("HALO"); BAR();
        if (tid == 0) __hip_atomic_store(flags + (FG_PF2 * 16 + k.bid) * 32, k.tag, RLX_AGENT);
      }
      if constexpr (Fr == 80) { WAITL0(); BAR(); }
      if constexpr (Fr == 120) {
        if (w == 0 && it == 0) {
          const int cgg = (B0 >> 1) + ((lane & 15) >> 1);
          const bool isg = lane < 10, act = isg ? 32 * cgg < 16 * (B0 + k.nblk) : lane < 60;
          const u32 u2 = isg ? 124 + 54 * (lane & 1) + cgg : 10 * ((lane - 10) % 25) + (80 * c8) / 64 + (lane - 10) / 25;
          poll_flags(flags, (FG_PF2 * 16 + (act ? u2 : 0)) * 32, act, lane, k.tag);
        }
        WAITV(Q::upto(F) - Q::upto(89)); CERT("Q3a"); WAITL0(); BAR(); STAMP(2);
      }
      if constexpr (Fr == 140) { WAITV(Q::upto(F) - Q::upto(104)); CERT("Q3b"); BAR(); }
      if constexpr (Fr < 160) wb[Fr % NBQ] = RDW(LDSQ(Fr));
      if constexpr (F < 120) {
        constexpr int s = F / 3, b = F % 3;
        if constexpr (b == 0 && (s & 1) == 0) waitpin_a2<Q::xwait2(s)>(xf[s], xf[s + 1]);
#define QMMA(ACC) do { if constexpr (s == 0) { if constexpr ((F & 1) == 0) mma32w_0(ACC, wb[F % NBQ], xf[s], wb[(F + 1) % NBQ]); else mma32_0(ACC, wb[F % NBQ], xf[s]); } \
                       else { if constexpr ((F & 1) == 0) mma32w(ACC, wb[F % NBQ], xf[s], wb[(F + 1) % NBQ]); else mma32(ACC, wb[F % NBQ], xf[s]); } } while (0)
        if constexpr (b == 0) QMMA(acc0); else if constexpr (b == 1) QMMA(acc1); else QMMA(acc2);
        if constexpr (F == 62) {
          pin2(sa0); pin2(sa1); pin2(sa2); pin2(sa3);
          float nm;
          stats_finish(red32_add((u2f(sa0.x) + u2f(sa1.x)) + (u2f(sa2.x) + u2f(sa3.x))), red32_add((u2f(sa0.y) + u2f(sa1.y)) + (u2f(sa2.y) + u2f(sa3.y))), nm, rstd);
        }
        if constexpr (F == 110) {
          pin2(hs0); pin2(hs1);
          float nm;
          stats_finish(red16_32_add(u2f(hs0.x) + u2f(hs1.x)), red16_32_add(u2f(hs0.y) + u2f(hs1.y)), nm, hrs);
        }
        if constexpr (F == 113) {
#pragma unroll
          for (int nb = 0; nb < 5; ++nb) pin2(k.xr[nb]);
        }
        if constexpr (b == 1) {
          if constexpr (s == 15) mma16_0(hacc, hw[0], hx[0]);
          if constexpr (s > 15 && s <= 34) mma16(hacc, hw[(s - 15) % 3], hx[(s - 15) % 3]);
          if constexpr (s >= 13 && s <= 32) {
            constexpr int sg = s - 13;
            hw[sg % 3] = *(const LAS u32x4*)(lds + (sg < 10 ? hwlo : hwhi) + (sg % 10) * 6144);
            hx[sg % 3] = *(const LAS u32x4*)(lds + lbh + sg * 1024);
          }
        }
      } else {
        constexpr int s = F - 120;
        if constexpr (s == 0) mma32w_0(acc3, wb[F % NBQ], xf[s], wb[(F + 1) % NBQ]); else if constexpr (s & 1) mma32(acc3, wb[F % NBQ], xf[s]); else mma32w(acc3, wb[F % NBQ], xf[s], wb[(F + 1) % NBQ]);
        constexpr int ms = s - 2, gi = ms / 2, st = ms % 2;
        if constexpr (ms >= 0 && gi < 12) {
          constexpr int b = gi >> 2, q = gi & 3, n0 = 32 * b + 8 * q;
          if constexpr (b == 0) GRAN_MAIN(st, acc0, q, n0); else if constexpr (b == 1) GRAN_MAIN(st, acc1, q, n0); else GRAN_MAIN(st, acc2, q, n0);
        }
        if constexpr (s == 22) {
          const f32x4 p0 = *(const LAS f32x4*)(lds + L_H4 + l16), p1 = *(const LAS f32x4*)(lds + L_H4 + 1024 + l16);
          const f32x4 p2 = *(const LAS f32x4*)(lds + L_H4 + 2048 + l16), p3 = *(const LAS f32x4*)(lds + L_H4 + 3072 + l16);
          hacc4 = (p0 + p1) + (p2 + p3);
        }
        if constexpr (ms >= 0 && gi == 12) GRAN(st, hacc, 0, hrs, haddr, hzero);
        if constexpr (ms >= 0 && gi == 14) GRAN(st, hacc4, 0, hrs, haddr4, hzero);
        if constexpr (s == 33) *(LAS u32x2*)(lds + L_QST + (32 * w + tok) * 144 + (h ? 64 : 40)) = u32x2{0u, 0u};
        if constexpr (s == 35) *(LAS u32x2*)(lds + L_QST + (32 * w + tok) * 144 + (h ? 112 : 48)) = u32x2{0u, 0u};
        if constexpr (s == 37) *(LAS u32x2*)(lds + L_QST + (32 * w + tok) * 144 + (h ? 120 : 56)) = u32x2{0u, 0u};
      }
      if constexpr (F < 120 && F % 3 == 2) {
        constexpr int s = F / 3;
        if constexpr (s == 15) mma16_0(hacc4, h4w[0], h4x[0]);
        if constexpr (s > 15 && s <= 19) mma16(hacc4, h4w[(s - 15) % 3], h4x[(s - 15) % 3]);
        if constexpr (s >= 13 && s <= 17) {
          h4w[(s - 13) % 3] = *(const LAS u32x4*)(lds + hw4 + (s - 13) * 6144);
          h4x[(s - 13) % 3] = *(const LAS u32x4*)(lds + hx4 + (s - 13) * 1024);
        }
        if constexpr (s == 21) *(LAS f32x4*)(lds + L_H4 + w * 1024 + l16) = hacc4;
      }
      SB();
    });
    STAMP(3);
    asm volatile("s_nop 11" : "+v"(acc3));
    GRAN_MAIN(0, acc3, 0, 96); GRAN_MAIN(1, acc3, 0, 96); GRAN_MAIN(0, acc3, 1, 104); GRAN_MAIN(1, acc3, 1, 104); GRAN_MAIN(0, acc3, 2, 112); GRAN_MAIN(1, acc3, 2, 112);
    WAITL0(); BAR();
    STAMP(4);
    {
      const int par = t16 & 1;
      const bool ca = (4 * g + par) > t16, cb = (4 * g + 2 + par) > t16;
      const unsigned long long mca = __ballot(ca), mcb = __ballot(cb), mpar = __ballot(par != 0);
      const bool neg = (m128 == 0) && (w == 0);
      const unsigned long long nga = neg ? mca : 0ull, ngb = neg ? mcb : 0ull;
      const u32 gq = g < 2 ? g : 2;
      const u32 pla = par ? 0x01000c0cu : 0x0c0c0100u, plb = par ? 0x03020c0cu : 0x0c0c0302u;
      const u32 sel0 = ca ? pla : 0x0c0c0c0cu, sel2 = ca ? 0x0c0c0c0cu : pla, sel1 = cb ? plb : 0x0c0c0c0cu, sel3 = cb ? 0x0c0c0c0cu : plb;
      constexpr float cexp = 0.22360679774997896f * 1.4426950408889634f;
      float cx = cexp; asm volatile("" : "+v"(cx));
      const u32 qa = L_QST + (32 * w + t16) * 144 + g * 16, ka = L_KST + (32 * w + t16) * 80 + gq * 16;
      const u32 va = L_VST + (32 * w + 4 * g + (t16 >> 2)) * 80 + (t16 & 3) * 8;
      u32x4 qf[4], k0[4], k1[4];
      u32x4 v0[4], v1[4];
#pragma unroll
      for (int u = 0; u < 4; ++u) {
        const u32 o = (u >> 1) * 16 * 80 + (u & 1) * 32;
        qf[u] = *(const LAS u32x4*)(lds + qa + (u >> 1) * 16 * 144 + (u & 1) * 64);
        k0[u] = *(const LAS u32x4*)(lds + ka + o);
        k1[u] = *(const LAS u32x4*)(lds + ka + o + 16 * 80);
      }
      dma_g<0, 0>(rs, l16, sG, w, lds0);
      f32x4 s0[4], s1[4];
      asm volatile("s_nop 1\n\t"
                   "v_mfma_f32_16x16x32_bf16 %0, %8, %16, 0\n\tv_mfma_f32_16x16x32_bf16 %1, %12, %16, 0\n\t"
                   "v_mfma_f32_16x16x32_bf16 %2, %9, %17, 0\n\tv_mfma_f32_16x16x32_bf16 %3, %13, %17, 0\n\t"
                   "v_mfma_f32_16x16x32_bf16 %4, %10, %18, 0\n\tv_mfma_f32_16x16x32_bf16 %5, %14, %18, 0\n\t"
                   "v_mfma_f32_16x16x32_bf16 %6, %11, %19, 0\n\tv_mfma_f32_16x16x32_bf16 %7, %15, %19, 0"
                   : "=&v"(s0[0]), "=&v"(s1[0]), "=&v"(s0[1]), "=&v"(s1[1]), "=&v"(s0[2]), "=&v"(s1[2]), "=&v"(s0[3]), "=&v"(s1[3])
                   : "v"(k0[0]), "v"(k0[1]), "v"(k0[2]), "v"(k0[3]), "v"(k1[0]), "v"(k1[1]), "v"(k1[2]), "v"(k1[3]), "v"(qf[0]), "v"(qf[1]), "v"(qf[2]), "v"(qf[3]));
#pragma unroll
      for (int u = 0; u < 4; ++u) {
        const u32 o = (u >> 1) * 16 * 80 + (u & 1) * 40;
        const u32x2 t0 = __builtin_bit_cast(u32x2, __builtin_amdgcn_ds_read_tr16_b64_v4i16((LAS s16x4*)(lds + va + o)));
        const u32x2 t1 = __builtin_bit_cast(u32x2, __builtin_amdgcn_ds_read_tr16_b64_v4i16((LAS s16x4*)(lds + va + o + 16 * 80)));
        const u32x2 t2 = __builtin_bit_cast(u32x2, __builtin_amdgcn_ds_read_tr16_b64_v4i16((LAS s16x4*)(lds + va + o + 32)));
        const u32x2 t3 = __builtin_bit_cast(u32x2, __builtin_amdgcn_ds_read_tr16_b64_v4i16((LAS s16x4*)(lds + va + o + 32 + 16 * 80)));
        v0[u] = u32x4{t0.x, t0.y, t1.x, t1.y}; v1[u] = u32x4{t2.x, t2.y, t3.x, t3.y};
      }
      dma_g<0, 1>(rs, l16, sG, w, lds0);
      asm volatile("s_nop 7" : "+v"(s0[0]), "+v"(s1[0]), "+v"(s0[1]), "+v"(s1[1]), "+v"(s0[2]), "+v"(s1[2]), "+v"(s0[3]), "+v"(s1[3]));
      float a[4], b[4], mx[4], sm[4];
#pragma unroll
      for (int u = 0; u < 4; ++u) {
        const float x00 = s0[u][0], x01 = s0[u][1], x02 = s0[u][2], x03 = s0[u][3], x10 = s1[u][0], x11 = s1[u][1], x12 = s1[u][2], x13 = s1[u][3];
        a[u] = sel_s(sel_s(x10, x11, mpar), sel_s(x00, x01, mpar), mca);
        b[u] = sel_s(sel_s(x12, x13, mpar), sel_s(x02, x03, mpar), mcb);
        if (u < 2) { a[u] = sel_s(a[u], -__builtin_inff(), nga); b[u] = sel_s(b[u], -__builtin_inff(), ngb); }
        mx[u] = max_s(a[u], b[u]);
      }
      dma_g<0, 2>(rs, l16, sG, w, lds0);
      {
        auto r01 = __builtin_amdgcn_permlane16_swap(f2u(mx[0]), f2u(mx[1]), false, false), r23 = __builtin_amdgcn_permlane16_swap(f2u(mx[2]), f2u(mx[3]), false, false);
        const float c01 = __builtin_fmaxf(u2f(r01[0]), u2f(r01[1])), c23 = __builtin_fmaxf(u2f(r23[0]), u2f(r23[1]));
        auto rt = __builtin_amdgcn_permlane32_swap(f2u(c01), f2u(c23), false, false);
        const u32 t = f2u(__builtin_fmaxf(u2f(rt[0]), u2f(rt[1])));
        dma_g<0, 3>(rs, l16, sG, w, lds0);
        auto ab = __builtin_amdgcn_permlane32_swap(t, t, false, false);
        auto m01 = __builtin_amdgcn_permlane16_swap(ab[0], ab[0], false, false), m23 = __builtin_amdgcn_permlane16_swap(ab[1], ab[1], false, false);
        mx[0] = u2f(m01[0]); mx[1] = u2f(m01[1]); mx[2] = u2f(m23[0]); mx[3] = u2f(m23[1]);
      }
      dma_g<0, 4>(rs, l16, sG, w, lds0);
#pragma unroll
      for (int u = 0; u < 4; ++u) {
        const float t = -mul_s(mx[u], cx);
        a[u] = __builtin_amdgcn_exp2f(fma_s(a[u], cx, t)); b[u] = __builtin_amdgcn_exp2f(fma_s(b[u], cx, t));
        sm[u] = a[u] + b[u];
      }
      dma_g<1, 0>(rs, l16, sG, w, lds0);
      {
        auto r01 = __builtin_amdgcn_permlane16_swap(f2u(sm[0]), f2u(sm[1]), false, false), r23 = __builtin_amdgcn_permlane16_swap(f2u(sm[2]), f2u(sm[3]), false, false);
        const float c01 = u2f(r01[0]) + u2f(r01[1]), c23 = u2f(r23[0]) + u2f(r23[1]);
        auto rt = __builtin_amdgcn_permlane32_swap(f2u(c01), f2u(c23), false, false);
        const u32 t = f2u(__builtin_amdgcn_rcpf(u2f(rt[0]) + u2f(rt[1])));
        dma_g<1, 1>(rs, l16, sG, w, lds0);
        auto ab = __builtin_amdgcn_permlane32_swap(t, t, false, false);
        auto m01 = __builtin_amdgcn_permlane16_swap(ab[0], ab[0], false, false), m23 = __builtin_amdgcn_permlane16_swap(ab[1], ab[1], false, false);
        sm[0] = u2f(m01[0]); sm[1] = u2f(m01[1]); sm[2] = u2f(m23[0]); sm[3] = u2f(m23[1]);
      }
      dma_g<1, 2>(rs, l16, sG, w, lds0);
      u32x4 pf[4];
#pragma unroll
      for (int u = 0; u < 4; ++u) {
        const float inv = sm[u];
        const u32 pk = cvtpk(a[u] * inv, b[u] * inv);
        pf[u] = u32x4{__builtin_amdgcn_perm(pk, pk, sel0), __builtin_amdgcn_perm(pk, pk, sel1), __builtin_amdgcn_perm(pk, pk, sel2), __builtin_amdgcn_perm(pk, pk, sel3)};
      }
      dma_g<1, 3>(rs, l16, sG, w, lds0);
      f32x4 o0[4], o1[4];
      asm volatile("s_nop 1\n\t"
                   "v_mfma_f32_16x16x32_bf16 %0, %8, %16, 0\n\tv_mfma_f32_16x16x32_bf16 %1, %12, %16, 0\n\t"
                   "v_mfma_f32_16x16x32_bf16 %2, %9, %17, 0\n\tv_mfma_f32_16x16x32_bf16 %3, %13, %17, 0\n\t"
                   "v_mfma_f32_16x16x32_bf16 %4, %10, %18, 0\n\tv_mfma_f32_16x16x32_bf16 %5, %14, %18, 0\n\t"
                   "v_mfma_f32_16x16x32_bf16 %6, %11, %19, 0\n\tv_mfma_f32_16x16x32_bf16 %7, %15, %19, 0\n\ts_nop 7"
                   : "=&v"(o0[0]), "=&v"(o1[0]), "=&v"(o0[1]), "=&v"(o1[1]), "=&v"(o0[2]), "=&v"(o1[2]), "=&v"(o0[3]), "=&v"(o1[3])
                   : "v"(v0[0]), "v"(v0[1]), "v"(v0[2]), "v"(v0[3]), "v"(v1[0]), "v"(v1[1]), "v"(v1[2]), "v"(v1[3]), "v"(pf[0]), "v"(pf[1]), "v"(pf[2]), "v"(pf[3]));
      dma_g<1, 4>(rs, l16, sG, w, lds0);
      const u32 sg = 5 * p + g, sg4 = 5 * p + 4;
      const u32 oa = (sg >> 2) * 1024 + (t16 + 16 * (sg & 3)) * 16, oa4 = (sg4 >> 2) * 1024 + (t16 + 16 * (sg4 & 3)) * 16;
#pragma unroll
      for (int qb = 0; qb < 2; ++qb) {
        const u32 so = ofo + (8 * m128 + 2 * w + qb) * 20480;
        const f32x4 A0 = o0[2 * qb], B0_ = o0[2 * qb + 1], A1 = o1[2 * qb], B1 = o1[2 * qb + 1];
        stg_sc1(u32x4{cvtpk(A0[0], A0[1]), cvtpk(A0[2], A0[3]), cvtpk(B0_[0], B0_[1]), cvtpk(B0_[2], B0_[3])}, rs, oa, so);
        if (g == 0) stg_sc1(u32x4{cvtpk(A1[0], A1[1]), cvtpk(A1[2], A1[3]), cvtpk(B1[0], B1[1]), cvtpk(B1[2], B1[3])}, rs, oa4, so);
      }
    }
    SB();
    STAMP(5);
    WAITV(0); CERT("G0"); CERT("G1");
    WAITL0();
    BAR();
    if (tid == 0) __hip_atomic_store(flags + ((fgb + FG_O + m128) * 16 + p) * 32, k.tag, RLX_AGENT);
    STAMP(6); RSTAMP(0);
    sfor<5>([&](auto I) { dma_g<2, decltype(I)::value>(rs, l16, sG, w, lds0); });
    sfor<5>([&](auto I) { dma_g<3, decltype(I)::value>(rs, l16, sG, w, lds0); });
  }
  {
    PHASE_IDS();
    if (w == 0) poll_flags(flags, ((fgb + FG_O + (m64 >> 1)) * 16 + (lane & 15)) * 32, lane < 16, lane, k.tag);
    BAR();
    STAMP(7); RSTAMP(1);
#pragma unroll
    for (int s = 0; s < 5; ++s)
#pragma unroll
      for (int nb = 0; nb < 5; ++nb) { if (s * 5 + nb < 24) pin_a(k.wo_r[s][nb]); else pin_v(k.wo_r[s][nb]); }
    u32x4 af[20];
    u32 tbo[4];
#pragma unroll
    for (int tbi = 0; tbi < 4; ++tbi) tbo[tbi] = ofo + ((4 * m64 + ((w + 1 + tbi) & 3)) * 20 + 5 * w) * 1024;
    constexpr int OW = 10;
#define OLOAD(q) ald_v_sc1<((q) % 5 % 4) * 1024>(af[q], rs, l16, tbo[(q) / 5] + ((q) % 5 / 4) * 4096)
    sfor<OW>([&](auto I) { constexpr int q = decltype(I)::value; OLOAD(q); });
    f32x4 acc[2][5];
    f32x4 part[3][5];
    sfor<20>([&](auto QC) {
      constexpr int q = decltype(QC)::value, tbi = q / 5, s = q % 5;
      if constexpr (q + OW < 20) OLOAD(q + OW);
      waitpin_v<(q < 10 ? OW : (19 - q))>(af[q]);
#pragma unroll
      for (int nb = 0; nb < 5; ++nb) acc[tbi & 1][nb] = mfma16(k.wo_r[s][nb], af[q], s == 0 ? f32x4{0.f, 0.f, 0.f, 0.f} : acc[tbi & 1][nb]);
      if constexpr (tbi > 0 && s < 3) {
        const u32 tbp = (w + tbi) & 3;
        *(LAS f32x4*)(lds + L_RED + ((tbp * 3 + (3 - tbi)) * 5 + 2 * s) * 1024 + l16) = acc[(tbi - 1) & 1][2 * s];
        if constexpr (2 * s + 1 < 5) *(LAS f32x4*)(lds + L_RED + ((tbp * 3 + (3 - tbi)) * 5 + 2 * s + 1) * 1024 + l16) = acc[(tbi - 1) & 1][2 * s + 1];
      }
      if constexpr (q == 18) {
        WAITL0(); BAR();
#pragma unroll
        for (int sl = 0; sl < 3; ++sl)
#pragma unroll
          for (int nb = 0; nb < 5; ++nb) part[sl][nb] = *(const LAS f32x4*)(lds + L_RED + ((w * 3 + sl) * 5 + nb) * 1024 + l16);
      }
      SB();
    });
    STAMP(8);
    float s1 = 0.f, s2 = 0.f;
#pragma unroll
    for (int nb = 0; nb < 5; ++nb) {
      const f32x4 v = (acc[1][nb] + part[0][nb]) + (part[1][nb] + part[2][nb]);
      const u32 a = cvtpk(bflo(k.xr[nb].x) + v[0], bfhi(k.xr[nb].x) + v[1]), b = cvtpk(bflo(k.xr[nb].y) + v[2], bfhi(k.xr[nb].y) + v[3]);
      k.xr[nb] = u32x2{a, b};
    }
    const u32 token = 64 * m64 + 16 * w + t16;
    const u32 xo = ((token >> 5) * 40 + 5 * c8) * 1024 + ((token & 31) + 32 * (g >> 1)) * 16;
#pragma unroll
    for (int pr = 0; pr < 3; ++pr) {
      const int A = 2 * pr, Bn = pr < 2 ? 2 * pr + 1 : 4;
      auto r0 = __builtin_amdgcn_permlane16_swap(k.xr[A].x, k.xr[Bn].x, false, false);
      auto r1 = __builtin_amdgcn_permlane16_swap(k.xr[A].y, k.xr[Bn].y, false, false);
      const u32x4 v = {r0[0], r1[0], r0[1], r1[1]};
      if (pr < 2) stg_sc1(v, rs, xo + (A + (g & 1)) * 1024, x1o);
      else if ((g & 1) == 0) stg_sc1(v, rs, xo + 4 * 1024, x1o);
    }
#pragma unroll
    for (int nb = 0; nb < 5; ++nb) {
      const float a = bflo(k.xr[nb].x), b = bfhi(k.xr[nb].x), c = bflo(k.xr[nb].y), d = bfhi(k.xr[nb].y);
      s1 += (a + b) + (c + d); s2 += (a * a + b * b) + (c * c + d * d);
    }
    s1 = red16_32_add(s1); s2 = red16_32_add(s2);
    if (g == 0) stg2_sc1(u32x2{f2u(s1), f2u(s2)}, rs, (c8 * 2048 + token) * 8, st1o);
    SB();
    STAMP(9);
    WAITV(0); CERT("G2"); CERT("G3");
    WAITL0();
    BAR();
    if (tid == 0) __hip_atomic_store(flags + ((fgb + FG_X1 + m64) * 16 + c8) * 32, k.tag, RLX_AGENT);
    STAMP(10); RSTAMP(2);
  }
  {
    PHASE_IDS();
    const int nblk = k.nblk;
    const int ds0 = w < 2 ? 14 * w : 28 + 13 * (w - 2);
    const u32 sG = OFF_WG + p * 286720 + w * 5120;
    Polls pl = {0u, 0u, 0u};
    if (w != 0) { dma_g3<4>(rs, l16, sG - w * 5120, w, lds0); dma_g3<5>(rs, l16, sG - w * 5120, w, lds0); dma_g3<6>(rs, l16, sG - w * 5120, w, lds0); }
    if (w == 0) poll_flags_p(flags, ((fgb + FG_X1 + 2 * m128 + ((lane >> 3) & 1)) * 16 + (lane & 7)) * 32, lane < 16, lane, k.tag, pl);
    BAR();
    STAMP(11); RSTAMP(3);
    u32x2 sa0, sa1, sa2, sa3;
    u32x4 xf[40];
    const u32 xbase = x1o + (4 * m128 + w) * 40960;
    const u32 sso = (4 * h * 2048 + 128 * m128 + 32 * w + tok) * 8;
    sfor<XW>([&](auto I) { constexpr int i = decltype(I)::value; XLOAD(i); });
    constexpr int NWD = GS::NWD;
    float r2 = 0.f;
    constexpr int DG = 4, NBG = 6;
    u32x4 wb[NBG];
    f32x16 aA0, aA1, aA2, aB0, aB1;
    u32 lb1 = l16 + 65536, lb2 = l16 + 131072; asm volatile("" : "+v"(lb1), "+v"(lb2));
    const u32 hbase = (8 * m128 + 2 * w + (tok >> 4)) * 54 * 1024 + ((tok & 15) + 16 * h) * 16;
    const u32 hlast = nblk == 7 ? hbase : 0x7fff0000u;
    const u32 wd_o = OFF_WD + (c8 * 54 + ds0) * 5120;
#define LDSG(F) ((((F) / 20) % 7) * 20480 + ((F) % 20) * 1024)
#define RDG(off) (*(const LAS u32x4*)(lds + ((off) < 65536 ? l16 : (off) < 131072 ? lb1 : lb2) + ((off) & 65535)))
    float hv0 = 0.f;
    u32 hp[4];
#define GU_STAGE(ACC, idx, t) do { \
      if constexpr ((t) < 8) { const float hv_ = mul_s(mul_s(ACC[t], r2), ACC[8 + (t)]); if constexpr ((t) & 1) hp[(t) >> 1] = cvtpk(hv0, hv_); else hv0 = hv_; } \
      if constexpr ((t) == 8) { const u32 fb_ = B0 + (idx); stg_sc1(u32x4{hp[0], hp[1], hp[2], hp[3]}, rs, ((idx) == 6 ? hlast : hbase) + (fb_ & 1) * 512, hfo + (fb_ >> 1) * 1024); } \
    } while (0)
    auto wd_load = [&](auto OC) {
      constexpr int i = decltype(OC)::value, s = i / 5, nb = i % 5;
      if constexpr (i < 24 || i >= 50) ald_a<(i % 4) * 1024>(k.wd_r[s][nb], rs, l16, wd_o + (i / 4) * 4096);
      else ald_v<(i % 4) * 1024>(k.wd_r[s][nb], rs, l16, wd_o + (i / 4) * 4096);
    };
#pragma unroll
    for (int F = 0; F < DG; ++F) wb[F % NBG] = RDG(LDSG(F));
    sfor<280>([&](auto FC) {
      constexpr int F = decltype(FC)::value, Fr = F + DG;
      if constexpr (GS::x_at(F)) XLOAD(F / 3 + XW);
      if constexpr (GS::op(F) >= 0 && GS::op(F) < 1000) dma_g<GS::op(F) / 8, GS::op(F) % 8>(rs, l16, sG, w, lds0);
      if constexpr (GS::op(F) >= 1000 && GS::op(F) < 2000) wd_load(std::integral_constant<int, GS::op(F) - 1000>{});
      if constexpr (GS::op(F) == 3000) ald2_sc1(sa0, rs, sso, st1o);
      if constexpr (GS::op(F) == 3001) ald2_sc1(sa1, rs, sso, st1o + 16384);
      if constexpr (GS::op(F) == 3002) ald2_sc1(sa2, rs, sso, st1o + 32768);
      if constexpr (GS::op(F) == 3003) ald2_sc1(sa3, rs, sso, st1o + 49152);
      if constexpr (Fr == 40) { WAITL0(); BAR(); }
      if constexpr (Fr == 80) { CERT("G4"); CERT("G5"); WAITL0(); BAR(); }
      if constexpr (Fr == 120) { WAITV(GS::upto(F) - GS::upto(GS::slot(7 * 8 + 4))); CERT("G6"); CERT("G7"); WAITL0(); BAR(); STAMP(12); }
      if constexpr (Fr == 160) { GSTAMP(23); WAITV(GS::upto(F) - GS::upto(GS::slot(9 * 8 + 4))); CERT("G8"); CERT("G9"); WAITL0(); BAR(); GSTAMP(24); }
      if constexpr (Fr == 200) { GSTAMP(25); WAITV(GS::upto(F) - GS::upto(GS::slot(11 * 8 + 4))); CERT("G10"); CERT("G11"); BAR(); GSTAMP(26); }
      if constexpr (Fr == 240) { WAITV(GS::upto(F) - GS::upto(GS::slot(13 * 8 + 4))); CERT("G12"); CERT("G13"); BAR(); STAMP(13); }
      if constexpr (Fr < 280) wb[Fr % NBG] = RDG(LDSG(Fr));
      constexpr int s = F < 120 ? F / 3 : F < 200 ? (F - 120) / 2 : (F - 200) / 2, b = F < 120 ? F % 3 : F & 1;
      if constexpr (F < 120 && b == 0 && (s & 1) == 0) waitpin_a2<GS::xwait2(s)>(xf[s], xf[s + 1]);
#define GMMA(ACC) do { if constexpr (s == 0) { if constexpr ((F & 1) == 0) mma32w_0(ACC, wb[F % NBG], xf[s], wb[(F + 1) % NBG]); else mma32_0(ACC, wb[F % NBG], xf[s]); } \
                       else { if constexpr ((F & 1) == 0) mma32w(ACC, wb[F % NBG], xf[s], wb[(F + 1) % NBG]); else mma32(ACC, wb[F % NBG], xf[s]); } } while (0)
      if constexpr (F < 120) { if constexpr (b == 0) GMMA(aA0); else if constexpr (b == 1) GMMA(aA1); else GMMA(aA2); }
      else if constexpr (F < 200) { if constexpr (b == 0) GMMA(aB0); else GMMA(aB1); }
      else { if constexpr (b == 0) GMMA(aA0); else GMMA(aA1); }
      if constexpr (F == 3) poll_pin(pl);
      if constexpr (F == 62) {
        pin2(sa0); pin2(sa1); pin2(sa2); pin2(sa3);
        float rstd, nm;
        stats_finish(red32_add((u2f(sa0.x) + u2f(sa1.x)) + (u2f(sa2.x) + u2f(sa3.x))), red32_add((u2f(sa0.y) + u2f(sa1.y)) + (u2f(sa2.y) + u2f(sa3.y))), nm, rstd);
        r2 = rstd * rstd;
      }
      if constexpr ((F & 1) && F >= 123 && F <= 139) GU_STAGE(aA0, 0, (F - 123) / 2);
      if constexpr ((F & 1) && F >= 141 && F <= 157) GU_STAGE(aA1, 1, (F - 141) / 2);
      if constexpr ((F & 1) && F >= 159 && F <= 175) GU_STAGE(aA2, 2, (F - 159) / 2);
      if constexpr ((F & 1) && F >= 203 && F <= 219) GU_STAGE(aB0, 3, (F - 203) / 2);
      if constexpr ((F & 1) && F >= 221 && F <= 237) GU_STAGE(aB1, 4, (F - 221) / 2);
      SB();
    });
    asm volatile("s_nop 11" : "+v"(aA0), "+v"(aA1));
    sfor<9>([&](auto T) { GU_STAGE(aA0, 5, decltype(T)::value); });
    sfor<9>([&](auto T) { GU_STAGE(aA1, 6, decltype(T)::value); });
    SB();
    STAMP(14);
    WAITV(0);
    WAITL0();
    BAR();
    if (tid == 0) __hip_atomic_store(flags + ((fgb + FG_H + m128) * 16 + p) * 32, k.tag, RLX_AGENT);
    {
      const u32 wd_o = OFF_WD + (c8 * 54 + ds0) * 5120;
      sfor<70 - GS::NWD>([&](auto I) { constexpr int i = GS::NWD + decltype(I)::value; ald_a<(i % 4) * 1024>(k.wd_r[i / 5][i % 5], rs, l16, wd_o + (i / 4) * 4096); });
    }
    STAMP(15); RSTAMP(4);
  }
  {
    PHASE_IDS();
    const int ds0 = w < 2 ? 14 * w : 28 + 13 * (w - 2);
    const u32 sQ = OFF_WQ + p * 163840;
    STAMP(16);
    if (w == 0) poll_flags(flags, ((fgb + FG_H + (m64 >> 1)) * 16 + (lane & 15)) * 32, lane < 16, lane, k.tag);
    else WAITV(0);
    BAR();
    STAMP(17); RSTAMP(5);
    sfor<70>([&](auto I) { constexpr int i = decltype(I)::value; if constexpr (i < 24 || i >= 50) pin_a(k.wd_r[i / 5][i % 5]); else pin_v(k.wd_r[i / 5][i % 5]); });
    constexpr int DH = 12;
    u32x4 af[DH];
    const bool s13 = w < 2;
    u32 tbo[4];
#pragma unroll
    for (int tbi = 0; tbi < 4; ++tbi) tbo[tbi] = hfo + ((4 * m64 + ((w + 1 + tbi) & 3)) * 54 + ds0) * 1024;
#define HLOAD(q) ald_a_sc1<((q) % 14 % 4) * 1024>(af[(q) % DH], rs, l16, tbo[(q) / 14] + ((q) % 14 / 4) * 4096)
    sfor<DH>([&](auto I) { constexpr int q = decltype(I)::value; HLOAD(q); });
    const rsrc_t rsq = it == 0 ? rs : k.rsnull;
    f32x4 acc[2][5];
    f32x4 part[3][5];
    sfor<56>([&](auto QC) {
      constexpr int q = decltype(QC)::value, tbi = q / 14, s = q % 14;
      constexpr int yf = (q + DH - 1 < 55 ? q + DH - 1 : 55) - q;
      constexpr int nd = q - 44;
      constexpr int yd = (nd > 0) ? (nd + (nd < 8 ? nd : 8)) : 0;
      waitpin_a<yf + yd>(af[q % DH]);
      if constexpr (s == 0) {
#pragma unroll
        for (int nb = 0; nb < 5; ++nb) acc[tbi & 1][nb] = mfma16(k.wd_r[s][nb], af[q % DH], f32x4{0.f, 0.f, 0.f, 0.f});
      } else if (s < 13 || s13) {
#pragma unroll
        for (int nb = 0; nb < 5; ++nb) acc[tbi & 1][nb] = mfma16(k.wd_r[s][nb], af[q % DH], acc[tbi & 1][nb]);
      }
      if constexpr (tbi > 0 && s >= 1 && s < 6) {
        const u32 tbp = (w + tbi) & 3;
        *(LAS f32x4*)(lds + L_RED + ((tbp * 3 + (3 - tbi)) * 5 + (s - 1)) * 1024 + l16) = acc[(tbi - 1) & 1][s - 1];
      }
      if constexpr (q == 50) {
        WAITL0(); BAR();
#pragma unroll
        for (int sl = 0; sl < 3; ++sl)
#pragma unroll
          for (int nb = 0; nb < 5; ++nb) part[sl][nb] = *(const LAS f32x4*)(lds + L_RED + ((w * 3 + sl) * 5 + nb) * 1024 + l16);
      }
      SB();
      if constexpr (q + DH < 56) HLOAD(q + DH);
      if constexpr (q >= 44) {
        constexpr int j0 = nd + (nd < 8 ? nd : 8);
        if constexpr (j0 < 10) dma_q<0, 0, j0>(rsq, l16, sQ, w, lds0); else dma_q<1, 1, j0 - 10>(rsq, l16, sQ, w, lds0);
        if constexpr (nd < 8) {
          constexpr int j1 = j0 + 1;
          if constexpr (j1 < 10) dma_q<0, 0, j1>(rsq, l16, sQ, w, lds0); else dma_q<1, 1, j1 - 10>(rsq, l16, sQ, w, lds0);
        }
      }
      SB();
    });
    STAMP(18);
#pragma unroll
    for (int nb = 0; nb < 5; ++nb) {
      const f32x4 v = (acc[1][nb] + part[0][nb]) + (part[1][nb] + part[2][nb]);
      const u32 a = cvtpk(bflo(k.xr[nb].x) + v[0], bfhi(k.xr[nb].x) + v[1]), b = cvtpk(bflo(k.xr[nb].y) + v[2], bfhi(k.xr[nb].y) + v[3]);
      k.xr[nb] = u32x2{a, b};
    }
    const u32 token = 64 * m64 + 16 * w + t16;
    STAMP(19);
    if (it == 0) {
      float s1 = 0.f, s2 = 0.f;
      const u32 xo = ((token >> 5) * 40 + 5 * c8) * 1024 + ((token & 31) + 32 * (g >> 1)) * 16;
#pragma unroll
      for (int pr = 0; pr < 3; ++pr) {
        const int A = 2 * pr, Bn = pr < 2 ? 2 * pr + 1 : 4;
        auto r0 = __builtin_amdgcn_permlane16_swap(k.xr[A].x, k.xr[Bn].x, false, false);
        auto r1 = __builtin_amdgcn_permlane16_swap(k.xr[A].y, k.xr[Bn].y, false, false);
        const u32x4 v = {r0[0], r1[0], r0[1], r1[1]};
        if (pr < 2) stg_sc1(v, rs, xo + (A + (g & 1)) * 1024, OFF_XF + 2 * SZ_XF);
        else if ((g & 1) == 0) stg_sc1(v, rs, xo + 4 * 1024, OFF_XF + 2 * SZ_XF);
      }
#pragma unroll
      for (int nb = 0; nb < 5; ++nb) {
        const float a = bflo(k.xr[nb].x), b = bfhi(k.xr[nb].x), c = bflo(k.xr[nb].y), d = bfhi(k.xr[nb].y);
        s1 += (a + b) + (c + d); s2 += (a * a + b * b) + (c * c + d * d);
      }
      s1 = red16_32_add(s1); s2 = red16_32_add(s2);
      if (g == 0) stg2_sc1(u32x2{f2u(s1), f2u(s2)}, rs, (c8 * 2048 + token) * 8, OFF_ST + 2 * SZ_ST);
      SB();
      WAITV(0);
      WAITL0();
      BAR();
      if (tid == 0) __hip_atomic_store(flags + ((FG_X2 + m64) * 16 + c8) * 32, k.tag, RLX_AGENT);
      sfor<10>([&](auto I) { dma_q<2, 2, decltype(I)::value>(rs, l16, sQ, w, lds0); });
    } else {
      const u32 oo = token * 1280 + (80 * c8 + 8 * (g >> 1)) * 2;
#pragma unroll
      for (int pr = 0; pr < 3; ++pr) {
        const int A = 2 * pr, Bn = pr < 2 ? 2 * pr + 1 : 4;
        auto r0 = __builtin_amdgcn_permlane16_swap(k.xr[A].x, k.xr[Bn].x, false, false);
        auto r1 = __builtin_amdgcn_permlane16_swap(k.xr[A].y, k.xr[Bn].y, false, false);
        const u32x4 v = {r0[0], r1[0], r0[1], r1[1]};
        if (pr < 2) __builtin_amdgcn_raw_buffer_store_b128(v, k.rso, oo + (A + (g & 1)) * 32, 0, 0);
        else if ((g & 1) == 0) __builtin_amdgcn_raw_buffer_store_b128(v, k.rso, oo + 4 * 32, 0, 0);
      }
    }
    STAMP(20);
  }
}

DI void stw_sc1(u32x4 v, rsrc_t rs, u32 off) { __builtin_amdgcn_raw_buffer_store_b128(v, rs, off, 0, AUX_SC1); }
DI float dot2ones(u32 v, float c) { return __builtin_amdgcn_fdot2_f32_bf16(__builtin_bit_cast(bf16x2, v), __builtin_bit_cast(bf16x2, 0x3f803f80u), c, false); }
DI void setup(K& k, int u, const u16* x0, const u16* wqkv, const u16* wo, const u16* wg, const u16* wu, const u16* wd) {
  const int l = k.lane, w = k.w;
  const rsrc_t rs = k.rs;
  LAS char* const lds = k.lds;
  const u32 lds0 = k.lds0;
  const int type = u < 60 ? 0 : u < 124 ? 3 : u < 178 ? 1 : u < 232 ? 2 : u < 252 ? 4 : 5;
  const int idx = u < 60 ? u : u < 124 ? u - 60 : u < 178 ? u - 124 : u < 232 ? u - 178 : u - 232;
  const u16* src = type == 0 ? wqkv : type == 1 ? wg : type == 2 ? wu : type == 3 ? x0 : wo;
  const u32 pitch = type == 0 ? 3840 : type <= 2 ? 3456 : 1280;
  const u32 nrec = type == 0 ? 640 * 3840 : type <= 2 ? 640 * 3456 : type == 3 ? 2048 * 1280 : type == 4 ? 640 * 1280 : 0;
  const rsrc_t rsrcA = __builtin_amdgcn_make_buffer_rsrc((void*)src, 0, nrec, 0x00020000);
  const rsrc_t rsrcD = __builtin_amdgcn_make_buffer_rsrc((void*)wd, 0, 1728 * 1280, 0x00020000);
  {
    const u32 voW = (16 * w + (l >> 2)) * pitch + idx * 64 + (l & 3) * 16;
    const u32 voO = ((idx & 1) * 320 + 8 * w + (l >> 3)) * 1280 + (idx >> 1) * 128 + (l & 7) * 16;
    const u32 vo = type == 4 ? voO : voW, step = type == 4 ? 32 * 1280 : 64 * pitch;
#pragma unroll
    for (int j = 0; j < 10; ++j) {
      const u32 cidx = 64 * (4 * j + w) + l, row = (cidx * 52429u) >> 22, cc = cidx - row * 80;
      const u32 vx = (32 * idx + row) * 1280 + ((cc ^ (row & 7)) * 16);
      const u32 v = type == 3 ? vx : vo + j * step;
      DMA_F("SA", 0, "", rsrcA, v, 0u, lds0 + (4 * j + w) * 1024);
    }
#pragma unroll
    for (int t = 0; t < 3; ++t) {
      const u32 ds_ = u / 10 + 25 * t;
      const u32 v = (u < 250 && ds_ < 54) ? (ds_ * 32 + 8 * w + (l >> 3)) * 1280 + (u % 10) * 128 + (l & 7) * 16 : 0x7fffffffu;
      DMA_F("SB", 0, "", rsrcD, v, 0u, lds0 + 40960 + t * 4096 + w * 1024);
    }
  }
  u32 ep;
  asm volatile("global_load_dword %0, %1, off sc1" : "=v"(ep) : "v"(k.flags + FG_EPOCH * 512) : "memory");
  SSTAMP(21);
  WAITV(0);
  asm volatile("" : "+v"(ep));
  BAR();
  SSTAMP(22);
  k.tag = __builtin_amdgcn_readfirstlane(ep * 2654435761u + 0x9E3779B9u);
  const int gam = l >> 4, q = (l & 15) >> 2, pp = l & 3;
  if (type <= 2) {
    const u32 ra = ((gam >> 1) * 8 + q) * 64 + (gam & 1) * 32 + pp * 8;
    u32x4 sl[10];
#pragma unroll
    for (int j = 0; j < 10; ++j) {
      const u32 o = ra + (4 * j + w) * 1024;
      const u32x2 a = __builtin_bit_cast(u32x2, __builtin_amdgcn_ds_read_tr16_b64_v4i16((LAS s16x4*)(lds + o)));
      const u32x2 b2 = __builtin_bit_cast(u32x2, __builtin_amdgcn_ds_read_tr16_b64_v4i16((LAS s16x4*)(lds + o + 256)));
      sl[j] = u32x4{a.x, a.y, b2.x, b2.y};
    }
    const int c = 32 * idx + (l & 31), hh = l >> 5;
    u32 dbase; int dstep;
    if (type == 0) {
      const int sec = c / 640, cc = c % 640, p = cc / 40, r40 = cc % 40;
      const int n = (sec == 1 ? 0 : sec == 2 ? 40 : 80) + r40;
      const int b = n >> 5, nl = n & 31;
      dbase = OFF_WQ + p * 163840 + (b < 3 ? b * 1024 : 122880) + nl * 16;
      dstep = b < 3 ? 3072 : 1024;
    } else {
      const int fb = c >> 4, nl = (c & 15) + 16 * (type - 1);
      const int half = fb / 54, rem = fb % 54;
      const int sl_ = 8 * half + (rem < 42 ? rem / 7 : 6 + (rem - 42) / 6), ix = rem < 42 ? rem % 7 : (rem - 42) % 6;
      dbase = OFF_WG + sl_ * 286720 + (ix < 3 ? ix * 1024 : ix < 5 ? 122880 + (ix - 3) * 1024 : 204800 + (ix - 5) * 1024) + nl * 16;
      dstep = ix < 3 ? 3072 : 2048;
    }
    float c0 = 0.f, c1 = 0.f;
#pragma unroll
    for (int j = 0; j < 10; ++j) { c0 = dot2ones(sl[j][0], c0); c1 = dot2ones(sl[j][1], c1); c0 = dot2ones(sl[j][2], c0); c1 = dot2ones(sl[j][3], c1); }
    const float cs = red32_add(c0 + c1);
    LAS float* red = (LAS float*)(lds + 81920);
    if (l < 32) red[w * 32 + l] = cs;
    WAITL0(); BAR();
    const float mean = ((red[l & 31] + red[32 + (l & 31)]) + (red[64 + (l & 31)] + red[96 + (l & 31)])) * (1.0f / 640.0f);
#pragma unroll
    for (int j = 0; j < 10; ++j) {
      u32x4 v;
#pragma unroll
      for (int e = 0; e < 4; ++e) v[e] = cvtpk(bflo(sl[j][e]) - mean, bfhi(sl[j][e]) - mean);
      stw_sc1(v, rs, dbase + (4 * j + w) * dstep + hh * 512);
    }
  } else if (type == 3) {
    const int tok = l & 31, h = l >> 5;
    u32x4 sl[10];
#pragma unroll
    for (int i = 0; i < 10; ++i) sl[i] = *(const LAS u32x4*)(lds + tok * 1280 + (((2 * (10 * w + i) + h) ^ (tok & 7)) * 16));
    float s1 = 0.f, s2 = 0.f;
#pragma unroll
    for (int i = 0; i < 10; ++i) {
      stw_sc1(sl[i], rs, OFF_XF + (idx * 40 + 10 * w + i) * 1024 + l * 16);
#pragma unroll
      for (int e = 0; e < 4; ++e) { s1 = dot2ones(sl[i][e], s1); const float a_ = bflo(sl[i][e]), b_ = bfhi(sl[i][e]); s2 += a_ * a_ + b_ * b_; }
    }
    s1 = red32_add(s1); s2 = red32_add(s2);
    if (l < 32) {
      stg2_sc1(u32x2{f2u(s1), f2u(s2)}, rs, (w * 2048 + 32 * idx + tok) * 8, OFF_ST);
      stg2_sc1(u32x2{0u, 0u}, rs, ((w + 4) * 2048 + 32 * idx + tok) * 8, OFF_ST);
    }
  } else if (type == 4) {
    const int cg = idx >> 1, kk = idx & 1, c = 64 * cg + l, c8 = c / 80, nb = (c % 80) >> 4, n = c & 15;
    const u32 dbase = OFF_WO + c8 * 20 * 5120 + nb * 1024 + n * 16;
#pragma unroll
    for (int j = 0; j < 10; ++j) {
      const int o = 4 * j + w, pl = o / 5, ii = o % 5, sg = 40 * kk + o, r0 = 40 * pl + 4 * ii + q;
      const u32x2 a = __builtin_bit_cast(u32x2, __builtin_amdgcn_ds_read_tr16_b64_v4i16((LAS s16x4*)(lds + r0 * 128 + gam * 32 + pp * 8)));
      const u32x2 b2 = __builtin_bit_cast(u32x2, __builtin_amdgcn_ds_read_tr16_b64_v4i16((LAS s16x4*)(lds + (r0 + 20) * 128 + gam * 32 + pp * 8)));
      stw_sc1(u32x4{a.x, a.y, b2.x, b2.y}, rs, dbase + (sg >> 2) * 5120 + (sg & 3) * 256);
    }
  }
  const bool crit = type == 0 || type == 3 || type == 4;
  SSTAMP(24);
  if (crit) { WAITV(0); }
  WAITL0();
  BAR();
  if (crit && k.tid == 0) __hip_atomic_store(k.flags + (FG_PF1 * 16 + u) * 32, k.tag, RLX_AGENT);
  SSTAMP(25);
#pragma unroll
  for (int t = 0; t < 3; ++t) {
    const u32 s_ = u / 10 + 25 * t;
    if (u < 250 && s_ < 54) {
      const u32 cg = u % 10, c = 64 * cg + l, c8 = c / 80, nb = (c % 80) >> 4, n = c & 15;
      const u32 r0 = 16 * (w >> 1) + 4 * (w & 1) + q;
      const u32x2 a = __builtin_bit_cast(u32x2, __builtin_amdgcn_ds_read_tr16_b64_v4i16((LAS s16x4*)(lds + 40960 + t * 4096 + r0 * 128 + gam * 32 + pp * 8)));
      const u32x2 b2 = __builtin_bit_cast(u32x2, __builtin_amdgcn_ds_read_tr16_b64_v4i16((LAS s16x4*)(lds + 40960 + t * 4096 + (r0 + 8) * 128 + gam * 32 + pp * 8)));
      stw_sc1(u32x4{a.x, a.y, b2.x, b2.y}, rs, OFF_WD + (c8 * 54 + s_) * 5120 + nb * 1024 + n * 16 + w * 256);
    }
  }
  WAITL0();
  if (w == 0) {
    u32 fo; bool act = true;
    if (l < 6) fo = 20 * (l >> 1) + (40 * k.p) / 32 + (l & 1);
    else if (l < 11) { const int rb = 4 * k.m128 - 1 + (l - 6); act = rb >= 0; fo = 60 + rb; }
    else if (l < 15) fo = 232 + 2 * ((80 * k.c8) / 64 + ((l - 11) >> 1)) + ((l - 11) & 1);
    else { act = false; fo = 0; }
    poll_flags(k.flags, (FG_PF1 * 16 + (act ? fo : 0)) * 32, act, l, k.tag);
  }
  BAR();
  SSTAMP(26);
}

__global__ __launch_bounds__(256, 1) void main_kernel(const u16* __restrict__ x0, const u16* __restrict__ wqkv, const u16* __restrict__ wo, const u16* __restrict__ wg,
                                                      const u16* __restrict__ wu, const u16* __restrict__ wd, char* __restrict__ ws, u16* __restrict__ out) {
  extern __shared__ __attribute__((aligned(16))) char smem[];
  K k;
  k.lds = (LAS char*)smem;
  k.lds0 = (u32)(size_t)k.lds;
  k.tid = threadIdx.x; k.lane = k.tid & 63; k.w = __builtin_amdgcn_readfirstlane(k.tid >> 6);
  k.rs = __builtin_amdgcn_make_buffer_rsrc(ws, 0, WS_USED, 0x00020000);
  k.rso = __builtin_amdgcn_make_buffer_rsrc(out, 0, 2048 * 640 * 2, 0x00020000);
  k.rsnull = __builtin_amdgcn_make_buffer_rsrc(ws, 0, 0, 0x00020000);
  k.flags = (GAS u32*)ws;
  const int bid = blockIdx.x, xc = bid & 7, j = bid >> 3, mg = xc >> 1, half = xc & 1;
  k.m128 = 4 * mg + (j >> 3); k.p = 8 * half + (j & 7);
  k.m64 = 8 * mg + (j >> 2); k.c8 = 4 * half + (j & 3);
  const int p7 = j & 7;
  k.B0 = 54 * half + (p7 < 6 ? 7 * p7 : 42 + 6 * (p7 - 6));
  k.nblk = p7 < 6 ? 7 : 6;
  k.bid = bid;
  setup(k, bid, x0, wqkv, wo, wg, wu, wd);
  {
    PHASE_IDS();
    const u32 sQ = OFF_WQ + p * 163840;
    sfor<10>([&](auto I) { dma_q<0, 0, decltype(I)::value>(rs, l16, sQ, w, lds0); });
    sfor<10>([&](auto I) { dma_q<1, 1, decltype(I)::value>(rs, l16, sQ, w, lds0); });
    sfor<10>([&](auto I) { dma_q<2, 2, decltype(I)::value>(rs, l16, sQ, w, lds0); });
    const u32 token = 64 * m64 + 16 * w + t16;
    const u32 o = ((token >> 5) * 40 + 5 * c8) * 1024 + ((token & 31) + 32 * (g >> 1)) * 16 + (g & 1) * 8;
#pragma unroll
    for (int nb = 0; nb < 5; ++nb) ald2(k.xr[nb], rs, o, OFF_XF + nb * 1024);
  }
#pragma nounroll
  for (int it = 0; it < 2; ++it) { k.it = it; iteration(k); }
  if (bid == 0 && k.w == 0) {
#pragma unroll
    for (int r = 0; r < 4; ++r) poll_flags(k.flags, (FG_PF2 * 16 + 64 * r + k.lane) * 32, true, k.lane, k.tag);
    if (k.lane == 0) __hip_atomic_fetch_add(k.flags + FG_EPOCH * 512, 1u, RLX_AGENT);
  }
}

extern "C" void kernel_launch(void* const* d_in, const int* in_sizes, int n_in, void* d_out, int out_size, void* d_ws, size_t ws_size,
                              hipStream_t stream) {
  const u16* x0 = (const u16*)d_in[0];
  const u16* wqkv = (const u16*)d_in[1];
  const u16* wo = (const u16*)d_in[2];
  const u16* wg = (const u16*)d_in[4];
  const u16* wu = (const u16*)d_in[5];
  const u16* wd = (const u16*)d_in[6];
  (void)hipFuncSetAttribute((const void*)main_kernel, hipFuncAttributeMaxDynamicSharedMemorySize, LDS_BYTES);
  main_kernel<<<256, 256, LDS_BYTES, stream>>>(x0, wqkv, wo, wg, wu, wd, (char*)d_ws, (u16*)d_out);
}
```

```cpp
#include <hip/hip_runtime.h>
#include <stdint.h>
#include <utility>

#define LAS __attribute__((address_space(3)))
#define GAS __attribute__((address_space(1)))
#define DI __device__ __forceinline__
typedef unsigned u32;
typedef unsigned short u16;
typedef unsigned long long u64;
typedef u32 u32x4 __attribute__((ext_vector_type(4)));
typedef u32 u32x2 __attribute__((ext_vector_type(2)));
typedef float f32x2 __attribute__((ext_vector_type(2)));
typedef float f32x4 __attribute__((ext_vector_type(4)));
typedef float f32x16 __attribute__((ext_vector_type(16)));
typedef short s16x8 __attribute__((ext_vector_type(8)));
typedef short s16x4 __attribute__((ext_vector_type(4)));
typedef __bf16 bf16x2 __attribute__((ext_vector_type(2)));
typedef __amdgpu_buffer_rsrc_t rsrc_t;

constexpr u32 FLAG_BYTES = 200 * 2048 + 34 * 2048;
constexpr u32 OFF_WQ = 483328;
constexpr u32 OFF_WO = OFF_WQ + 16 * 163840;
constexpr u32 OFF_WG = OFF_WO + 8 * 20 * 5 * 1024;
constexpr u32 OFF_WD = OFF_WG + 16 * 286720;
constexpr u32 OFF_CSQ = OFF_WD + 8 * 54 * 5 * 1024;
constexpr u32 OFF_CSG = OFF_CSQ + 16384;
constexpr u32 SZ_XF = 64 * 40 * 1024;
constexpr u32 OFF_XF = OFF_CSG + 28672;
constexpr u32 SZ_ST = 8 * 2048 * 8;
constexpr u32 OFF_ST = OFF_XF + 4 * SZ_XF;
constexpr u32 SZ_OF = 128 * 20 * 1024;
constexpr u32 OFF_OF = OFF_ST + 4 * SZ_ST;
constexpr u32 SZ_HF = 128 * 54 * 1024;
constexpr u32 OFF_HF = OFF_OF + 2 * SZ_OF;
constexpr u32 WS_USED = OFF_HF + 2 * SZ_HF;
constexpr u32 FG_O = 0, FG_X1 = 16, FG_H = 48, FG_X2 = 64, FG_PER_IT = 96;
constexpr u32 FG_TMO = 2 * FG_PER_IT;
constexpr u32 FG_EPOCH = FG_TMO + 1;
constexpr u32 FG_PF1 = FG_EPOCH + 1;
constexpr u32 FG_PF2 = FG_PF1 + 16;

constexpr u32 L_HALO = 122880;
constexpr u32 L_KST = 40960;
constexpr u32 L_VST = L_KST + 144 * 80;
constexpr u32 L_QST = L_VST + 144 * 80;
constexpr u32 L_RED = 100352;
constexpr u32 L_H4 = 143360;
constexpr u32 LDS_BYTES = 163840;

constexpr u32 AUX_SC1 = 16;
#define RLX_AGENT __ATOMIC_RELAXED, __HIP_MEMORY_SCOPE_AGENT
#define SB() __builtin_amdgcn_sched_barrier(0)
#define MEMFENCE() asm volatile("" ::: "memory")
#define WAITV(n) asm volatile("s_waitcnt vmcnt(%0)" ::"i"(n) : "memory")
#define WAITL0() asm volatile("s_waitcnt lgkmcnt(0)" ::: "memory")
#define BAR() do { MEMFENCE(); __builtin_amdgcn_s_barrier(); MEMFENCE(); } while (0)
#define CERT(tag) asm volatile("; CERT " tag ::: "memory")

template <int... I, class Fn> DI void sfor_impl(std::integer_sequence<int, I...>, Fn&& f) { (f(std::integral_constant<int, I>{}), ...); }
template <int N, class Fn> DI void sfor(Fn&& f) { sfor_impl(std::make_integer_sequence<int, N>{}, f); }
DI f32x16 mfma32(u32x4 a, u32x4 b, f32x16 c) { return __builtin_amdgcn_mfma_f32_32x32x16_bf16(__builtin_bit_cast(s16x8, a), __builtin_bit_cast(s16x8, b), c, 0, 0, 0); }
DI f32x4 mfma16(u32x4 a, u32x4 b, f32x4 c) { return __builtin_amdgcn_mfma_f32_16x16x32_bf16(__builtin_bit_cast(s16x8, a), __builtin_bit_cast(s16x8, b), c, 0, 0, 0); }
DI u32 cvtpk(float lo, float hi) { f32x2 v = {lo, hi}; bf16x2 b = __builtin_convertvector(v, bf16x2); return __builtin_bit_cast(u32, b); }
DI float u2f(u32 v) { return __builtin_bit_cast(float, v); }
DI u32 f2u(float v) { return __builtin_bit_cast(u32, v); }
DI float bflo(u32 v) { return __builtin_bit_cast(float, v << 16); }
DI float bfhi(u32 v) { return __builtin_bit_cast(float, v & 0xffff0000u); }
DI float fma_s(float a, float b, float c) { float r; asm("v_fma_f32 %0, %1, %2, %3" : "=v"(r) : "v"(a), "v"(b), "v"(c)); return r; }
DI float mul_s(float a, float b) { float r; asm("v_mul_f32_e32 %0, %1, %2" : "=v"(r) : "v"(a), "v"(b)); return r; }
DI float add_s(float a, float b) { float r; asm("v_add_f32_e32 %0, %1, %2" : "=v"(r) : "v"(a), "v"(b)); return r; }
DI float max_s(float a, float b) { float r; asm("v_max_f32_e32 %0, %1, %2" : "=v"(r) : "v"(a), "v"(b)); return r; }
DI float sel_s(float f, float t, unsigned long long m) { float r; asm("v_cndmask_b32_e64 %0, %1, %2, %3" : "=v"(r) : "v"(f), "v"(t), "s"(m)); return r; }
DI float red16_32_add(float v) {
  auto a = __builtin_amdgcn_permlane16_swap(f2u(v), f2u(v), false, false);
  v = u2f(a[0]) + u2f(a[1]);
  auto b = __builtin_amdgcn_permlane32_swap(f2u(v), f2u(v), false, false);
  return u2f(b[0]) + u2f(b[1]);
}
DI float red16_32_max(float v) {
  auto a = __builtin_amdgcn_permlane16_swap(f2u(v), f2u(v), false, false);
  v = __builtin_fmaxf(u2f(a[0]), u2f(a[1]));
  auto b = __builtin_amdgcn_permlane32_swap(f2u(v), f2u(v), false, false);
  return __builtin_fmaxf(u2f(b[0]), u2f(b[1]));
}
DI float red32_add(float v) {
  auto b = __builtin_amdgcn_permlane32_swap(f2u(v), f2u(v), false, false);
  return u2f(b[0]) + u2f(b[1]);
}
DI void stg_sc1(u32x4 v, rsrc_t rs, u32 voff, u32 soff) { __builtin_amdgcn_raw_buffer_store_b128(v, rs, voff, soff, AUX_SC1); }
DI void stg2_sc1(u32x2 v, rsrc_t rs, u32 voff, u32 soff) { __builtin_amdgcn_raw_buffer_store_b64(v, rs, voff, soff, AUX_SC1); }

template <int IMM> DI void ald_v_sc1(u32x4& d, rsrc_t rs, u32 voff, u32 soff) {
  asm volatile("buffer_load_dwordx4 %0, %1, %2, %3 offen offset:%4 sc1" : "=v"(d) : "v"(voff), "s"(rs), "s"(soff), "i"(IMM) : "memory");
}
template <int IMM> DI void ald_a(u32x4& d, rsrc_t rs, u32 voff, u32 soff) {
  asm volatile("buffer_load_dwordx4 %0, %1, %2, %3 offen offset:%4 sc1" : "=a"(d) : "v"(voff), "s"(rs), "s"(soff), "i"(IMM) : "memory");
}
template <int IMM> DI void ald_v(u32x4& d, rsrc_t rs, u32 voff, u32 soff) {
  asm volatile("buffer_load_dwordx4 %0, %1, %2, %3 offen offset:%4 sc1" : "=v"(d) : "v"(voff), "s"(rs), "s"(soff), "i"(IMM) : "memory");
}
DI void ald2_sc1(u32x2& d, rsrc_t rs, u32 voff, u32 soff) {
  asm volatile("buffer_load_dwordx2 %0, %1, %2, %3 offen sc1" : "=v"(d) : "v"(voff), "s"(rs), "s"(soff) : "memory");
}
DI void ald2(u32x2& d, rsrc_t rs, u32 voff, u32 soff) {
  asm volatile("buffer_load_dwordx2 %0, %1, %2, %3 offen sc1" : "=v"(d) : "v"(voff), "s"(rs), "s"(soff) : "memory");
}
DI void ald1(u32& d, rsrc_t rs, u32 voff, u32 soff) {
  asm volatile("buffer_load_dword %0, %1, %2, %3 offen" : "=v"(d) : "v"(voff), "s"(rs), "s"(soff) : "memory");
}
template <int IMM> DI void ald_a_sc1(u32x4& d, rsrc_t rs, u32 voff, u32 soff) {
  asm volatile("buffer_load_dwordx4 %0, %1, %2, %3 offen offset:%4 sc1" : "=a"(d) : "v"(voff), "s"(rs), "s"(soff), "i"(IMM) : "memory");
}
template <int N> DI void waitpin_a(u32x4& r) { asm volatile("s_waitcnt vmcnt(%1)" : "+a"(r) : "i"(N) : "memory"); }
template <int N> DI void waitpin_a2(u32x4& r0, u32x4& r1) { asm volatile("s_waitcnt vmcnt(%2)" : "+a"(r0), "+a"(r1) : "i"(N) : "memory"); }
DI void mma32(f32x16& acc, const u32x4& wv, const u32x4& xa) { asm volatile("v_mfma_f32_32x32x16_bf16 %0, %1, %2, %0" : "+v"(acc) : "v"(wv), "a"(xa)); }
DI void mma32_0(f32x16& acc, const u32x4& wv, const u32x4& xa) { asm volatile("v_mfma_f32_32x32x16_bf16 %0, %1, %2, 0" : "=&v"(acc) : "v"(wv), "a"(xa)); }
DI void mma32w(f32x16& acc, const u32x4& wv, const u32x4& xa, const u32x4& wn) { asm volatile("v_mfma_f32_32x32x16_bf16 %0, %1, %2, %0" : "+v"(acc) : "v"(wv), "a"(xa), "v"(wn)); }
DI void mma32w_0(f32x16& acc, const u32x4& wv, const u32x4& xa, const u32x4& wn) { asm volatile("v_mfma_f32_32x32x16_bf16 %0, %1, %2, 0" : "=&v"(acc) : "v"(wv), "a"(xa), "v"(wn)); }
DI void mma16(f32x4& acc, const u32x4& av, const u32x4& bv) { asm volatile("v_mfma_f32_16x16x32_bf16 %0, %1, %2, %0" : "+v"(acc) : "v"(av), "v"(bv)); }
DI void mma16_0(f32x4& acc, const u32x4& av, const u32x4& bv) { asm volatile("v_mfma_f32_16x16x32_bf16 %0, %1, %2, 0" : "=&v"(acc) : "v"(av), "v"(bv)); }
DI void pin_vv(u32x4& a, u32x4& b) { asm volatile("" : "+v"(a), "+v"(b)); }
template <int N> DI void waitpin_v(u32x4& r) { asm volatile("s_waitcnt vmcnt(%1)" : "+v"(r) : "i"(N) : "memory"); }
DI void pin_v(u32x4& r) { asm volatile("" : "+v"(r)::"memory"); }
DI void pin_a(u32x4& r) { asm volatile("" : "+a"(r)::"memory"); }
DI void pin2(u32x2& r) { asm volatile("" : "+v"(r)::"memory"); }
DI void pin1(u32& r) { asm volatile("" : "+v"(r)::"memory"); }
#define DMA_F(TAG, IMM, SC, rs, voff, soff, m0v) \
  asm volatile("s_mov_b32 m0, %2\n\ts_nop 0\n\tbuffer_load_dwordx4 %0, %1, %3 offen offset:%4 " SC "lds ; DMA " TAG ::"v"(voff), "s"(rs), "s"(m0v), "s"(soff), "i"(IMM) : "memory")
#define DMA_N(TAG, IMM, SC, rs, voff, soff) \
  asm volatile("buffer_load_dwordx4 %0, %1, %2 offen offset:%3 " SC "lds ; DMA " TAG ::"v"(voff), "s"(rs), "s"(soff), "i"(IMM) : "memory")

constexpr u32 kSpinCap = 1u << 16;

struct K {
  rsrc_t rs, rso, rsnull;
  LAS char* lds;
  u32 lds0;
  GAS u32* flags;
  int bid, lane, w, m128, p, m64, c8, B0, nblk, tid, it;
  u64 tag;
  u32x2 xr[5];
  u32x4 wo_r[5][5];
  u32x4 wd_r[14][5];
};

#define PHASE_IDS() int lane = k.lane; asm volatile("" : "+v"(lane)); \
  int it = k.it; asm volatile("" : "+s"(it)); \
  const u32 xin = OFF_XF + it * 2 * SZ_XF, stin = OFF_ST + it * 2 * SZ_ST, x1o = xin + SZ_XF, st1o = stin + SZ_ST; \
  const u32 ofo = OFF_OF + it * SZ_OF, hfo = OFF_HF + it * SZ_HF, fgb = it * FG_PER_IT; \
  (void)xin; (void)stin; (void)x1o; (void)st1o; (void)ofo; (void)hfo; (void)fgb; \
  int w = k.w, m128 = k.m128, p = k.p, m64 = k.m64, c8 = k.c8, B0 = k.B0; \
  asm volatile("" : "+s"(w), "+s"(m128), "+s"(p), "+s"(m64), "+s"(c8), "+s"(B0)); \
  const u32 l16 = lane * 16; const int tok = lane & 31, h = lane >> 5, t16 = lane & 15, g = lane >> 4; \
  const rsrc_t rs = k.rs; LAS char* const lds = k.lds; const u32 lds0 = k.lds0; \
  (void)tok; (void)h; (void)t16; (void)g; (void)l16; (void)m128; (void)p; (void)m64; (void)c8; (void)B0; (void)lds; (void)lds0; (void)rs

DI void flag_set(GAS u32* flags, u32 idx, u64 tag) { __hip_atomic_store((GAS u64*)(flags + idx), tag, RLX_AGENT); }
DI u64 flag_get(GAS u32* flags, u32 idx) { return __hip_atomic_load((GAS u64*)(flags + idx), RLX_AGENT); }
DI bool timed_out(GAS u32* flags, u64 tag) {
  const u64 v = flag_get(flags, FG_TMO * 512);
  return ((u64)(u32)__builtin_amdgcn_readfirstlane((u32)v) | ((u64)(u32)__builtin_amdgcn_readfirstlane((u32)(v >> 32)) << 32)) == tag;
}
DI void poll_flags(GAS u32* flags, u32 fo, bool act, int lane, u64 tag) {
  for (u32 spins = 0;;) {
    const u64 v = act ? flag_get(flags, fo) : tag;
    if (__all(v == tag)) break;
    __builtin_amdgcn_s_sleep(1);
    if (++spins > kSpinCap) {
      if (lane == 0) flag_set(flags, FG_TMO * 512, tag);
      break;
    }
    if ((spins & 255u) == 0u && timed_out(flags, tag)) break;
  }
}
constexpr int POLL_GAP = 2;
struct Polls { u64 a, b, c; };
DI void poll_issue(u64& d, const GAS u32* p) { asm volatile("global_load_dwordx2 %0, %1, off sc1 ; POLL" : "=v"(d) : "v"(p) : "memory"); }
DI bool poll_check(u64& d, bool act, u64 want) { asm volatile("s_waitcnt vmcnt(2) ; POLLWAIT" : "+v"(d)::"memory"); return __all(!act || d == want); }
DI void poll_flags_p(GAS u32* flags, u32 fo, bool act, int lane, u64 want, Polls& pl) {
  const GAS u32* p = flags + (act ? fo : FG_TMO * 512);
  asm volatile("; POLL_BEGIN" ::: "memory");
  poll_issue(pl.a, p); __builtin_amdgcn_s_sleep(POLL_GAP); poll_issue(pl.b, p); __builtin_amdgcn_s_sleep(POLL_GAP); poll_issue(pl.c, p);
  for (u32 spins = 0;;) {
    if (poll_check(pl.a, act, want)) break;
    poll_issue(pl.a, p);
    if (poll_check(pl.b, act, want)) break;
    poll_issue(pl.b, p);
    if (poll_check(pl.c, act, want)) break;
    poll_issue(pl.c, p);
    if (++spins > kSpinCap) {
      if (lane == 0) flag_set(flags, FG_TMO * 512, want);
      break;
    }
    if ((spins & 63u) == 0u && timed_out(flags, want)) break;
  }
  asm volatile("; POLL_END" ::: "memory");
}
DI void poll_pin(Polls& pl) { asm volatile("; POLL_PIN" : "+v"(pl.a), "+v"(pl.b), "+v"(pl.c)::"memory"); }
DI void stats_finish(float s1, float s2, float& nmean, float& rstd) {
  const float mean = s1 * (1.0f / 640.0f);
  const float var = __builtin_fmaxf(s2 * (1.0f / 640.0f) - mean * mean, 0.f);
  nmean = -mean; rstd = __builtin_amdgcn_rsqf(var + 1e-5f);
}

template <int C, int SLOT, int I> DI void dma_q(rsrc_t rs, u32 l16, u32 sQ, int w, u32 lds0) {
#define Q_EMIT(TAG, FIRST, IMM, SO, MO) do { if constexpr (FIRST) DMA_F(TAG, IMM, "sc1 ", rs, l16, SO, MO); else DMA_N(TAG, IMM, "sc1 ", rs, l16, SO); } while (0)
  if constexpr (C < 3) {
    constexpr int grp = I / 4, imm = (I % 4) * 1024;
    const u32 so = sQ + w * 10240 + C * 40960 + grp * 4096, mo = lds0 + SLOT * 40960 + w * 10240 + grp * 4096;
    if constexpr (C == 0) Q_EMIT("Q0", I % 4 == 0, imm, so, mo); else if constexpr (C == 1) Q_EMIT("Q1", I % 4 == 0, imm, so, mo); else Q_EMIT("Q2", I % 4 == 0, imm, so, mo);
  } else {
    constexpr int hf = I / 5, ii = I % 5, grp = ii / 4, imm = (ii % 4) * 1024;
    const u32 so = sQ + w * 5120 + 122880 + hf * 20480 + grp * 4096, mo = lds0 + SLOT * 40960 + w * 5120 + hf * 20480 + grp * 4096;
    if constexpr (hf == 0) Q_EMIT("Q3a", ii % 4 == 0, imm, so, mo); else Q_EMIT("Q3b", ii % 4 == 0, imm, so, mo);
  }
#undef Q_EMIT
}
template <int HH, int I> DI void dma_g(rsrc_t rs, u32 l16, u32 sG, int w, u32 lds0) {
  constexpr int grp = I / 4, imm = (I % 4) * 1024;
  const u32 so = sG + HH * 20480 + grp * 4096, mo = lds0 + w * 5120 + (HH % 7) * 20480 + grp * 4096;
#define G_CASE(n) if constexpr (HH == n) { if constexpr (I % 4 == 0) DMA_F("G" #n, imm, "sc1 ", rs, l16, so, mo); else DMA_N("G" #n, imm, "sc1 ", rs, l16, so); }
  G_CASE(0) G_CASE(1) G_CASE(2) G_CASE(3) G_CASE(4) G_CASE(5) G_CASE(6) G_CASE(7) G_CASE(8) G_CASE(9) G_CASE(10) G_CASE(11) G_CASE(12) G_CASE(13)
#undef G_CASE
}

template <int HH> DI void dma_g3(rsrc_t rs, u32 l16, u32 sG0, int w, u32 lds0) {
  const u32 f0 = 7 * (w - 1);
  const u32 so = sG0 + HH * 20480 + f0 * 1024, mo = lds0 + (HH % 7) * 20480 + f0 * 1024;
#define G3_CASE(n) if constexpr (HH == n) { DMA_F("G" #n, 0, "sc1 ", rs, l16, so, mo); DMA_N("G" #n, 1024, "sc1 ", rs, l16, so); DMA_N("G" #n, 2048, "sc1 ", rs, l16, so); DMA_N("G" #n, 3072, "sc1 ", rs, l16, so); \
    DMA_F("G" #n, 0, "sc1 ", rs, l16, so + 4096, mo + 4096); DMA_N("G" #n, 1024, "sc1 ", rs, l16, so + 4096); if (w < 3) DMA_N("G" #n, 2048, "sc1 ", rs, l16, so + 4096); }
  G3_CASE(2) G3_CASE(3) G3_CASE(4) G3_CASE(5) G3_CASE(6)
#undef G3_CASE
}
constexpr int XW = 10;
struct QS {
  static constexpr bool x_at(int f) { return f < 120 && f % 3 == 0 && f / 3 + XW < 40; }
  static constexpr int aux_at(int f) { return (f < 32 && f % 3 == 1) ? f / 3 : -1; }
  static constexpr bool pf_at(int f) { return f == 85; }
  static constexpr int q3_at(int f) { return (f >= 77 && f <= 104 && f % 3 == 2) ? (f - 77) / 3 : -1; }
  static constexpr int wo_at(int f) {
    if (f >= 34 && f <= 67 && f % 3 == 1) return (f - 34) / 3;
    if (f >= 121 && f <= 157 && (f - 121) % 3 == 0) return 12 + (f - 121) / 3;
    return -1;
  }
  static constexpr int upto(int F) { int n = 0; for (int f = 0; f <= F; ++f) n += x_at(f) + (aux_at(f) >= 0) + pf_at(f) + (q3_at(f) >= 0) + (wo_at(f) >= 0); return n; }
  static constexpr int xidx(int i) { return i < XW ? i : XW + upto(3 * (i - XW)) - 1; }
  static constexpr int xwait2(int s) { return XW + upto(3 * s) - 1 - xidx(s + 1); }
};
struct GS {
  static constexpr int NWD = 65;
  static constexpr bool x_at(int f) { return f < 120 && f % 3 == 0 && f / 3 + XW < 40; }
  static constexpr int op(int F) {
    if (F <= 10 && F % 3 == 1) return 3000 + F / 3;
    if (F >= 40 && F <= 67 && F % 3 == 1) return (7 + (F - 40) / 15) * 8 + ((F - 40) % 15) / 3;
    if (F >= 79 && F <= 106 && F % 3 == 1) return (9 + (F - 79) / 15) * 8 + ((F - 79) % 15) / 3;
    if (F < 120 || (F & 1)) return -1;
    if (F < 140) return (11 + (F - 120) / 10) * 8 + ((F - 120) % 10) / 2;
    if (F < 160) return 1000 + (F - 140) / 2;
    if (F < 170) return 13 * 8 + (F - 160) / 2;
    if (F < 280) return 1010 + (F - 170) / 2;
    return -1;
  }
  static constexpr bool st_at(int F) { return F == 139 || F == 157 || F == 175 || F == 219 || F == 237; }
  static constexpr int upto(int F) { int n = 0; for (int f = 0; f <= F; ++f) n += x_at(f) + (op(f) >= 0) + st_at(f); return n; }
  static constexpr int slot(int o) { for (int f = 0; f < 300; ++f) if (op(f) == o) return f; return -1; }
  static constexpr int xidx(int i) { return i < XW ? i : XW + upto(3 * (i - XW)) - 1; }
  static constexpr int xwait2(int s) { return XW + upto(3 * s) - 1 - xidx(s + 1); }
};
static_assert(GS::op(278) == 1000 + GS::NWD - 1, "down-weight prefetch schedule");

DI void iteration(K& k) {
  GAS u32* const flags = k.flags;
  const int tid = k.tid;
  {
    PHASE_IDS();
    const u32 sQ = OFF_WQ + p * 163840, sG = OFF_WG + p * 286720 + w * 5120;
    if (it > 0 && w == 0) {
      const int grp = 2 * m128 - 1 + (lane >> 3);
      const bool act = lane < 24 && grp >= 0;
      poll_flags(flags, ((FG_X2 + grp) * 16 + (lane & 7)) * 32, act, lane, k.tag);
    }
    WAITV(0); CERT("Q0"); CERT("Q1"); CERT("Q2");
    BAR();
    u32x4 xf[40];
    const u32 xbase = xin + (4 * m128 + w) * 40960;
#define XLOAD(i) ald_a_sc1<((i) % 4) * 1024>(xf[i], rs, l16, xbase + ((i) / 4) * 4096)
    u32x2 hs0, hs1, sa0, sa1, sa2, sa3;
    const int rbh = m128 > 0 ? 4 * m128 - 1 : 0;
    const u32 hvo = (g >> 1) * 1024 + (g & 1) * 512 + (16 + t16) * 16;
    const int ht = 128 * m128 - 16 + t16;
    const u32 hso = ((2 * g) * 2048 + (ht < 0 ? 0 : ht)) * 8, sso = (4 * h * 2048 + 128 * m128 + 32 * w + tok) * 8;
    auto aux_load = [&](auto AC) {
      constexpr int a = decltype(AC)::value;
      if constexpr (a < 5) DMA_F("HALO", 0, "sc1 ", rs, hvo, xin + rbh * 40960 + (5 * w + a) * 2048, lds0 + L_HALO + (5 * w + a) * 1024);
      if constexpr (a == 5) ald2_sc1(sa0, rs, sso, stin);
      if constexpr (a == 6) ald2_sc1(sa1, rs, sso, stin + 16384);
      if constexpr (a == 7) ald2_sc1(sa2, rs, sso, stin + 32768);
      if constexpr (a == 8) ald2_sc1(sa3, rs, sso, stin + 49152);
      if constexpr (a == 9) ald2_sc1(hs0, rs, hso, stin);
      if constexpr (a == 10) ald2_sc1(hs1, rs, hso, stin + 16384);
    };
    sfor<XW>([&](auto I) { constexpr int i = decltype(I)::value; XLOAD(i); });
    using Q = QS;
    u32x2 pf2 = {0u, 0u};
    const int cgg = (B0 >> 1) + ((lane & 15) >> 1);
    const bool pfact = lane < 10 ? 32 * cgg < 16 * (B0 + k.nblk) : lane < 60;
    const u32 pfo = (FG_PF2 * 16 + (!pfact ? 0 : lane < 10 ? 124 + 54 * (lane & 1) + cgg : 10 * ((lane - 10) % 25) + (80 * c8) / 64 + (lane - 10) / 25)) * 128;
    constexpr int DQ = 4, NBQ = 6;
    u32x4 wb[NBQ];
    f32x16 acc0, acc1, acc2, acc3;
    f32x4 hacc, hacc4;
    u32x4 hw[3], hx[3], h4w[3], h4x[3];
    float rstd = 0.f, hrs = 0.f;
    u32 lb1 = l16 + 65536, lbh = l16 + L_HALO; asm volatile("" : "+v"(lb1), "+v"(lbh));
    u32 hwlo = (w >> 1) * 1024 + (g >> 1) * 3072 + (g & 1) * 512 + (16 * (w & 1) + t16) * 16, hwhi = hwlo + 61440;
    u32 hw4 = 2048 + (g >> 1) * 3072 + (g & 1) * 512 + t16 * 16 + w * 30720, hx4 = l16 + L_HALO + w * 5120;
    asm volatile("" : "+v"(hwlo), "+v"(hwhi), "+v"(hw4), "+v"(hx4));
    const u32 kvrow = L_KST + (16 + 32 * w + tok) * 80 + h * 8;
    const int hn = 16 * w + 4 * g;
    const u32 haddr = L_KST + t16 * 80 + 2 * hn + (hn >= 40 ? (L_VST - L_KST - 80) : 0);
    const u32 haddr4 = L_VST + t16 * 80 + 2 * (64 + 4 * g - 40);
    const bool hzero = (m128 == 0);
    const u32 wo_o = OFF_WO + (c8 * 20 + 5 * w) * 5120;
#define LDSQ(F) (((F) / 40 % 3) * 40960 + ((F) % 40) * 1024)
#define RDW(off) (*(const LAS u32x4*)(lds + ((off) < 65536 ? l16 : lb1) + ((off) & 65535)))
    u32 gp0 = 0;
#define STG_OFF(n0) ((n0) < 40 ? 2 * (n0) : (L_VST - L_KST) + 2 * ((n0) - 40))
    const u32 qrow = L_QST + (32 * w + tok) * 144 + h * 8, qrow2 = L_QST + (32 * w + tok) * 144 + (h ? 72 : 32);
#define Q_ADDR(n0) ((n0) == 96 ? qrow2 : qrow + ((n0) < 96 ? 2 * ((n0) - 80) : 2 * ((n0) - 80) + 32))
#define GRAN(t, ACC, q, RS, STADDR, ZERO) do { \
      if constexpr ((t) == 0) gp0 = cvtpk(mul_s(ACC[4 * (q)], RS), mul_s(ACC[4 * (q) + 1], RS)); \
      if constexpr ((t) == 1) { u32x2 pk_ = {gp0, cvtpk(mul_s(ACC[4 * (q) + 2], RS), mul_s(ACC[4 * (q) + 3], RS))}; if (ZERO) pk_ = u32x2{0u, 0u}; *(LAS u32x2*)(lds + (STADDR)) = pk_; } \
    } while (0)
#define GRAN_MAIN(t, ACC, q, n0) GRAN(t, ACC, q, rstd, ((n0) < 80 ? kvrow + STG_OFF(n0) : Q_ADDR(n0)), false)
#pragma unroll
    for (int F = 0; F < DQ; ++F) wb[F % NBQ] = RDW(LDSQ(F));
    sfor<160>([&](auto FC) {
      constexpr int F = decltype(FC)::value, Fr = F + DQ;
      if constexpr (Q::x_at(F)) XLOAD(F / 3 + XW);
      if constexpr (Q::aux_at(F) >= 0) aux_load(std::integral_constant<int, Q::aux_at(F)>{});
      if constexpr (Q::pf_at(F)) asm volatile("buffer_load_dwordx2 %0, %1, %2, 0 offen sc1" : "=v"(pf2) : "v"(pfo), "s"(rs) : "memory");
      if constexpr (Q::q3_at(F) >= 0) dma_q<3, 0, Q::q3_at(F)>(rs, l16, sQ, w, lds0);
      if constexpr (Q::wo_at(F) >= 0) {
        constexpr int i = Q::wo_at(F), s = i / 5, nb = i % 5;
        if constexpr (i < 24) ald_a<(i % 4) * 1024>(k.wo_r[s][nb], rs, l16, wo_o + (i / 4) * 4096); else ald_v<(i % 4) * 1024>(k.wo_r[s][nb], rs, l16, wo_o + (i / 4) * 4096);
      }
      if constexpr (Fr == 40) {
        WAITV(Q::upto(F) - Q::upto(13)); CERT("HALO"); BAR();
        if (tid == 0) flag_set(flags, (FG_PF2 * 16 + k.bid) * 32, k.tag);
      }
      if constexpr (Fr == 80) { WAITL0(); BAR(); }
      if constexpr (Fr == 120) {
        WAITV(Q::upto(F) - Q::upto(89)); CERT("Q3a");
        asm volatile("" : "+v"(pf2));
        if (w == 0 && it == 0 && !__all(!pfact || (pf2.x == (u32)k.tag && pf2.y == (u32)(k.tag >> 32)))) poll_flags(flags, pfo / 4, pfact, lane, k.tag);
        WAITL0(); BAR();
      }
      if constexpr (Fr == 140) { WAITV(Q::upto(F) - Q::upto(104)); CERT("Q3b"); BAR(); }
      if constexpr (Fr < 160) wb[Fr % NBQ] = RDW(LDSQ(Fr));
      if constexpr (F < 120) {
        constexpr int s = F / 3, b = F % 3;
        if constexpr (b == 0 && (s & 1) == 0) waitpin_a2<Q::xwait2(s)>(xf[s], xf[s + 1]);
#define QMMA(ACC) do { if constexpr (s == 0) { if constexpr ((F & 1) == 0) mma32w_0(ACC, wb[F % NBQ], xf[s], wb[(F + 1) % NBQ]); else mma32_0(ACC, wb[F % NBQ], xf[s]); } \
                       else { if constexpr ((F & 1) == 0) mma32w(ACC, wb[F % NBQ], xf[s], wb[(F + 1) % NBQ]); else mma32(ACC, wb[F % NBQ], xf[s]); } } while (0)
        if constexpr (b == 0) QMMA(acc0); else if constexpr (b == 1) QMMA(acc1); else QMMA(acc2);
        if constexpr (F == 62) {
          pin2(sa0); pin2(sa1); pin2(sa2); pin2(sa3);
          float nm;
          stats_finish(red32_add((u2f(sa0.x) + u2f(sa1.x)) + (u2f(sa2.x) + u2f(sa3.x))), red32_add((u2f(sa0.y) + u2f(sa1.y)) + (u2f(sa2.y) + u2f(sa3.y))), nm, rstd);
        }
        if constexpr (F == 110) {
          pin2(hs0); pin2(hs1);
          float nm;
          stats_finish(red16_32_add(u2f(hs0.x) + u2f(hs1.x)), red16_32_add(u2f(hs0.y) + u2f(hs1.y)), nm, hrs);
        }
        if constexpr (F == 113) {
#pragma unroll
          for (int nb = 0; nb < 5; ++nb) pin2(k.xr[nb]);
        }
        if constexpr (b == 1) {
          if constexpr (s == 15) mma16_0(hacc, hw[0], hx[0]);
          if constexpr (s > 15 && s <= 34) mma16(hacc, hw[(s - 15) % 3], hx[(s - 15) % 3]);
          if constexpr (s >= 13 && s <= 32) {
            constexpr int sg = s - 13;
            hw[sg % 3] = *(const LAS u32x4*)(lds + (sg < 10 ? hwlo : hwhi) + (sg % 10) * 6144);
            hx[sg % 3] = *(const LAS u32x4*)(lds + lbh + sg * 1024);
          }
        }
      } else {
        constexpr int s = F - 120;
        if constexpr (s == 0) mma32w_0(acc3, wb[F % NBQ], xf[s], wb[(F + 1) % NBQ]); else if constexpr (s & 1) mma32(acc3, wb[F % NBQ], xf[s]); else mma32w(acc3, wb[F % NBQ], xf[s], wb[(F + 1) % NBQ]);
        constexpr int ms = s - 2, gi = ms / 2, st = ms % 2;
        if constexpr (ms >= 0 && gi < 12) {
          constexpr int b = gi >> 2, q = gi & 3, n0 = 32 * b + 8 * q;
          if constexpr (b == 0) GRAN_MAIN(st, acc0, q, n0); else if constexpr (b == 1) GRAN_MAIN(st, acc1, q, n0); else GRAN_MAIN(st, acc2, q, n0);
        }
        if constexpr (s == 22) {
          const f32x4 p0 = *(const LAS f32x4*)(lds + L_H4 + l16), p1 = *(const LAS f32x4*)(lds + L_H4 + 1024 + l16);
          const f32x4 p2 = *(const LAS f32x4*)(lds + L_H4 + 2048 + l16), p3 = *(const LAS f32x4*)(lds + L_H4 + 3072 + l16);
          hacc4 = (p0 + p1) + (p2 + p3);
        }
        if constexpr (ms >= 0 && gi == 12) GRAN(st, hacc, 0, hrs, haddr, hzero);
        if constexpr (ms >= 0 && gi == 14) GRAN(st, hacc4, 0, hrs, haddr4, hzero);
        if constexpr (s == 33) *(LAS u32x2*)(lds + L_QST + (32 * w + tok) * 144 + (h ? 64 : 40)) = u32x2{0u, 0u};
        if constexpr (s == 35) *(LAS u32x2*)(lds + L_QST + (32 * w + tok) * 144 + (h ? 112 : 48)) = u32x2{0u, 0u};
        if constexpr (s == 37) *(LAS u32x2*)(lds + L_QST + (32 * w + tok) * 144 + (h ? 120 : 56)) = u32x2{0u, 0u};
      }
      if constexpr (F < 120 && F % 3 == 2) {
        constexpr int s = F / 3;
        if constexpr (s == 15) mma16_0(hacc4, h4w[0], h4x[0]);
        if constexpr (s > 15 && s <= 19) mma16(hacc4, h4w[(s - 15) % 3], h4x[(s - 15) % 3]);
        if constexpr (s >= 13 && s <= 17) {
          h4w[(s - 13) % 3] = *(const LAS u32x4*)(lds + hw4 + (s - 13) * 6144);
          h4x[(s - 13) % 3] = *(const LAS u32x4*)(lds + hx4 + (s - 13) * 1024);
        }
        if constexpr (s == 21) *(LAS f32x4*)(lds + L_H4 + w * 1024 + l16) = hacc4;
      }
      SB();
    });
    asm volatile("s_nop 11" : "+v"(acc3));
    GRAN_MAIN(0, acc3, 0, 96); GRAN_MAIN(1, acc3, 0, 96); GRAN_MAIN(0, acc3, 1, 104); GRAN_MAIN(1, acc3, 1, 104); GRAN_MAIN(0, acc3, 2, 112); GRAN_MAIN(1, acc3, 2, 112);
    WAITL0(); BAR();
    {
      const int par = t16 & 1;
      const bool ca = (4 * g + par) > t16, cb = (4 * g + 2 + par) > t16;
      const unsigned long long mca = __ballot(ca), mcb = __ballot(cb), mpar = __ballot(par != 0);
      const bool neg = (m128 == 0) && (w == 0);
      const unsigned long long nga = neg ? mca : 0ull, ngb = neg ? mcb : 0ull;
      const u32 gq = g < 2 ? g : 2;
      const u32 pla = par ? 0x01000c0cu : 0x0c0c0100u, plb = par ? 0x03020c0cu : 0x0c0c0302u;
      const u32 sel0 = ca ? pla : 0x0c0c0c0cu, sel2 = ca ? 0x0c0c0c0cu : pla, sel1 = cb ? plb : 0x0c0c0c0cu, sel3 = cb ? 0x0c0c0c0cu : plb;
      constexpr float cexp = 0.22360679774997896f * 1.4426950408889634f;
      float cx = cexp; asm volatile("" : "+v"(cx));
      const u32 qa = L_QST + (32 * w + t16) * 144 + g * 16, ka = L_KST + (32 * w + t16) * 80 + gq * 16;
      const u32 va = L_VST + (32 * w + 4 * g + (t16 >> 2)) * 80 + (t16 & 3) * 8;
      u32x4 qf[4], k0[4], k1[4];
      u32x4 v0[4], v1[4];
#pragma unroll
      for (int u = 0; u < 4; ++u) {
        const u32 o = (u >> 1) * 16 * 80 + (u & 1) * 32;
        qf[u] = *(const LAS u32x4*)(lds + qa + (u >> 1) * 16 * 144 + (u & 1) * 64);
        k0[u] = *(const LAS u32x4*)(lds + ka + o);
        k1[u] = *(const LAS u32x4*)(lds + ka + o + 16 * 80);
      }
      dma_g<0, 0>(rs, l16, sG, w, lds0);
      f32x4 s0[4], s1[4];
      asm volatile("s_nop 1\n\t"
                   "v_mfma_f32_16x16x32_bf16 %0, %8, %16, 0\n\tv_mfma_f32_16x16x32_bf16 %1, %12, %16, 0\n\t"
                   "v_mfma_f32_16x16x32_bf16 %2, %9, %17, 0\n\tv_mfma_f32_16x16x32_bf16 %3, %13, %17, 0\n\t"
                   "v_mfma_f32_16x16x32_bf16 %4, %10, %18, 0\n\tv_mfma_f32_16x16x32_bf16 %5, %14, %18, 0\n\t"
                   "v_mfma_f32_16x16x32_bf16 %6, %11, %19, 0\n\tv_mfma_f32_16x16x32_bf16 %7, %15, %19, 0"
                   : "=&v"(s0[0]), "=&v"(s1[0]), "=&v"(s0[1]), "=&v"(s1[1]), "=&v"(s0[2]), "=&v"(s1[2]), "=&v"(s0[3]), "=&v"(s1[3])
                   : "v"(k0[0]), "v"(k0[1]), "v"(k0[2]), "v"(k0[3]), "v"(k1[0]), "v"(k1[1]), "v"(k1[2]), "v"(k1[3]), "v"(qf[0]), "v"(qf[1]), "v"(qf[2]), "v"(qf[3]));
#pragma unroll
      for (int u = 0; u < 4; ++u) {
        const u32 o = (u >> 1) * 16 * 80 + (u & 1) * 40;
        const u32x2 t0 = __builtin_bit_cast(u32x2, __builtin_amdgcn_ds_read_tr16_b64_v4i16((LAS s16x4*)(lds + va + o)));
        const u32x2 t1 = __builtin_bit_cast(u32x2, __builtin_amdgcn_ds_read_tr16_b64_v4i16((LAS s16x4*)(lds + va + o + 16 * 80)));
        const u32x2 t2 = __builtin_bit_cast(u32x2, __builtin_amdgcn_ds_read_tr16_b64_v4i16((LAS s16x4*)(lds + va + o + 32)));
        const u32x2 t3 = __builtin_bit_cast(u32x2, __builtin_amdgcn_ds_read_tr16_b64_v4i16((LAS s16x4*)(lds + va + o + 32 + 16 * 80)));
        v0[u] = u32x4{t0.x, t0.y, t1.x, t1.y}; v1[u] = u32x4{t2.x, t2.y, t3.x, t3.y};
      }
      dma_g<0, 1>(rs, l16, sG, w, lds0);
      asm volatile("s_nop 7" : "+v"(s0[0]), "+v"(s1[0]), "+v"(s0[1]), "+v"(s1[1]), "+v"(s0[2]), "+v"(s1[2]), "+v"(s0[3]), "+v"(s1[3]));
      float a[4], b[4], mx[4], sm[4];
#pragma unroll
      for (int u = 0; u < 4; ++u) {
        const float x00 = s0[u][0], x01 = s0[u][1], x02 = s0[u][2], x03 = s0[u][3], x10 = s1[u][0], x11 = s1[u][1], x12 = s1[u][2], x13 = s1[u][3];
        a[u] = sel_s(sel_s(x10, x11, mpar), sel_s(x00, x01, mpar), mca);
        b[u] = sel_s(sel_s(x12, x13, mpar), sel_s(x02, x03, mpar), mcb);
        if (u < 2) { a[u] = sel_s(a[u], -__builtin_inff(), nga); b[u] = sel_s(b[u], -__builtin_inff(), ngb); }
        mx[u] = max_s(a[u], b[u]);
      }
      dma_g<0, 2>(rs, l16, sG, w, lds0);
      {
        auto r01 = __builtin_amdgcn_permlane16_swap(f2u(mx[0]), f2u(mx[1]), false, false), r23 = __builtin_amdgcn_permlane16_swap(f2u(mx[2]), f2u(mx[3]), false, false);
        const float c01 = __builtin_fmaxf(u2f(r01[0]), u2f(r01[1])), c23 = __builtin_fmaxf(u2f(r23[0]), u2f(r23[1]));
        auto rt = __builtin_amdgcn_permlane32_swap(f2u(c01), f2u(c23), false, false);
        const u32 t = f2u(__builtin_fmaxf(u2f(rt[0]), u2f(rt[1])));
        dma_g<0, 3>(rs, l16, sG, w, lds0);
        auto ab = __builtin_amdgcn_permlane32_swap(t, t, false, false);
        auto m01 = __builtin_amdgcn_permlane16_swap(ab[0], ab[0], false, false), m23 = __builtin_amdgcn_permlane16_swap(ab[1], ab[1], false, false);
        mx[0] = u2f(m01[0]); mx[1] = u2f(m01[1]); mx[2] = u2f(m23[0]); mx[3] = u2f(m23[1]);
      }
      dma_g<0, 4>(rs, l16, sG, w, lds0);
#pragma unroll
      for (int u = 0; u < 4; ++u) {
        const float t = -mul_s(mx[u], cx);
        a[u] = __builtin_amdgcn_exp2f(fma_s(a[u], cx, t)); b[u] = __builtin_amdgcn_exp2f(fma_s(b[u], cx, t));
        sm[u] = a[u] + b[u];
      }
      dma_g<1, 0>(rs, l16, sG, w, lds0);
      {
        auto r01 = __builtin_amdgcn_permlane16_swap(f2u(sm[0]), f2u(sm[1]), false, false), r23 = __builtin_amdgcn_permlane16_swap(f2u(sm[2]), f2u(sm[3]), false, false);
        const float c01 = u2f(r01[0]) + u2f(r01[1]), c23 = u2f(r23[0]) + u2f(r23[1]);
        auto rt = __builtin_amdgcn_permlane32_swap(f2u(c01), f2u(c23), false, false);
        const u32 t = f2u(__builtin_amdgcn_rcpf(u2f(rt[0]) + u2f(rt[1])));
        dma_g<1, 1>(rs, l16, sG, w, lds0);
        auto ab = __builtin_amdgcn_permlane32_swap(t, t, false, false);
        auto m01 = __builtin_amdgcn_permlane16_swap(ab[0], ab[0], false, false), m23 = __builtin_amdgcn_permlane16_swap(ab[1], ab[1], false, false);
        sm[0] = u2f(m01[0]); sm[1] = u2f(m01[1]); sm[2] = u2f(m23[0]); sm[3] = u2f(m23[1]);
      }
      dma_g<1, 2>(rs, l16, sG, w, lds0);
      u32x4 pf[4];
#pragma unroll
      for (int u = 0; u < 4; ++u) {
        const float inv = sm[u];
        const u32 pk = cvtpk(a[u] * inv, b[u] * inv);
        pf[u] = u32x4{__builtin_amdgcn_perm(pk, pk, sel0), __builtin_amdgcn_perm(pk, pk, sel1), __builtin_amdgcn_perm(pk, pk, sel2), __builtin_amdgcn_perm(pk, pk, sel3)};
      }
      dma_g<1, 3>(rs, l16, sG, w, lds0);
      f32x4 o0[4], o1[4];
      asm volatile("s_nop 1\n\t"
                   "v_mfma_f32_16x16x32_bf16 %0, %8, %16, 0\n\tv_mfma_f32_16x16x32_bf16 %1, %12, %16, 0\n\t"
                   "v_mfma_f32_16x16x32_bf16 %2, %9, %17, 0\n\tv_mfma_f32_16x16x32_bf16 %3, %13, %17, 0\n\t"
                   "v_mfma_f32_16x16x32_bf16 %4, %10, %18, 0\n\tv_mfma_f32_16x16x32_bf16 %5, %14, %18, 0\n\t"
                   "v_mfma_f32_16x16x32_bf16 %6, %11, %19, 0\n\tv_mfma_f32_16x16x32_bf16 %7, %15, %19, 0\n\ts_nop 7"
                   : "=&v"(o0[0]), "=&v"(o1[0]), "=&v"(o0[1]), "=&v"(o1[1]), "=&v"(o0[2]), "=&v"(o1[2]), "=&v"(o0[3]), "=&v"(o1[3])
                   : "v"(v0[0]), "v"(v0[1]), "v"(v0[2]), "v"(v0[3]), "v"(v1[0]), "v"(v1[1]), "v"(v1[2]), "v"(v1[3]), "v"(pf[0]), "v"(pf[1]), "v"(pf[2]), "v"(pf[3]));
      dma_g<1, 4>(rs, l16, sG, w, lds0);
      const u32 sg = 5 * p + g, sg4 = 5 * p + 4;
      const u32 oa = (sg >> 2) * 1024 + (t16 + 16 * (sg & 3)) * 16, oa4 = (sg4 >> 2) * 1024 + (t16 + 16 * (sg4 & 3)) * 16;
#pragma unroll
      for (int qb = 0; qb < 2; ++qb) {
        const u32 so = ofo + (8 * m128 + 2 * w + qb) * 20480;
        const f32x4 A0 = o0[2 * qb], B0_ = o0[2 * qb + 1], A1 = o1[2 * qb], B1 = o1[2 * qb + 1];
        stg_sc1(u32x4{cvtpk(A0[0], A0[1]), cvtpk(A0[2], A0[3]), cvtpk(B0_[0], B0_[1]), cvtpk(B0_[2], B0_[3])}, rs, oa, so);
        if (g == 0) stg_sc1(u32x4{cvtpk(A1[0], A1[1]), cvtpk(A1[2], A1[3]), cvtpk(B1[0], B1[1]), cvtpk(B1[2], B1[3])}, rs, oa4, so);
      }
    }
    SB();
    WAITV(0); CERT("G0"); CERT("G1");
    WAITL0();
    BAR();
    if (tid == 0) flag_set(flags, ((fgb + FG_O + m128) * 16 + p) * 32, k.tag);
    sfor<5>([&](auto I) { dma_g<2, decltype(I)::value>(rs, l16, sG, w, lds0); });
    sfor<5>([&](auto I) { dma_g<3, decltype(I)::value>(rs, l16, sG, w, lds0); });
  }
  {
    PHASE_IDS();
    if (w == 0) poll_flags(flags, ((fgb + FG_O + (m64 >> 1)) * 16 + (lane & 15)) * 32, lane < 16, lane, k.tag);
    BAR();
#pragma unroll
    for (int s = 0; s < 5; ++s)
#pragma unroll
      for (int nb = 0; nb < 5; ++nb) { if (s * 5 + nb < 24) pin_a(k.wo_r[s][nb]); else pin_v(k.wo_r[s][nb]); }
    u32x4 af[20];
    u32 tbo[4];
#pragma unroll
    for (int tbi = 0; tbi < 4; ++tbi) tbo[tbi] = ofo + ((4 * m64 + ((w + 1 + tbi) & 3)) * 20 + 5 * w) * 1024;
    constexpr int OW = 10;
#define OLOAD(q) ald_v_sc1<((q) % 5 % 4) * 1024>(af[q], rs, l16, tbo[(q) / 5] + ((q) % 5 / 4) * 4096)
    sfor<OW>([&](auto I) { constexpr int q = decltype(I)::value; OLOAD(q); });
    f32x4 acc[2][5];
    f32x4 part[3][5];
    sfor<20>([&](auto QC) {
      constexpr int q = decltype(QC)::value, tbi = q / 5, s = q % 5;
      if constexpr (q + OW < 20) OLOAD(q + OW);
      waitpin_v<(q < 10 ? OW : (19 - q))>(af[q]);
#pragma unroll
      for (int nb = 0; nb < 5; ++nb) acc[tbi & 1][nb] = mfma16(k.wo_r[s][nb], af[q], s == 0 ? f32x4{0.f, 0.f, 0.f, 0.f} : acc[tbi & 1][nb]);
      if constexpr (tbi > 0 && s < 3) {
        const u32 tbp = (w + tbi) & 3;
        *(LAS f32x4*)(lds + L_RED + ((tbp * 3 + (3 - tbi)) * 5 + 2 * s) * 1024 + l16) = acc[(tbi - 1) & 1][2 * s];
        if constexpr (2 * s + 1 < 5) *(LAS f32x4*)(lds + L_RED + ((tbp * 3 + (3 - tbi)) * 5 + 2 * s + 1) * 1024 + l16) = acc[(tbi - 1) & 1][2 * s + 1];
      }
      if constexpr (q == 18) {
        WAITL0(); BAR();
#pragma unroll
        for (int sl = 0; sl < 3; ++sl)
#pragma unroll
          for (int nb = 0; nb < 5; ++nb) part[sl][nb] = *(const LAS f32x4*)(lds + L_RED + ((w * 3 + sl) * 5 + nb) * 1024 + l16);
      }
      SB();
    });
    float s1 = 0.f, s2 = 0.f;
#pragma unroll
    for (int nb = 0; nb < 5; ++nb) {
      const f32x4 v = (acc[1][nb] + part[0][nb]) + (part[1][nb] + part[2][nb]);
      const u32 a = cvtpk(bflo(k.xr[nb].x) + v[0], bfhi(k.xr[nb].x) + v[1]), b = cvtpk(bflo(k.xr[nb].y) + v[2], bfhi(k.xr[nb].y) + v[3]);
      k.xr[nb] = u32x2{a, b};
    }
    const u32 token = 64 * m64 + 16 * w + t16;
    const u32 xo = ((token >> 5) * 40 + 5 * c8) * 1024 + ((token & 31) + 32 * (g >> 1)) * 16;
#pragma unroll
    for (int pr = 0; pr < 3; ++pr) {
      const int A = 2 * pr, Bn = pr < 2 ? 2 * pr + 1 : 4;
      auto r0 = __builtin_amdgcn_permlane16_swap(k.xr[A].x, k.xr[Bn].x, false, false);
      auto r1 = __builtin_amdgcn_permlane16_swap(k.xr[A].y, k.xr[Bn].y, false, false);
      const u32x4 v = {r0[0], r1[0], r0[1], r1[1]};
      if (pr < 2) stg_sc1(v, rs, xo + (A + (g & 1)) * 1024, x1o);
      else if ((g & 1) == 0) stg_sc1(v, rs, xo + 4 * 1024, x1o);
    }
#pragma unroll
    for (int nb = 0; nb < 5; ++nb) {
      const float a = bflo(k.xr[nb].x), b = bfhi(k.xr[nb].x), c = bflo(k.xr[nb].y), d = bfhi(k.xr[nb].y);
      s1 += (a + b) + (c + d); s2 += (a * a + b * b) + (c * c + d * d);
    }
    s1 = red16_32_add(s1); s2 = red16_32_add(s2);
    if (g == 0) stg2_sc1(u32x2{f2u(s1), f2u(s2)}, rs, (c8 * 2048 + token) * 8, st1o);
    SB();
    WAITV(0); CERT("G2"); CERT("G3");
    WAITL0();
    BAR();
    if (tid == 0) flag_set(flags, ((fgb + FG_X1 + m64) * 16 + c8) * 32, k.tag);
  }
  {
    PHASE_IDS();
    const int nblk = k.nblk;
    const int ds0 = w < 2 ? 14 * w : 28 + 13 * (w - 2);
    const u32 sG = OFF_WG + p * 286720 + w * 5120;
    Polls pl = {0ull, 0ull, 0ull};
    if (w != 0) { dma_g3<4>(rs, l16, sG - w * 5120, w, lds0); dma_g3<5>(rs, l16, sG - w * 5120, w, lds0); dma_g3<6>(rs, l16, sG - w * 5120, w, lds0); }
    if (w == 0) poll_flags_p(flags, ((fgb + FG_X1 + 2 * m128 + ((lane >> 3) & 1)) * 16 + (lane & 7)) * 32, lane < 16, lane, k.tag, pl);
    BAR();
    u32x2 sa0, sa1, sa2, sa3;
    u32x4 xf[40];
    const u32 xbase = x1o + (4 * m128 + w) * 40960;
    const u32 sso = (4 * h * 2048 + 128 * m128 + 32 * w + tok) * 8;
    sfor<XW>([&](auto I) { constexpr int i = decltype(I)::value; XLOAD(i); });
    constexpr int NWD = GS::NWD;
    float r2 = 0.f;
    constexpr int DG = 4, NBG = 6;
    u32x4 wb[NBG];
    f32x16 aA0, aA1, aA2, aB0, aB1;
    u32 lb1 = l16 + 65536, lb2 = l16 + 131072; asm volatile("" : "+v"(lb1), "+v"(lb2));
    const u32 hbase = (8 * m128 + 2 * w + (tok >> 4)) * 54 * 1024 + ((tok & 15) + 16 * h) * 16;
    const u32 hlast = nblk == 7 ? hbase : 0x7fff0000u;
    const u32 wd_o = OFF_WD + (c8 * 54 + ds0) * 5120;
#define LDSG(F) ((((F) / 20) % 7) * 20480 + ((F) % 20) * 1024)
#define RDG(off) (*(const LAS u32x4*)(lds + ((off) < 65536 ? l16 : (off) < 131072 ? lb1 : lb2) + ((off) & 65535)))
    float hv0 = 0.f;
    u32 hp[4];
#define GU_STAGE(ACC, idx, t) do { \
      if constexpr ((t) < 8) { const float hv_ = mul_s(mul_s(ACC[t], r2), ACC[8 + (t)]); if constexpr ((t) & 1) hp[(t) >> 1] = cvtpk(hv0, hv_); else hv0 = hv_; } \
      if constexpr ((t) == 8) { const u32 fb_ = B0 + (idx); stg_sc1(u32x4{hp[0], hp[1], hp[2], hp[3]}, rs, ((idx) == 6 ? hlast : hbase) + (fb_ & 1) * 512, hfo + (fb_ >> 1) * 1024); } \
    } while (0)
    auto wd_load = [&](auto OC) {
      constexpr int i = decltype(OC)::value, s = i / 5, nb = i % 5;
      if constexpr (i < 24 || i >= 50) ald_a<(i % 4) * 1024>(k.wd_r[s][nb], rs, l16, wd_o + (i / 4) * 4096);
      else ald_v<(i % 4) * 1024>(k.wd_r[s][nb], rs, l16, wd_o + (i / 4) * 4096);
    };
#pragma unroll
    for (int F = 0; F < DG; ++F) wb[F % NBG] = RDG(LDSG(F));
    sfor<280>([&](auto FC) {
      constexpr int F = decltype(FC)::value, Fr = F + DG;
      if constexpr (GS::x_at(F)) XLOAD(F / 3 + XW);
      if constexpr (GS::op(F) >= 0 && GS::op(F) < 1000) dma_g<GS::op(F) / 8, GS::op(F) % 8>(rs, l16, sG, w, lds0);
      if constexpr (GS::op(F) >= 1000 && GS::op(F) < 2000) wd_load(std::integral_constant<int, GS::op(F) - 1000>{});
      if constexpr (GS::op(F) == 3000) ald2_sc1(sa0, rs, sso, st1o);
      if constexpr (GS::op(F) == 3001) ald2_sc1(sa1, rs, sso, st1o + 16384);
      if constexpr (GS::op(F) == 3002) ald2_sc1(sa2, rs, sso, st1o + 32768);
      if constexpr (GS::op(F) == 3003) ald2_sc1(sa3, rs, sso, st1o + 49152);
      if constexpr (Fr == 40) { WAITL0(); BAR(); }
      if constexpr (Fr == 80) { CERT("G4"); CERT("G5"); WAITL0(); BAR(); }
      if constexpr (Fr == 120) { WAITV(GS::upto(F) - GS::upto(GS::slot(7 * 8 + 4))); CERT("G6"); CERT("G7"); WAITL0(); BAR(); }
      if constexpr (Fr == 160) { WAITV(GS::upto(F) - GS::upto(GS::slot(9 * 8 + 4))); CERT("G8"); CERT("G9"); WAITL0(); BAR(); }
      if constexpr (Fr == 200) { WAITV(GS::upto(F) - GS::upto(GS::slot(11 * 8 + 4))); CERT("G10"); CERT("G11"); BAR(); }
      if constexpr (Fr == 240) { WAITV(GS::upto(F) - GS::upto(GS::slot(13 * 8 + 4))); CERT("G12"); CERT("G13"); BAR(); }
      if constexpr (Fr < 280) wb[Fr % NBG] = RDG(LDSG(Fr));
      constexpr int s = F < 120 ? F / 3 : F < 200 ? (F - 120) / 2 : (F - 200) / 2, b = F < 120 ? F % 3 : F & 1;
      if constexpr (F < 120 && b == 0 && (s & 1) == 0) waitpin_a2<GS::xwait2(s)>(xf[s], xf[s + 1]);
#define GMMA(ACC) do { if constexpr (s == 0) { if constexpr ((F & 1) == 0) mma32w_0(ACC, wb[F % NBG], xf[s], wb[(F + 1) % NBG]); else mma32_0(ACC, wb[F % NBG], xf[s]); } \
                       else { if constexpr ((F & 1) == 0) mma32w(ACC, wb[F % NBG], xf[s], wb[(F + 1) % NBG]); else mma32(ACC, wb[F % NBG], xf[s]); } } while (0)
      if constexpr (F < 120) { if constexpr (b == 0) GMMA(aA0); else if constexpr (b == 1) GMMA(aA1); else GMMA(aA2); }
      else if constexpr (F < 200) { if constexpr (b == 0) GMMA(aB0); else GMMA(aB1); }
      else { if constexpr (b == 0) GMMA(aA0); else GMMA(aA1); }
      if constexpr (F == 3) poll_pin(pl);
      if constexpr (F == 62) {
        pin2(sa0); pin2(sa1); pin2(sa2); pin2(sa3);
        float rstd, nm;
        stats_finish(red32_add((u2f(sa0.x) + u2f(sa1.x)) + (u2f(sa2.x) + u2f(sa3.x))), red32_add((u2f(sa0.y) + u2f(sa1.y)) + (u2f(sa2.y) + u2f(sa3.y))), nm, rstd);
        r2 = rstd * rstd;
      }
      if constexpr ((F & 1) && F >= 123 && F <= 139) GU_STAGE(aA0, 0, (F - 123) / 2);
      if constexpr ((F & 1) && F >= 141 && F <= 157) GU_STAGE(aA1, 1, (F - 141) / 2);
      if constexpr ((F & 1) && F >= 159 && F <= 175) GU_STAGE(aA2, 2, (F - 159) / 2);
      if constexpr ((F & 1) && F >= 203 && F <= 219) GU_STAGE(aB0, 3, (F - 203) / 2);
      if constexpr ((F & 1) && F >= 221 && F <= 237) GU_STAGE(aB1, 4, (F - 221) / 2);
      SB();
    });
    asm volatile("s_nop 11" : "+v"(aA0), "+v"(aA1));
    sfor<9>([&](auto T) { GU_STAGE(aA0, 5, decltype(T)::value); });
    sfor<9>([&](auto T) { GU_STAGE(aA1, 6, decltype(T)::value); });
    SB();
    WAITV(0);
    WAITL0();
    BAR();
    if (tid == 0) flag_set(flags, ((fgb + FG_H + m128) * 16 + p) * 32, k.tag);
    {
      const u32 wd_o = OFF_WD + (c8 * 54 + ds0) * 5120;
      const u32 l13 = w < 2 ? l16 : 0x7fff0000u;
      static_assert(GS::NWD == 65, "the fragments of step 13");
      sfor<70 - GS::NWD>([&](auto I) { constexpr int i = GS::NWD + decltype(I)::value; ald_a<(i % 4) * 1024>(k.wd_r[i / 5][i % 5], rs, l13, wd_o + (i / 4) * 4096); });
    }
  }
  {
    PHASE_IDS();
    const int ds0 = w < 2 ? 14 * w : 28 + 13 * (w - 2);
    const u32 sQ = OFF_WQ + p * 163840;
    if (w == 0) poll_flags(flags, ((fgb + FG_H + (m64 >> 1)) * 16 + (lane & 15)) * 32, lane < 16, lane, k.tag);
    else WAITV(0);
    BAR();
    sfor<70>([&](auto I) { constexpr int i = decltype(I)::value; if constexpr (i < 24 || i >= 50) pin_a(k.wd_r[i / 5][i % 5]); else pin_v(k.wd_r[i / 5][i % 5]); });
    constexpr int DH = 12;
    u32x4 af[DH];
    const bool s13 = w < 2;
    u32 tbo[4];
#pragma unroll
    for (int tbi = 0; tbi < 4; ++tbi) tbo[tbi] = hfo + ((4 * m64 + ((w + 1 + tbi) & 3)) * 54 + ds0) * 1024;
const u32 l13 = s13 ? l16 : 0x7fff0000u;
#define HLOAD(q) ald_a_sc1<((q) % 14 % 4) * 1024>(af[(q) % DH], rs, ((q) % 14 == 13 ? l13 : l16), tbo[(q) / 14] + ((q) % 14 / 4) * 4096)
    sfor<DH>([&](auto I) { constexpr int q = decltype(I)::value; HLOAD(q); });
    const rsrc_t rsq = it == 0 ? rs : k.rsnull;
    f32x4 acc[2][5];
    f32x4 part[3][5];
    sfor<56>([&](auto QC) {
      constexpr int q = decltype(QC)::value, tbi = q / 14, s = q % 14;
      constexpr int yf = (q + DH - 1 < 55 ? q + DH - 1 : 55) - q;
      constexpr int nd = q - 44;
      constexpr int yd = (nd > 0) ? (nd + (nd < 8 ? nd : 8)) : 0;
      waitpin_a<yf + yd>(af[q % DH]);
      if constexpr (s == 0) {
#pragma unroll
        for (int nb = 0; nb < 5; ++nb) acc[tbi & 1][nb] = mfma16(k.wd_r[s][nb], af[q % DH], f32x4{0.f, 0.f, 0.f, 0.f});
      } else if (s < 13 || s13) {
#pragma unroll
        for (int nb = 0; nb < 5; ++nb) acc[tbi & 1][nb] = mfma16(k.wd_r[s][nb], af[q % DH], acc[tbi & 1][nb]);
      }
      if constexpr (tbi > 0 && s >= 1 && s < 6) {
        const u32 tbp = (w + tbi) & 3;
        *(LAS f32x4*)(lds + L_RED + ((tbp * 3 + (3 - tbi)) * 5 + (s - 1)) * 1024 + l16) = acc[(tbi - 1) & 1][s - 1];
      }
      if constexpr (q == 50) {
        WAITL0(); BAR();
#pragma unroll
        for (int sl = 0; sl < 3; ++sl)
#pragma unroll
          for (int nb = 0; nb < 5; ++nb) part[sl][nb] = *(const LAS f32x4*)(lds + L_RED + ((w * 3 + sl) * 5 + nb) * 1024 + l16);
      }
      SB();
      if constexpr (q + DH < 56) HLOAD(q + DH);
      if constexpr (q >= 44) {
        constexpr int j0 = nd + (nd < 8 ? nd : 8);
        if constexpr (j0 < 10) dma_q<0, 0, j0>(rsq, l16, sQ, w, lds0); else dma_q<1, 1, j0 - 10>(rsq, l16, sQ, w, lds0);
        if constexpr (nd < 8) {
          constexpr int j1 = j0 + 1;
          if constexpr (j1 < 10) dma_q<0, 0, j1>(rsq, l16, sQ, w, lds0); else dma_q<1, 1, j1 - 10>(rsq, l16, sQ, w, lds0);
        }
      }
      SB();
    });
#pragma unroll
    for (int nb = 0; nb < 5; ++nb) {
      const f32x4 v = (acc[1][nb] + part[0][nb]) + (part[1][nb] + part[2][nb]);
      const u32 a = cvtpk(bflo(k.xr[nb].x) + v[0], bfhi(k.xr[nb].x) + v[1]), b = cvtpk(bflo(k.xr[nb].y) + v[2], bfhi(k.xr[nb].y) + v[3]);
      k.xr[nb] = u32x2{a, b};
    }
    const u32 token = 64 * m64 + 16 * w + t16;
    if (it == 0) {
      float s1 = 0.f, s2 = 0.f;
      const u32 xo = ((token >> 5) * 40 + 5 * c8) * 1024 + ((token & 31) + 32 * (g >> 1)) * 16;
#pragma unroll
      for (int pr = 0; pr < 3; ++pr) {
        const int A = 2 * pr, Bn = pr < 2 ? 2 * pr + 1 : 4;
        auto r0 = __builtin_amdgcn_permlane16_swap(k.xr[A].x, k.xr[Bn].x, false, false);
        auto r1 = __builtin_amdgcn_permlane16_swap(k.xr[A].y, k.xr[Bn].y, false, false);
        const u32x4 v = {r0[0], r1[0], r0[1], r1[1]};
        if (pr < 2) stg_sc1(v, rs, xo + (A + (g & 1)) * 1024, OFF_XF + 2 * SZ_XF);
        else if ((g & 1) == 0) stg_sc1(v, rs, xo + 4 * 1024, OFF_XF + 2 * SZ_XF);
      }
#pragma unroll
      for (int nb = 0; nb < 5; ++nb) {
        const float a = bflo(k.xr[nb].x), b = bfhi(k.xr[nb].x), c = bflo(k.xr[nb].y), d = bfhi(k.xr[nb].y);
        s1 += (a + b) + (c + d); s2 += (a * a + b * b) + (c * c + d * d);
      }
      s1 = red16_32_add(s1); s2 = red16_32_add(s2);
      if (g == 0) stg2_sc1(u32x2{f2u(s1), f2u(s2)}, rs, (c8 * 2048 + token) * 8, OFF_ST + 2 * SZ_ST);
      SB();
      WAITV(0);
      WAITL0();
      BAR();
      if (tid == 0) flag_set(flags, ((FG_X2 + m64) * 16 + c8) * 32, k.tag);
      sfor<10>([&](auto I) { dma_q<2, 2, decltype(I)::value>(rs, l16, sQ, w, lds0); });
    } else {
      const u32 oo = token * 1280 + (80 * c8 + 8 * (g >> 1)) * 2;
#pragma unroll
      for (int pr = 0; pr < 3; ++pr) {
        const int A = 2 * pr, Bn = pr < 2 ? 2 * pr + 1 : 4;
        auto r0 = __builtin_amdgcn_permlane16_swap(k.xr[A].x, k.xr[Bn].x, false, false);
        auto r1 = __builtin_amdgcn_permlane16_swap(k.xr[A].y, k.xr[Bn].y, false, false);
        const u32x4 v = {r0[0], r1[0], r0[1], r1[1]};
        if (pr < 2) __builtin_amdgcn_raw_buffer_store_b128(v, k.rso, oo + (A + (g & 1)) * 32, 0, 0);
        else if ((g & 1) == 0) __builtin_amdgcn_raw_buffer_store_b128(v, k.rso, oo + 4 * 32, 0, 0);
      }
    }
  }
}

DI void stw_sc1(u32x4 v, rsrc_t rs, u32 off) { __builtin_amdgcn_raw_buffer_store_b128(v, rs, off, 0, AUX_SC1); }
DI float dot2ones(u32 v, float c) { return __builtin_amdgcn_fdot2_f32_bf16(__builtin_bit_cast(bf16x2, v), __builtin_bit_cast(bf16x2, 0x3f803f80u), c, false); }
DI void setup(K& k, int u, const u16* x0, const u16* wqkv, const u16* wo, const u16* wg, const u16* wu, const u16* wd) {
  const int l = k.lane, w = k.w;
  const rsrc_t rs = k.rs;
  LAS char* const lds = k.lds;
  const u32 lds0 = k.lds0;
  const int type = u < 60 ? 0 : u < 124 ? 3 : u < 178 ? 1 : u < 232 ? 2 : u < 252 ? 4 : 5;
  const int idx = u < 60 ? u : u < 124 ? u - 60 : u < 178 ? u - 124 : u < 232 ? u - 178 : u - 232;
  const u16* src = type == 0 ? wqkv : type == 1 ? wg : type == 2 ? wu : type == 3 ? x0 : wo;
  const u32 pitch = type == 0 ? 3840 : type <= 2 ? 3456 : 1280;
  const u32 nrec = type == 0 ? 640 * 3840 : type <= 2 ? 640 * 3456 : type == 3 ? 2048 * 1280 : type == 4 ? 640 * 1280 : 0;
  const rsrc_t rsrcA = __builtin_amdgcn_make_buffer_rsrc((void*)src, 0, nrec, 0x00020000);
  const rsrc_t rsrcD = __builtin_amdgcn_make_buffer_rsrc((void*)wd, 0, 1728 * 1280, 0x00020000);
  {
    const u32 voW = (16 * w + (l >> 2)) * pitch + idx * 64 + (l & 3) * 16;
    const u32 voO = ((idx & 1) * 320 + 8 * w + (l >> 3)) * 1280 + (idx >> 1) * 128 + (l & 7) * 16;
    const u32 vo = type == 4 ? voO : voW, step = type == 4 ? 32 * 1280 : 64 * pitch;
#pragma unroll
    for (int j = 0; j < 10; ++j) {
      const u32 cidx = 64 * (4 * j + w) + l, row = (cidx * 52429u) >> 22, cc = cidx - row * 80;
      const u32 vx = (32 * idx + row) * 1280 + ((cc ^ (row & 7)) * 16);
      const u32 v = type == 3 ? vx : vo + j * step;
      DMA_F("SA", 0, "", rsrcA, v, 0u, lds0 + (4 * j + w) * 1024);
    }
#pragma unroll
    for (int t = 0; t < 3; ++t) {
      const u32 ds_ = u / 10 + 25 * t;
      const u32 v = (u < 250 && ds_ < 54) ? (ds_ * 32 + 8 * w + (l >> 3)) * 1280 + (u % 10) * 128 + (l & 7) * 16 : 0x7fffffffu;
      DMA_F("SB", 0, "", rsrcD, v, 0u, lds0 + 40960 + t * 4096 + w * 1024);
    }
  }
  const int hh = l >> 5;
  u32 dbase = 0; int dstep = 0;
  {
    const int c = 32 * idx + (l & 31);
    if (type == 0) {
      const int sec = c / 640, cc = c % 640, p = cc / 40, r40 = cc % 40;
      const int n = (sec == 1 ? 0 : sec == 2 ? 40 : 80) + r40;
      const int b = n >> 5, nl = n & 31;
      dbase = OFF_WQ + p * 163840 + (b < 3 ? b * 1024 : 122880) + nl * 16;
      dstep = b < 3 ? 3072 : 1024;
    } else if (type <= 2) {
      const int fb = c >> 4, nl = (c & 15) + 16 * (type - 1);
      const int half = fb / 54, rem = fb % 54;
      const int sl_ = 8 * half + (rem < 42 ? rem / 7 : 6 + (rem - 42) / 6), ix = rem < 42 ? rem % 7 : (rem - 42) % 6;
      dbase = OFF_WG + sl_ * 286720 + (ix < 3 ? ix * 1024 : ix < 5 ? 122880 + (ix - 3) * 1024 : 204800 + (ix - 5) * 1024) + nl * 16;
      dstep = ix < 3 ? 3072 : 2048;
    }
  }
  asm volatile("" : "+v"(dbase), "+v"(dstep));
  u64 ep;
  asm volatile("global_load_dwordx2 %0, %1, off sc1" : "=v"(ep) : "v"(k.flags + FG_EPOCH * 512) : "memory");
  WAITV(0);
  asm volatile("" : "+v"(ep));
  BAR();
  {
    const u64 t = ep * 0x9E3779B97F4A7C15ull + 0xD1B54A32D192ED03ull;
    k.tag = (u64)(u32)__builtin_amdgcn_readfirstlane((u32)t) | ((u64)(u32)__builtin_amdgcn_readfirstlane((u32)(t >> 32)) << 32);
  }
  const int gam = l >> 4, q = (l & 15) >> 2, pp = l & 3;
  if (type <= 2) {
    const u32 ra = ((gam >> 1) * 8 + q) * 64 + (gam & 1) * 32 + pp * 8;
    u32x4 sl[10];
#pragma unroll
    for (int j = 0; j < 10; ++j) {
      const u32 o = ra + (4 * j + w) * 1024;
      const u32x2 a = __builtin_bit_cast(u32x2, __builtin_amdgcn_ds_read_tr16_b64_v4i16((LAS s16x4*)(lds + o)));
      const u32x2 b2 = __builtin_bit_cast(u32x2, __builtin_amdgcn_ds_read_tr16_b64_v4i16((LAS s16x4*)(lds + o + 256)));
      sl[j] = u32x4{a.x, a.y, b2.x, b2.y};
    }
    float c0 = 0.f, c1 = 0.f;
#pragma unroll
    for (int j = 0; j < 10; ++j) { c0 = dot2ones(sl[j][0], c0); c1 = dot2ones(sl[j][1], c1); c0 = dot2ones(sl[j][2], c0); c1 = dot2ones(sl[j][3], c1); }
    const float cs = red32_add(c0 + c1);
    LAS float* red = (LAS float*)(lds + 81920);
    if (l < 32) red[w * 32 + l] = cs;
    WAITL0(); BAR();
    const float mean = ((red[l & 31] + red[32 + (l & 31)]) + (red[64 + (l & 31)] + red[96 + (l & 31)])) * (1.0f / 640.0f);
#pragma unroll
    for (int j = 0; j < 10; ++j) {
      u32x4 v;
#pragma unroll
      for (int e = 0; e < 4; ++e) v[e] = cvtpk(bflo(sl[j][e]) - mean, bfhi(sl[j][e]) - mean);
      stw_sc1(v, rs, dbase + (4 * j + w) * dstep + hh * 512);
    }
  } else if (type == 3) {
    const int tok = l & 31, h = l >> 5;
    u32x4 sl[10];
#pragma unroll
    for (int i = 0; i < 10; ++i) sl[i] = *(const LAS u32x4*)(lds + tok * 1280 + (((2 * (10 * w + i) + h) ^ (tok & 7)) * 16));
    float s1 = 0.f, s2 = 0.f;
#pragma unroll
    for (int i = 0; i < 10; ++i) {
      stw_sc1(sl[i], rs, OFF_XF + (idx * 40 + 10 * w + i) * 1024 + l * 16);
#pragma unroll
      for (int e = 0; e < 4; ++e) { s1 = dot2ones(sl[i][e], s1); const float a_ = bflo(sl[i][e]), b_ = bfhi(sl[i][e]); s2 += a_ * a_ + b_ * b_; }
    }
    s1 = red32_add(s1); s2 = red32_add(s2);
    if (l < 32) {
      stg2_sc1(u32x2{f2u(s1), f2u(s2)}, rs, (w * 2048 + 32 * idx + tok) * 8, OFF_ST);
      stg2_sc1(u32x2{0u, 0u}, rs, ((w + 4) * 2048 + 32 * idx + tok) * 8, OFF_ST);
    }
  } else if (type == 4) {
    const int cg = idx >> 1, kk = idx & 1, c = 64 * cg + l, c8 = c / 80, nb = (c % 80) >> 4, n = c & 15;
    const u32 dbase = OFF_WO + c8 * 20 * 5120 + nb * 1024 + n * 16;
#pragma unroll
    for (int j = 0; j < 10; ++j) {
      const int o = 4 * j + w, pl = o / 5, ii = o % 5, sg = 40 * kk + o, r0 = 40 * pl + 4 * ii + q;
      const u32x2 a = __builtin_bit_cast(u32x2, __builtin_amdgcn_ds_read_tr16_b64_v4i16((LAS s16x4*)(lds + r0 * 128 + gam * 32 + pp * 8)));
      const u32x2 b2 = __builtin_bit_cast(u32x2, __builtin_amdgcn_ds_read_tr16_b64_v4i16((LAS s16x4*)(lds + (r0 + 20) * 128 + gam * 32 + pp * 8)));
      stw_sc1(u32x4{a.x, a.y, b2.x, b2.y}, rs, dbase + (sg >> 2) * 5120 + (sg & 3) * 256);
    }
  }
  const bool crit = type == 0 || type == 3 || type == 4;
  if (crit) { WAITV(0); }
  WAITL0();
  BAR();
  if (crit && k.tid == 0) flag_set(k.flags, (FG_PF1 * 16 + u) * 32, k.tag);
#pragma unroll
  for (int t = 0; t < 3; ++t) {
    const u32 s_ = u / 10 + 25 * t;
    if (u < 250 && s_ < 54) {
      const u32 cg = u % 10, c = 64 * cg + l, c8 = c / 80, nb = (c % 80) >> 4, n = c & 15;
      const u32 r0 = 16 * (w >> 1) + 4 * (w & 1) + q;
      const u32x2 a = __builtin_bit_cast(u32x2, __builtin_amdgcn_ds_read_tr16_b64_v4i16((LAS s16x4*)(lds + 40960 + t * 4096 + r0 * 128 + gam * 32 + pp * 8)));
      const u32x2 b2 = __builtin_bit_cast(u32x2, __builtin_amdgcn_ds_read_tr16_b64_v4i16((LAS s16x4*)(lds + 40960 + t * 4096 + (r0 + 8) * 128 + gam * 32 + pp * 8)));
      stw_sc1(u32x4{a.x, a.y, b2.x, b2.y}, rs, OFF_WD + (c8 * 54 + s_) * 5120 + nb * 1024 + n * 16 + w * 256);
    }
  }
  WAITL0();
  if (w == 0) {
    u32 fo; bool act = true;
    if (l < 6) fo = 20 * (l >> 1) + (40 * k.p) / 32 + (l & 1);
    else if (l < 11) { const int rb = 4 * k.m128 - 1 + (l - 6); act = rb >= 0; fo = 60 + rb; }
    else if (l < 15) fo = 232 + 2 * ((80 * k.c8) / 64 + ((l - 11) >> 1)) + ((l - 11) & 1);
    else { act = false; fo = 0; }
    poll_flags(k.flags, (FG_PF1 * 16 + (act ? fo : 0)) * 32, act, l, k.tag);
  }
  BAR();
}

__global__ __launch_bounds__(256, 1) void main_kernel(const u16* __restrict__ x0, const u16* __restrict__ wqkv, const u16* __restrict__ wo, const u16* __restrict__ wg,
                                                      const u16* __restrict__ wu, const u16* __restrict__ wd, char* __restrict__ ws, u16* __restrict__ out) {
  extern __shared__ __attribute__((aligned(16))) char smem[];
  K k;
  k.lds = (LAS char*)smem;
  k.lds0 = (u32)(size_t)k.lds;
  k.tid = threadIdx.x; k.lane = k.tid & 63; k.w = __builtin_amdgcn_readfirstlane(k.tid >> 6);
  k.rs = __builtin_amdgcn_make_buffer_rsrc(ws, 0, WS_USED, 0x00020000);
  k.rso = __builtin_amdgcn_make_buffer_rsrc(out, 0, 2048 * 640 * 2, 0x00020000);
  k.rsnull = __builtin_amdgcn_make_buffer_rsrc(ws, 0, 0, 0x00020000);
  k.flags = (GAS u32*)ws;
  const int bid = blockIdx.x, xc = bid & 7, j = bid >> 3, mg = xc >> 1, half = xc & 1;
  k.m128 = 4 * mg + (j >> 3); k.p = 8 * half + (j & 7);
  k.m64 = 8 * mg + (j >> 2); k.c8 = 4 * half + (j & 3);
  const int p7 = j & 7;
  k.B0 = 54 * half + (p7 < 6 ? 7 * p7 : 42 + 6 * (p7 - 6));
  k.nblk = p7 < 6 ? 7 : 6;
  k.bid = bid;
  setup(k, bid, x0, wqkv, wo, wg, wu, wd);
  {
    PHASE_IDS();
    const u32 sQ = OFF_WQ + p * 163840;
    sfor<10>([&](auto I) { dma_q<0, 0, decltype(I)::value>(rs, l16, sQ, w, lds0); });
    sfor<10>([&](auto I) { dma_q<1, 1, decltype(I)::value>(rs, l16, sQ, w, lds0); });
    sfor<10>([&](auto I) { dma_q<2, 2, decltype(I)::value>(rs, l16, sQ, w, lds0); });
    const u32 token = 64 * m64 + 16 * w + t16;
    const u32 o = ((token >> 5) * 40 + 5 * c8) * 1024 + ((token & 31) + 32 * (g >> 1)) * 16 + (g & 1) * 8;
#pragma unroll
    for (int nb = 0; nb < 5; ++nb) ald2(k.xr[nb], rs, o, OFF_XF + nb * 1024);
  }
#pragma nounroll
  for (int it = 0; it < 2; ++it) { k.it = it; iteration(k); }
  if (bid == 0 && k.w == 0) {
#pragma unroll
    for (int r = 0; r < 4; ++r) poll_flags(k.flags, (FG_PF2 * 16 + 64 * r + k.lane) * 32, true, k.lane, k.tag);
    if (k.lane == 0) __hip_atomic_fetch_add((GAS u64*)(k.flags + FG_EPOCH * 512), 1ull, RLX_AGENT);
  }
}

extern "C" void kernel_launch(void* const* d_in, const int* in_sizes, int n_in, void* d_out, int out_size, void* d_ws, size_t ws_size,
                              hipStream_t stream) {
  const u16* x0 = (const u16*)d_in[0];
  const u16* wqkv = (const u16*)d_in[1];
  const u16* wo = (const u16*)d_in[2];
  const u16* wg = (const u16*)d_in[4];
  const u16* wu = (const u16*)d_in[5];
  const u16* wd = (const u16*)d_in[6];
  (void)hipFuncSetAttribute((const void*)main_kernel, hipFuncAttributeMaxDynamicSharedMemorySize, LDS_BYTES);
  main_kernel<<<256, 256, LDS_BYTES, stream>>>(x0, wqkv, wo, wg, wu, wd, (char*)d_ws, (u16*)d_out);
}
```

```cpp
#include <hip/hip_runtime.h>
#include <stdint.h>
#include <utility>

#define LAS __attribute__((address_space(3)))
#define GAS __attribute__((address_space(1)))
#define DI __device__ __forceinline__
typedef unsigned u32;
typedef unsigned short u16;
typedef unsigned long long u64;
typedef u32 u32x4 __attribute__((ext_vector_type(4)));
typedef u32 u32x2 __attribute__((ext_vector_type(2)));
typedef float f32x2 __attribute__((ext_vector_type(2)));
typedef float f32x4 __attribute__((ext_vector_type(4)));
typedef float f32x16 __attribute__((ext_vector_type(16)));
typedef short s16x8 __attribute__((ext_vector_type(8)));
typedef short s16x4 __attribute__((ext_vector_type(4)));
typedef __bf16 bf16x2 __attribute__((ext_vector_type(2)));
typedef __amdgpu_buffer_rsrc_t rsrc_t;

constexpr u32 FLAG_BYTES = 200 * 2048 + 34 * 2048;
constexpr u32 OFF_WQ = 483328;
constexpr u32 OFF_WO = OFF_WQ + 16 * 163840;
constexpr u32 OFF_WG = OFF_WO + 8 * 20 * 5 * 1024;
constexpr u32 OFF_WD = OFF_WG + 16 * 286720;
constexpr u32 OFF_CSQ = OFF_WD + 8 * 54 * 5 * 1024;
constexpr u32 OFF_CSG = OFF_CSQ + 16384;
constexpr u32 SZ_XF = 64 * 40 * 1024;
constexpr u32 OFF_XF = OFF_CSG + 28672;
constexpr u32 SZ_ST = 8 * 2048 * 8;
constexpr u32 OFF_ST = OFF_XF + 4 * SZ_XF;
constexpr u32 SZ_OF = 128 * 20 * 1024;
constexpr u32 OFF_OF = OFF_ST + 4 * SZ_ST;
constexpr u32 SZ_HF = 128 * 54 * 1024;
constexpr u32 OFF_HF = OFF_OF + 2 * SZ_OF;
constexpr u32 WS_USED = OFF_HF + 2 * SZ_HF;
constexpr u32 FG_O = 0, FG_X1 = 16, FG_H = 48, FG_X2 = 64, FG_PER_IT = 96;
constexpr u32 FG_TMO = 2 * FG_PER_IT;
constexpr u32 FG_EPOCH = FG_TMO + 1;
constexpr u32 FG_PF1 = FG_EPOCH + 1;
constexpr u32 FG_PF2 = FG_PF1 + 16;

constexpr u32 L_HALO = 122880;
constexpr u32 L_KST = 40960;
constexpr u32 L_VST = L_KST + 144 * 80;
constexpr u32 L_QST = L_VST + 144 * 80;
constexpr u32 L_RED = 100352;
constexpr u32 L_H4 = 143360;
constexpr u32 LDS_BYTES = 163840;

constexpr u32 AUX_SC1 = 16;
#define RLX_AGENT __ATOMIC_RELAXED, __HIP_MEMORY_SCOPE_AGENT
#define SB() __builtin_amdgcn_sched_barrier(0)
#define MEMFENCE() asm volatile("" ::: "memory")
#define WAITV(n) asm volatile("s_waitcnt vmcnt(%0)" ::"i"(n) : "memory")
#define WAITL0() asm volatile("s_waitcnt lgkmcnt(0)" ::: "memory")
#define BAR() do { MEMFENCE(); __builtin_amdgcn_s_barrier(); MEMFENCE(); } while (0)
#define CERT(tag) asm volatile("; CERT " tag ::: "memory")

template <int... I, class Fn> DI void sfor_impl(std::integer_sequence<int, I...>, Fn&& f) { (f(std::integral_constant<int, I>{}), ...); }
template <int N, class Fn> DI void sfor(Fn&& f) { sfor_impl(std::make_integer_sequence<int, N>{}, f); }
DI f32x16 mfma32(u32x4 a, u32x4 b, f32x16 c) { return __builtin_amdgcn_mfma_f32_32x32x16_bf16(__builtin_bit_cast(s16x8, a), __builtin_bit_cast(s16x8, b), c, 0, 0, 0); }
DI f32x4 mfma16(u32x4 a, u32x4 b, f32x4 c) { return __builtin_amdgcn_mfma_f32_16x16x32_bf16(__builtin_bit_cast(s16x8, a), __builtin_bit_cast(s16x8, b), c, 0, 0, 0); }
DI u32 cvtpk(float lo, float hi) { f32x2 v = {lo, hi}; bf16x2 b = __builtin_convertvector(v, bf16x2); return __builtin_bit_cast(u32, b); }
DI float u2f(u32 v) { return __builtin_bit_cast(float, v); }
DI u32 f2u(float v) { return __builtin_bit_cast(u32, v); }
DI float bflo(u32 v) { return __builtin_bit_cast(float, v << 16); }
DI float bfhi(u32 v) { return __builtin_bit_cast(float, v & 0xffff0000u); }
DI float fma_s(float a, float b, float c) { float r; asm("v_fma_f32 %0, %1, %2, %3" : "=v"(r) : "v"(a), "v"(b), "v"(c)); return r; }
DI float mul_s(float a, float b) { float r; asm("v_mul_f32_e32 %0, %1, %2" : "=v"(r) : "v"(a), "v"(b)); return r; }
DI float add_s(float a, float b) { float r; asm("v_add_f32_e32 %0, %1, %2" : "=v"(r) : "v"(a), "v"(b)); return r; }
DI float max_s(float a, float b) { float r; asm("v_max_f32_e32 %0, %1, %2" : "=v"(r) : "v"(a), "v"(b)); return r; }
DI float sel_s(float f, float t, unsigned long long m) { float r; asm("v_cndmask_b32_e64 %0, %1, %2, %3" : "=v"(r) : "v"(f), "v"(t), "s"(m)); return r; }
DI float red16_32_add(float v) {
  auto a = __builtin_amdgcn_permlane16_swap(f2u(v), f2u(v), false, false);
  v = u2f(a[0]) + u2f(a[1]);
  auto b = __builtin_amdgcn_permlane32_swap(f2u(v), f2u(v), false, false);
  return u2f(b[0]) + u2f(b[1]);
}
DI float red16_32_max(float v) {
  auto a = __builtin_amdgcn_permlane16_swap(f2u(v), f2u(v), false, false);
  v = __builtin_fmaxf(u2f(a[0]), u2f(a[1]));
  auto b = __builtin_amdgcn_permlane32_swap(f2u(v), f2u(v), false, false);
  return __builtin_fmaxf(u2f(b[0]), u2f(b[1]));
}
DI float red32_add(float v) {
  auto b = __builtin_amdgcn_permlane32_swap(f2u(v), f2u(v), false, false);
  return u2f(b[0]) + u2f(b[1]);
}
DI void stg_sc1(u32x4 v, rsrc_t rs, u32 voff, u32 soff) { __builtin_amdgcn_raw_buffer_store_b128(v, rs, voff, soff, AUX_SC1); }
DI void stg2_sc1(u32x2 v, rsrc_t rs, u32 voff, u32 soff) { __builtin_amdgcn_raw_buffer_store_b64(v, rs, voff, soff, AUX_SC1); }

template <int IMM> DI void ald_v_sc1(u32x4& d, rsrc_t rs, u32 voff, u32 soff) {
  asm volatile("buffer_load_dwordx4 %0, %1, %2, %3 offen offset:%4 sc1" : "=v"(d) : "v"(voff), "s"(rs), "s"(soff), "i"(IMM) : "memory");
}
template <int IMM> DI void ald_a(u32x4& d, rsrc_t rs, u32 voff, u32 soff) {
  asm volatile("buffer_load_dwordx4 %0, %1, %2, %3 offen offset:%4 sc1" : "=a"(d) : "v"(voff), "s"(rs), "s"(soff), "i"(IMM) : "memory");
}
template <int IMM> DI void ald_v(u32x4& d, rsrc_t rs, u32 voff, u32 soff) {
  asm volatile("buffer_load_dwordx4 %0, %1, %2, %3 offen offset:%4 sc1" : "=v"(d) : "v"(voff), "s"(rs), "s"(soff), "i"(IMM) : "memory");
}
DI void ald2_sc1(u32x2& d, rsrc_t rs, u32 voff, u32 soff) {
  asm volatile("buffer_load_dwordx2 %0, %1, %2, %3 offen sc1" : "=v"(d) : "v"(voff), "s"(rs), "s"(soff) : "memory");
}
DI void ald2(u32x2& d, rsrc_t rs, u32 voff, u32 soff) {
  asm volatile("buffer_load_dwordx2 %0, %1, %2, %3 offen sc1" : "=v"(d) : "v"(voff), "s"(rs), "s"(soff) : "memory");
}
DI void ald1(u32& d, rsrc_t rs, u32 voff, u32 soff) {
  asm volatile("buffer_load_dword %0, %1, %2, %3 offen" : "=v"(d) : "v"(voff), "s"(rs), "s"(soff) : "memory");
}
template <int IMM> DI void ald_a_sc1(u32x4& d, rsrc_t rs, u32 voff, u32 soff) {
  asm volatile("buffer_load_dwordx4 %0, %1, %2, %3 offen offset:%4 sc1" : "=a"(d) : "v"(voff), "s"(rs), "s"(soff), "i"(IMM) : "memory");
}
template <int N> DI void waitpin_a(u32x4& r) { asm volatile("s_waitcnt vmcnt(%1)" : "+a"(r) : "i"(N) : "memory"); }
template <int N> DI void waitpin_a2(u32x4& r0, u32x4& r1) { asm volatile("s_waitcnt vmcnt(%2)" : "+a"(r0), "+a"(r1) : "i"(N) : "memory"); }
DI void mma32(f32x16& acc, const u32x4& wv, const u32x4& xa) { asm volatile("v_mfma_f32_32x32x16_bf16 %0, %1, %2, %0" : "+v"(acc) : "v"(wv), "a"(xa)); }
DI void mma32_0(f32x16& acc, const u32x4& wv, const u32x4& xa) { asm volatile("v_mfma_f32_32x32x16_bf16 %0, %1, %2, 0" : "=&v"(acc) : "v"(wv), "a"(xa)); }
DI void mma32w(f32x16& acc, const u32x4& wv, const u32x4& xa, const u32x4& wn) { asm volatile("v_mfma_f32_32x32x16_bf16 %0, %1, %2, %0" : "+v"(acc) : "v"(wv), "a"(xa), "v"(wn)); }
DI void mma32w_0(f32x16& acc, const u32x4& wv, const u32x4& xa, const u32x4& wn) { asm volatile("v_mfma_f32_32x32x16_bf16 %0, %1, %2, 0" : "=&v"(acc) : "v"(wv), "a"(xa), "v"(wn)); }
DI void mma16(f32x4& acc, const u32x4& av, const u32x4& bv) { asm volatile("v_mfma_f32_16x16x32_bf16 %0, %1, %2, %0" : "+v"(acc) : "v"(av), "v"(bv)); }
DI void mma16_0(f32x4& acc, const u32x4& av, const u32x4& bv) { asm volatile("v_mfma_f32_16x16x32_bf16 %0, %1, %2, 0" : "=&v"(acc) : "v"(av), "v"(bv)); }
DI void pin_vv(u32x4& a, u32x4& b) { asm volatile("" : "+v"(a), "+v"(b)); }
template <int N> DI void waitpin_v(u32x4& r) { asm volatile("s_waitcnt vmcnt(%1)" : "+v"(r) : "i"(N) : "memory"); }
DI void pin_v(u32x4& r) { asm volatile("" : "+v"(r)::"memory"); }
DI void pin_a(u32x4& r) { asm volatile("" : "+a"(r)::"memory"); }
DI void pin2(u32x2& r) { asm volatile("" : "+v"(r)::"memory"); }
DI void pin1(u32& r) { asm volatile("" : "+v"(r)::"memory"); }
#define DMA_F(TAG, IMM, SC, rs, voff, soff, m0v) \
  asm volatile("s_mov_b32 m0, %2\n\ts_nop 0\n\tbuffer_load_dwordx4 %0, %1, %3 offen offset:%4 " SC "lds ; DMA " TAG ::"v"(voff), "s"(rs), "s"(m0v), "s"(soff), "i"(IMM) : "memory")
#define DMA_N(TAG, IMM, SC, rs, voff, soff) \
  asm volatile("buffer_load_dwordx4 %0, %1, %2 offen offset:%3 " SC "lds ; DMA " TAG ::"v"(voff), "s"(rs), "s"(soff), "i"(IMM) : "memory")

#define STAMP(i) do {} while (0)
#define RSTAMP(i) do {} while (0)
#define GSTAMP(i) STAMP(i)
#define QSTAMP(i) do {} while (0)
#define SSTAMP(i) do {} while (0)
constexpr u32 OFF_STAMP = WS_USED + 4096;
constexpr u32 kSpinCap = 1u << 16;

struct K {
  rsrc_t rs, rso, rsnull;
  LAS char* lds;
  u32 lds0;
  GAS u32* flags;
  int bid, lane, w, m128, p, m64, c8, B0, nblk, tid, it;
  u64 tag;
  u32x2 xr[5];
  u32x4 wo_r[5][5];
  u32x4 wd_r[14][5];
};

#define PHASE_IDS() int lane = k.lane; asm volatile("" : "+v"(lane)); \
  int it = k.it; asm volatile("" : "+s"(it)); \
  const u32 xin = OFF_XF + it * 2 * SZ_XF, stin = OFF_ST + it * 2 * SZ_ST, x1o = xin + SZ_XF, st1o = stin + SZ_ST; \
  const u32 ofo = OFF_OF + it * SZ_OF, hfo = OFF_HF + it * SZ_HF, fgb = it * FG_PER_IT; \
  (void)xin; (void)stin; (void)x1o; (void)st1o; (void)ofo; (void)hfo; (void)fgb; \
  int w = k.w, m128 = k.m128, p = k.p, m64 = k.m64, c8 = k.c8, B0 = k.B0; \
  asm volatile("" : "+s"(w), "+s"(m128), "+s"(p), "+s"(m64), "+s"(c8), "+s"(B0)); \
  const u32 l16 = lane * 16; const int tok = lane & 31, h = lane >> 5, t16 = lane & 15, g = lane >> 4; \
  const rsrc_t rs = k.rs; LAS char* const lds = k.lds; const u32 lds0 = k.lds0; \
  (void)tok; (void)h; (void)t16; (void)g; (void)l16; (void)m128; (void)p; (void)m64; (void)c8; (void)B0; (void)lds; (void)lds0; (void)rs

DI void flag_set(GAS u32* flags, u32 idx, u64 tag) { __hip_atomic_store((GAS u64*)(flags + idx), tag, RLX_AGENT); }
DI u64 flag_get(GAS u32* flags, u32 idx) { return __hip_atomic_load((GAS u64*)(flags + idx), RLX_AGENT); }
DI bool timed_out(GAS u32* flags, u64 tag) {
  const u64 v = flag_get(flags, FG_TMO * 512);
  return ((u64)(u32)__builtin_amdgcn_readfirstlane((u32)v) | ((u64)(u32)__builtin_amdgcn_readfirstlane((u32)(v >> 32)) << 32)) == tag;
}
DI void poll_flags(GAS u32* flags, u32 fo, bool act, int lane, u64 tag) {
  for (u32 spins = 0;;) {
    const u64 v = act ? flag_get(flags, fo) : tag;
    if (__all(v == tag)) break;
    __builtin_amdgcn_s_sleep(1);
    if (++spins > kSpinCap) {
      if (lane == 0) flag_set(flags, FG_TMO * 512, tag);
      break;
    }
    if ((spins & 255u) == 0u && timed_out(flags, tag)) break;
  }
}
#define POLL_GAP 2
struct Polls { u64 a, b, c; };
DI void poll_issue(u64& d, const GAS u32* p) { asm volatile("global_load_dwordx2 %0, %1, off sc1 ; POLL" : "=v"(d) : "v"(p) : "memory"); }
DI bool poll_check(u64& d, bool act, u64 want) { asm volatile("s_waitcnt vmcnt(2) ; POLLWAIT" : "+v"(d)::"memory"); return __all(!act || d == want); }
DI void poll_flags_p(GAS u32* flags, u32 fo, bool act, int lane, u64 want, Polls& pl) {
  const GAS u32* p = flags + (act ? fo : FG_TMO * 512);
  asm volatile("; POLL_BEGIN" ::: "memory");
  poll_issue(pl.a, p); __builtin_amdgcn_s_sleep(POLL_GAP); poll_issue(pl.b, p); __builtin_amdgcn_s_sleep(POLL_GAP); poll_issue(pl.c, p);
  for (u32 spins = 0;;) {
    if (poll_check(pl.a, act, want)) break;
    poll_issue(pl.a, p);
    if (poll_check(pl.b, act, want)) break;
    poll_issue(pl.b, p);
    if (poll_check(pl.c, act, want)) break;
    poll_issue(pl.c, p);
    if (++spins > kSpinCap) {
      if (lane == 0) flag_set(flags, FG_TMO * 512, want);
      break;
    }
    if ((spins & 63u) == 0u && timed_out(flags, want)) break;
  }
  asm volatile("; POLL_END" ::: "memory");
}
DI void poll_pin(Polls& pl) { asm volatile("; POLL_PIN" : "+v"(pl.a), "+v"(pl.b), "+v"(pl.c)::"memory"); }
DI void stats_finish(float s1, float s2, float& nmean, float& rstd) {
  const float mean = s1 * (1.0f / 640.0f);
  const float var = __builtin_fmaxf(s2 * (1.0f / 640.0f) - mean * mean, 0.f);
  nmean = -mean; rstd = __builtin_amdgcn_rsqf(var + 1e-5f);
}

template <int C, int SLOT, int I> DI void dma_q(rsrc_t rs, u32 l16, u32 sQ, int w, u32 lds0) {
#define Q_EMIT(TAG, FIRST, IMM, SO, MO) do { if constexpr (FIRST) DMA_F(TAG, IMM, "sc1 ", rs, l16, SO, MO); else DMA_N(TAG, IMM, "sc1 ", rs, l16, SO); } while (0)
  if constexpr (C < 3) {
    constexpr int grp = I / 4, imm = (I % 4) * 1024;
    const u32 so = sQ + w * 10240 + C * 40960 + grp * 4096, mo = lds0 + SLOT * 40960 + w * 10240 + grp * 4096;
    if constexpr (C == 0) Q_EMIT("Q0", I % 4 == 0, imm, so, mo); else if constexpr (C == 1) Q_EMIT("Q1", I % 4 == 0, imm, so, mo); else Q_EMIT("Q2", I % 4 == 0, imm, so, mo);
  } else {
    constexpr int hf = I / 5, ii = I % 5, grp = ii / 4, imm = (ii % 4) * 1024;
    const u32 so = sQ + w * 5120 + 122880 + hf * 20480 + grp * 4096, mo = lds0 + SLOT * 40960 + w * 5120 + hf * 20480 + grp * 4096;
    if constexpr (hf == 0) Q_EMIT("Q3a", ii % 4 == 0, imm, so, mo); else Q_EMIT("Q3b", ii % 4 == 0, imm, so, mo);
  }
#undef Q_EMIT
}
template <int HH, int I> DI void dma_g(rsrc_t rs, u32 l16, u32 sG, int w, u32 lds0) {
  constexpr int grp = I / 4, imm = (I % 4) * 1024;
  const u32 so = sG + HH * 20480 + grp * 4096, mo = lds0 + w * 5120 + (HH % 7) * 20480 + grp * 4096;
#define G_CASE(n) if constexpr (HH == n) { if constexpr (I % 4 == 0) DMA_F("G" #n, imm, "sc1 ", rs, l16, so, mo); else DMA_N("G" #n, imm, "sc1 ", rs, l16, so); }
  G_CASE(0) G_CASE(1) G_CASE(2) G_CASE(3) G_CASE(4) G_CASE(5) G_CASE(6) G_CASE(7) G_CASE(8) G_CASE(9) G_CASE(10) G_CASE(11) G_CASE(12) G_CASE(13)
#undef G_CASE
}

template <int HH> DI void dma_g3(rsrc_t rs, u32 l16, u32 sG0, int w, u32 lds0) {
  const u32 f0 = 7 * (w - 1);
  const u32 so = sG0 + HH * 20480 + f0 * 1024, mo = lds0 + (HH % 7) * 20480 + f0 * 1024;
#define G3_CASE(n) if constexpr (HH == n) { DMA_F("G" #n, 0, "sc1 ", rs, l16, so, mo); DMA_N("G" #n, 1024, "sc1 ", rs, l16, so); DMA_N("G" #n, 2048, "sc1 ", rs, l16, so); DMA_N("G" #n, 3072, "sc1 ", rs, l16, so); \
    DMA_F("G" #n, 0, "sc1 ", rs, l16, so + 4096, mo + 4096); DMA_N("G" #n, 1024, "sc1 ", rs, l16, so + 4096); if (w < 3) DMA_N("G" #n, 2048, "sc1 ", rs, l16, so + 4096); }
  G3_CASE(2) G3_CASE(3) G3_CASE(4) G3_CASE(5) G3_CASE(6)
#undef G3_CASE
}
constexpr int XW = 10;
struct QS {
  static constexpr bool x_at(int f) { return f < 120 && f % 3 == 0 && f / 3 + XW < 40; }
  static constexpr int aux_at(int f) { return (f < 32 && f % 3 == 1) ? f / 3 : -1; }
  static constexpr bool pf_at(int f) { return f == 85; }
  static constexpr int q3_at(int f) { return (f >= 77 && f <= 104 && f % 3 == 2) ? (f - 77) / 3 : -1; }
  static constexpr int wo_at(int f) {
    if (f >= 34 && f <= 67 && f % 3 == 1) return (f - 34) / 3;
    if (f >= 121 && f <= 157 && (f - 121) % 3 == 0) return 12 + (f - 121) / 3;
    return -1;
  }
  static constexpr int upto(int F) { int n = 0; for (int f = 0; f <= F; ++f) n += x_at(f) + (aux_at(f) >= 0) + pf_at(f) + (q3_at(f) >= 0) + (wo_at(f) >= 0); return n; }
  static constexpr int xidx(int i) { return i < XW ? i : XW + upto(3 * (i - XW)) - 1; }
  static constexpr int xwait2(int s) { return XW + upto(3 * s) - 1 - xidx(s + 1); }
};
struct GS {
  static constexpr int NWD = 65;
  static constexpr bool x_at(int f) { return f < 120 && f % 3 == 0 && f / 3 + XW < 40; }
  static constexpr int op(int F) {
    if (F <= 10 && F % 3 == 1) return 3000 + F / 3;
    if (F >= 40 && F <= 67 && F % 3 == 1) return (7 + (F - 40) / 15) * 8 + ((F - 40) % 15) / 3;
    if (F >= 79 && F <= 106 && F % 3 == 1) return (9 + (F - 79) / 15) * 8 + ((F - 79) % 15) / 3;
    if (F < 120 || (F & 1)) return -1;
    if (F < 140) return (11 + (F - 120) / 10) * 8 + ((F - 120) % 10) / 2;
    if (F < 160) return 1000 + (F - 140) / 2;
    if (F < 170) return 13 * 8 + (F - 160) / 2;
    if (F < 280) return 1010 + (F - 170) / 2;
    return -1;
  }
  static constexpr bool st_at(int F) { return F == 139 || F == 157 || F == 175 || F == 219 || F == 237; }
  static constexpr int upto(int F) { int n = 0; for (int f = 0; f <= F; ++f) n += x_at(f) + (op(f) >= 0) + st_at(f); return n; }
  static constexpr int slot(int o) { for (int f = 0; f < 300; ++f) if (op(f) == o) return f; return -1; }
  static constexpr int xidx(int i) { return i < XW ? i : XW + upto(3 * (i - XW)) - 1; }
  static constexpr int xwait2(int s) { return XW + upto(3 * s) - 1 - xidx(s + 1); }
};
static_assert(GS::op(278) == 1000 + GS::NWD - 1, "down-weight prefetch schedule");

DI void iteration(K& k) {
  GAS u32* const flags = k.flags;
  const int tid = k.tid;
  {
    PHASE_IDS();
    const u32 sQ = OFF_WQ + p * 163840, sG = OFF_WG + p * 286720 + w * 5120;
    STAMP(0);
    if (it > 0 && w == 0) {
      const int grp = 2 * m128 - 1 + (lane >> 3);
      const bool act = lane < 24 && grp >= 0;
      poll_flags(flags, ((FG_X2 + grp) * 16 + (lane & 7)) * 32, act, lane, k.tag);
    }
    WAITV(0); CERT("Q0"); CERT("Q1"); CERT("Q2");
    BAR();
    STAMP(1);
    u32x4 xf[40];
    const u32 xbase = xin + (4 * m128 + w) * 40960;
#define XLOAD(i) ald_a_sc1<((i) % 4) * 1024>(xf[i], rs, l16, xbase + ((i) / 4) * 4096)
    u32x2 hs0, hs1, sa0, sa1, sa2, sa3;
    const int rbh = m128 > 0 ? 4 * m128 - 1 : 0;
    const u32 hvo = (g >> 1) * 1024 + (g & 1) * 512 + (16 + t16) * 16;
    const int ht = 128 * m128 - 16 + t16;
    const u32 hso = ((2 * g) * 2048 + (ht < 0 ? 0 : ht)) * 8, sso = (4 * h * 2048 + 128 * m128 + 32 * w + tok) * 8;
    auto aux_load = [&](auto AC) {
      constexpr int a = decltype(AC)::value;
      if constexpr (a < 5) DMA_F("HALO", 0, "sc1 ", rs, hvo, xin + rbh * 40960 + (5 * w + a) * 2048, lds0 + L_HALO + (5 * w + a) * 1024);
      if constexpr (a == 5) ald2_sc1(sa0, rs, sso, stin);
      if constexpr (a == 6) ald2_sc1(sa1, rs, sso, stin + 16384);
      if constexpr (a == 7) ald2_sc1(sa2, rs, sso, stin + 32768);
      if constexpr (a == 8) ald2_sc1(sa3, rs, sso, stin + 49152);
      if constexpr (a == 9) ald2_sc1(hs0, rs, hso, stin);
      if constexpr (a == 10) ald2_sc1(hs1, rs, hso, stin + 16384);
    };
    sfor<XW>([&](auto I) { constexpr int i = decltype(I)::value; XLOAD(i); });
    using Q = QS;
    u32x2 pf2 = {0u, 0u};
    const int cgg = (B0 >> 1) + ((lane & 15) >> 1);
    const bool pfact = lane < 10 ? 32 * cgg < 16 * (B0 + k.nblk) : lane < 60;
    const u32 pfo = (FG_PF2 * 16 + (!pfact ? 0 : lane < 10 ? 124 + 54 * (lane & 1) + cgg : 10 * ((lane - 10) % 25) + (80 * c8) / 64 + (lane - 10) / 25)) * 128;
    constexpr int DQ = 4, NBQ = 6;
    u32x4 wb[NBQ];
    f32x16 acc0, acc1, acc2, acc3;
    f32x4 hacc, hacc4;
    u32x4 hw[3], hx[3], h4w[3], h4x[3];
    float rstd = 0.f, hrs = 0.f;
    u32 lb1 = l16 + 65536, lbh = l16 + L_HALO; asm volatile("" : "+v"(lb1), "+v"(lbh));
    u32 hwlo = (w >> 1) * 1024 + (g >> 1) * 3072 + (g & 1) * 512 + (16 * (w & 1) + t16) * 16, hwhi = hwlo + 61440;
    u32 hw4 = 2048 + (g >> 1) * 3072 + (g & 1) * 512 + t16 * 16 + w * 30720, hx4 = l16 + L_HALO + w * 5120;
    asm volatile("" : "+v"(hwlo), "+v"(hwhi), "+v"(hw4), "+v"(hx4));
    const u32 kvrow = L_KST + (16 + 32 * w + tok) * 80 + h * 8;
    const int hn = 16 * w + 4 * g;
    const u32 haddr = L_KST + t16 * 80 + 2 * hn + (hn >= 40 ? (L_VST - L_KST - 80) : 0);
    const u32 haddr4 = L_VST + t16 * 80 + 2 * (64 + 4 * g - 40);
    const bool hzero = (m128 == 0);
    const u32 wo_o = OFF_WO + (c8 * 20 + 5 * w) * 5120;
#define LDSQ(F) (((F) / 40 % 3) * 40960 + ((F) % 40) * 1024)
#define RDW(off) (*(const LAS u32x4*)(lds + ((off) < 65536 ? l16 : lb1) + ((off) & 65535)))
    u32 gp0 = 0;
#define STG_OFF(n0) ((n0) < 40 ? 2 * (n0) : (L_VST - L_KST) + 2 * ((n0) - 40))
    const u32 qrow = L_QST + (32 * w + tok) * 144 + h * 8, qrow2 = L_QST + (32 * w + tok) * 144 + (h ? 72 : 32);
#define Q_ADDR(n0) ((n0) == 96 ? qrow2 : qrow + ((n0) < 96 ? 2 * ((n0) - 80) : 2 * ((n0) - 80) + 32))
#define GRAN(t, ACC, q, RS, STADDR, ZERO) do { \
      if constexpr ((t) == 0) gp0 = cvtpk(mul_s(ACC[4 * (q)], RS), mul_s(ACC[4 * (q) + 1], RS)); \
      if constexpr ((t) == 1) { u32x2 pk_ = {gp0, cvtpk(mul_s(ACC[4 * (q) + 2], RS), mul_s(ACC[4 * (q) + 3], RS))}; if (ZERO) pk_ = u32x2{0u, 0u}; *(LAS u32x2*)(lds + (STADDR)) = pk_; } \
    } while (0)
#define GRAN_MAIN(t, ACC, q, n0) GRAN(t, ACC, q, rstd, ((n0) < 80 ? kvrow + STG_OFF(n0) : Q_ADDR(n0)), false)
#pragma unroll
    for (int F = 0; F < DQ; ++F) wb[F % NBQ] = RDW(LDSQ(F));
    sfor<160>([&](auto FC) {
      constexpr int F = decltype(FC)::value, Fr = F + DQ;
      if constexpr (F == 30) QSTAMP(21);
      if constexpr (F == 60) QSTAMP(22);
      if constexpr (F == 90) QSTAMP(23);
      if constexpr (F == 140) QSTAMP(26);
      if constexpr (Q::x_at(F)) XLOAD(F / 3 + XW);
      if constexpr (Q::aux_at(F) >= 0) aux_load(std::integral_constant<int, Q::aux_at(F)>{});
      if constexpr (Q::pf_at(F)) asm volatile("buffer_load_dwordx2 %0, %1, %2, 0 offen sc1" : "=v"(pf2) : "v"(pfo), "s"(rs) : "memory");
      if constexpr (Q::q3_at(F) >= 0) dma_q<3, 0, Q::q3_at(F)>(rs, l16, sQ, w, lds0);
      if constexpr (Q::wo_at(F) >= 0) {
        constexpr int i = Q::wo_at(F), s = i / 5, nb = i % 5;
        if constexpr (i < 24) ald_a<(i % 4) * 1024>(k.wo_r[s][nb], rs, l16, wo_o + (i / 4) * 4096); else ald_v<(i % 4) * 1024>(k.wo_r[s][nb], rs, l16, wo_o + (i / 4) * 4096);
      }
      if constexpr (Fr == 40) {
        WAITV(Q::upto(F) - Q::upto(13)); CERT("HALO"); BAR();
        if (tid == 0) flag_set(flags, (FG_PF2 * 16 + k.bid) * 32, k.tag);
      }
      if constexpr (Fr == 80) { WAITL0(); BAR(); }
      if constexpr (Fr == 120) {
        WAITV(Q::upto(F) - Q::upto(89)); CERT("Q3a");
        asm volatile("" : "+v"(pf2));
        if (w == 0 && it == 0 && !__all(!pfact || (pf2.x == (u32)k.tag && pf2.y == (u32)(k.tag >> 32)))) poll_flags(flags, pfo / 4, pfact, lane, k.tag);
        WAITL0(); BAR(); STAMP(2);
      }
      if constexpr (Fr == 140) { WAITV(Q::upto(F) - Q::upto(104)); CERT("Q3b"); BAR(); }
      if constexpr (Fr < 160) wb[Fr % NBQ] = RDW(LDSQ(Fr));
      if constexpr (F < 120) {
        constexpr int s = F / 3, b = F % 3;
        if constexpr (b == 0 && (s & 1) == 0) waitpin_a2<Q::xwait2(s)>(xf[s], xf[s + 1]);
#define QMMA(ACC) do { if constexpr (s == 0) { if constexpr ((F & 1) == 0) mma32w_0(ACC, wb[F % NBQ], xf[s], wb[(F + 1) % NBQ]); else mma32_0(ACC, wb[F % NBQ], xf[s]); } \
                       else { if constexpr ((F & 1) == 0) mma32w(ACC, wb[F % NBQ], xf[s], wb[(F + 1) % NBQ]); else mma32(ACC, wb[F % NBQ], xf[s]); } } while (0)
        if constexpr (b == 0) QMMA(acc0); else if constexpr (b == 1) QMMA(acc1); else QMMA(acc2);
        if constexpr (F == 62) {
          pin2(sa0); pin2(sa1); pin2(sa2); pin2(sa3);
          float nm;
          stats_finish(red32_add((u2f(sa0.x) + u2f(sa1.x)) + (u2f(sa2.x) + u2f(sa3.x))), red32_add((u2f(sa0.y) + u2f(sa1.y)) + (u2f(sa2.y) + u2f(sa3.y))), nm, rstd);
        }
        if constexpr (F == 110) {
          pin2(hs0); pin2(hs1);
          float nm;
          stats_finish(red16_32_add(u2f(hs0.x) + u2f(hs1.x)), red16_32_add(u2f(hs0.y) + u2f(hs1.y)), nm, hrs);
        }
        if constexpr (F == 113) {
#pragma unroll
          for (int nb = 0; nb < 5; ++nb) pin2(k.xr[nb]);
        }
        if constexpr (b == 1) {
          if constexpr (s == 15) mma16_0(hacc, hw[0], hx[0]);
          if constexpr (s > 15 && s <= 34) mma16(hacc, hw[(s - 15) % 3], hx[(s - 15) % 3]);
          if constexpr (s >= 13 && s <= 32) {
            constexpr int sg = s - 13;
            hw[sg % 3] = *(const LAS u32x4*)(lds + (sg < 10 ? hwlo : hwhi) + (sg % 10) * 6144);
            hx[sg % 3] = *(const LAS u32x4*)(lds + lbh + sg * 1024);
          }
        }
      } else {
        constexpr int s = F - 120;
        if constexpr (s == 0) mma32w_0(acc3, wb[F % NBQ], xf[s], wb[(F + 1) % NBQ]); else if constexpr (s & 1) mma32(acc3, wb[F % NBQ], xf[s]); else mma32w(acc3, wb[F % NBQ], xf[s], wb[(F + 1) % NBQ]);
        constexpr int ms = s - 2, gi = ms / 2, st = ms % 2;
        if constexpr (ms >= 0 && gi < 12) {
          constexpr int b = gi >> 2, q = gi & 3, n0 = 32 * b + 8 * q;
          if constexpr (b == 0) GRAN_MAIN(st, acc0, q, n0); else if constexpr (b == 1) GRAN_MAIN(st, acc1, q, n0); else GRAN_MAIN(st, acc2, q, n0);
        }
        if constexpr (s == 22) {
          const f32x4 p0 = *(const LAS f32x4*)(lds + L_H4 + l16), p1 = *(const LAS f32x4*)(lds + L_H4 + 1024 + l16);
          const f32x4 p2 = *(const LAS f32x4*)(lds + L_H4 + 2048 + l16), p3 = *(const LAS f32x4*)(lds + L_H4 + 3072 + l16);
          hacc4 = (p0 + p1) + (p2 + p3);
        }
        if constexpr (ms >= 0 && gi == 12) GRAN(st, hacc, 0, hrs, haddr, hzero);
        if constexpr (ms >= 0 && gi == 14) GRAN(st, hacc4, 0, hrs, haddr4, hzero);
        if constexpr (s == 33) *(LAS u32x2*)(lds + L_QST + (32 * w + tok) * 144 + (h ? 64 : 40)) = u32x2{0u, 0u};
        if constexpr (s == 35) *(LAS u32x2*)(lds + L_QST + (32 * w + tok) * 144 + (h ? 112 : 48)) = u32x2{0u, 0u};
        if constexpr (s == 37) *(LAS u32x2*)(lds + L_QST + (32 * w + tok) * 144 + (h ? 120 : 56)) = u32x2{0u, 0u};
      }
      if constexpr (F < 120 && F % 3 == 2) {
        constexpr int s = F / 3;
        if constexpr (s == 15) mma16_0(hacc4, h4w[0], h4x[0]);
        if constexpr (s > 15 && s <= 19) mma16(hacc4, h4w[(s - 15) % 3], h4x[(s - 15) % 3]);
        if constexpr (s >= 13 && s <= 17) {
          h4w[(s - 13) % 3] = *(const LAS u32x4*)(lds + hw4 + (s - 13) * 6144);
          h4x[(s - 13) % 3] = *(const LAS u32x4*)(lds + hx4 + (s - 13) * 1024);
        }
        if constexpr (s == 21) *(LAS f32x4*)(lds + L_H4 + w * 1024 + l16) = hacc4;
      }
      SB();
    });
    STAMP(3);
    asm volatile("s_nop 11" : "+v"(acc3));
    GRAN_MAIN(0, acc3, 0, 96); GRAN_MAIN(1, acc3, 0, 96); GRAN_MAIN(0, acc3, 1, 104); GRAN_MAIN(1, acc3, 1, 104); GRAN_MAIN(0, acc3, 2, 112); GRAN_MAIN(1, acc3, 2, 112);
    WAITL0(); BAR();
    STAMP(4);
    {
      const int par = t16 & 1;
      const bool ca = (4 * g + par) > t16, cb = (4 * g + 2 + par) > t16;
      const unsigned long long mca = __ballot(ca), mcb = __ballot(cb), mpar = __ballot(par != 0);
      const bool neg = (m128 == 0) && (w == 0);
      const unsigned long long nga = neg ? mca : 0ull, ngb = neg ? mcb : 0ull;
      const u32 gq = g < 2 ? g : 2;
      const u32 pla = par ? 0x01000c0cu : 0x0c0c0100u, plb = par ? 0x03020c0cu : 0x0c0c0302u;
      const u32 sel0 = ca ? pla : 0x0c0c0c0cu, sel2 = ca ? 0x0c0c0c0cu : pla, sel1 = cb ? plb : 0x0c0c0c0cu, sel3 = cb ? 0x0c0c0c0cu : plb;
      constexpr float cexp = 0.22360679774997896f * 1.4426950408889634f;
      float cx = cexp; asm volatile("" : "+v"(cx));
      const u32 qa = L_QST + (32 * w + t16) * 144 + g * 16, ka = L_KST + (32 * w + t16) * 80 + gq * 16;
      const u32 va = L_VST + (32 * w + 4 * g + (t16 >> 2)) * 80 + (t16 & 3) * 8;
      u32x4 qf[4], k0[4], k1[4];
      u32x4 v0[4], v1[4];
#pragma unroll
      for (int u = 0; u < 4; ++u) {
        const u32 o = (u >> 1) * 16 * 80 + (u & 1) * 32;
        qf[u] = *(const LAS u32x4*)(lds + qa + (u >> 1) * 16 * 144 + (u & 1) * 64);
        k0[u] = *(const LAS u32x4*)(lds + ka + o);
        k1[u] = *(const LAS u32x4*)(lds + ka + o + 16 * 80);
      }
      dma_g<0, 0>(rs, l16, sG, w, lds0);
      f32x4 s0[4], s1[4];
      asm volatile("s_nop 1\n\t"
                   "v_mfma_f32_16x16x32_bf16 %0, %8, %16, 0\n\tv_mfma_f32_16x16x32_bf16 %1, %12, %16, 0\n\t"
                   "v_mfma_f32_16x16x32_bf16 %2, %9, %17, 0\n\tv_mfma_f32_16x16x32_bf16 %3, %13, %17, 0\n\t"
                   "v_mfma_f32_16x16x32_bf16 %4, %10, %18, 0\n\tv_mfma_f32_16x16x32_bf16 %5, %14, %18, 0\n\t"
                   "v_mfma_f32_16x16x32_bf16 %6, %11, %19, 0\n\tv_mfma_f32_16x16x32_bf16 %7, %15, %19, 0"
                   : "=&v"(s0[0]), "=&v"(s1[0]), "=&v"(s0[1]), "=&v"(s1[1]), "=&v"(s0[2]), "=&v"(s1[2]), "=&v"(s0[3]), "=&v"(s1[3])
                   : "v"(k0[0]), "v"(k0[1]), "v"(k0[2]), "v"(k0[3]), "v"(k1[0]), "v"(k1[1]), "v"(k1[2]), "v"(k1[3]), "v"(qf[0]), "v"(qf[1]), "v"(qf[2]), "v"(qf[3]));
#pragma unroll
      for (int u = 0; u < 4; ++u) {
        const u32 o = (u >> 1) * 16 * 80 + (u & 1) * 40;
        const u32x2 t0 = __builtin_bit_cast(u32x2, __builtin_amdgcn_ds_read_tr16_b64_v4i16((LAS s16x4*)(lds + va + o)));
        const u32x2 t1 = __builtin_bit_cast(u32x2, __builtin_amdgcn_ds_read_tr16_b64_v4i16((LAS s16x4*)(lds + va + o + 16 * 80)));
        const u32x2 t2 = __builtin_bit_cast(u32x2, __builtin_amdgcn_ds_read_tr16_b64_v4i16((LAS s16x4*)(lds + va + o + 32)));
        const u32x2 t3 = __builtin_bit_cast(u32x2, __builtin_amdgcn_ds_read_tr16_b64_v4i16((LAS s16x4*)(lds + va + o + 32 + 16 * 80)));
        v0[u] = u32x4{t0.x, t0.y, t1.x, t1.y}; v1[u] = u32x4{t2.x, t2.y, t3.x, t3.y};
      }
      dma_g<0, 1>(rs, l16, sG, w, lds0);
      asm volatile("s_nop 7" : "+v"(s0[0]), "+v"(s1[0]), "+v"(s0[1]), "+v"(s1[1]), "+v"(s0[2]), "+v"(s1[2]), "+v"(s0[3]), "+v"(s1[3]));
      float a[4], b[4], mx[4], sm[4];
#pragma unroll
      for (int u = 0; u < 4; ++u) {
        const float x00 = s0[u][0], x01 = s0[u][1], x02 = s0[u][2], x03 = s0[u][3], x10 = s1[u][0], x11 = s1[u][1], x12 = s1[u][2], x13 = s1[u][3];
        a[u] = sel_s(sel_s(x10, x11, mpar), sel_s(x00, x01, mpar), mca);
        b[u] = sel_s(sel_s(x12, x13, mpar), sel_s(x02, x03, mpar), mcb);
        if (u < 2) { a[u] = sel_s(a[u], -__builtin_inff(), nga); b[u] = sel_s(b[u], -__builtin_inff(), ngb); }
        mx[u] = max_s(a[u], b[u]);
      }
      dma_g<0, 2>(rs, l16, sG, w, lds0);
      {
        auto r01 = __builtin_amdgcn_permlane16_swap(f2u(mx[0]), f2u(mx[1]), false, false), r23 = __builtin_amdgcn_permlane16_swap(f2u(mx[2]), f2u(mx[3]), false, false);
        const float c01 = __builtin_fmaxf(u2f(r01[0]), u2f(r01[1])), c23 = __builtin_fmaxf(u2f(r23[0]), u2f(r23[1]));
        auto rt = __builtin_amdgcn_permlane32_swap(f2u(c01), f2u(c23), false, false);
        const u32 t = f2u(__builtin_fmaxf(u2f(rt[0]), u2f(rt[1])));
        dma_g<0, 3>(rs, l16, sG, w, lds0);
        auto ab = __builtin_amdgcn_permlane32_swap(t, t, false, false);
        auto m01 = __builtin_amdgcn_permlane16_swap(ab[0], ab[0], false, false), m23 = __builtin_amdgcn_permlane16_swap(ab[1], ab[1], false, false);
        mx[0] = u2f(m01[0]); mx[1] = u2f(m01[1]); mx[2] = u2f(m23[0]); mx[3] = u2f(m23[1]);
      }
      dma_g<0, 4>(rs, l16, sG, w, lds0);
#pragma unroll
      for (int u = 0; u < 4; ++u) {
        const float t = -mul_s(mx[u], cx);
        a[u] = __builtin_amdgcn_exp2f(fma_s(a[u], cx, t)); b[u] = __builtin_amdgcn_exp2f(fma_s(b[u], cx, t));
        sm[u] = a[u] + b[u];
      }
      dma_g<1, 0>(rs, l16, sG, w, lds0);
      {
        auto r01 = __builtin_amdgcn_permlane16_swap(f2u(sm[0]), f2u(sm[1]), false, false), r23 = __builtin_amdgcn_permlane16_swap(f2u(sm[2]), f2u(sm[3]), false, false);
        const float c01 = u2f(r01[0]) + u2f(r01[1]), c23 = u2f(r23[0]) + u2f(r23[1]);
        auto rt = __builtin_amdgcn_permlane32_swap(f2u(c01), f2u(c23), false, false);
        const u32 t = f2u(__builtin_amdgcn_rcpf(u2f(rt[0]) + u2f(rt[1])));
        dma_g<1, 1>(rs, l16, sG, w, lds0);
        auto ab = __builtin_amdgcn_permlane32_swap(t, t, false, false);
        auto m01 = __builtin_amdgcn_permlane16_swap(ab[0], ab[0], false, false), m23 = __builtin_amdgcn_permlane16_swap(ab[1], ab[1], false, false);
        sm[0] = u2f(m01[0]); sm[1] = u2f(m01[1]); sm[2] = u2f(m23[0]); sm[3] = u2f(m23[1]);
      }
      dma_g<1, 2>(rs, l16, sG, w, lds0);
      u32x4 pf[4];
#pragma unroll
      for (int u = 0; u < 4; ++u) {
        const float inv = sm[u];
        const u32 pk = cvtpk(a[u] * inv, b[u] * inv);
        pf[u] = u32x4{__builtin_amdgcn_perm(pk, pk, sel0), __builtin_amdgcn_perm(pk, pk, sel1), __builtin_amdgcn_perm(pk, pk, sel2), __builtin_amdgcn_perm(pk, pk, sel3)};
      }
      dma_g<1, 3>(rs, l16, sG, w, lds0);
      f32x4 o0[4], o1[4];
      asm volatile("s_nop 1\n\t"
                   "v_mfma_f32_16x16x32_bf16 %0, %8, %16, 0\n\tv_mfma_f32_16x16x32_bf16 %1, %12, %16, 0\n\t"
                   "v_mfma_f32_16x16x32_bf16 %2, %9, %17, 0\n\tv_mfma_f32_16x16x32_bf16 %3, %13, %17, 0\n\t"
                   "v_mfma_f32_16x16x32_bf16 %4, %10, %18, 0\n\tv_mfma_f32_16x16x32_bf16 %5, %14, %18, 0\n\t"
                   "v_mfma_f32_16x16x32_bf16 %6, %11, %19, 0\n\tv_mfma_f32_16x16x32_bf16 %7, %15, %19, 0\n\ts_nop 7"
                   : "=&v"(o0[0]), "=&v"(o1[0]), "=&v"(o0[1]), "=&v"(o1[1]), "=&v"(o0[2]), "=&v"(o1[2]), "=&v"(o0[3]), "=&v"(o1[3])
                   : "v"(v0[0]), "v"(v0[1]), "v"(v0[2]), "v"(v0[3]), "v"(v1[0]), "v"(v1[1]), "v"(v1[2]), "v"(v1[3]), "v"(pf[0]), "v"(pf[1]), "v"(pf[2]), "v"(pf[3]));
      dma_g<1, 4>(rs, l16, sG, w, lds0);
      const u32 sg = 5 * p + g, sg4 = 5 * p + 4;
      const u32 oa = (sg >> 2) * 1024 + (t16 + 16 * (sg & 3)) * 16, oa4 = (sg4 >> 2) * 1024 + (t16 + 16 * (sg4 & 3)) * 16;
#pragma unroll
      for (int qb = 0; qb < 2; ++qb) {
        const u32 so = ofo + (8 * m128 + 2 * w + qb) * 20480;
        const f32x4 A0 = o0[2 * qb], B0_ = o0[2 * qb + 1], A1 = o1[2 * qb], B1 = o1[2 * qb + 1];
        stg_sc1(u32x4{cvtpk(A0[0], A0[1]), cvtpk(A0[2], A0[3]), cvtpk(B0_[0], B0_[1]), cvtpk(B0_[2], B0_[3])}, rs, oa, so);
        if (g == 0) stg_sc1(u32x4{cvtpk(A1[0], A1[1]), cvtpk(A1[2], A1[3]), cvtpk(B1[0], B1[1]), cvtpk(B1[2], B1[3])}, rs, oa4, so);
      }
    }
    SB();
    STAMP(5);
    WAITV(0); CERT("G0"); CERT("G1");
    WAITL0();
    BAR();
    if (tid == 0) flag_set(flags, ((fgb + FG_O + m128) * 16 + p) * 32, k.tag);
    STAMP(6); RSTAMP(0);
    sfor<5>([&](auto I) { dma_g<2, decltype(I)::value>(rs, l16, sG, w, lds0); });
    sfor<5>([&](auto I) { dma_g<3, decltype(I)::value>(rs, l16, sG, w, lds0); });
  }
  {
    PHASE_IDS();
    if (w == 0) poll_flags(flags, ((fgb + FG_O + (m64 >> 1)) * 16 + (lane & 15)) * 32, lane < 16, lane, k.tag);
    BAR();
    STAMP(7); RSTAMP(1);
#pragma unroll
    for (int s = 0; s < 5; ++s)
#pragma unroll
      for (int nb = 0; nb < 5; ++nb) { if (s * 5 + nb < 24) pin_a(k.wo_r[s][nb]); else pin_v(k.wo_r[s][nb]); }
    u32x4 af[20];
    u32 tbo[4];
#pragma unroll
    for (int tbi = 0; tbi < 4; ++tbi) tbo[tbi] = ofo + ((4 * m64 + ((w + 1 + tbi) & 3)) * 20 + 5 * w) * 1024;
    constexpr int OW = 10;
#define OLOAD(q) ald_v_sc1<((q) % 5 % 4) * 1024>(af[q], rs, l16, tbo[(q) / 5] + ((q) % 5 / 4) * 4096)
    sfor<OW>([&](auto I) { constexpr int q = decltype(I)::value; OLOAD(q); });
    f32x4 acc[2][5];
    f32x4 part[3][5];
    sfor<20>([&](auto QC) {
      constexpr int q = decltype(QC)::value, tbi = q / 5, s = q % 5;
      if constexpr (q + OW < 20) OLOAD(q + OW);
      waitpin_v<(q < 10 ? OW : (19 - q))>(af[q]);
#pragma unroll
      for (int nb = 0; nb < 5; ++nb) acc[tbi & 1][nb] = mfma16(k.wo_r[s][nb], af[q], s == 0 ? f32x4{0.f, 0.f, 0.f, 0.f} : acc[tbi & 1][nb]);
      if constexpr (tbi > 0 && s < 3) {
        const u32 tbp = (w + tbi) & 3;
        *(LAS f32x4*)(lds + L_RED + ((tbp * 3 + (3 - tbi)) * 5 + 2 * s) * 1024 + l16) = acc[(tbi - 1) & 1][2 * s];
        if constexpr (2 * s + 1 < 5) *(LAS f32x4*)(lds + L_RED + ((tbp * 3 + (3 - tbi)) * 5 + 2 * s + 1) * 1024 + l16) = acc[(tbi - 1) & 1][2 * s + 1];
      }
      if constexpr (q == 18) {
        WAITL0(); BAR();
#pragma unroll
        for (int sl = 0; sl < 3; ++sl)
#pragma unroll
          for (int nb = 0; nb < 5; ++nb) part[sl][nb] = *(const LAS f32x4*)(lds + L_RED + ((w * 3 + sl) * 5 + nb) * 1024 + l16);
      }
      SB();
    });
    STAMP(8);
    float s1 = 0.f, s2 = 0.f;
#pragma unroll
    for (int nb = 0; nb < 5; ++nb) {
      const f32x4 v = (acc[1][nb] + part[0][nb]) + (part[1][nb] + part[2][nb]);
      const u32 a = cvtpk(bflo(k.xr[nb].x) + v[0], bfhi(k.xr[nb].x) + v[1]), b = cvtpk(bflo(k.xr[nb].y) + v[2], bfhi(k.xr[nb].y) + v[3]);
      k.xr[nb] = u32x2{a, b};
    }
    const u32 token = 64 * m64 + 16 * w + t16;
    const u32 xo = ((token >> 5) * 40 + 5 * c8) * 1024 + ((token & 31) + 32 * (g >> 1)) * 16;
#pragma unroll
    for (int pr = 0; pr < 3; ++pr) {
      const int A = 2 * pr, Bn = pr < 2 ? 2 * pr + 1 : 4;
      auto r0 = __builtin_amdgcn_permlane16_swap(k.xr[A].x, k.xr[Bn].x, false, false);
      auto r1 = __builtin_amdgcn_permlane16_swap(k.xr[A].y, k.xr[Bn].y, false, false);
      const u32x4 v = {r0[0], r1[0], r0[1], r1[1]};
      if (pr < 2) stg_sc1(v, rs, xo + (A + (g & 1)) * 1024, x1o);
      else if ((g & 1) == 0) stg_sc1(v, rs, xo + 4 * 1024, x1o);
    }
#pragma unroll
    for (int nb = 0; nb < 5; ++nb) {
      const float a = bflo(k.xr[nb].x), b = bfhi(k.xr[nb].x), c = bflo(k.xr[nb].y), d = bfhi(k.xr[nb].y);
      s1 += (a + b) + (c + d); s2 += (a * a + b * b) + (c * c + d * d);
    }
    s1 = red16_32_add(s1); s2 = red16_32_add(s2);
    if (g == 0) stg2_sc1(u32x2{f2u(s1), f2u(s2)}, rs, (c8 * 2048 + token) * 8, st1o);
    SB();
    STAMP(9);
    WAITV(0); CERT("G2"); CERT("G3");
    WAITL0();
    BAR();
    if (tid == 0) flag_set(flags, ((fgb + FG_X1 + m64) * 16 + c8) * 32, k.tag);
    STAMP(10); RSTAMP(2);
  }
  {
    PHASE_IDS();
    const int nblk = k.nblk;
    const int ds0 = w < 2 ? 14 * w : 28 + 13 * (w - 2);
    const u32 sG = OFF_WG + p * 286720 + w * 5120;
    Polls pl = {0ull, 0ull, 0ull};
    if (w != 0) { dma_g3<4>(rs, l16, sG - w * 5120, w, lds0); dma_g3<5>(rs, l16, sG - w * 5120, w, lds0); dma_g3<6>(rs, l16, sG - w * 5120, w, lds0); }
    if (w == 0) poll_flags_p(flags, ((fgb + FG_X1 + 2 * m128 + ((lane >> 3) & 1)) * 16 + (lane & 7)) * 32, lane < 16, lane, k.tag, pl);
    BAR();
    STAMP(11); RSTAMP(3);
    u32x2 sa0, sa1, sa2, sa3;
    u32x4 xf[40];
    const u32 xbase = x1o + (4 * m128 + w) * 40960;
    const u32 sso = (4 * h * 2048 + 128 * m128 + 32 * w + tok) * 8;
    sfor<XW>([&](auto I) { constexpr int i = decltype(I)::value; XLOAD(i); });
    constexpr int NWD = GS::NWD;
    float r2 = 0.f;
    constexpr int DG = 4, NBG = 6;
    u32x4 wb[NBG];
    f32x16 aA0, aA1, aA2, aB0, aB1;
    u32 lb1 = l16 + 65536, lb2 = l16 + 131072; asm volatile("" : "+v"(lb1), "+v"(lb2));
    const u32 hbase = (8 * m128 + 2 * w + (tok >> 4)) * 54 * 1024 + ((tok & 15) + 16 * h) * 16;
    const u32 hlast = nblk == 7 ? hbase : 0x7fff0000u;
    const u32 wd_o = OFF_WD + (c8 * 54 + ds0) * 5120;
#define LDSG(F) ((((F) / 20) % 7) * 20480 + ((F) % 20) * 1024)
#define RDG(off) (*(const LAS u32x4*)(lds + ((off) < 65536 ? l16 : (off) < 131072 ? lb1 : lb2) + ((off) & 65535)))
    float hv0 = 0.f;
    u32 hp[4];
#define GU_STAGE(ACC, idx, t) do { \
      if constexpr ((t) < 8) { const float hv_ = mul_s(mul_s(ACC[t], r2), ACC[8 + (t)]); if constexpr ((t) & 1) hp[(t) >> 1] = cvtpk(hv0, hv_); else hv0 = hv_; } \
      if constexpr ((t) == 8) { const u32 fb_ = B0 + (idx); stg_sc1(u32x4{hp[0], hp[1], hp[2], hp[3]}, rs, ((idx) == 6 ? hlast : hbase) + (fb_ & 1) * 512, hfo + (fb_ >> 1) * 1024); } \
    } while (0)
    auto wd_load = [&](auto OC) {
      constexpr int i = decltype(OC)::value, s = i / 5, nb = i % 5;
      if constexpr (i < 24 || i >= 50) ald_a<(i % 4) * 1024>(k.wd_r[s][nb], rs, l16, wd_o + (i / 4) * 4096);
      else ald_v<(i % 4) * 1024>(k.wd_r[s][nb], rs, l16, wd_o + (i / 4) * 4096);
    };
#pragma unroll
    for (int F = 0; F < DG; ++F) wb[F % NBG] = RDG(LDSG(F));
    sfor<280>([&](auto FC) {
      constexpr int F = decltype(FC)::value, Fr = F + DG;
      if constexpr (GS::x_at(F)) XLOAD(F / 3 + XW);
      if constexpr (GS::op(F) >= 0 && GS::op(F) < 1000) dma_g<GS::op(F) / 8, GS::op(F) % 8>(rs, l16, sG, w, lds0);
      if constexpr (GS::op(F) >= 1000 && GS::op(F) < 2000) wd_load(std::integral_constant<int, GS::op(F) - 1000>{});
      if constexpr (GS::op(F) == 3000) ald2_sc1(sa0, rs, sso, st1o);
      if constexpr (GS::op(F) == 3001) ald2_sc1(sa1, rs, sso, st1o + 16384);
      if constexpr (GS::op(F) == 3002) ald2_sc1(sa2, rs, sso, st1o + 32768);
      if constexpr (GS::op(F) == 3003) ald2_sc1(sa3, rs, sso, st1o + 49152);
      if constexpr (Fr == 40) { WAITL0(); BAR(); }
      if constexpr (Fr == 80) { CERT("G4"); CERT("G5"); WAITL0(); BAR(); }
      if constexpr (Fr == 120) { WAITV(GS::upto(F) - GS::upto(GS::slot(7 * 8 + 4))); CERT("G6"); CERT("G7"); WAITL0(); BAR(); STAMP(12); }
      if constexpr (Fr == 160) { GSTAMP(23); WAITV(GS::upto(F) - GS::upto(GS::slot(9 * 8 + 4))); CERT("G8"); CERT("G9"); WAITL0(); BAR(); GSTAMP(24); }
      if constexpr (Fr == 200) { GSTAMP(25); WAITV(GS::upto(F) - GS::upto(GS::slot(11 * 8 + 4))); CERT("G10"); CERT("G11"); BAR(); GSTAMP(26); }
      if constexpr (Fr == 240) { WAITV(GS::upto(F) - GS::upto(GS::slot(13 * 8 + 4))); CERT("G12"); CERT("G13"); BAR(); STAMP(13); }
      if constexpr (Fr < 280) wb[Fr % NBG] = RDG(LDSG(Fr));
      constexpr int s = F < 120 ? F / 3 : F < 200 ? (F - 120) / 2 : (F - 200) / 2, b = F < 120 ? F % 3 : F & 1;
      if constexpr (F < 120 && b == 0 && (s & 1) == 0) waitpin_a2<GS::xwait2(s)>(xf[s], xf[s + 1]);
#define GMMA(ACC) do { if constexpr (s == 0) { if constexpr ((F & 1) == 0) mma32w_0(ACC, wb[F % NBG], xf[s], wb[(F + 1) % NBG]); else mma32_0(ACC, wb[F % NBG], xf[s]); } \
                       else { if constexpr ((F & 1) == 0) mma32w(ACC, wb[F % NBG], xf[s], wb[(F + 1) % NBG]); else mma32(ACC, wb[F % NBG], xf[s]); } } while (0)
      if constexpr (F < 120) { if constexpr (b == 0) GMMA(aA0); else if constexpr (b == 1) GMMA(aA1); else GMMA(aA2); }
      else if constexpr (F < 200) { if constexpr (b == 0) GMMA(aB0); else GMMA(aB1); }
      else { if constexpr (b == 0) GMMA(aA0); else GMMA(aA1); }
      if constexpr (F == 3) poll_pin(pl);
      if constexpr (F == 62) {
        pin2(sa0); pin2(sa1); pin2(sa2); pin2(sa3);
        float rstd, nm;
        stats_finish(red32_add((u2f(sa0.x) + u2f(sa1.x)) + (u2f(sa2.x) + u2f(sa3.x))), red32_add((u2f(sa0.y) + u2f(sa1.y)) + (u2f(sa2.y) + u2f(sa3.y))), nm, rstd);
        r2 = rstd * rstd;
      }
      if constexpr ((F & 1) && F >= 123 && F <= 139) GU_STAGE(aA0, 0, (F - 123) / 2);
      if constexpr ((F & 1) && F >= 141 && F <= 157) GU_STAGE(aA1, 1, (F - 141) / 2);
      if constexpr ((F & 1) && F >= 159 && F <= 175) GU_STAGE(aA2, 2, (F - 159) / 2);
      if constexpr ((F & 1) && F >= 203 && F <= 219) GU_STAGE(aB0, 3, (F - 203) / 2);
      if constexpr ((F & 1) && F >= 221 && F <= 237) GU_STAGE(aB1, 4, (F - 221) / 2);
      SB();
    });
    asm volatile("s_nop 11" : "+v"(aA0), "+v"(aA1));
    sfor<9>([&](auto T) { GU_STAGE(aA0, 5, decltype(T)::value); });
    sfor<9>([&](auto T) { GU_STAGE(aA1, 6, decltype(T)::value); });
    SB();
    STAMP(14);
    WAITV(0);
    WAITL0();
    BAR();
    if (tid == 0) flag_set(flags, ((fgb + FG_H + m128) * 16 + p) * 32, k.tag);
    {
      const u32 wd_o = OFF_WD + (c8 * 54 + ds0) * 5120;
      const u32 l13 = w < 2 ? l16 : 0x7fff0000u;
      static_assert(GS::NWD == 65, "the fragments of step 13");
      sfor<70 - GS::NWD>([&](auto I) { constexpr int i = GS::NWD + decltype(I)::value; ald_a<(i % 4) * 1024>(k.wd_r[i / 5][i % 5], rs, l13, wd_o + (i / 4) * 4096); });
    }
    STAMP(15); RSTAMP(4);
  }
  {
    PHASE_IDS();
    const int ds0 = w < 2 ? 14 * w : 28 + 13 * (w - 2);
    const u32 sQ = OFF_WQ + p * 163840;
    STAMP(16);
    if (w == 0) poll_flags(flags, ((fgb + FG_H + (m64 >> 1)) * 16 + (lane & 15)) * 32, lane < 16, lane, k.tag);
    else WAITV(0);
    BAR();
    STAMP(17); RSTAMP(5);
    sfor<70>([&](auto I) { constexpr int i = decltype(I)::value; if constexpr (i < 24 || i >= 50) pin_a(k.wd_r[i / 5][i % 5]); else pin_v(k.wd_r[i / 5][i % 5]); });
    constexpr int DH = 12;
    u32x4 af[DH];
    const bool s13 = w < 2;
    u32 tbo[4];
#pragma unroll
    for (int tbi = 0; tbi < 4; ++tbi) tbo[tbi] = hfo + ((4 * m64 + ((w + 1 + tbi) & 3)) * 54 + ds0) * 1024;
const u32 l13 = s13 ? l16 : 0x7fff0000u;
#define HLOAD(q) ald_a_sc1<((q) % 14 % 4) * 1024>(af[(q) % DH], rs, ((q) % 14 == 13 ? l13 : l16), tbo[(q) / 14] + ((q) % 14 / 4) * 4096)
    sfor<DH>([&](auto I) { constexpr int q = decltype(I)::value; HLOAD(q); });
    const rsrc_t rsq = it == 0 ? rs : k.rsnull;
    f32x4 acc[2][5];
    f32x4 part[3][5];
    sfor<56>([&](auto QC) {
      constexpr int q = decltype(QC)::value, tbi = q / 14, s = q % 14;
      constexpr int yf = (q + DH - 1 < 55 ? q + DH - 1 : 55) - q;
      constexpr int nd = q - 44;
      constexpr int yd = (nd > 0) ? (nd + (nd < 8 ? nd : 8)) : 0;
      waitpin_a<yf + yd>(af[q % DH]);
      if constexpr (s == 0) {
#pragma unroll
        for (int nb = 0; nb < 5; ++nb) acc[tbi & 1][nb] = mfma16(k.wd_r[s][nb], af[q % DH], f32x4{0.f, 0.f, 0.f, 0.f});
      } else if (s < 13 || s13) {
#pragma unroll
        for (int nb = 0; nb < 5; ++nb) acc[tbi & 1][nb] = mfma16(k.wd_r[s][nb], af[q % DH], acc[tbi & 1][nb]);
      }
      if constexpr (tbi > 0 && s >= 1 && s < 6) {
        const u32 tbp = (w + tbi) & 3;
        *(LAS f32x4*)(lds + L_RED + ((tbp * 3 + (3 - tbi)) * 5 + (s - 1)) * 1024 + l16) = acc[(tbi - 1) & 1][s - 1];
      }
      if constexpr (q == 50) {
        WAITL0(); BAR();
#pragma unroll
        for (int sl = 0; sl < 3; ++sl)
#pragma unroll
          for (int nb = 0; nb < 5; ++nb) part[sl][nb] = *(const LAS f32x4*)(lds + L_RED + ((w * 3 + sl) * 5 + nb) * 1024 + l16);
      }
      SB();
      if constexpr (q + DH < 56) HLOAD(q + DH);
      if constexpr (q >= 44) {
        constexpr int j0 = nd + (nd < 8 ? nd : 8);
        if constexpr (j0 < 10) dma_q<0, 0, j0>(rsq, l16, sQ, w, lds0); else dma_q<1, 1, j0 - 10>(rsq, l16, sQ, w, lds0);
        if constexpr (nd < 8) {
          constexpr int j1 = j0 + 1;
          if constexpr (j1 < 10) dma_q<0, 0, j1>(rsq, l16, sQ, w, lds0); else dma_q<1, 1, j1 - 10>(rsq, l16, sQ, w, lds0);
        }
      }
      SB();
    });
    STAMP(18);
#pragma unroll
    for (int nb = 0; nb < 5; ++nb) {
      const f32x4 v = (acc[1][nb] + part[0][nb]) + (part[1][nb] + part[2][nb]);
      const u32 a = cvtpk(bflo(k.xr[nb].x) + v[0], bfhi(k.xr[nb].x) + v[1]), b = cvtpk(bflo(k.xr[nb].y) + v[2], bfhi(k.xr[nb].y) + v[3]);
      k.xr[nb] = u32x2{a, b};
    }
    const u32 token = 64 * m64 + 16 * w + t16;
    STAMP(19);
    if (it == 0) {
      float s1 = 0.f, s2 = 0.f;
      const u32 xo = ((token >> 5) * 40 + 5 * c8) * 1024 + ((token & 31) + 32 * (g >> 1)) * 16;
#pragma unroll
      for (int pr = 0; pr < 3; ++pr) {
        const int A = 2 * pr, Bn = pr < 2 ? 2 * pr + 1 : 4;
        auto r0 = __builtin_amdgcn_permlane16_swap(k.xr[A].x, k.xr[Bn].x, false, false);
        auto r1 = __builtin_amdgcn_permlane16_swap(k.xr[A].y, k.xr[Bn].y, false, false);
        const u32x4 v = {r0[0], r1[0], r0[1], r1[1]};
        if (pr < 2) stg_sc1(v, rs, xo + (A + (g & 1)) * 1024, OFF_XF + 2 * SZ_XF);
        else if ((g & 1) == 0) stg_sc1(v, rs, xo + 4 * 1024, OFF_XF + 2 * SZ_XF);
      }
#pragma unroll
      for (int nb = 0; nb < 5; ++nb) {
        const float a = bflo(k.xr[nb].x), b = bfhi(k.xr[nb].x), c = bflo(k.xr[nb].y), d = bfhi(k.xr[nb].y);
        s1 += (a + b) + (c + d); s2 += (a * a + b * b) + (c * c + d * d);
      }
      s1 = red16_32_add(s1); s2 = red16_32_add(s2);
      if (g == 0) stg2_sc1(u32x2{f2u(s1), f2u(s2)}, rs, (c8 * 2048 + token) * 8, OFF_ST + 2 * SZ_ST);
      SB();
      WAITV(0);
      WAITL0();
      BAR();
      if (tid == 0) flag_set(flags, ((FG_X2 + m64) * 16 + c8) * 32, k.tag);
      sfor<10>([&](auto I) { dma_q<2, 2, decltype(I)::value>(rs, l16, sQ, w, lds0); });
    } else {
      const u32 oo = token * 1280 + (80 * c8 + 8 * (g >> 1)) * 2;
#pragma unroll
      for (int pr = 0; pr < 3; ++pr) {
        const int A = 2 * pr, Bn = pr < 2 ? 2 * pr + 1 : 4;
        auto r0 = __builtin_amdgcn_permlane16_swap(k.xr[A].x, k.xr[Bn].x, false, false);
        auto r1 = __builtin_amdgcn_permlane16_swap(k.xr[A].y, k.xr[Bn].y, false, false);
        const u32x4 v = {r0[0], r1[0], r0[1], r1[1]};
        if (pr < 2) __builtin_amdgcn_raw_buffer_store_b128(v, k.rso, oo + (A + (g & 1)) * 32, 0, 0);
        else if ((g & 1) == 0) __builtin_amdgcn_raw_buffer_store_b128(v, k.rso, oo + 4 * 32, 0, 0);
      }
    }
    STAMP(20);
  }
}

DI void stw_sc1(u32x4 v, rsrc_t rs, u32 off) { __builtin_amdgcn_raw_buffer_store_b128(v, rs, off, 0, AUX_SC1); }
DI float dot2ones(u32 v, float c) { return __builtin_amdgcn_fdot2_f32_bf16(__builtin_bit_cast(bf16x2, v), __builtin_bit_cast(bf16x2, 0x3f803f80u), c, false); }
DI void setup(K& k, int u, const u16* x0, const u16* wqkv, const u16* wo, const u16* wg, const u16* wu, const u16* wd) {
  const int l = k.lane, w = k.w;
  const rsrc_t rs = k.rs;
  LAS char* const lds = k.lds;
  const u32 lds0 = k.lds0;
  const int type = u < 60 ? 0 : u < 124 ? 3 : u < 178 ? 1 : u < 232 ? 2 : u < 252 ? 4 : 5;
  const int idx = u < 60 ? u : u < 124 ? u - 60 : u < 178 ? u - 124 : u < 232 ? u - 178 : u - 232;
  const u16* src = type == 0 ? wqkv : type == 1 ? wg : type == 2 ? wu : type == 3 ? x0 : wo;
  const u32 pitch = type == 0 ? 3840 : type <= 2 ? 3456 : 1280;
  const u32 nrec = type == 0 ? 640 * 3840 : type <= 2 ? 640 * 3456 : type == 3 ? 2048 * 1280 : type == 4 ? 640 * 1280 : 0;
  const rsrc_t rsrcA = __builtin_amdgcn_make_buffer_rsrc((void*)src, 0, nrec, 0x00020000);
  const rsrc_t rsrcD = __builtin_amdgcn_make_buffer_rsrc((void*)wd, 0, 1728 * 1280, 0x00020000);
  {
    const u32 voW = (16 * w + (l >> 2)) * pitch + idx * 64 + (l & 3) * 16;
    const u32 voO = ((idx & 1) * 320 + 8 * w + (l >> 3)) * 1280 + (idx >> 1) * 128 + (l & 7) * 16;
    const u32 vo = type == 4 ? voO : voW, step = type == 4 ? 32 * 1280 : 64 * pitch;
#pragma unroll
    for (int j = 0; j < 10; ++j) {
      const u32 cidx = 64 * (4 * j + w) + l, row = (cidx * 52429u) >> 22, cc = cidx - row * 80;
      const u32 vx = (32 * idx + row) * 1280 + ((cc ^ (row & 7)) * 16);
      const u32 v = type == 3 ? vx : vo + j * step;
      DMA_F("SA", 0, "", rsrcA, v, 0u, lds0 + (4 * j + w) * 1024);
    }
#pragma unroll
    for (int t = 0; t < 3; ++t) {
      const u32 ds_ = u / 10 + 25 * t;
      const u32 v = (u < 250 && ds_ < 54) ? (ds_ * 32 + 8 * w + (l >> 3)) * 1280 + (u % 10) * 128 + (l & 7) * 16 : 0x7fffffffu;
      DMA_F("SB", 0, "", rsrcD, v, 0u, lds0 + 40960 + t * 4096 + w * 1024);
    }
  }
  const int hh = l >> 5;
  u32 dbase = 0; int dstep = 0;
  {
    const int c = 32 * idx + (l & 31);
    if (type == 0) {
      const int sec = c / 640, cc = c % 640, p = cc / 40, r40 = cc % 40;
      const int n = (sec == 1 ? 0 : sec == 2 ? 40 : 80) + r40;
      const int b = n >> 5, nl = n & 31;
      dbase = OFF_WQ + p * 163840 + (b < 3 ? b * 1024 : 122880) + nl * 16;
      dstep = b < 3 ? 3072 : 1024;
    } else if (type <= 2) {
      const int fb = c >> 4, nl = (c & 15) + 16 * (type - 1);
      const int half = fb / 54, rem = fb % 54;
      const int sl_ = 8 * half + (rem < 42 ? rem / 7 : 6 + (rem - 42) / 6), ix = rem < 42 ? rem % 7 : (rem - 42) % 6;
      dbase = OFF_WG + sl_ * 286720 + (ix < 3 ? ix * 1024 : ix < 5 ? 122880 + (ix - 3) * 1024 : 204800 + (ix - 5) * 1024) + nl * 16;
      dstep = ix < 3 ? 3072 : 2048;
    }
  }
  asm volatile("" : "+v"(dbase), "+v"(dstep));
  u64 ep;
  asm volatile("global_load_dwordx2 %0, %1, off sc1" : "=v"(ep) : "v"(k.flags + FG_EPOCH * 512) : "memory");
  SSTAMP(21);
  WAITV(0);
  asm volatile("" : "+v"(ep));
  BAR();
  SSTAMP(22);
  {
    const u64 t = ep * 0x9E3779B97F4A7C15ull + 0xD1B54A32D192ED03ull;
    k.tag = (u64)(u32)__builtin_amdgcn_readfirstlane((u32)t) | ((u64)(u32)__builtin_amdgcn_readfirstlane((u32)(t >> 32)) << 32);
  }
  const int gam = l >> 4, q = (l & 15) >> 2, pp = l & 3;
  if (type <= 2) {
    const u32 ra = ((gam >> 1) * 8 + q) * 64 + (gam & 1) * 32 + pp * 8;
    u32x4 sl[10];
#pragma unroll
    for (int j = 0; j < 10; ++j) {
      const u32 o = ra + (4 * j + w) * 1024;
      const u32x2 a = __builtin_bit_cast(u32x2, __builtin_amdgcn_ds_read_tr16_b64_v4i16((LAS s16x4*)(lds + o)));
      const u32x2 b2 = __builtin_bit_cast(u32x2, __builtin_amdgcn_ds_read_tr16_b64_v4i16((LAS s16x4*)(lds + o + 256)));
      sl[j] = u32x4{a.x, a.y, b2.x, b2.y};
    }
    float c0 = 0.f, c1 = 0.f;
#pragma unroll
    for (int j = 0; j < 10; ++j) { c0 = dot2ones(sl[j][0], c0); c1 = dot2ones(sl[j][1], c1); c0 = dot2ones(sl[j][2], c0); c1 = dot2ones(sl[j][3], c1); }
    const float cs = red32_add(c0 + c1);
    LAS float* red = (LAS float*)(lds + 81920);
    if (l < 32) red[w * 32 + l] = cs;
    WAITL0(); BAR();
    const float mean = ((red[l & 31] + red[32 + (l & 31)]) + (red[64 + (l & 31)] + red[96 + (l & 31)])) * (1.0f / 640.0f);
#pragma unroll
    for (int j = 0; j < 10; ++j) {
      u32x4 v;
#pragma unroll
      for (int e = 0; e < 4; ++e) v[e] = cvtpk(bflo(sl[j][e]) - mean, bfhi(sl[j][e]) - mean);
      stw_sc1(v, rs, dbase + (4 * j + w) * dstep + hh * 512);
    }
  } else if (type == 3) {
    const int tok = l & 31, h = l >> 5;
    u32x4 sl[10];
#pragma unroll
    for (int i = 0; i < 10; ++i) sl[i] = *(const LAS u32x4*)(lds + tok * 1280 + (((2 * (10 * w + i) + h) ^ (tok & 7)) * 16));
    float s1 = 0.f, s2 = 0.f;
#pragma unroll
    for (int i = 0; i < 10; ++i) {
      stw_sc1(sl[i], rs, OFF_XF + (idx * 40 + 10 * w + i) * 1024 + l * 16);
#pragma unroll
      for (int e = 0; e < 4; ++e) { s1 = dot2ones(sl[i][e], s1); const float a_ = bflo(sl[i][e]), b_ = bfhi(sl[i][e]); s2 += a_ * a_ + b_ * b_; }
    }
    s1 = red32_add(s1); s2 = red32_add(s2);
    if (l < 32) {
      stg2_sc1(u32x2{f2u(s1), f2u(s2)}, rs, (w * 2048 + 32 * idx + tok) * 8, OFF_ST);
      stg2_sc1(u32x2{0u, 0u}, rs, ((w + 4) * 2048 + 32 * idx + tok) * 8, OFF_ST);
    }
  } else if (type == 4) {
    const int cg = idx >> 1, kk = idx & 1, c = 64 * cg + l, c8 = c / 80, nb = (c % 80) >> 4, n = c & 15;
    const u32 dbase = OFF_WO + c8 * 20 * 5120 + nb * 1024 + n * 16;
#pragma unroll
    for (int j = 0; j < 10; ++j) {
      const int o = 4 * j + w, pl = o / 5, ii = o % 5, sg = 40 * kk + o, r0 = 40 * pl + 4 * ii + q;
      const u32x2 a = __builtin_bit_cast(u32x2, __builtin_amdgcn_ds_read_tr16_b64_v4i16((LAS s16x4*)(lds + r0 * 128 + gam * 32 + pp * 8)));
      const u32x2 b2 = __builtin_bit_cast(u32x2, __builtin_amdgcn_ds_read_tr16_b64_v4i16((LAS s16x4*)(lds + (r0 + 20) * 128 + gam * 32 + pp * 8)));
      stw_sc1(u32x4{a.x, a.y, b2.x, b2.y}, rs, dbase + (sg >> 2) * 5120 + (sg & 3) * 256);
    }
  }
  const bool crit = type == 0 || type == 3 || type == 4;
  SSTAMP(24);
  if (crit) { WAITV(0); }
  WAITL0();
  BAR();
  if (crit && k.tid == 0) flag_set(k.flags, (FG_PF1 * 16 + u) * 32, k.tag);
  SSTAMP(25);
#pragma unroll
  for (int t = 0; t < 3; ++t) {
    const u32 s_ = u / 10 + 25 * t;
    if (u < 250 && s_ < 54) {
      const u32 cg = u % 10, c = 64 * cg + l, c8 = c / 80, nb = (c % 80) >> 4, n = c & 15;
      const u32 r0 = 16 * (w >> 1) + 4 * (w & 1) + q;
      const u32x2 a = __builtin_bit_cast(u32x2, __builtin_amdgcn_ds_read_tr16_b64_v4i16((LAS s16x4*)(lds + 40960 + t * 4096 + r0 * 128 + gam * 32 + pp * 8)));
      const u32x2 b2 = __builtin_bit_cast(u32x2, __builtin_amdgcn_ds_read_tr16_b64_v4i16((LAS s16x4*)(lds + 40960 + t * 4096 + (r0 + 8) * 128 + gam * 32 + pp * 8)));
      stw_sc1(u32x4{a.x, a.y, b2.x, b2.y}, rs, OFF_WD + (c8 * 54 + s_) * 5120 + nb * 1024 + n * 16 + w * 256);
    }
  }
  WAITL0();
  if (w == 0) {
    u32 fo; bool act = true;
    if (l < 6) fo = 20 * (l >> 1) + (40 * k.p) / 32 + (l & 1);
    else if (l < 11) { const int rb = 4 * k.m128 - 1 + (l - 6); act = rb >= 0; fo = 60 + rb; }
    else if (l < 15) fo = 232 + 2 * ((80 * k.c8) / 64 + ((l - 11) >> 1)) + ((l - 11) & 1);
    else { act = false; fo = 0; }
    poll_flags(k.flags, (FG_PF1 * 16 + (act ? fo : 0)) * 32, act, l, k.tag);
  }
  BAR();
  SSTAMP(26);
}

__global__ __launch_bounds__(256, 1) void main_kernel(const u16* __restrict__ x0, const u16* __restrict__ wqkv, const u16* __restrict__ wo, const u16* __restrict__ wg,
                                                      const u16* __restrict__ wu, const u16* __restrict__ wd, char* __restrict__ ws, u16* __restrict__ out) {
  extern __shared__ __attribute__((aligned(16))) char smem[];
  K k;
  k.lds = (LAS char*)smem;
  k.lds0 = (u32)(size_t)k.lds;
  k.tid = threadIdx.x; k.lane = k.tid & 63; k.w = __builtin_amdgcn_readfirstlane(k.tid >> 6);
  k.rs = __builtin_amdgcn_make_buffer_rsrc(ws, 0, WS_USED, 0x00020000);
  k.rso = __builtin_amdgcn_make_buffer_rsrc(out, 0, 2048 * 640 * 2, 0x00020000);
  k.rsnull = __builtin_amdgcn_make_buffer_rsrc(ws, 0, 0, 0x00020000);
  k.flags = (GAS u32*)ws;
  const int bid = blockIdx.x, xc = bid & 7, j = bid >> 3, mg = xc >> 1, half = xc & 1;
  k.m128 = 4 * mg + (j >> 3); k.p = 8 * half + (j & 7);
  k.m64 = 8 * mg + (j >> 2); k.c8 = 4 * half + (j & 3);
  const int p7 = j & 7;
  k.B0 = 54 * half + (p7 < 6 ? 7 * p7 : 42 + 6 * (p7 - 6));
  k.nblk = p7 < 6 ? 7 : 6;
  k.bid = bid;
  setup(k, bid, x0, wqkv, wo, wg, wu, wd);
  {
    PHASE_IDS();
    const u32 sQ = OFF_WQ + p * 163840;
    sfor<10>([&](auto I) { dma_q<0, 0, decltype(I)::value>(rs, l16, sQ, w, lds0); });
    sfor<10>([&](auto I) { dma_q<1, 1, decltype(I)::value>(rs, l16, sQ, w, lds0); });
    sfor<10>([&](auto I) { dma_q<2, 2, decltype(I)::value>(rs, l16, sQ, w, lds0); });
    const u32 token = 64 * m64 + 16 * w + t16;
    const u32 o = ((token >> 5) * 40 + 5 * c8) * 1024 + ((token & 31) + 32 * (g >> 1)) * 16 + (g & 1) * 8;
#pragma unroll
    for (int nb = 0; nb < 5; ++nb) ald2(k.xr[nb], rs, o, OFF_XF + nb * 1024);
  }
#pragma nounroll
  for (int it = 0; it < 2; ++it) { k.it = it; iteration(k); }
  if (bid == 0 && k.w == 0) {
#pragma unroll
    for (int r = 0; r < 4; ++r) poll_flags(k.flags, (FG_PF2 * 16 + 64 * r + k.lane) * 32, true, k.lane, k.tag);
    if (k.lane == 0) __hip_atomic_fetch_add((GAS u64*)(k.flags + FG_EPOCH * 512), 1ull, RLX_AGENT);
  }
}

extern "C" void kernel_launch(void* const* d_in, const int* in_sizes, int n_in, void* d_out, int out_size, void* d_ws, size_t ws_size,
                              hipStream_t stream) {
  const u16* x0 = (const u16*)d_in[0];
  const u16* wqkv = (const u16*)d_in[1];
  const u16* wo = (const u16*)d_in[2];
  const u16* wg = (const u16*)d_in[4];
  const u16* wu = (const u16*)d_in[5];
  const u16* wd = (const u16*)d_in[6];
  (void)hipFuncSetAttribute((const void*)main_kernel, hipFuncAttributeMaxDynamicSharedMemorySize, LDS_BYTES);
  main_kernel<<<256, 256, LDS_BYTES, stream>>>(x0, wqkv, wo, wg, wu, wd, (char*)d_ws, (u16*)d_out);
}
```

```cpp
#include <hip/hip_runtime.h>
#include <stdint.h>
#include <utility>

#define LAS __attribute__((address_space(3)))
#define GAS __attribute__((address_space(1)))
#define DI __device__ __forceinline__
typedef unsigned u32;
typedef unsigned short u16;
typedef unsigned long long u64;
typedef u32 u32x4 __attribute__((ext_vector_type(4)));
typedef u32 u32x2 __attribute__((ext_vector_type(2)));
typedef float f32x2 __attribute__((ext_vector_type(2)));
typedef float f32x4 __attribute__((ext_vector_type(4)));
typedef float f32x16 __attribute__((ext_vector_type(16)));
typedef short s16x8 __attribute__((ext_vector_type(8)));
typedef short s16x4 __attribute__((ext_vector_type(4)));
typedef __bf16 bf16x2 __attribute__((ext_vector_type(2)));
typedef __amdgpu_buffer_rsrc_t rsrc_t;

constexpr u32 OFF_WQ = 483328;
constexpr u32 OFF_WO = OFF_WQ + 16 * 163840;
constexpr u32 OFF_WG = OFF_WO + 8 * 20 * 5 * 1024;
constexpr u32 OFF_WD = OFF_WG + 16 * 286720;
constexpr u32 SZ_XF = 64 * 40 * 1024;
constexpr u32 OFF_XF = OFF_WD + 8 * 54 * 5 * 1024 + 45056;
constexpr u32 SZ_ST = 8 * 2048 * 8;
constexpr u32 OFF_ST = OFF_XF + 4 * SZ_XF;
constexpr u32 SZ_OF = 128 * 20 * 1024;
constexpr u32 OFF_OF = OFF_ST + 4 * SZ_ST;
constexpr u32 SZ_HF = 128 * 54 * 1024;
constexpr u32 OFF_HF = OFF_OF + 2 * SZ_OF;
constexpr u32 WS_USED = OFF_HF + 2 * SZ_HF;
constexpr u32 FG_O = 0, FG_X1 = 16, FG_H = 48, FG_X2 = 64, FG_PER_IT = 96;
constexpr u32 FG_TMO = 2 * FG_PER_IT;
constexpr u32 FG_EPOCH = FG_TMO + 1;
constexpr u32 FG_PF1 = FG_EPOCH + 1;
constexpr u32 FG_PF2 = FG_PF1 + 16;

constexpr u32 L_HALO = 122880;
constexpr u32 L_KST = 40960;
constexpr u32 L_VST = L_KST + 144 * 80;
constexpr u32 L_QST = L_VST + 144 * 80;
constexpr u32 L_RED = 100352;
constexpr u32 L_H4 = 143360;
constexpr u32 LDS_BYTES = 163840;

constexpr u32 AUX_SC1 = 16;
#define RLX_AGENT __ATOMIC_RELAXED, __HIP_MEMORY_SCOPE_AGENT
#define SB() __builtin_amdgcn_sched_barrier(0)
#define MEMFENCE() asm volatile("" ::: "memory")
#define WAITV(n) asm volatile("s_waitcnt vmcnt(%0)" ::"i"(n) : "memory")
#define WAITL0() asm volatile("s_waitcnt lgkmcnt(0)" ::: "memory")
#define BAR() do { MEMFENCE(); __builtin_amdgcn_s_barrier(); MEMFENCE(); } while (0)
#define CERT(tag) asm volatile("; CERT " tag ::: "memory")

template <int... I, class Fn> DI void sfor_impl(std::integer_sequence<int, I...>, Fn&& f) { (f(std::integral_constant<int, I>{}), ...); }
template <int N, class Fn> DI void sfor(Fn&& f) { sfor_impl(std::make_integer_sequence<int, N>{}, f); }
DI f32x4 mfma16(u32x4 a, u32x4 b, f32x4 c) { return __builtin_amdgcn_mfma_f32_16x16x32_bf16(__builtin_bit_cast(s16x8, a), __builtin_bit_cast(s16x8, b), c, 0, 0, 0); }
DI u32 cvtpk(float lo, float hi) { f32x2 v = {lo, hi}; bf16x2 b = __builtin_convertvector(v, bf16x2); return __builtin_bit_cast(u32, b); }
DI float u2f(u32 v) { return __builtin_bit_cast(float, v); }
DI u32 f2u(float v) { return __builtin_bit_cast(u32, v); }
DI float bflo(u32 v) { return __builtin_bit_cast(float, v << 16); }
DI float bfhi(u32 v) { return __builtin_bit_cast(float, v & 0xffff0000u); }
DI float fma_s(float a, float b, float c) { float r; asm("v_fma_f32 %0, %1, %2, %3" : "=v"(r) : "v"(a), "v"(b), "v"(c)); return r; }
DI float mul_s(float a, float b) { float r; asm("v_mul_f32_e32 %0, %1, %2" : "=v"(r) : "v"(a), "v"(b)); return r; }
DI float max_s(float a, float b) { float r; asm("v_max_f32_e32 %0, %1, %2" : "=v"(r) : "v"(a), "v"(b)); return r; }
DI float sel_s(float f, float t, unsigned long long m) { float r; asm("v_cndmask_b32_e64 %0, %1, %2, %3" : "=v"(r) : "v"(f), "v"(t), "s"(m)); return r; }
DI float red16_32_add(float v) {
  auto a = __builtin_amdgcn_permlane16_swap(f2u(v), f2u(v), false, false);
  v = u2f(a[0]) + u2f(a[1]);
  auto b = __builtin_amdgcn_permlane32_swap(f2u(v), f2u(v), false, false);
  return u2f(b[0]) + u2f(b[1]);
}
DI float red32_add(float v) {
  auto b = __builtin_amdgcn_permlane32_swap(f2u(v), f2u(v), false, false);
  return u2f(b[0]) + u2f(b[1]);
}
DI void stg_sc1(u32x4 v, rsrc_t rs, u32 voff, u32 soff) { __builtin_amdgcn_raw_buffer_store_b128(v, rs, voff, soff, AUX_SC1); }
DI void stg2_sc1(u32x2 v, rsrc_t rs, u32 voff, u32 soff) { __builtin_amdgcn_raw_buffer_store_b64(v, rs, voff, soff, AUX_SC1); }

template <int IMM> DI void ald_v_sc1(u32x4& d, rsrc_t rs, u32 voff, u32 soff) {
  asm volatile("buffer_load_dwordx4 %0, %1, %2, %3 offen offset:%4 sc1" : "=v"(d) : "v"(voff), "s"(rs), "s"(soff), "i"(IMM) : "memory");
}
DI void ald2_sc1(u32x2& d, rsrc_t rs, u32 voff, u32 soff) {
  asm volatile("buffer_load_dwordx2 %0, %1, %2, %3 offen sc1" : "=v"(d) : "v"(voff), "s"(rs), "s"(soff) : "memory");
}
template <int IMM> DI void ald_a_sc1(u32x4& d, rsrc_t rs, u32 voff, u32 soff) {
  asm volatile("buffer_load_dwordx4 %0, %1, %2, %3 offen offset:%4 sc1" : "=a"(d) : "v"(voff), "s"(rs), "s"(soff), "i"(IMM) : "memory");
}
template <int N> DI void waitpin_a(u32x4& r) { asm volatile("s_waitcnt vmcnt(%1)" : "+a"(r) : "i"(N) : "memory"); }
template <int N> DI void waitpin_a2(u32x4& r0, u32x4& r1) { asm volatile("s_waitcnt vmcnt(%2)" : "+a"(r0), "+a"(r1) : "i"(N) : "memory"); }
DI void mma32(f32x16& acc, const u32x4& wv, const u32x4& xa) { asm volatile("v_mfma_f32_32x32x16_bf16 %0, %1, %2, %0" : "+v"(acc) : "v"(wv), "a"(xa)); }
DI void mma32_0(f32x16& acc, const u32x4& wv, const u32x4& xa) { asm volatile("v_mfma_f32_32x32x16_bf16 %0, %1, %2, 0" : "=&v"(acc) : "v"(wv), "a"(xa)); }
DI void mma32w(f32x16& acc, const u32x4& wv, const u32x4& xa, const u32x4& wn) { asm volatile("v_mfma_f32_32x32x16_bf16 %0, %1, %2, %0" : "+v"(acc) : "v"(wv), "a"(xa), "v"(wn)); }
DI void mma32w_0(f32x16& acc, const u32x4& wv, const u32x4& xa, const u32x4& wn) { asm volatile("v_mfma_f32_32x32x16_bf16 %0, %1, %2, 0" : "=&v"(acc) : "v"(wv), "a"(xa), "v"(wn)); }
DI void mma16(f32x4& acc, const u32x4& av, const u32x4& bv) { asm volatile("v_mfma_f32_16x16x32_bf16 %0, %1, %2, %0" : "+v"(acc) : "v"(av), "v"(bv)); }
DI void mma16_0(f32x4& acc, const u32x4& av, const u32x4& bv) { asm volatile("v_mfma_f32_16x16x32_bf16 %0, %1, %2, 0" : "=&v"(acc) : "v"(av), "v"(bv)); }
template <int N> DI void waitpin_v(u32x4& r) { asm volatile("s_waitcnt vmcnt(%1)" : "+v"(r) : "i"(N) : "memory"); }
DI void pin_v(u32x4& r) { asm volatile("" : "+v"(r)::"memory"); }
DI void pin_a(u32x4& r) { asm volatile("" : "+a"(r)::"memory"); }
DI void pin2(u32x2& r) { asm volatile("" : "+v"(r)::"memory"); }
#define DMA_F(TAG, IMM, SC, rs, voff, soff, m0v) \
  asm volatile("s_mov_b32 m0, %2\n\ts_nop 0\n\tbuffer_load_dwordx4 %0, %1, %3 offen offset:%4 " SC "lds ; DMA " TAG ::"v"(voff), "s"(rs), "s"(m0v), "s"(soff), "i"(IMM) : "memory")
#define DMA_N(TAG, IMM, SC, rs, voff, soff) \
  asm volatile("buffer_load_dwordx4 %0, %1, %2 offen offset:%3 " SC "lds ; DMA " TAG ::"v"(voff), "s"(rs), "s"(soff), "i"(IMM) : "memory")

constexpr u32 kSpinCap = 1u << 16;

struct K {
  rsrc_t rs, rso, rsnull;
  LAS char* lds;
  u32 lds0;
  GAS u32* flags;
  int bid, lane, w, m128, p, m64, c8, B0, nblk, tid, it;
  u64 tag;
  u32x2 xr[5];
  u32x4 wo_r[5][5];
  u32x4 wd_r[14][5];
};

#define PHASE_IDS() int lane = k.lane; asm volatile("" : "+v"(lane)); \
  int it = k.it; asm volatile("" : "+s"(it)); \
  const u32 xin = OFF_XF + it * 2 * SZ_XF, stin = OFF_ST + it * 2 * SZ_ST, x1o = xin + SZ_XF, st1o = stin + SZ_ST; \
  const u32 ofo = OFF_OF + it * SZ_OF, hfo = OFF_HF + it * SZ_HF, fgb = it * FG_PER_IT; \
  (void)xin; (void)stin; (void)x1o; (void)st1o; (void)ofo; (void)hfo; (void)fgb; \
  int w = k.w, m128 = k.m128, p = k.p, m64 = k.m64, c8 = k.c8, B0 = k.B0; \
  asm volatile("" : "+s"(w), "+s"(m128), "+s"(p), "+s"(m64), "+s"(c8), "+s"(B0)); \
  const u32 l16 = lane * 16; const int tok = lane & 31, h = lane >> 5, t16 = lane & 15, g = lane >> 4; \
  const rsrc_t rs = k.rs; LAS char* const lds = k.lds; const u32 lds0 = k.lds0; \
  (void)tok; (void)h; (void)t16; (void)g; (void)l16; (void)m128; (void)p; (void)m64; (void)c8; (void)B0; (void)lds; (void)lds0; (void)rs

DI void flag_set(GAS u32* flags, u32 idx, u64 tag) { __hip_atomic_store((GAS u64*)(flags + idx), tag, RLX_AGENT); }
DI u64 flag_get(GAS u32* flags, u32 idx) { return __hip_atomic_load((GAS u64*)(flags + idx), RLX_AGENT); }
DI bool timed_out(GAS u32* flags, u64 tag) {
  const u64 v = flag_get(flags, FG_TMO * 512);
  return ((u64)(u32)__builtin_amdgcn_readfirstlane((u32)v) | ((u64)(u32)__builtin_amdgcn_readfirstlane((u32)(v >> 32)) << 32)) == tag;
}
DI void poll_flags(GAS u32* flags, u32 fo, bool act, int lane, u64 tag) {
  for (u32 spins = 0;;) {
    const u64 v = act ? flag_get(flags, fo) : tag;
    if (__all(v == tag)) break;
    __builtin_amdgcn_s_sleep(1);
    if (++spins > kSpinCap) {
      if (lane == 0) flag_set(flags, FG_TMO * 512, tag);
      break;
    }
    if ((spins & 255u) == 0u && timed_out(flags, tag)) break;
  }
}
constexpr int POLL_GAP = 2;
struct Polls { u64 a, b, c; };
DI void poll_issue(u64& d, const GAS u32* p) { asm volatile("global_load_dwordx2 %0, %1, off sc1 ; POLL" : "=v"(d) : "v"(p) : "memory"); }
DI bool poll_check(u64& d, bool act, u64 want) { asm volatile("s_waitcnt vmcnt(2) ; POLLWAIT" : "+v"(d)::"memory"); return __all(!act || d == want); }
DI void poll_flags_p(GAS u32* flags, u32 fo, bool act, int lane, u64 want, Polls& pl) {
  const GAS u32* p = flags + (act ? fo : FG_TMO * 512);
  asm volatile("; POLL_BEGIN" ::: "memory");
  poll_issue(pl.a, p); __builtin_amdgcn_s_sleep(POLL_GAP); poll_issue(pl.b, p); __builtin_amdgcn_s_sleep(POLL_GAP); poll_issue(pl.c, p);
  for (u32 spins = 0;;) {
    if (poll_check(pl.a, act, want)) break;
    poll_issue(pl.a, p);
    if (poll_check(pl.b, act, want)) break;
    poll_issue(pl.b, p);
    if (poll_check(pl.c, act, want)) break;
    poll_issue(pl.c, p);
    if (++spins > kSpinCap) {
      if (lane == 0) flag_set(flags, FG_TMO * 512, want);
      break;
    }
    if ((spins & 63u) == 0u && timed_out(flags, want)) break;
  }
  asm volatile("; POLL_END" ::: "memory");
}
DI void poll_pin(Polls& pl) { asm volatile("; POLL_PIN" : "+v"(pl.a), "+v"(pl.b), "+v"(pl.c)::"memory"); }
DI void stats_finish(float s1, float s2, float& nmean, float& rstd) {
  const float mean = s1 * (1.0f / 640.0f);
  const float var = __builtin_fmaxf(s2 * (1.0f / 640.0f) - mean * mean, 0.f);
  nmean = -mean; rstd = __builtin_amdgcn_rsqf(var + 1e-5f);
}

template <int C, int SLOT, int I> DI void dma_q(rsrc_t rs, u32 l16, u32 sQ, int w, u32 lds0) {
#define Q_EMIT(TAG, FIRST, IMM, SO, MO) do { if constexpr (FIRST) DMA_F(TAG, IMM, "sc1 ", rs, l16, SO, MO); else DMA_N(TAG, IMM, "sc1 ", rs, l16, SO); } while (0)
  if constexpr (C < 3) {
    constexpr int grp = I / 4, imm = (I % 4) * 1024;
    const u32 so = sQ + w * 10240 + C * 40960 + grp * 4096, mo = lds0 + SLOT * 40960 + w * 10240 + grp * 4096;
    if constexpr (C == 0) Q_EMIT("Q0", I % 4 == 0, imm, so, mo); else if constexpr (C == 1) Q_EMIT("Q1", I % 4 == 0, imm, so, mo); else Q_EMIT("Q2", I % 4 == 0, imm, so, mo);
  } else {
    constexpr int hf = I / 5, ii = I % 5, grp = ii / 4, imm = (ii % 4) * 1024;
    const u32 so = sQ + w * 5120 + 122880 + hf * 20480 + grp * 4096, mo = lds0 + SLOT * 40960 + w * 5120 + hf * 20480 + grp * 4096;
    if constexpr (hf == 0) Q_EMIT("Q3a", ii % 4 == 0, imm, so, mo); else Q_EMIT("Q3b", ii % 4 == 0, imm, so, mo);
  }
#undef Q_EMIT
}
template <int HH, int I> DI void dma_g(rsrc_t rs, u32 l16, u32 sG, int w, u32 lds0) {
  constexpr int grp = I / 4, imm = (I % 4) * 1024;
  const u32 so = sG + HH * 20480 + grp * 4096, mo = lds0 + w * 5120 + (HH % 7) * 20480 + grp * 4096;
#define G_CASE(n) if constexpr (HH == n) { if constexpr (I % 4 == 0) DMA_F("G" #n, imm, "sc1 ", rs, l16, so, mo); else DMA_N("G" #n, imm, "sc1 ", rs, l16, so); }
  G_CASE(0) G_CASE(1) G_CASE(2) G_CASE(3) G_CASE(4) G_CASE(5) G_CASE(6) G_CASE(7) G_CASE(8) G_CASE(9) G_CASE(10) G_CASE(11) G_CASE(12) G_CASE(13)
#undef G_CASE
}

template <int HH> DI void dma_g3(rsrc_t rs, u32 l16, u32 sG0, int w, u32 lds0) {
  const u32 f0 = 7 * (w - 1);
  const u32 so = sG0 + HH * 20480 + f0 * 1024, mo = lds0 + (HH % 7) * 20480 + f0 * 1024;
#define G3_CASE(n) if constexpr (HH == n) { DMA_F("G" #n, 0, "sc1 ", rs, l16, so, mo); DMA_N("G" #n, 1024, "sc1 ", rs, l16, so); DMA_N("G" #n, 2048, "sc1 ", rs, l16, so); DMA_N("G" #n, 3072, "sc1 ", rs, l16, so); \
    DMA_F("G" #n, 0, "sc1 ", rs, l16, so + 4096, mo + 4096); DMA_N("G" #n, 1024, "sc1 ", rs, l16, so + 4096); if (w < 3) DMA_N("G" #n, 2048, "sc1 ", rs, l16, so + 4096); }
  G3_CASE(2) G3_CASE(3) G3_CASE(4) G3_CASE(5) G3_CASE(6)
#undef G3_CASE
}
constexpr int XW = 10;
struct QS {
  static constexpr bool x_at(int f) { return f < 120 && f % 3 == 0 && f / 3 + XW < 40; }
  static constexpr int aux_at(int f) { return (f < 32 && f % 3 == 1) ? f / 3 : -1; }
  static constexpr bool pf_at(int f) { return f == 85; }
  static constexpr int q3_at(int f) { return (f >= 77 && f <= 104 && f % 3 == 2) ? (f - 77) / 3 : -1; }
  static constexpr int wo_at(int f) {
    if (f >= 34 && f <= 67 && f % 3 == 1) return (f - 34) / 3;
    if (f >= 121 && f <= 157 && (f - 121) % 3 == 0) return 12 + (f - 121) / 3;
    return -1;
  }
  static constexpr int upto(int F) { int n = 0; for (int f = 0; f <= F; ++f) n += x_at(f) + (aux_at(f) >= 0) + pf_at(f) + (q3_at(f) >= 0) + (wo_at(f) >= 0); return n; }
  static constexpr int xidx(int i) { return i < XW ? i : XW + upto(3 * (i - XW)) - 1; }
  static constexpr int xwait2(int s) { return XW + upto(3 * s) - 1 - xidx(s + 1); }
};
struct GS {
  static constexpr int NWD = 65;
  static constexpr bool x_at(int f) { return f < 120 && f % 3 == 0 && f / 3 + XW < 40; }
  static constexpr int op(int F) {
    if (F <= 10 && F % 3 == 1) return 3000 + F / 3;
    if (F >= 40 && F <= 67 && F % 3 == 1) return (7 + (F - 40) / 15) * 8 + ((F - 40) % 15) / 3;
    if (F >= 79 && F <= 106 && F % 3 == 1) return (9 + (F - 79) / 15) * 8 + ((F - 79) % 15) / 3;
    if (F < 120 || (F & 1)) return -1;
    if (F < 140) return (11 + (F - 120) / 10) * 8 + ((F - 120) % 10) / 2;
    if (F < 160) return 1000 + (F - 140) / 2;
    if (F < 170) return 13 * 8 + (F - 160) / 2;
    if (F < 280) return 1010 + (F - 170) / 2;
    return -1;
  }
  static constexpr bool st_at(int F) { return F == 139 || F == 157 || F == 175 || F == 219 || F == 237; }
  static constexpr int upto(int F) { int n = 0; for (int f = 0; f <= F; ++f) n += x_at(f) + (op(f) >= 0) + st_at(f); return n; }
  static constexpr int slot(int o) { for (int f = 0; f < 300; ++f) if (op(f) == o) return f; return -1; }
  static constexpr int xidx(int i) { return i < XW ? i : XW + upto(3 * (i - XW)) - 1; }
  static constexpr int xwait2(int s) { return XW + upto(3 * s) - 1 - xidx(s + 1); }
};
static_assert(GS::op(278) == 1000 + GS::NWD - 1, "down-weight prefetch schedule");

DI void iteration(K& k) {
  GAS u32* const flags = k.flags;
  const int tid = k.tid;
  {
    PHASE_IDS();
    const u32 sQ = OFF_WQ + p * 163840, sG = OFF_WG + p * 286720 + w * 5120;
    if (it > 0 && w == 0) {
      const int grp = 2 * m128 - 1 + (lane >> 3);
      const bool act = lane < 24 && grp >= 0;
      poll_flags(flags, ((FG_X2 + grp) * 16 + (lane & 7)) * 32, act, lane, k.tag);
    }
    WAITV(0); CERT("Q0"); CERT("Q1"); CERT("Q2");
    BAR();
    u32x4 xf[40];
    const u32 xbase = xin + (4 * m128 + w) * 40960;
#define XLOAD(i) ald_a_sc1<((i) % 4) * 1024>(xf[i], rs, l16, xbase + ((i) / 4) * 4096)
    u32x2 hs0, hs1, sa0, sa1, sa2, sa3;
    const int rbh = m128 > 0 ? 4 * m128 - 1 : 0;
    const u32 hvo = (g >> 1) * 1024 + (g & 1) * 512 + (16 + t16) * 16;
    const int ht = 128 * m128 - 16 + t16;
    const u32 hso = ((2 * g) * 2048 + (ht < 0 ? 0 : ht)) * 8, sso = (4 * h * 2048 + 128 * m128 + 32 * w + tok) * 8;
    auto aux_load = [&](auto AC) {
      constexpr int a = decltype(AC)::value;
      if constexpr (a < 5) DMA_F("HALO", 0, "sc1 ", rs, hvo, xin + rbh * 40960 + (5 * w + a) * 2048, lds0 + L_HALO + (5 * w + a) * 1024);
      if constexpr (a == 5) ald2_sc1(sa0, rs, sso, stin);
      if constexpr (a == 6) ald2_sc1(sa1, rs, sso, stin + 16384);
      if constexpr (a == 7) ald2_sc1(sa2, rs, sso, stin + 32768);
      if constexpr (a == 8) ald2_sc1(sa3, rs, sso, stin + 49152);
      if constexpr (a == 9) ald2_sc1(hs0, rs, hso, stin);
      if constexpr (a == 10) ald2_sc1(hs1, rs, hso, stin + 16384);
    };
    sfor<XW>([&](auto I) { constexpr int i = decltype(I)::value; XLOAD(i); });
    using Q = QS;
    u32x2 pf2 = {0u, 0u};
    const int cgg = (B0 >> 1) + ((lane & 15) >> 1);
    const bool pfact = lane < 10 ? 32 * cgg < 16 * (B0 + k.nblk) : lane < 60;
    const u32 pfo = (FG_PF2 * 16 + (!pfact ? 0 : lane < 10 ? 124 + 54 * (lane & 1) + cgg : 10 * ((lane - 10) % 25) + (80 * c8) / 64 + (lane - 10) / 25)) * 128;
    constexpr int DQ = 4, NBQ = 6;
    u32x4 wb[NBQ];
    f32x16 acc0, acc1, acc2, acc3;
    f32x4 hacc, hacc4;
    u32x4 hw[3], hx[3], h4w[3], h4x[3];
    float rstd = 0.f, hrs = 0.f;
    u32 lb1 = l16 + 65536, lbh = l16 + L_HALO; asm volatile("" : "+v"(lb1), "+v"(lbh));
    u32 hwlo = (w >> 1) * 1024 + (g >> 1) * 3072 + (g & 1) * 512 + (16 * (w & 1) + t16) * 16, hwhi = hwlo + 61440;
    u32 hw4 = 2048 + (g >> 1) * 3072 + (g & 1) * 512 + t16 * 16 + w * 30720, hx4 = l16 + L_HALO + w * 5120;
    asm volatile("" : "+v"(hwlo), "+v"(hwhi), "+v"(hw4), "+v"(hx4));
    const u32 kvrow = L_KST + (16 + 32 * w + tok) * 80 + h * 8;
    const int hn = 16 * w + 4 * g;
    const u32 haddr = L_KST + t16 * 80 + 2 * hn + (hn >= 40 ? (L_VST - L_KST - 80) : 0);
    const u32 haddr4 = L_VST + t16 * 80 + 2 * (64 + 4 * g - 40);
    const bool hzero = (m128 == 0);
    const u32 wo_o = OFF_WO + (c8 * 20 + 5 * w) * 5120;
#define LDSQ(F) (((F) / 40 % 3) * 40960 + ((F) % 40) * 1024)
#define RDW(off) (*(const LAS u32x4*)(lds + ((off) < 65536 ? l16 : lb1) + ((off) & 65535)))
    u32 gp0 = 0;
#define STG_OFF(n0) ((n0) < 40 ? 2 * (n0) : (L_VST - L_KST) + 2 * ((n0) - 40))
    const u32 qrow = L_QST + (32 * w + tok) * 144 + h * 8, qrow2 = L_QST + (32 * w + tok) * 144 + (h ? 72 : 32);
#define Q_ADDR(n0) ((n0) == 96 ? qrow2 : qrow + ((n0) < 96 ? 2 * ((n0) - 80) : 2 * ((n0) - 80) + 32))
#define GRAN(t, ACC, q, RS, STADDR, ZERO) do { \
      if constexpr ((t) == 0) gp0 = cvtpk(mul_s(ACC[4 * (q)], RS), mul_s(ACC[4 * (q) + 1], RS)); \
      if constexpr ((t) == 1) { u32x2 pk_ = {gp0, cvtpk(mul_s(ACC[4 * (q) + 2], RS), mul_s(ACC[4 * (q) + 3], RS))}; if (ZERO) pk_ = u32x2{0u, 0u}; *(LAS u32x2*)(lds + (STADDR)) = pk_; } \
    } while (0)
#define GRAN_MAIN(t, ACC, q, n0) GRAN(t, ACC, q, rstd, ((n0) < 80 ? kvrow + STG_OFF(n0) : Q_ADDR(n0)), false)
#pragma unroll
    for (int F = 0; F < DQ; ++F) wb[F % NBQ] = RDW(LDSQ(F));
    sfor<160>([&](auto FC) {
      constexpr int F = decltype(FC)::value, Fr = F + DQ;
      if constexpr (Q::x_at(F)) XLOAD(F / 3 + XW);
      if constexpr (Q::aux_at(F) >= 0) aux_load(std::integral_constant<int, Q::aux_at(F)>{});
      if constexpr (Q::pf_at(F)) asm volatile("buffer_load_dwordx2 %0, %1, %2, 0 offen sc1" : "=v"(pf2) : "v"(pfo), "s"(rs) : "memory");
      if constexpr (Q::q3_at(F) >= 0) dma_q<3, 0, Q::q3_at(F)>(rs, l16, sQ, w, lds0);
      if constexpr (Q::wo_at(F) >= 0) {
        constexpr int i = Q::wo_at(F), s = i / 5, nb = i % 5;
        if constexpr (i < 24) ald_a_sc1<(i % 4) * 1024>(k.wo_r[s][nb], rs, l16, wo_o + (i / 4) * 4096); else ald_v_sc1<(i % 4) * 1024>(k.wo_r[s][nb], rs, l16, wo_o + (i / 4) * 4096);
      }
      if constexpr (Fr == 40) {
        WAITV(Q::upto(F) - Q::upto(13)); CERT("HALO"); BAR();
        if (tid == 0) flag_set(flags, (FG_PF2 * 16 + k.bid) * 32, k.tag);
      }
      if constexpr (Fr == 80) { WAITL0(); BAR(); }
      if constexpr (Fr == 120) {
        WAITV(Q::upto(F) - Q::upto(89)); CERT("Q3a");
        asm volatile("" : "+v"(pf2));
        if (w == 0 && it == 0 && !__all(!pfact || (pf2.x == (u32)k.tag && pf2.y == (u32)(k.tag >> 32)))) poll_flags(flags, pfo / 4, pfact, lane, k.tag);
        WAITL0(); BAR();
      }
      if constexpr (Fr == 140) { WAITV(Q::upto(F) - Q::upto(104)); CERT("Q3b"); BAR(); }
      if constexpr (Fr < 160) wb[Fr % NBQ] = RDW(LDSQ(Fr));
      if constexpr (F < 120) {
        constexpr int s = F / 3, b = F % 3;
        if constexpr (b == 0 && (s & 1) == 0) waitpin_a2<Q::xwait2(s)>(xf[s], xf[s + 1]);
#define QMMA(ACC) do { if constexpr (s == 0) { if constexpr ((F & 1) == 0) mma32w_0(ACC, wb[F % NBQ], xf[s], wb[(F + 1) % NBQ]); else mma32_0(ACC, wb[F % NBQ], xf[s]); } \
                       else { if constexpr ((F & 1) == 0) mma32w(ACC, wb[F % NBQ], xf[s], wb[(F + 1) % NBQ]); else mma32(ACC, wb[F % NBQ], xf[s]); } } while (0)
        if constexpr (b == 0) QMMA(acc0); else if constexpr (b == 1) QMMA(acc1); else QMMA(acc2);
        if constexpr (F == 62) {
          pin2(sa0); pin2(sa1); pin2(sa2); pin2(sa3);
          float nm;
          stats_finish(red32_add((u2f(sa0.x) + u2f(sa1.x)) + (u2f(sa2.x) + u2f(sa3.x))), red32_add((u2f(sa0.y) + u2f(sa1.y)) + (u2f(sa2.y) + u2f(sa3.y))), nm, rstd);
        }
        if constexpr (F == 110) {
          pin2(hs0); pin2(hs1);
          float nm;
          stats_finish(red16_32_add(u2f(hs0.x) + u2f(hs1.x)), red16_32_add(u2f(hs0.y) + u2f(hs1.y)), nm, hrs);
        }
        if constexpr (F == 113) {
#pragma unroll
          for (int nb = 0; nb < 5; ++nb) pin2(k.xr[nb]);
        }
        if constexpr (b == 1) {
          if constexpr (s == 15) mma16_0(hacc, hw[0], hx[0]);
          if constexpr (s > 15 && s <= 34) mma16(hacc, hw[(s - 15) % 3], hx[(s - 15) % 3]);
          if constexpr (s >= 13 && s <= 32) {
            constexpr int sg = s - 13;
            hw[sg % 3] = *(const LAS u32x4*)(lds + (sg < 10 ? hwlo : hwhi) + (sg % 10) * 6144);
            hx[sg % 3] = *(const LAS u32x4*)(lds + lbh + sg * 1024);
          }
        }
      } else {
        constexpr int s = F - 120;
        if constexpr (s == 0) mma32w_0(acc3, wb[F % NBQ], xf[s], wb[(F + 1) % NBQ]); else if constexpr (s & 1) mma32(acc3, wb[F % NBQ], xf[s]); else mma32w(acc3, wb[F % NBQ], xf[s], wb[(F + 1) % NBQ]);
        constexpr int ms = s - 2, gi = ms / 2, st = ms % 2;
        if constexpr (ms >= 0 && gi < 12) {
          constexpr int b = gi >> 2, q = gi & 3, n0 = 32 * b + 8 * q;
          if constexpr (b == 0) GRAN_MAIN(st, acc0, q, n0); else if constexpr (b == 1) GRAN_MAIN(st, acc1, q, n0); else GRAN_MAIN(st, acc2, q, n0);
        }
        if constexpr (s == 22) {
          const f32x4 p0 = *(const LAS f32x4*)(lds + L_H4 + l16), p1 = *(const LAS f32x4*)(lds + L_H4 + 1024 + l16);
          const f32x4 p2 = *(const LAS f32x4*)(lds + L_H4 + 2048 + l16), p3 = *(const LAS f32x4*)(lds + L_H4 + 3072 + l16);
          hacc4 = (p0 + p1) + (p2 + p3);
        }
        if constexpr (ms >= 0 && gi == 12) GRAN(st, hacc, 0, hrs, haddr, hzero);
        if constexpr (ms >= 0 && gi == 14) GRAN(st, hacc4, 0, hrs, haddr4, hzero);
        if constexpr (s == 33) *(LAS u32x2*)(lds + L_QST + (32 * w + tok) * 144 + (h ? 64 : 40)) = u32x2{0u, 0u};
        if constexpr (s == 35) *(LAS u32x2*)(lds + L_QST + (32 * w + tok) * 144 + (h ? 112 : 48)) = u32x2{0u, 0u};
        if constexpr (s == 37) *(LAS u32x2*)(lds + L_QST + (32 * w + tok) * 144 + (h ? 120 : 56)) = u32x2{0u, 0u};
      }
      if constexpr (F < 120 && F % 3 == 2) {
        constexpr int s = F / 3;
        if constexpr (s == 15) mma16_0(hacc4, h4w[0], h4x[0]);
        if constexpr (s > 15 && s <= 19) mma16(hacc4, h4w[(s - 15) % 3], h4x[(s - 15) % 3]);
        if constexpr (s >= 13 && s <= 17) {
          h4w[(s - 13) % 3] = *(const LAS u32x4*)(lds + hw4 + (s - 13) * 6144);
          h4x[(s - 13) % 3] = *(const LAS u32x4*)(lds + hx4 + (s - 13) * 1024);
        }
        if constexpr (s == 21) *(LAS f32x4*)(lds + L_H4 + w * 1024 + l16) = hacc4;
      }
      SB();
    });
    asm volatile("s_nop 11" : "+v"(acc3));
    GRAN_MAIN(0, acc3, 0, 96); GRAN_MAIN(1, acc3, 0, 96); GRAN_MAIN(0, acc3, 1, 104); GRAN_MAIN(1, acc3, 1, 104); GRAN_MAIN(0, acc3, 2, 112); GRAN_MAIN(1, acc3, 2, 112);
    WAITL0(); BAR();
    {
      const int par = t16 & 1;
      const bool ca = (4 * g + par) > t16, cb = (4 * g + 2 + par) > t16;
      const unsigned long long mca = __ballot(ca), mcb = __ballot(cb), mpar = __ballot(par != 0);
      const bool neg = (m128 == 0) && (w == 0);
      const unsigned long long nga = neg ? mca : 0ull, ngb = neg ? mcb : 0ull;
      const u32 gq = g < 2 ? g : 2;
      const u32 pla = par ? 0x01000c0cu : 0x0c0c0100u, plb = par ? 0x03020c0cu : 0x0c0c0302u;
      const u32 sel0 = ca ? pla : 0x0c0c0c0cu, sel2 = ca ? 0x0c0c0c0cu : pla, sel1 = cb ? plb : 0x0c0c0c0cu, sel3 = cb ? 0x0c0c0c0cu : plb;
      constexpr float cexp = 0.22360679774997896f * 1.4426950408889634f;
      float cx = cexp; asm volatile("" : "+v"(cx));
      const u32 qa = L_QST + (32 * w + t16) * 144 + g * 16, ka = L_KST + (32 * w + t16) * 80 + gq * 16;
      const u32 va = L_VST + (32 * w + 4 * g + (t16 >> 2)) * 80 + (t16 & 3) * 8;
      u32x4 qf[4], k0[4], k1[4];
      u32x4 v0[4], v1[4];
#pragma unroll
      for (int u = 0; u < 4; ++u) {
        const u32 o = (u >> 1) * 16 * 80 + (u & 1) * 32;
        qf[u] = *(const LAS u32x4*)(lds + qa + (u >> 1) * 16 * 144 + (u & 1) * 64);
        k0[u] = *(const LAS u32x4*)(lds + ka + o);
        k1[u] = *(const LAS u32x4*)(lds + ka + o + 16 * 80);
      }
      dma_g<0, 0>(rs, l16, sG, w, lds0);
      f32x4 s0[4], s1[4];
      asm volatile("s_nop 1\n\t"
                   "v_mfma_f32_16x16x32_bf16 %0, %8, %16, 0\n\tv_mfma_f32_16x16x32_bf16 %1, %12, %16, 0\n\t"
                   "v_mfma_f32_16x16x32_bf16 %2, %9, %17, 0\n\tv_mfma_f32_16x16x32_bf16 %3, %13, %17, 0\n\t"
                   "v_mfma_f32_16x16x32_bf16 %4, %10, %18, 0\n\tv_mfma_f32_16x16x32_bf16 %5, %14, %18, 0\n\t"
                   "v_mfma_f32_16x16x32_bf16 %6, %11, %19, 0\n\tv_mfma_f32_16x16x32_bf16 %7, %15, %19, 0"
                   : "=&v"(s0[0]), "=&v"(s1[0]), "=&v"(s0[1]), "=&v"(s1[1]), "=&v"(s0[2]), "=&v"(s1[2]), "=&v"(s0[3]), "=&v"(s1[3])
                   : "v"(k0[0]), "v"(k0[1]), "v"(k0[2]), "v"(k0[3]), "v"(k1[0]), "v"(k1[1]), "v"(k1[2]), "v"(k1[3]), "v"(qf[0]), "v"(qf[1]), "v"(qf[2]), "v"(qf[3]));
#pragma unroll
      for (int u = 0; u < 4; ++u) {
        const u32 o = (u >> 1) * 16 * 80 + (u & 1) * 40;
        const u32x2 t0 = __builtin_bit_cast(u32x2, __builtin_amdgcn_ds_read_tr16_b64_v4i16((LAS s16x4*)(lds + va + o)));
        const u32x2 t1 = __builtin_bit_cast(u32x2, __builtin_amdgcn_ds_read_tr16_b64_v4i16((LAS s16x4*)(lds + va + o + 16 * 80)));
        const u32x2 t2 = __builtin_bit_cast(u32x2, __builtin_amdgcn_ds_read_tr16_b64_v4i16((LAS s16x4*)(lds + va + o + 32)));
        const u32x2 t3 = __builtin_bit_cast(u32x2, __builtin_amdgcn_ds_read_tr16_b64_v4i16((LAS s16x4*)(lds + va + o + 32 + 16 * 80)));
        v0[u] = u32x4{t0.x, t0.y, t1.x, t1.y}; v1[u] = u32x4{t2.x, t2.y, t3.x, t3.y};
      }
      dma_g<0, 1>(rs, l16, sG, w, lds0);
      asm volatile("s_nop 7" : "+v"(s0[0]), "+v"(s1[0]), "+v"(s0[1]), "+v"(s1[1]), "+v"(s0[2]), "+v"(s1[2]), "+v"(s0[3]), "+v"(s1[3]));
      float a[4], b[4], mx[4], sm[4];
#pragma unroll
      for (int u = 0; u < 4; ++u) {
        const float x00 = s0[u][0], x01 = s0[u][1], x02 = s0[u][2], x03 = s0[u][3], x10 = s1[u][0], x11 = s1[u][1], x12 = s1[u][2], x13 = s1[u][3];
        a[u] = sel_s(sel_s(x10, x11, mpar), sel_s(x00, x01, mpar), mca);
        b[u] = sel_s(sel_s(x12, x13, mpar), sel_s(x02, x03, mpar), mcb);
        if (u < 2) { a[u] = sel_s(a[u], -__builtin_inff(), nga); b[u] = sel_s(b[u], -__builtin_inff(), ngb); }
        mx[u] = max_s(a[u], b[u]);
      }
      dma_g<0, 2>(rs, l16, sG, w, lds0);
      {
        auto r01 = __builtin_amdgcn_permlane16_swap(f2u(mx[0]), f2u(mx[1]), false, false), r23 = __builtin_amdgcn_permlane16_swap(f2u(mx[2]), f2u(mx[3]), false, false);
        const float c01 = __builtin_fmaxf(u2f(r01[0]), u2f(r01[1])), c23 = __builtin_fmaxf(u2f(r23[0]), u2f(r23[1]));
        auto rt = __builtin_amdgcn_permlane32_swap(f2u(c01), f2u(c23), false, false);
        const u32 t = f2u(__builtin_fmaxf(u2f(rt[0]), u2f(rt[1])));
        dma_g<0, 3>(rs, l16, sG, w, lds0);
        auto ab = __builtin_amdgcn_permlane32_swap(t, t, false, false);
        auto m01 = __builtin_amdgcn_permlane16_swap(ab[0], ab[0], false, false), m23 = __builtin_amdgcn_permlane16_swap(ab[1], ab[1], false, false);
        mx[0] = u2f(m01[0]); mx[1] = u2f(m01[1]); mx[2] = u2f(m23[0]); mx[3] = u2f(m23[1]);
      }
      dma_g<0, 4>(rs, l16, sG, w, lds0);
#pragma unroll
      for (int u = 0; u < 4; ++u) {
        const float t = -mul_s(mx[u], cx);
        a[u] = __builtin_amdgcn_exp2f(fma_s(a[u], cx, t)); b[u] = __builtin_amdgcn_exp2f(fma_s(b[u], cx, t));
        sm[u] = a[u] + b[u];
      }
      dma_g<1, 0>(rs, l16, sG, w, lds0);
      {
        auto r01 = __builtin_amdgcn_permlane16_swap(f2u(sm[0]), f2u(sm[1]), false, false), r23 = __builtin_amdgcn_permlane16_swap(f2u(sm[2]), f2u(sm[3]), false, false);
        const float c01 = u2f(r01[0]) + u2f(r01[1]), c23 = u2f(r23[0]) + u2f(r23[1]);
        auto rt = __builtin_amdgcn_permlane32_swap(f2u(c01), f2u(c23), false, false);
        const u32 t = f2u(__builtin_amdgcn_rcpf(u2f(rt[0]) + u2f(rt[1])));
        dma_g<1, 1>(rs, l16, sG, w, lds0);
        auto ab = __builtin_amdgcn_permlane32_swap(t, t, false, false);
        auto m01 = __builtin_amdgcn_permlane16_swap(ab[0], ab[0], false, false), m23 = __builtin_amdgcn_permlane16_swap(ab[1], ab[1], false, false);
        sm[0] = u2f(m01[0]); sm[1] = u2f(m01[1]); sm[2] = u2f(m23[0]); sm[3] = u2f(m23[1]);
      }
      dma_g<1, 2>(rs, l16, sG, w, lds0);
      u32x4 pf[4];
#pragma unroll
      for (int u = 0; u < 4; ++u) {
        const float inv = sm[u];
        const u32 pk = cvtpk(a[u] * inv, b[u] * inv);
        pf[u] = u32x4{__builtin_amdgcn_perm(pk, pk, sel0), __builtin_amdgcn_perm(pk, pk, sel1), __builtin_amdgcn_perm(pk, pk, sel2), __builtin_amdgcn_perm(pk, pk, sel3)};
      }
      dma_g<1, 3>(rs, l16, sG, w, lds0);
      f32x4 o0[4], o1[4];
      asm volatile("s_nop 1\n\t"
                   "v_mfma_f32_16x16x32_bf16 %0, %8, %16, 0\n\tv_mfma_f32_16x16x32_bf16 %1, %12, %16, 0\n\t"
                   "v_mfma_f32_16x16x32_bf16 %2, %9, %17, 0\n\tv_mfma_f32_16x16x32_bf16 %3, %13, %17, 0\n\t"
                   "v_mfma_f32_16x16x32_bf16 %4, %10, %18, 0\n\tv_mfma_f32_16x16x32_bf16 %5, %14, %18, 0\n\t"
                   "v_mfma_f32_16x16x32_bf16 %6, %11, %19, 0\n\tv_mfma_f32_16x16x32_bf16 %7, %15, %19, 0\n\ts_nop 7"
                   : "=&v"(o0[0]), "=&v"(o1[0]), "=&v"(o0[1]), "=&v"(o1[1]), "=&v"(o0[2]), "=&v"(o1[2]), "=&v"(o0[3]), "=&v"(o1[3])
                   : "v"(v0[0]), "v"(v0[1]), "v"(v0[2]), "v"(v0[3]), "v"(v1[0]), "v"(v1[1]), "v"(v1[2]), "v"(v1[3]), "v"(pf[0]), "v"(pf[1]), "v"(pf[2]), "v"(pf[3]));
      dma_g<1, 4>(rs, l16, sG, w, lds0);
      const u32 sg = 5 * p + g, sg4 = 5 * p + 4;
      const u32 oa = (sg >> 2) * 1024 + (t16 + 16 * (sg & 3)) * 16, oa4 = (sg4 >> 2) * 1024 + (t16 + 16 * (sg4 & 3)) * 16;
#pragma unroll
      for (int qb = 0; qb < 2; ++qb) {
        const u32 so = ofo + (8 * m128 + 2 * w + qb) * 20480;
        const f32x4 A0 = o0[2 * qb], B0_ = o0[2 * qb + 1], A1 = o1[2 * qb], B1 = o1[2 * qb + 1];
        stg_sc1(u32x4{cvtpk(A0[0], A0[1]), cvtpk(A0[2], A0[3]), cvtpk(B0_[0], B0_[1]), cvtpk(B0_[2], B0_[3])}, rs, oa, so);
        if (g == 0) stg_sc1(u32x4{cvtpk(A1[0], A1[1]), cvtpk(A1[2], A1[3]), cvtpk(B1[0], B1[1]), cvtpk(B1[2], B1[3])}, rs, oa4, so);
      }
    }
    SB();
    WAITV(0); CERT("G0"); CERT("G1");
    WAITL0();
    BAR();
    if (tid == 0) flag_set(flags, ((fgb + FG_O + m128) * 16 + p) * 32, k.tag);
    sfor<5>([&](auto I) { dma_g<2, decltype(I)::value>(rs, l16, sG, w, lds0); });
    sfor<5>([&](auto I) { dma_g<3, decltype(I)::value>(rs, l16, sG, w, lds0); });
  }
  {
    PHASE_IDS();
    if (w == 0) poll_flags(flags, ((fgb + FG_O + (m64 >> 1)) * 16 + (lane & 15)) * 32, lane < 16, lane, k.tag);
    BAR();
#pragma unroll
    for (int s = 0; s < 5; ++s)
#pragma unroll
      for (int nb = 0; nb < 5; ++nb) { if (s * 5 + nb < 24) pin_a(k.wo_r[s][nb]); else pin_v(k.wo_r[s][nb]); }
    u32x4 af[20];
    u32 tbo[4];
#pragma unroll
    for (int tbi = 0; tbi < 4; ++tbi) tbo[tbi] = ofo + ((4 * m64 + ((w + 1 + tbi) & 3)) * 20 + 5 * w) * 1024;
    constexpr int OW = 10;
#define OLOAD(q) ald_v_sc1<((q) % 5 % 4) * 1024>(af[q], rs, l16, tbo[(q) / 5] + ((q) % 5 / 4) * 4096)
    sfor<OW>([&](auto I) { constexpr int q = decltype(I)::value; OLOAD(q); });
    f32x4 acc[2][5];
    f32x4 part[3][5];
    sfor<20>([&](auto QC) {
      constexpr int q = decltype(QC)::value, tbi = q / 5, s = q % 5;
      if constexpr (q + OW < 20) OLOAD(q + OW);
      waitpin_v<(q < 10 ? OW : (19 - q))>(af[q]);
#pragma unroll
      for (int nb = 0; nb < 5; ++nb) acc[tbi & 1][nb] = mfma16(k.wo_r[s][nb], af[q], s == 0 ? f32x4{0.f, 0.f, 0.f, 0.f} : acc[tbi & 1][nb]);
      if constexpr (tbi > 0 && s < 3) {
        const u32 tbp = (w + tbi) & 3;
        *(LAS f32x4*)(lds + L_RED + ((tbp * 3 + (3 - tbi)) * 5 + 2 * s) * 1024 + l16) = acc[(tbi - 1) & 1][2 * s];
        if constexpr (2 * s + 1 < 5) *(LAS f32x4*)(lds + L_RED + ((tbp * 3 + (3 - tbi)) * 5 + 2 * s + 1) * 1024 + l16) = acc[(tbi - 1) & 1][2 * s + 1];
      }
      if constexpr (q == 18) {
        WAITL0(); BAR();
#pragma unroll
        for (int sl = 0; sl < 3; ++sl)
#pragma unroll
          for (int nb = 0; nb < 5; ++nb) part[sl][nb] = *(const LAS f32x4*)(lds + L_RED + ((w * 3 + sl) * 5 + nb) * 1024 + l16);
      }
      SB();
    });
    float s1 = 0.f, s2 = 0.f;
#pragma unroll
    for (int nb = 0; nb < 5; ++nb) {
      const f32x4 v = (acc[1][nb] + part[0][nb]) + (part[1][nb] + part[2][nb]);
      const u32 a = cvtpk(bflo(k.xr[nb].x) + v[0], bfhi(k.xr[nb].x) + v[1]), b = cvtpk(bflo(k.xr[nb].y) + v[2], bfhi(k.xr[nb].y) + v[3]);
      k.xr[nb] = u32x2{a, b};
    }
    const u32 token = 64 * m64 + 16 * w + t16;
    const u32 xo = ((token >> 5) * 40 + 5 * c8) * 1024 + ((token & 31) + 32 * (g >> 1)) * 16;
#pragma unroll
    for (int pr = 0; pr < 3; ++pr) {
      const int A = 2 * pr, Bn = pr < 2 ? 2 * pr + 1 : 4;
      auto r0 = __builtin_amdgcn_permlane16_swap(k.xr[A].x, k.xr[Bn].x, false, false);
      auto r1 = __builtin_amdgcn_permlane16_swap(k.xr[A].y, k.xr[Bn].y, false, false);
      const u32x4 v = {r0[0], r1[0], r0[1], r1[1]};
      if (pr < 2) stg_sc1(v, rs, xo + (A + (g & 1)) * 1024, x1o);
      else if ((g & 1) == 0) stg_sc1(v, rs, xo + 4 * 1024, x1o);
    }
#pragma unroll
    for (int nb = 0; nb < 5; ++nb) {
      const float a = bflo(k.xr[nb].x), b = bfhi(k.xr[nb].x), c = bflo(k.xr[nb].y), d = bfhi(k.xr[nb].y);
      s1 += (a + b) + (c + d); s2 += (a * a + b * b) + (c * c + d * d);
    }
    s1 = red16_32_add(s1); s2 = red16_32_add(s2);
    if (g == 0) stg2_sc1(u32x2{f2u(s1), f2u(s2)}, rs, (c8 * 2048 + token) * 8, st1o);
    SB();
    WAITV(0); CERT("G2"); CERT("G3");
    WAITL0();
    BAR();
    if (tid == 0) flag_set(flags, ((fgb + FG_X1 + m64) * 16 + c8) * 32, k.tag);
  }
  {
    PHASE_IDS();
    const int nblk = k.nblk;
    const int ds0 = w < 2 ? 14 * w : 28 + 13 * (w - 2);
    const u32 sG = OFF_WG + p * 286720 + w * 5120;
    Polls pl = {0ull, 0ull, 0ull};
    if (w != 0) { dma_g3<4>(rs, l16, sG - w * 5120, w, lds0); dma_g3<5>(rs, l16, sG - w * 5120, w, lds0); dma_g3<6>(rs, l16, sG - w * 5120, w, lds0); }
    if (w == 0) poll_flags_p(flags, ((fgb + FG_X1 + 2 * m128 + ((lane >> 3) & 1)) * 16 + (lane & 7)) * 32, lane < 16, lane, k.tag, pl);
    BAR();
    u32x2 sa0, sa1, sa2, sa3;
    u32x4 xf[40];
    const u32 xbase = x1o + (4 * m128 + w) * 40960;
    const u32 sso = (4 * h * 2048 + 128 * m128 + 32 * w + tok) * 8;
    sfor<XW>([&](auto I) { constexpr int i = decltype(I)::value; XLOAD(i); });
    constexpr int NWD = GS::NWD;
    float r2 = 0.f;
    constexpr int DG = 4, NBG = 6;
    u32x4 wb[NBG];
    f32x16 aA0, aA1, aA2, aB0, aB1;
    u32 lb1 = l16 + 65536, lb2 = l16 + 131072; asm volatile("" : "+v"(lb1), "+v"(lb2));
    const u32 hbase = (8 * m128 + 2 * w + (tok >> 4)) * 54 * 1024 + ((tok & 15) + 16 * h) * 16;
    const u32 hlast = nblk == 7 ? hbase : 0x7fff0000u;
    const u32 wd_o = OFF_WD + (c8 * 54 + ds0) * 5120;
#define LDSG(F) ((((F) / 20) % 7) * 20480 + ((F) % 20) * 1024)
#define RDG(off) (*(const LAS u32x4*)(lds + ((off) < 65536 ? l16 : (off) < 131072 ? lb1 : lb2) + ((off) & 65535)))
    float hv0 = 0.f;
    u32 hp[4];
#define GU_STAGE(ACC, idx, t) do { \
      if constexpr ((t) < 8) { const float hv_ = mul_s(mul_s(ACC[t], r2), ACC[8 + (t)]); if constexpr ((t) & 1) hp[(t) >> 1] = cvtpk(hv0, hv_); else hv0 = hv_; } \
      if constexpr ((t) == 8) { const u32 fb_ = B0 + (idx); stg_sc1(u32x4{hp[0], hp[1], hp[2], hp[3]}, rs, ((idx) == 6 ? hlast : hbase) + (fb_ & 1) * 512, hfo + (fb_ >> 1) * 1024); } \
    } while (0)
    auto wd_load = [&](auto OC) {
      constexpr int i = decltype(OC)::value, s = i / 5, nb = i % 5;
      if constexpr (i < 24 || i >= 50) ald_a_sc1<(i % 4) * 1024>(k.wd_r[s][nb], rs, l16, wd_o + (i / 4) * 4096);
      else ald_v_sc1<(i % 4) * 1024>(k.wd_r[s][nb], rs, l16, wd_o + (i / 4) * 4096);
    };
#pragma unroll
    for (int F = 0; F < DG; ++F) wb[F % NBG] = RDG(LDSG(F));
    sfor<280>([&](auto FC) {
      constexpr int F = decltype(FC)::value, Fr = F + DG;
      if constexpr (GS::x_at(F)) XLOAD(F / 3 + XW);
      if constexpr (GS::op(F) >= 0 && GS::op(F) < 1000) dma_g<GS::op(F) / 8, GS::op(F) % 8>(rs, l16, sG, w, lds0);
      if constexpr (GS::op(F) >= 1000 && GS::op(F) < 2000) wd_load(std::integral_constant<int, GS::op(F) - 1000>{});
      if constexpr (GS::op(F) == 3000) ald2_sc1(sa0, rs, sso, st1o);
      if constexpr (GS::op(F) == 3001) ald2_sc1(sa1, rs, sso, st1o + 16384);
      if constexpr (GS::op(F) == 3002) ald2_sc1(sa2, rs, sso, st1o + 32768);
      if constexpr (GS::op(F) == 3003) ald2_sc1(sa3, rs, sso, st1o + 49152);
      if constexpr (Fr == 40) { WAITL0(); BAR(); }
      if constexpr (Fr == 80) { CERT("G4"); CERT("G5"); WAITL0(); BAR(); }
      if constexpr (Fr == 120) { WAITV(GS::upto(F) - GS::upto(GS::slot(7 * 8 + 4))); CERT("G6"); CERT("G7"); WAITL0(); BAR(); }
      if constexpr (Fr == 160) { WAITV(GS::upto(F) - GS::upto(GS::slot(9 * 8 + 4))); CERT("G8"); CERT("G9"); WAITL0(); BAR(); }
      if constexpr (Fr == 200) { WAITV(GS::upto(F) - GS::upto(GS::slot(11 * 8 + 4))); CERT("G10"); CERT("G11"); BAR(); }
      if constexpr (Fr == 240) { WAITV(GS::upto(F) - GS::upto(GS::slot(13 * 8 + 4))); CERT("G12"); CERT("G13"); BAR(); }
      if constexpr (Fr < 280) wb[Fr % NBG] = RDG(LDSG(Fr));
      constexpr int s = F < 120 ? F / 3 : F < 200 ? (F - 120) / 2 : (F - 200) / 2, b = F < 120 ? F % 3 : F & 1;
      if constexpr (F < 120 && b == 0 && (s & 1) == 0) waitpin_a2<GS::xwait2(s)>(xf[s], xf[s + 1]);
#define GMMA(ACC) do { if constexpr (s == 0) { if constexpr ((F & 1) == 0) mma32w_0(ACC, wb[F % NBG], xf[s], wb[(F + 1) % NBG]); else mma32_0(ACC, wb[F % NBG], xf[s]); } \
                       else { if constexpr ((F & 1) == 0) mma32w(ACC, wb[F % NBG], xf[s], wb[(F + 1) % NBG]); else mma32(ACC, wb[F % NBG], xf[s]); } } while (0)
      if constexpr (F < 120) { if constexpr (b == 0) GMMA(aA0); else if constexpr (b == 1) GMMA(aA1); else GMMA(aA2); }
      else if constexpr (F < 200) { if constexpr (b == 0) GMMA(aB0); else GMMA(aB1); }
      else { if constexpr (b == 0) GMMA(aA0); else GMMA(aA1); }
      if constexpr (F == 3) poll_pin(pl);
      if constexpr (F == 62) {
        pin2(sa0); pin2(sa1); pin2(sa2); pin2(sa3);
        float rstd, nm;
        stats_finish(red32_add((u2f(sa0.x) + u2f(sa1.x)) + (u2f(sa2.x) + u2f(sa3.x))), red32_add((u2f(sa0.y) + u2f(sa1.y)) + (u2f(sa2.y) + u2f(sa3.y))), nm, rstd);
        r2 = rstd * rstd;
      }
      if constexpr ((F & 1) && F >= 123 && F <= 139) GU_STAGE(aA0, 0, (F - 123) / 2);
      if constexpr ((F & 1) && F >= 141 && F <= 157) GU_STAGE(aA1, 1, (F - 141) / 2);
      if constexpr ((F & 1) && F >= 159 && F <= 175) GU_STAGE(aA2, 2, (F - 159) / 2);
      if constexpr ((F & 1) && F >= 203 && F <= 219) GU_STAGE(aB0, 3, (F - 203) / 2);
      if constexpr ((F & 1) && F >= 221 && F <= 237) GU_STAGE(aB1, 4, (F - 221) / 2);
      SB();
    });
    asm volatile("s_nop 11" : "+v"(aA0), "+v"(aA1));
    sfor<9>([&](auto T) { GU_STAGE(aA0, 5, decltype(T)::value); });
    sfor<9>([&](auto T) { GU_STAGE(aA1, 6, decltype(T)::value); });
    SB();
    WAITV(0);
    WAITL0();
    BAR();
    if (tid == 0) flag_set(flags, ((fgb + FG_H + m128) * 16 + p) * 32, k.tag);
    {
      const u32 wd_o = OFF_WD + (c8 * 54 + ds0) * 5120;
      const u32 l13 = w < 2 ? l16 : 0x7fff0000u;
      static_assert(GS::NWD == 65, "the fragments of step 13");
      sfor<70 - GS::NWD>([&](auto I) { constexpr int i = GS::NWD + decltype(I)::value; ald_a_sc1<(i % 4) * 1024>(k.wd_r[i / 5][i % 5], rs, l13, wd_o + (i / 4) * 4096); });
    }
  }
  {
    PHASE_IDS();
    const int ds0 = w < 2 ? 14 * w : 28 + 13 * (w - 2);
    const u32 sQ = OFF_WQ + p * 163840;
    if (w == 0) poll_flags(flags, ((fgb + FG_H + (m64 >> 1)) * 16 + (lane & 15)) * 32, lane < 16, lane, k.tag);
    else WAITV(0);
    BAR();
    sfor<70>([&](auto I) { constexpr int i = decltype(I)::value; if constexpr (i < 24 || i >= 50) pin_a(k.wd_r[i / 5][i % 5]); else pin_v(k.wd_r[i / 5][i % 5]); });
    constexpr int DH = 12;
    u32x4 af[DH];
    const bool s13 = w < 2;
    u32 tbo[4];
#pragma unroll
    for (int tbi = 0; tbi < 4; ++tbi) tbo[tbi] = hfo + ((4 * m64 + ((w + 1 + tbi) & 3)) * 54 + ds0) * 1024;
    const u32 l13 = s13 ? l16 : 0x7fff0000u;
#define HLOAD(q) ald_a_sc1<((q) % 14 % 4) * 1024>(af[(q) % DH], rs, ((q) % 14 == 13 ? l13 : l16), tbo[(q) / 14] + ((q) % 14 / 4) * 4096)
    sfor<DH>([&](auto I) { constexpr int q = decltype(I)::value; HLOAD(q); });
    const rsrc_t rsq = it == 0 ? rs : k.rsnull;
    f32x4 acc[2][5];
    f32x4 part[3][5];
    sfor<56>([&](auto QC) {
      constexpr int q = decltype(QC)::value, tbi = q / 14, s = q % 14;
      constexpr int yf = (q + DH - 1 < 55 ? q + DH - 1 : 55) - q;
      constexpr int nd = q - 44;
      constexpr int yd = (nd > 0) ? (nd + (nd < 8 ? nd : 8)) : 0;
      waitpin_a<yf + yd>(af[q % DH]);
      if constexpr (s == 0) {
#pragma unroll
        for (int nb = 0; nb < 5; ++nb) acc[tbi & 1][nb] = mfma16(k.wd_r[s][nb], af[q % DH], f32x4{0.f, 0.f, 0.f, 0.f});
      } else if (s < 13 || s13) {
#pragma unroll
        for (int nb = 0; nb < 5; ++nb) acc[tbi & 1][nb] = mfma16(k.wd_r[s][nb], af[q % DH], acc[tbi & 1][nb]);
      }
      if constexpr (tbi > 0 && s >= 1 && s < 6) {
        const u32 tbp = (w + tbi) & 3;
        *(LAS f32x4*)(lds + L_RED + ((tbp * 3 + (3 - tbi)) * 5 + (s - 1)) * 1024 + l16) = acc[(tbi - 1) & 1][s - 1];
      }
      if constexpr (q == 50) {
        WAITL0(); BAR();
#pragma unroll
        for (int sl = 0; sl < 3; ++sl)
#pragma unroll
          for (int nb = 0; nb < 5; ++nb) part[sl][nb] = *(const LAS f32x4*)(lds + L_RED + ((w * 3 + sl) * 5 + nb) * 1024 + l16);
      }
      SB();
      if constexpr (q + DH < 56) HLOAD(q + DH);
      if constexpr (q >= 44) {
        constexpr int j0 = nd + (nd < 8 ? nd : 8);
        if constexpr (j0 < 10) dma_q<0, 0, j0>(rsq, l16, sQ, w, lds0); else dma_q<1, 1, j0 - 10>(rsq, l16, sQ, w, lds0);
        if constexpr (nd < 8) {
          constexpr int j1 = j0 + 1;
          if constexpr (j1 < 10) dma_q<0, 0, j1>(rsq, l16, sQ, w, lds0); else dma_q<1, 1, j1 - 10>(rsq, l16, sQ, w, lds0);
        }
      }
      SB();
    });
#pragma unroll
    for (int nb = 0; nb < 5; ++nb) {
      const f32x4 v = (acc[1][nb] + part[0][nb]) + (part[1][nb] + part[2][nb]);
      const u32 a = cvtpk(bflo(k.xr[nb].x) + v[0], bfhi(k.xr[nb].x) + v[1]), b = cvtpk(bflo(k.xr[nb].y) + v[2], bfhi(k.xr[nb].y) + v[3]);
      k.xr[nb] = u32x2{a, b};
    }
    const u32 token = 64 * m64 + 16 * w + t16;
    if (it == 0) {
      float s1 = 0.f, s2 = 0.f;
      const u32 xo = ((token >> 5) * 40 + 5 * c8) * 1024 + ((token & 31) + 32 * (g >> 1)) * 16;
#pragma unroll
      for (int pr = 0; pr < 3; ++pr) {
        const int A = 2 * pr, Bn = pr < 2 ? 2 * pr + 1 : 4;
        auto r0 = __builtin_amdgcn_permlane16_swap(k.xr[A].x, k.xr[Bn].x, false, false);
        auto r1 = __builtin_amdgcn_permlane16_swap(k.xr[A].y, k.xr[Bn].y, false, false);
        const u32x4 v = {r0[0], r1[0], r0[1], r1[1]};
        if (pr < 2) stg_sc1(v, rs, xo + (A + (g & 1)) * 1024, OFF_XF + 2 * SZ_XF);
        else if ((g & 1) == 0) stg_sc1(v, rs, xo + 4 * 1024, OFF_XF + 2 * SZ_XF);
      }
#pragma unroll
      for (int nb = 0; nb < 5; ++nb) {
        const float a = bflo(k.xr[nb].x), b = bfhi(k.xr[nb].x), c = bflo(k.xr[nb].y), d = bfhi(k.xr[nb].y);
        s1 += (a + b) + (c + d); s2 += (a * a + b * b) + (c * c + d * d);
      }
      s1 = red16_32_add(s1); s2 = red16_32_add(s2);
      if (g == 0) stg2_sc1(u32x2{f2u(s1), f2u(s2)}, rs, (c8 * 2048 + token) * 8, OFF_ST + 2 * SZ_ST);
      SB();
      WAITV(0);
      WAITL0();
      BAR();
      if (tid == 0) flag_set(flags, ((FG_X2 + m64) * 16 + c8) * 32, k.tag);
      sfor<10>([&](auto I) { dma_q<2, 2, decltype(I)::value>(rs, l16, sQ, w, lds0); });
    } else {
      const u32 oo = token * 1280 + (80 * c8 + 8 * (g >> 1)) * 2;
#pragma unroll
      for (int pr = 0; pr < 3; ++pr) {
        const int A = 2 * pr, Bn = pr < 2 ? 2 * pr + 1 : 4;
        auto r0 = __builtin_amdgcn_permlane16_swap(k.xr[A].x, k.xr[Bn].x, false, false);
        auto r1 = __builtin_amdgcn_permlane16_swap(k.xr[A].y, k.xr[Bn].y, false, false);
        const u32x4 v = {r0[0], r1[0], r0[1], r1[1]};
        if (pr < 2) __builtin_amdgcn_raw_buffer_store_b128(v, k.rso, oo + (A + (g & 1)) * 32, 0, 0);
        else if ((g & 1) == 0) __builtin_amdgcn_raw_buffer_store_b128(v, k.rso, oo + 4 * 32, 0, 0);
      }
    }
  }
}

DI void stw_sc1(u32x4 v, rsrc_t rs, u32 off) { __builtin_amdgcn_raw_buffer_store_b128(v, rs, off, 0, AUX_SC1); }
DI float dot2ones(u32 v, float c) { return __builtin_amdgcn_fdot2_f32_bf16(__builtin_bit_cast(bf16x2, v), __builtin_bit_cast(bf16x2, 0x3f803f80u), c, false); }
DI void setup(K& k, int u, const u16* x0, const u16* wqkv, const u16* wo, const u16* wg, const u16* wu, const u16* wd) {
  const int l = k.lane, w = k.w;
  const rsrc_t rs = k.rs;
  LAS char* const lds = k.lds;
  const u32 lds0 = k.lds0;
  const int type = u < 60 ? 0 : u < 124 ? 3 : u < 178 ? 1 : u < 232 ? 2 : u < 252 ? 4 : 5;
  const int idx = u < 60 ? u : u < 124 ? u - 60 : u < 178 ? u - 124 : u < 232 ? u - 178 : u - 232;
  const u16* src = type == 0 ? wqkv : type == 1 ? wg : type == 2 ? wu : type == 3 ? x0 : wo;
  const u32 pitch = type == 0 ? 3840 : type <= 2 ? 3456 : 1280;
  const u32 nrec = type == 0 ? 640 * 3840 : type <= 2 ? 640 * 3456 : type == 3 ? 2048 * 1280 : type == 4 ? 640 * 1280 : 0;
  const rsrc_t rsrcA = __builtin_amdgcn_make_buffer_rsrc((void*)src, 0, nrec, 0x00020000);
  const rsrc_t rsrcD = __builtin_amdgcn_make_buffer_rsrc((void*)wd, 0, 1728 * 1280, 0x00020000);
  if (type == 1 || type == 2) __builtin_amdgcn_s_sleep(24);
  {
    const u32 voW = (16 * w + (l >> 2)) * pitch + idx * 64 + (l & 3) * 16;
    const u32 voO = ((idx & 1) * 320 + 8 * w + (l >> 3)) * 1280 + (idx >> 1) * 128 + (l & 7) * 16;
    const u32 vo = type == 4 ? voO : voW, step = type == 4 ? 32 * 1280 : 64 * pitch;
#pragma unroll
    for (int j = 0; j < 10; ++j) {
      const u32 cidx = 64 * (4 * j + w) + l, row = (cidx * 52429u) >> 22, cc = cidx - row * 80;
      const u32 vx = (32 * idx + row) * 1280 + ((cc ^ (row & 7)) * 16);
      const u32 v = type == 3 ? vx : vo + j * step;
      DMA_F("SA", 0, "", rsrcA, v, 0u, lds0 + (4 * j + w) * 1024);
    }
  }
  const int hh = l >> 5;
  u32 dbase = 0; int dstep = 0;
  {
    const int c = 32 * idx + (l & 31);
    if (type == 0) {
      const int sec = c / 640, cc = c % 640, p = cc / 40, r40 = cc % 40;
      const int n = (sec == 1 ? 0 : sec == 2 ? 40 : 80) + r40;
      const int b = n >> 5, nl = n & 31;
      dbase = OFF_WQ + p * 163840 + (b < 3 ? b * 1024 : 122880) + nl * 16;
      dstep = b < 3 ? 3072 : 1024;
    } else if (type <= 2) {
      const int fb = c >> 4, nl = (c & 15) + 16 * (type - 1);
      const int half = fb / 54, rem = fb % 54;
      const int sl_ = 8 * half + (rem < 42 ? rem / 7 : 6 + (rem - 42) / 6), ix = rem < 42 ? rem % 7 : (rem - 42) % 6;
      dbase = OFF_WG + sl_ * 286720 + (ix < 3 ? ix * 1024 : ix < 5 ? 122880 + (ix - 3) * 1024 : 204800 + (ix - 5) * 1024) + nl * 16;
      dstep = ix < 3 ? 3072 : 2048;
    }
  }
  asm volatile("" : "+v"(dbase), "+v"(dstep));
  u64 ep;
  asm volatile("global_load_dwordx2 %0, %1, off sc1" : "=v"(ep) : "v"(k.flags + FG_EPOCH * 512) : "memory");
  WAITV(0);
  asm volatile("" : "+v"(ep));
  BAR();
  {
    const u64 t = ep * 0x9E3779B97F4A7C15ull + 0xD1B54A32D192ED03ull;
    k.tag = (u64)(u32)__builtin_amdgcn_readfirstlane((u32)t) | ((u64)(u32)__builtin_amdgcn_readfirstlane((u32)(t >> 32)) << 32);
  }
  const int gam = l >> 4, q = (l & 15) >> 2, pp = l & 3;
  if (type <= 2) {
    const u32 ra = ((gam >> 1) * 8 + q) * 64 + (gam & 1) * 32 + pp * 8;
    u32x4 sl[10];
#pragma unroll
    for (int j = 0; j < 10; ++j) {
      const u32 o = ra + (4 * j + w) * 1024;
      const u32x2 a = __builtin_bit_cast(u32x2, __builtin_amdgcn_ds_read_tr16_b64_v4i16((LAS s16x4*)(lds + o)));
      const u32x2 b2 = __builtin_bit_cast(u32x2, __builtin_amdgcn_ds_read_tr16_b64_v4i16((LAS s16x4*)(lds + o + 256)));
      sl[j] = u32x4{a.x, a.y, b2.x, b2.y};
    }
    float c0 = 0.f, c1 = 0.f;
#pragma unroll
    for (int j = 0; j < 10; ++j) { c0 = dot2ones(sl[j][0], c0); c1 = dot2ones(sl[j][1], c1); c0 = dot2ones(sl[j][2], c0); c1 = dot2ones(sl[j][3], c1); }
    const float cs = red32_add(c0 + c1);
    LAS float* red = (LAS float*)(lds + 81920);
    if (l < 32) red[w * 32 + l] = cs;
    WAITL0(); BAR();
    const float mean = ((red[l & 31] + red[32 + (l & 31)]) + (red[64 + (l & 31)] + red[96 + (l & 31)])) * (1.0f / 640.0f);
#pragma unroll
    for (int j = 0; j < 10; ++j) {
      u32x4 v;
#pragma unroll
      for (int e = 0; e < 4; ++e) v[e] = cvtpk(bflo(sl[j][e]) - mean, bfhi(sl[j][e]) - mean);
      stw_sc1(v, rs, dbase + (4 * j + w) * dstep + hh * 512);
    }
  } else if (type == 3) {
    const int tok = l & 31, h = l >> 5;
    u32x4 sl[10];
#pragma unroll
    for (int i = 0; i < 10; ++i) sl[i] = *(const LAS u32x4*)(lds + tok * 1280 + (((2 * (10 * w + i) + h) ^ (tok & 7)) * 16));
    float s1 = 0.f, s2 = 0.f;
#pragma unroll
    for (int i = 0; i < 10; ++i) {
      stw_sc1(sl[i], rs, OFF_XF + (idx * 40 + 10 * w + i) * 1024 + l * 16);
#pragma unroll
      for (int e = 0; e < 4; ++e) { s1 = dot2ones(sl[i][e], s1); const float a_ = bflo(sl[i][e]), b_ = bfhi(sl[i][e]); s2 += a_ * a_ + b_ * b_; }
    }
    s1 = red32_add(s1); s2 = red32_add(s2);
    if (l < 32) {
      stg2_sc1(u32x2{f2u(s1), f2u(s2)}, rs, (w * 2048 + 32 * idx + tok) * 8, OFF_ST);
      stg2_sc1(u32x2{0u, 0u}, rs, ((w + 4) * 2048 + 32 * idx + tok) * 8, OFF_ST);
    }
  } else if (type == 4) {
    const int cg = idx >> 1, kk = idx & 1, c = 64 * cg + l, c8 = c / 80, nb = (c % 80) >> 4, n = c & 15;
    const u32 dbase = OFF_WO + c8 * 20 * 5120 + nb * 1024 + n * 16;
#pragma unroll
    for (int j = 0; j < 10; ++j) {
      const int o = 4 * j + w, pl = o / 5, ii = o % 5, sg = 40 * kk + o, r0 = 40 * pl + 4 * ii + q;
      const u32x2 a = __builtin_bit_cast(u32x2, __builtin_amdgcn_ds_read_tr16_b64_v4i16((LAS s16x4*)(lds + r0 * 128 + gam * 32 + pp * 8)));
      const u32x2 b2 = __builtin_bit_cast(u32x2, __builtin_amdgcn_ds_read_tr16_b64_v4i16((LAS s16x4*)(lds + (r0 + 20) * 128 + gam * 32 + pp * 8)));
      stw_sc1(u32x4{a.x, a.y, b2.x, b2.y}, rs, dbase + (sg >> 2) * 5120 + (sg & 3) * 256);
    }
  }
  const bool crit = type == 0 || type == 3 || type == 4;
  {
#pragma unroll
    for (int t = 0; t < 3; ++t) {
      const u32 ds_ = u / 10 + 25 * t;
      const u32 v = (u < 250 && ds_ < 54) ? (ds_ * 32 + 8 * w + (l >> 3)) * 1280 + (u % 10) * 128 + (l & 7) * 16 : 0x7fffffffu;
      DMA_F("SB", 0, "", rsrcD, v, 0u, lds0 + 40960 + t * 4096 + w * 1024);
    }
  }
  if (crit) { WAITV(3); }
  WAITL0();
  BAR();
  if (crit && k.tid == 0) flag_set(k.flags, (FG_PF1 * 16 + u) * 32, k.tag);
  WAITV(0); BAR();
#pragma unroll
  for (int t = 0; t < 3; ++t) {
    const u32 s_ = u / 10 + 25 * t;
    if (u < 250 && s_ < 54) {
      const u32 cg = u % 10, c = 64 * cg + l, c8 = c / 80, nb = (c % 80) >> 4, n = c & 15;
      const u32 r0 = 16 * (w >> 1) + 4 * (w & 1) + q;
      const u32x2 a = __builtin_bit_cast(u32x2, __builtin_amdgcn_ds_read_tr16_b64_v4i16((LAS s16x4*)(lds + 40960 + t * 4096 + r0 * 128 + gam * 32 + pp * 8)));
      const u32x2 b2 = __builtin_bit_cast(u32x2, __builtin_amdgcn_ds_read_tr16_b64_v4i16((LAS s16x4*)(lds + 40960 + t * 4096 + (r0 + 8) * 128 + gam * 32 + pp * 8)));
      stw_sc1(u32x4{a.x, a.y, b2.x, b2.y}, rs, OFF_WD + (c8 * 54 + s_) * 5120 + nb * 1024 + n * 16 + w * 256);
    }
  }
  WAITL0();
  if (w == 0) {
    u32 fo; bool act = true;
    if (l < 6) fo = 20 * (l >> 1) + (40 * k.p) / 32 + (l & 1);
    else if (l < 11) { const int rb = 4 * k.m128 - 1 + (l - 6); act = rb >= 0; fo = 60 + rb; }
    else if (l < 15) fo = 232 + 2 * ((80 * k.c8) / 64 + ((l - 11) >> 1)) + ((l - 11) & 1);
    else { act = false; fo = 0; }
    poll_flags(k.flags, (FG_PF1 * 16 + (act ? fo : 0)) * 32, act, l, k.tag);
  }
  BAR();
}

__global__ __launch_bounds__(256, 1) void main_kernel(const u16* __restrict__ x0, const u16* __restrict__ wqkv, const u16* __restrict__ wo, const u16* __restrict__ wg,
                                                      const u16* __restrict__ wu, const u16* __restrict__ wd, char* __restrict__ ws, u16* __restrict__ out) {
  extern __shared__ __attribute__((aligned(16))) char smem[];
  K k;
  k.lds = (LAS char*)smem;
  k.lds0 = (u32)(size_t)k.lds;
  k.tid = threadIdx.x; k.lane = k.tid & 63; k.w = __builtin_amdgcn_readfirstlane(k.tid >> 6);
  k.rs = __builtin_amdgcn_make_buffer_rsrc(ws, 0, WS_USED, 0x00020000);
  k.rso = __builtin_amdgcn_make_buffer_rsrc(out, 0, 2048 * 640 * 2, 0x00020000);
  k.rsnull = __builtin_amdgcn_make_buffer_rsrc(ws, 0, 0, 0x00020000);
  k.flags = (GAS u32*)ws;
  const int bid = blockIdx.x, xc = bid & 7, j = bid >> 3, mg = xc >> 1, half = xc & 1;
  k.m128 = 4 * mg + (j >> 3); k.p = 8 * half + (j & 7);
  k.m64 = 8 * mg + (j >> 2); k.c8 = 4 * half + (j & 3);
  const int p7 = j & 7;
  k.B0 = 54 * half + (p7 < 6 ? 7 * p7 : 42 + 6 * (p7 - 6));
  k.nblk = p7 < 6 ? 7 : 6;
  k.bid = bid; k.it = 0;
  setup(k, bid, x0, wqkv, wo, wg, wu, wd);
  {
    PHASE_IDS();
    const u32 sQ = OFF_WQ + p * 163840;
    sfor<10>([&](auto I) { dma_q<0, 0, decltype(I)::value>(rs, l16, sQ, w, lds0); });
    sfor<10>([&](auto I) { dma_q<1, 1, decltype(I)::value>(rs, l16, sQ, w, lds0); });
    sfor<10>([&](auto I) { dma_q<2, 2, decltype(I)::value>(rs, l16, sQ, w, lds0); });
    const u32 token = 64 * m64 + 16 * w + t16;
    const u32 o = ((token >> 5) * 40 + 5 * c8) * 1024 + ((token & 31) + 32 * (g >> 1)) * 16 + (g & 1) * 8;
#pragma unroll
    for (int nb = 0; nb < 5; ++nb) ald2_sc1(k.xr[nb], rs, o, OFF_XF + nb * 1024);
  }
#pragma nounroll
  for (int it = 0; it < 2; ++it) { k.it = it; iteration(k); }
  if (bid == 0 && k.w == 0) {
#pragma unroll
    for (int r = 0; r < 4; ++r) poll_flags(k.flags, (FG_PF2 * 16 + 64 * r + k.lane) * 32, true, k.lane, k.tag);
    if (k.lane == 0) __hip_atomic_fetch_add((GAS u64*)(k.flags + FG_EPOCH * 512), 1ull, RLX_AGENT);
  }
}

extern "C" void kernel_launch(void* const* d_in, const int* in_sizes, int n_in, void* d_out, int out_size, void* d_ws, size_t ws_size,
                              hipStream_t stream) {
  const u16* x0 = (const u16*)d_in[0];
  const u16* wqkv = (const u16*)d_in[1];
  const u16* wo = (const u16*)d_in[2];
  const u16* wg = (const u16*)d_in[4];
  const u16* wu = (const u16*)d_in[5];
  const u16* wd = (const u16*)d_in[6];
  (void)hipFuncSetAttribute((const void*)main_kernel, hipFuncAttributeMaxDynamicSharedMemorySize, LDS_BYTES);
  main_kernel<<<256, 256, LDS_BYTES, stream>>>(x0, wqkv, wo, wg, wu, wd, (char*)d_ws, (u16*)d_out);
}
```
